```python
import torch

def _softmax(x, axis=-1):
    return torch.softmax(x, dim=axis)
def _gelu(x):
    return torch.nn.functional.gelu(x, approximate="tanh")
def _silu(x):
    return torch.nn.functional.silu(x)
def _sigmoid(x):
    return torch.sigmoid(x)
def _layernorm(x, axis=-1, eps=1e-5):
    m = x.mean(dim=axis, keepdim=True)
    v = x.var(dim=axis, keepdim=True, unbiased=False)
    return (x - m) / torch.sqrt(v + eps)
def _scatter_add(tgt, idx, src, axis=0):
    return tgt.clone().index_add_(axis, idx, src)
def _segment_sum(src, seg, n):
    out = torch.zeros((n,) + src.shape[1:], dtype=src.dtype)
    return out.index_add_(0, seg, src)
def _pad(x, pw):
    flat = tuple(v for pair in reversed(pw) for v in pair)
    return torch.nn.functional.pad(x, flat)

kernel_name = 'concat_heads__compressed_kv_cla__linear_kernel_hedgehog__prompt'

def setup_inputs():

    _w = (lambda a: (lambda t: t.to(torch.bfloat16) if t.is_floating_point() else t)(torch.as_tensor(a)))
    def _A_setup():
        B, H, Sq, Sk, D, M = 1, 32, 2048, 2048, 128, 576
        Dc = 4 * D
        return {
            "x": _w((lambda *s: torch.randn(*s, dtype=torch.bfloat16) * 0.8)(B, Sq, M)),
            "c_kv": _w((lambda *s: torch.randn(*s, dtype=torch.bfloat16) * 0.4)(B, Sk, Dc)),
            "w_q": _w((lambda *s: torch.randn(*s, dtype=torch.bfloat16) * 6.4)(M, H, D) * (M ** -0.5)),
            "w_uk": _w((lambda *s: torch.randn(*s, dtype=torch.bfloat16) * 6.4)(Dc, H, D) * (Dc ** -0.5)),
            "w_uv": _w((lambda *s: torch.randn(*s, dtype=torch.bfloat16) * 0.4)(Dc, H, D) * (Dc ** -0.5)),
            "w_o": _w((lambda *s: torch.randn(*s, dtype=torch.bfloat16) * 0.4)(H, D, M) * ((H * D) ** -0.5)),
        }
    _A_in = _A_setup()

    def _B_setup():
        B, S, D, H = 1, 2048, 576, 32
        Dh = D // H
        return {
            "x": _w((lambda *s: torch.randn(*s, dtype=torch.bfloat16) * 0.4)(B, S, D)),
            "wqkv": _w((lambda *s: torch.randn(*s, dtype=torch.bfloat16) * 0.4)(D, 3 * D)),
            "w_phi": _w((lambda *s: torch.randn(*s, dtype=torch.bfloat16) * 0.4)(H, Dh, Dh) * (Dh ** -0.5)),
            "wo": _w((lambda *s: torch.randn(*s, dtype=torch.bfloat16) * 0.05)(D, D)),
        }
    _B_in = _B_setup()

    return {
        "A_x": _A_in["x"],
        "A_c_kv": _A_in["c_kv"],
        "A_w_q": _A_in["w_q"],
        "A_w_uk": _A_in["w_uk"],
        "A_w_uv": _A_in["w_uv"],
        "A_w_o": _A_in["w_o"],
        "B_x": _B_in["x"],
        "B_wqkv": _B_in["wqkv"],
        "B_w_phi": _B_in["w_phi"],
        "B_wo": _B_in["wo"],
    }

def reference(A_x, A_c_kv, A_w_q, A_w_uk, A_w_uv, A_w_o, B_x, B_wqkv, B_w_phi, B_wo):
    def _A_ref(x, c_kv, w_q, w_uk, w_uv, w_o):
        B, Sq, M = x.shape[0], x.shape[1], x.shape[2]
        Sk = c_kv.shape[1]
        H, D = w_q.shape[1], w_q.shape[2]
        q = torch.einsum("bqm,mhd->bhqd", x, w_q)
        k = torch.einsum("bkc,chd->bhkd", c_kv, w_uk)
        v = torch.einsum("bkc,chd->bhkd", c_kv, w_uv)
        scores = torch.einsum("bhqd,bhkd->bhqk", q, k) * (D ** -0.5)
        off = Sk - Sq
        qi = torch.arange(Sq)[:, None]
        kj = torch.arange(Sk)[None, :]
        cmask = kj <= (qi + off)
        scores = torch.where(cmask[None, None], scores, -1e9)
        p = _softmax(scores, axis=-1)
        ctx = torch.einsum("bhqk,bhkd->bhqd", p, v)
        return torch.einsum("bhqd,hdm->bqm", ctx, w_o)

    def _B_ref(x, wqkv, w_phi, wo):
        B, S, D = x.shape
        H = 32; Dh = D // H
        qkv = (lambda x, *d: x.permute(*d))((x @ wqkv).reshape(B, S, 3, H, Dh), 2, 0, 3, 1, 4)
        q, k, v = qkv[0], qkv[1], qkv[2]
        pq = torch.einsum("bhsd,hdf->bhsf", q, w_phi)
        pk = torch.einsum("bhsd,hdf->bhsf", k, w_phi)
        phi_q = torch.concatenate([_softmax(pq, axis=-1), _softmax(-pq, axis=-1)], axis=-1)
        phi_k = torch.concatenate([_softmax(pk, axis=-1), _softmax(-pk, axis=-1)], axis=-1)
        kv = torch.einsum("bhsf,bhsd->bhfd", phi_k, v)
        z  = torch.einsum("bhsf->bhf", phi_k)
        num = torch.einsum("bhqf,bhfd->bhqd", phi_q, kv)
        den = torch.einsum("bhqf,bhf->bhq", phi_q, z)[..., None] + 1e-6
        o = num / den
        return (lambda x, *d: x.permute(*d))(o, 0, 2, 1, 3).reshape(B, S, D) @ wo

    a = _A_ref(A_x, A_c_kv, A_w_q, A_w_uk, A_w_uv, A_w_o)
    b = _B_ref(B_x, B_wqkv, B_w_phi, B_wo)
    return torch.concatenate([a, b], axis=-1)
```

```cpp
#include <hip/hip_runtime.h>
#include <hip/hip_bf16.h>
#include <stdint.h>
typedef unsigned short u16;
__device__ __forceinline__ float bf2f(u16 b) { return __uint_as_float(((unsigned)b) << 16); }
__device__ __forceinline__ u16 f2bf(float f) { unsigned u = __float_as_uint(f); u += 0x7fffu + ((u >> 16) & 1u); return (u16)(u >> 16); }
__device__ __forceinline__ float rbf(float f) { return bf2f(f2bf(f)); }

__global__ __launch_bounds__(256) void n_gemm(const u16* A, int lda, const u16* B, int ldb, u16* C, int ldc, int M, int N, int K) {
  __shared__ float As[16][65], Bs[16][65];
  int tx = threadIdx.x & 15, ty = threadIdx.x >> 4, m0 = blockIdx.y * 64, n0 = blockIdx.x * 64;
  float acc[4][4] = {};
  for (int k0 = 0; k0 < K; k0 += 16) {
    for (int i = threadIdx.x; i < 1024; i += 256) { int r = i >> 4, c = i & 15; As[c][r] = bf2f(A[(size_t)(m0 + r) * lda + k0 + c]); }
    for (int i = threadIdx.x; i < 1024; i += 256) { int r = i >> 6, c = i & 63; Bs[r][c] = (n0 + c < N) ? bf2f(B[(size_t)(k0 + r) * ldb + n0 + c]) : 0.f; }
    __syncthreads();
    for (int k = 0; k < 16; ++k)
      for (int i = 0; i < 4; ++i) for (int j = 0; j < 4; ++j) acc[i][j] = fmaf(As[k][ty * 4 + i], Bs[k][tx * 4 + j], acc[i][j]);
    __syncthreads();
  }
  for (int i = 0; i < 4; ++i) for (int j = 0; j < 4; ++j) if (n0 + tx * 4 + j < N) C[(size_t)(m0 + ty * 4 + i) * ldc + n0 + tx * 4 + j] = f2bf(acc[i][j]);
}
#define MIMIC 0
__global__ __launch_bounds__(256) void n_attn(const u16* Q, const u16* K, const u16* V, u16* O) {
  __shared__ float s[2048]; __shared__ float qv[128]; __shared__ float red[256];
  int q = blockIdx.x, h = blockIdx.y, t = threadIdx.x;
  if (t < 128) qv[t] = bf2f(Q[(size_t)q * 4096 + h * 128 + t]);
  __syncthreads();
  float mx = -1e30f;
  for (int k = t; k <= q; k += 256) { const u16* kp = K + (size_t)k * 4096 + h * 128; float a = 0; for (int d = 0; d < 128; ++d) a = fmaf(qv[d], bf2f(kp[d]), a);
    if (MIMIC) a = rbf(rbf(a) * 0.08838834764831845f); else a *= 0.08838834764831845f;
    s[k] = a; mx = fmaxf(mx, a); }
  red[t] = mx; __syncthreads();
  for (int o = 128; o > 0; o >>= 1) { if (t < o) red[t] = fmaxf(red[t], red[t + o]); __syncthreads(); }
  mx = red[0]; __syncthreads();
  float sum = 0;
  for (int k = t; k <= q; k += 256) { float e = __expf(s[k] - mx); s[k] = e; sum += e; }
  red[t] = sum; __syncthreads();
  for (int o = 128; o > 0; o >>= 1) { if (t < o) red[t] += red[t + o]; __syncthreads(); }
  sum = red[0]; __syncthreads();
  int d = t & 127, half = t >> 7; float a = 0;
  for (int k = half; k <= q; k += 2) { float p = s[k] / sum; if (MIMIC) p = rbf(p); a = fmaf(p, bf2f(V[(size_t)k * 4096 + h * 128 + d]), a); }
  red[t] = a; __syncthreads();
  if (t < 128) O[(size_t)q * 4096 + h * 128 + d] = f2bf(red[t] + red[t + 128]);
}
__global__ void n_phi(const u16* qkv, const u16* wphi, float* phi  ) {
  int i = blockIdx.x * blockDim.x + threadIdx.x; if (i >= 2 * 2048 * 32) return;
  int which = i / (2048 * 32), r = i % (2048 * 32), s = r / 32, h = r % 32;
  float x[18], p[18];
  for (int d = 0; d < 18; ++d) x[d] = bf2f(qkv[(size_t)s * 1728 + which * 576 + h * 18 + d]);
  float mx = -1e30f, mn = 1e30f;
  for (int f = 0; f < 18; ++f) { float a = 0; for (int d = 0; d < 18; ++d) a = fmaf(x[d], bf2f(wphi[h * 324 + d * 18 + f]), a); if (MIMIC) a = rbf(a); p[f] = a; mx = fmaxf(mx, a); mn = fminf(mn, a); }
  float s1 = 0, s2 = 0; for (int f = 0; f < 18; ++f) { s1 += __expf(p[f] - mx); s2 += __expf(mn - p[f]); }
  float* o = phi + (size_t)i * 36;
  for (int f = 0; f < 18; ++f) { float a = __expf(p[f] - mx) / s1, b = __expf(mn - p[f]) / s2; if (MIMIC) { a = rbf(a); b = rbf(b); } o[f] = a; o[18 + f] = b; }
}
__global__ void n_kv(const float* phik, const u16* qkv, float* kv) {
  int h = blockIdx.x, t = threadIdx.x; if (t >= 36 * 19) return; int f = t / 19, d = t % 19; float a = 0;
  for (int s = 0; s < 2048; ++s) { float p = phik[((size_t)s * 32 + h) * 36 + f]; float v = d < 18 ? bf2f(qkv[(size_t)s * 1728 + 1152 + h * 18 + d]) : 1.f; a = fmaf(p, v, a); }
  if (MIMIC) a = rbf(a);
  kv[h * 684 + t] = a;
}
__global__ void n_o(const float* phiq, const float* kv, u16* o) {
  int i = blockIdx.x * blockDim.x + threadIdx.x; if (i >= 2048 * 32) return; int s = i / 32, h = i % 32;
  const float* p = phiq + (size_t)i * 36; const float* m = kv + h * 684;
  float den = 0; for (int f = 0; f < 36; ++f) den = fmaf(p[f], m[f * 19 + 18], den);
  if (MIMIC) den = rbf(rbf(den) + 1e-6f); else den += 1e-6f;
  for (int d = 0; d < 18; ++d) { float a = 0; for (int f = 0; f < 36; ++f) a = fmaf(p[f], m[f * 19 + d], a); if (MIMIC) a = rbf(a); o[(size_t)s * 576 + h * 18 + d] = f2bf(a / den); }
}
#define NAIVE_NAME kernel_launch
extern "C" void NAIVE_NAME(void* const* d_in, const int* in_sizes, int n_in, void* d_out, int out_size, void* d_ws, size_t ws_size, hipStream_t stream) {
  const u16 *x = (const u16*)d_in[0], *ckv = (const u16*)d_in[1], *wq = (const u16*)d_in[2], *wuk = (const u16*)d_in[3], *wuv = (const u16*)d_in[4], *wo = (const u16*)d_in[5];
  const u16 *bx = (const u16*)d_in[6], *bwqkv = (const u16*)d_in[7], *bwphi = (const u16*)d_in[8], *bwo = (const u16*)d_in[9];
  u16* out = (u16*)d_out; char* ws = (char*)d_ws;
  const size_t T = (size_t)2048 * 4096 * 2;
  u16 *Q = (u16*)ws, *K = (u16*)(ws + T), *V = (u16*)(ws + 2 * T), *C = (u16*)(ws + 3 * T), *qkv = (u16*)(ws + 4 * T);
  float* phi = (float*)(ws + 5 * T); float* kv = (float*)(ws + 7 * T); u16* o = (u16*)(ws + 7 * T + (1 << 20));
  n_gemm<<<dim3(64, 32), 256, 0, stream>>>(x, 576, wq, 4096, Q, 4096, 2048, 4096, 576);
  n_gemm<<<dim3(64, 32), 256, 0, stream>>>(ckv, 512, wuk, 4096, K, 4096, 2048, 4096, 512);
  n_gemm<<<dim3(64, 32), 256, 0, stream>>>(ckv, 512, wuv, 4096, V, 4096, 2048, 4096, 512);
  n_attn<<<dim3(2048, 32), 256, 0, stream>>>(Q, K, V, C);
  n_gemm<<<dim3(9, 32), 256, 0, stream>>>(C, 4096, wo, 576, out, 1152, 2048, 576, 4096);
  n_gemm<<<dim3(27, 32), 256, 0, stream>>>(bx, 576, bwqkv, 1728, qkv, 1728, 2048, 1728, 576);
  n_phi<<<512, 256, 0, stream>>>(qkv, bwphi, phi);
  n_kv<<<32, 704, 0, stream>>>(phi + (size_t)2048 * 32 * 36, qkv, kv);
  n_o<<<256, 256, 0, stream>>>(phi, kv, o);
  n_gemm<<<dim3(9, 32), 256, 0, stream>>>(o, 576, bwo, 576, out + 576, 1152, 2048, 576, 576);
}
```

```cpp
#include <hip/hip_runtime.h>
#include <hip/hip_bf16.h>
#include <stdint.h>
typedef unsigned short u16;
typedef unsigned long long u64;
typedef float f32x4 __attribute__((ext_vector_type(4)));
typedef float f32x2 __attribute__((ext_vector_type(2)));
typedef unsigned u32x2 __attribute__((ext_vector_type(2)));
typedef unsigned u32x4 __attribute__((ext_vector_type(4)));
typedef short s16x4 __attribute__((ext_vector_type(4)));
typedef __bf16 bf16x8 __attribute__((ext_vector_type(8)));
typedef __bf16 bf16x2 __attribute__((ext_vector_type(2)));
#define LDSP(T, a) ((__attribute__((address_space(3))) T*)(a))
__device__ __forceinline__ unsigned pk2(float lo, float hi) { f32x2 v = {lo, hi}; return __builtin_bit_cast(unsigned, __builtin_convertvector(v, bf16x2)); }
__device__ __forceinline__ f32x4 mma(u32x4 a, u32x4 b, f32x4 c) { return __builtin_amdgcn_mfma_f32_16x16x32_bf16(__builtin_bit_cast(bf16x8, a), __builtin_bit_cast(bf16x8, b), c, 0, 0, 0); }
__device__ __forceinline__ u32x2 trd(unsigned a) { return __builtin_bit_cast(u32x2, __builtin_amdgcn_ds_read_tr16_b64_v4i16(LDSP(s16x4, a))); }
__device__ __forceinline__ int fresh_lane() { int l; asm volatile("v_mbcnt_lo_u32_b32 %0, -1, 0\n\tv_mbcnt_hi_u32_b32 %0, -1, %0" : "=v"(l)); return l; }
__device__ __forceinline__ unsigned bt_voff(int l) {
  const unsigned half = l >> 5, r8 = (l >> 2) & 7;
  const unsigned c = (l & 3) ^ (2 * (r8 >> 2));
  return c == 3 ? 0x7ff00000u : (16 * half + 8 * (r8 >> 2) + (r8 & 3)) * 3456 + c * 16;
}
#define RFL(x) __builtin_amdgcn_readfirstlane(x)
__device__ __forceinline__ u64 uni64(const void* p) { u64 a = (u64)p; unsigned lo = RFL((unsigned)a), hi = RFL((unsigned)(a >> 32)); return ((u64)hi << 32) | lo; }
__device__ __forceinline__ float bf2f(u16 b) { return __uint_as_float(((unsigned)b) << 16); }
__device__ __forceinline__ u16 f2bf(float f) { unsigned u = __float_as_uint(f); u += 0x7fffu + ((u >> 16) & 1u); return (u16)(u >> 16); }

#define DBG 0
#define DBGOP(k)
#define DBGARG
#define DBGPASS
#define DBGA2
#define TSTAMP(v)
#define TDUMP(i, v)
#define CSTAMP(k, i)
constexpr size_t WS_KV = 0, WS_CTX = 32u << 20, WS_PART = 48u << 20, WS_OF = 68u << 20, WS_PHIQ = 72u << 20, WS_KVP = 78u << 20, WS_FLAG = 80u << 20;
constexpr unsigned FL_KV = 0, FL_B = 32768, FL_CTX = 65536, FL_O = 98304, FL_P = 131072, FL_SEED = 163840;

#define RSRC(p, n) __builtin_amdgcn_make_buffer_rsrc((void*)(p), 0, (n), 0x00020000)
#define SC1 16
typedef __attribute__((address_space(1))) u64 gu64;
#define RLX_AGENT __ATOMIC_RELAXED, __HIP_MEMORY_SCOPE_AGENT
#define CODE_WARM(n, dst, off) asm volatile("s_getpc_b64 s[40:41]\n\tkmain_pf" #n ":\n\ts_mov_b32 s42, kmain_code_end-kmain_pf" #n "\n\ts_mov_b32 s43, 0x20000\n\tbuffer_load_dword " dst ", %0, s[40:43], 0 offen" \
                                           :: "v"(off) : "s40", "s41", "s42", "s43", dst, "memory")
__global__ __launch_bounds__(256, 1) void k_main(const char* x, const char* ckv, const char* wq, const char* wuk, const char* wuv, const char* wo, const char* bx, const char* bwqkv,
                                                  const char* bwphi, const char* bwo, char* ws, char* out) {
  const int bid = blockIdx.x;
  CODE_WARM(0, "a255", (bid >> 3) * 4096 + threadIdx.x * 16);
  __builtin_amdgcn_sched_barrier(0);
  const int w = RFL(threadIdx.x >> 6);
  const int h = (bid & 7) * 4 + ((bid >> 3) & 3), xb = bid >> 5, qh = w & 1, g = w >> 1, b1 = 15 - xb, b2 = xb;
  const unsigned offRb = (256 * (bid & 7) + 64 * w) * 1152u, offTb = ((36u * (bid >> 3)) & ~15u) + (32u * (w >> 1) + 4u * (w & 1)) * 3456u;
  const u64 pRb = uni64(bx + offRb), pTb = uni64(bwqkv + offTb);
  u64 tag;
  {
    const int l = threadIdx.x & 63;
    const unsigned offR = (256 * xb + 64 * w) * 1024u, offT = h * 256u + (w & 1) * 128u;
    const unsigned offRq = (128 * (g ? b2 : b1) + 64 * qh) * 1152u, offTq = h * 256u + qh * 128u + 32u * g * 8192u;
    const unsigned c15 = l & 15, g4 = l >> 4, r8 = ((l >> 4) & 1) * 4 + ((l >> 2) & 3), dr = l >> 3, dc = l & 7;
    const unsigned po = (bid < 83 ? bid : bid - 83) * 28672u, pn = bid < 83 ? 2048u * 1152u : 576u * 8192u;
    const unsigned po2 = (bid < 116 ? bid : bid - 116) * 20480u, pn2 = bid < 116 ? 2048u * 1152u : 576u * 3456u;
    asm volatile(
    "; ---- setup\n\t"
    "s_mov_b64 s[40:41], %2\n\t"
    "s_mov_b32 s42, %3\n\t"
    "s_mov_b32 s43, 0x20000\n\t"
    "s_mov_b64 s[44:45], %4\n\t"
    "s_mov_b32 s46, %5\n\t"
    "s_mov_b32 s47, 0x20000\n\t"
    "s_lshl_b32 s62, %1, 13\n\t"
    "v_add_u32 v22, 0, %9\n\t"
    "v_add_u32 v23, 0x1c00, %10\n\t"
    "v_add_u32 v24, 0x3800, %9\n\t"
    "v_add_u32 v25, 0x5400, %10\n\t"
    "v_add_u32 v26, 0x8000, %9\n\t"
    "v_add_u32 v27, 0x9c00, %10\n\t"
    "v_add_u32 v28, 0xb800, %9\n\t"
    "v_add_u32 v29, 0xd400, %10\n\t"
    "v_add_u32 v30, 0, %11\n\t"
    "v_add_u32 v31, 0x1fc00, %11\n\t"
    "v_add_u32 v32, 0x7800, %11\n\t"
    "v_add_u32 v33, 0x27400, %11\n\t"
    "v_add_u32 v34, 0x40000, %11\n\t"
    "v_add_u32 v35, 0x5fc00, %11\n\t"
    "v_add_u32 v36, 0x47800, %11\n\t"
    "v_add_u32 v37, 0x67400, %11\n\t"
    "s_mov_b32 s52, 0\n\t"
    "s_mov_b32 s53, 0\n\t"
    "s_add_u32 s57, s62, 0x10000\n\t"
    "s_mov_b32 s58, s62\n\t"
    "s_add_u32 m0, s57, 0\n\t"
    "s_nop 0\n\t"
    "buffer_load_dwordx4 v22, s[40:43], s52 offen offset:0 lds\n\t"
    "buffer_load_dwordx4 v23, s[40:43], s52 offen offset:1024 lds\n\t"
    "buffer_load_dwordx4 v24, s[40:43], s52 offen offset:2048 lds\n\t"
    "buffer_load_dwordx4 v25, s[40:43], s52 offen offset:3072 lds\n\t"
    "s_add_u32 m0, s57, 0x1000\n\t"
    "s_nop 0\n\t"
    "buffer_load_dwordx4 v26, s[40:43], s52 offen offset:0 lds\n\t"
    "buffer_load_dwordx4 v27, s[40:43], s52 offen offset:1024 lds\n\t"
    "buffer_load_dwordx4 v28, s[40:43], s52 offen offset:2048 lds\n\t"
    "buffer_load_dwordx4 v29, s[40:43], s52 offen offset:3072 lds\n\t"
    "s_add_u32 m0, s58, 0\n\t"
    "s_nop 0\n\t"
    "buffer_load_dwordx4 v30, s[44:47], s53 offen offset:0 lds\n\t"
    "buffer_load_dwordx4 v31, s[44:47], s53 offen offset:1024 lds\n\t"
    "buffer_load_dwordx4 v32, s[44:47], s53 offen offset:2048 lds\n\t"
    "buffer_load_dwordx4 v33, s[44:47], s53 offen offset:3072 lds\n\t"
    "s_add_u32 m0, s58, 0x1000\n\t"
    "s_nop 0\n\t"
    "buffer_load_dwordx4 v34, s[44:47], s53 offen offset:0 lds\n\t"
    "buffer_load_dwordx4 v35, s[44:47], s53 offen offset:1024 lds\n\t"
    "buffer_load_dwordx4 v36, s[44:47], s53 offen offset:2048 lds\n\t"
    "buffer_load_dwordx4 v37, s[44:47], s53 offen offset:3072 lds\n\t"
    "s_load_dwordx2 %0, %25, 0x0\n\t"
    "s_mov_b32 s52, 0x80\n\t"
    "s_mov_b32 s53, 0x80000\n\t"
    "s_add_u32 s57, s62, 0x18000\n\t"
    "s_add_u32 s58, s62, 0x8000\n\t"
    "s_add_u32 m0, s57, 0\n\t"
    "s_nop 0\n\t"
    "buffer_load_dwordx4 v22, s[40:43], s52 offen offset:0 lds\n\t"
    "buffer_load_dwordx4 v23, s[40:43], s52 offen offset:1024 lds\n\t"
    "buffer_load_dwordx4 v24, s[40:43], s52 offen offset:2048 lds\n\t"
    "buffer_load_dwordx4 v25, s[40:43], s52 offen offset:3072 lds\n\t"
    "s_add_u32 m0, s57, 0x1000\n\t"
    "s_nop 0\n\t"
    "buffer_load_dwordx4 v26, s[40:43], s52 offen offset:0 lds\n\t"
    "buffer_load_dwordx4 v27, s[40:43], s52 offen offset:1024 lds\n\t"
    "buffer_load_dwordx4 v28, s[40:43], s52 offen offset:2048 lds\n\t"
    "buffer_load_dwordx4 v29, s[40:43], s52 offen offset:3072 lds\n\t"
    "s_add_u32 m0, s58, 0\n\t"
    "s_nop 0\n\t"
    "buffer_load_dwordx4 v30, s[44:47], s53 offen offset:0 lds\n\t"
    "buffer_load_dwordx4 v31, s[44:47], s53 offen offset:1024 lds\n\t"
    "buffer_load_dwordx4 v32, s[44:47], s53 offen offset:2048 lds\n\t"
    "buffer_load_dwordx4 v33, s[44:47], s53 offen offset:3072 lds\n\t"
    "s_add_u32 m0, s58, 0x1000\n\t"
    "s_nop 0\n\t"
    "buffer_load_dwordx4 v34, s[44:47], s53 offen offset:0 lds\n\t"
    "buffer_load_dwordx4 v35, s[44:47], s53 offen offset:1024 lds\n\t"
    "buffer_load_dwordx4 v36, s[44:47], s53 offen offset:2048 lds\n\t"
    "buffer_load_dwordx4 v37, s[44:47], s53 offen offset:3072 lds\n\t"
    "s_mov_b64 s[48:49], %6\n\t"
    "s_mov_b32 s50, 0x10000\n\t"
    "s_mov_b32 s51, 0x20000\n\t"
    "s_mov_b64 s[68:69], %23\n\t"
    "s_mov_b32 s70, %24\n\t"
    "s_mov_b32 s71, 0x20000\n\t"
    "v_mov_b32 v45, %7\n\t"
    "v_xor_b32 v46, 64, %7\n\t"
    "v_xor_b32 v18, 0, %8\n\t"
    "v_xor_b32 v19, 32, %8\n\t"
    "v_xor_b32 v20, 64, %8\n\t"
    "v_xor_b32 v21, 0x60, %8\n\t"
    "s_mov_b32 s54, 0x10000\n\t"
    "s_mov_b32 s55, 0x18000\n\t"
    "s_mov_b32 s56, 0x20000\n\t"
    "v_add_u32 v16, 0x10000, v45\n\t"
    "v_add_u32 v17, 0x10000, v46\n\t"
    "s_mov_b32 s52, 0x100\n\t"
    "s_mov_b32 s53, 0x100000\n\t"
    "s_add_u32 s57, s62, 0x20000\n\t"
    "s_mov_b32 s58, s62\n\t"
    "s_lshl_b32 s63, %1, 10\n\t"
    "s_mov_b32 s59, 5\n\t"
    "s_mov_b32 s76, 0x3e0293ee\n\t"
    "s_mul_i32 s66, %1, 0xc00\n\t"
    "s_waitcnt vmcnt(16) lgkmcnt(0)\n\t"
    "s_barrier\n\t"
    "ds_read_b128 a[0:3], v16 offset:0\n\t"
    "ds_read_b64_tr_b16 a[32:33], v18 offset:0\n\t"
    "ds_read_b64_tr_b16 a[34:35], v18 offset:2048\n\t"
    "ds_read_b128 a[4:7], v16 offset:2048\n\t"
    "ds_read_b64_tr_b16 a[36:37], v19 offset:0\n\t"
    "ds_read_b64_tr_b16 a[38:39], v19 offset:2048\n\t"
    "ds_read_b128 a[8:11], v16 offset:4096\n\t"
    "ds_read_b64_tr_b16 a[40:41], v20 offset:0\n\t"
    "ds_read_b64_tr_b16 a[42:43], v20 offset:2048\n\t"
    "ds_read_b128 a[12:15], v16 offset:6144\n\t"
    "ds_read_b64_tr_b16 a[44:45], v21 offset:0\n\t"
    "ds_read_b64_tr_b16 a[46:47], v21 offset:2048\n\t"
    "ds_read_b128 a[16:19], v16 offset:8192\n\t"
    "ds_read_b64_tr_b16 a[48:49], v18 offset:8192\n\t"
    "ds_read_b64_tr_b16 a[50:51], v18 offset:10240\n\t"
    "ds_read_b128 a[20:23], v16 offset:10240\n\t"
    "ds_read_b64_tr_b16 a[52:53], v19 offset:8192\n\t"
    "ds_read_b64_tr_b16 a[54:55], v19 offset:10240\n\t"
    "ds_read_b128 a[24:27], v16 offset:12288\n\t"
    "ds_read_b64_tr_b16 a[56:57], v20 offset:8192\n\t"
    "ds_read_b64_tr_b16 a[58:59], v20 offset:10240\n\t"
    "ds_read_b128 a[28:31], v16 offset:14336\n\t"
    "ds_read_b64_tr_b16 a[60:61], v21 offset:8192\n\t"
    "ds_read_b64_tr_b16 a[62:63], v21 offset:10240\n\t"
    "s_bitcmp1_b32 %1, 0\n\t"
    "s_cbranch_scc1 kvV_first_%=\n\t"
    "; ---- kvK_first\n\t"
    "kvK_first_%=:\n\t"
    "s_waitcnt lgkmcnt(13)\n\t"
    "v_mfma_f32_16x16x32_bf16 v[48:51], a[32:35], a[0:3], 0\n\t"
    "ds_read_b128 a[64:67], v17 offset:0\n\t"
    "v_mfma_f32_16x16x32_bf16 v[52:55], a[36:39], a[0:3], 0\n\t"
    "ds_read_b64_tr_b16 a[96:97], v18 offset:4096\n\t"
    "s_add_u32 m0, s57, 0\n\t"
    "v_mfma_f32_16x16x32_bf16 v[56:59], a[40:43], a[0:3], 0\n\t"
    "buffer_load_dwordx4 v22, s[40:43], s52 offen offset:0 lds\n\t"
    "s_waitcnt lgkmcnt(8)\n\t"
    "v_mfma_f32_16x16x32_bf16 v[60:63], a[44:47], a[0:3], 0\n\t"
    "ds_read_b64_tr_b16 a[98:99], v18 offset:6144\n\t"
    "v_mfma_f32_16x16x32_bf16 v[64:67], a[48:51], a[0:3], 0\n\t"
    "v_mfma_f32_16x16x32_bf16 v[68:71], a[52:55], a[0:3], 0\n\t"
    "ds_read_b128 a[68:71], v17 offset:2048\n\t"
    "s_waitcnt lgkmcnt(5)\n\t"
    "v_mfma_f32_16x16x32_bf16 v[72:75], a[56:59], a[0:3], 0\n\t"
    "s_waitcnt lgkmcnt(4)\n\t"
    "v_mfma_f32_16x16x32_bf16 v[76:79], a[60:63], a[0:3], 0\n\t"
    "ds_read_b64_tr_b16 a[100:101], v19 offset:4096\n\t"
    "v_mfma_f32_16x16x32_bf16 v[80:83], a[32:35], a[4:7], 0\n\t"
    "v_mfma_f32_16x16x32_bf16 v[84:87], a[36:39], a[4:7], 0\n\t"
    "ds_read_b64_tr_b16 a[102:103], v19 offset:6144\n\t"
    "buffer_load_dwordx4 v23, s[40:43], s52 offen offset:1024 lds\n\t"
    "v_mfma_f32_16x16x32_bf16 v[88:91], a[40:43], a[4:7], 0\n\t"
    "v_mfma_f32_16x16x32_bf16 v[92:95], a[44:47], a[4:7], 0\n\t"
    "ds_read_b128 a[72:75], v17 offset:4096\n\t"
    "v_mfma_f32_16x16x32_bf16 v[96:99], a[48:51], a[4:7], 0\n\t"
    "ds_read_b64_tr_b16 a[104:105], v20 offset:4096\n\t"
    "v_mfma_f32_16x16x32_bf16 v[100:103], a[52:55], a[4:7], 0\n\t"
    "v_mfma_f32_16x16x32_bf16 v[104:107], a[56:59], a[4:7], 0\n\t"
    "ds_read_b64_tr_b16 a[106:107], v20 offset:6144\n\t"
    "v_mfma_f32_16x16x32_bf16 v[108:111], a[60:63], a[4:7], 0\n\t"
    "v_mfma_f32_16x16x32_bf16 v[112:115], a[32:35], a[8:11], 0\n\t"
    "ds_read_b128 a[76:79], v17 offset:6144\n\t"
    "buffer_load_dwordx4 v24, s[40:43], s52 offen offset:2048 lds\n\t"
    "v_mfma_f32_16x16x32_bf16 v[116:119], a[36:39], a[8:11], 0\n\t"
    "v_mfma_f32_16x16x32_bf16 v[120:123], a[40:43], a[8:11], 0\n\t"
    "ds_read_b64_tr_b16 a[108:109], v21 offset:4096\n\t"
    "v_mfma_f32_16x16x32_bf16 v[124:127], a[44:47], a[8:11], 0\n\t"
    "v_mfma_f32_16x16x32_bf16 v[128:131], a[48:51], a[8:11], 0\n\t"
    "ds_read_b64_tr_b16 a[110:111], v21 offset:6144\n\t"
    "v_mfma_f32_16x16x32_bf16 v[132:135], a[52:55], a[8:11], 0\n\t"
    "v_mfma_f32_16x16x32_bf16 v[136:139], a[56:59], a[8:11], 0\n\t"
    "ds_read_b128 a[80:83], v17 offset:8192\n\t"
    "v_mfma_f32_16x16x32_bf16 v[140:143], a[60:63], a[8:11], 0\n\t"
    "ds_read_b64_tr_b16 a[112:113], v18 offset:12288\n\t"
    "buffer_load_dwordx4 v25, s[40:43], s52 offen offset:3072 lds\n\t"
    "v_mfma_f32_16x16x32_bf16 v[144:147], a[32:35], a[12:15], 0\n\t"
    "v_mfma_f32_16x16x32_bf16 v[148:151], a[36:39], a[12:15], 0\n\t"
    "ds_read_b64_tr_b16 a[114:115], v18 offset:14336\n\t"
    "v_mfma_f32_16x16x32_bf16 v[152:155], a[40:43], a[12:15], 0\n\t"
    "v_mfma_f32_16x16x32_bf16 v[156:159], a[44:47], a[12:15], 0\n\t"
    "ds_read_b128 a[84:87], v17 offset:10240\n\t"
    "v_mfma_f32_16x16x32_bf16 v[160:163], a[48:51], a[12:15], 0\n\t"
    "v_mfma_f32_16x16x32_bf16 v[164:167], a[52:55], a[12:15], 0\n\t"
    "ds_read_b64_tr_b16 a[116:117], v19 offset:12288\n\t"
    "s_add_u32 m0, s57, 0x1000\n\t"
    "v_mfma_f32_16x16x32_bf16 v[168:171], a[56:59], a[12:15], 0\n\t"
    "buffer_load_dwordx4 v26, s[40:43], s52 offen offset:0 lds\n\t"
    "v_mfma_f32_16x16x32_bf16 v[172:175], a[60:63], a[12:15], 0\n\t"
    "ds_read_b64_tr_b16 a[118:119], v19 offset:14336\n\t"
    "v_mfma_f32_16x16x32_bf16 v[176:179], a[32:35], a[16:19], 0\n\t"
    "v_mfma_f32_16x16x32_bf16 v[180:183], a[36:39], a[16:19], 0\n\t"
    "ds_read_b128 a[88:91], v17 offset:12288\n\t"
    "v_mfma_f32_16x16x32_bf16 v[184:187], a[40:43], a[16:19], 0\n\t"
    "ds_read_b64_tr_b16 a[120:121], v20 offset:12288\n\t"
    "v_mfma_f32_16x16x32_bf16 v[188:191], a[44:47], a[16:19], 0\n\t"
    "v_mfma_f32_16x16x32_bf16 v[192:195], a[48:51], a[16:19], 0\n\t"
    "ds_read_b64_tr_b16 a[122:123], v20 offset:14336\n\t"
    "v_mfma_f32_16x16x32_bf16 v[196:199], a[52:55], a[16:19], 0\n\t"
    "buffer_load_dwordx4 v27, s[40:43], s52 offen offset:1024 lds\n\t"
    "v_mfma_f32_16x16x32_bf16 v[200:203], a[56:59], a[16:19], 0\n\t"
    "ds_read_b128 a[92:95], v17 offset:14336\n\t"
    "v_mfma_f32_16x16x32_bf16 v[204:207], a[60:63], a[16:19], 0\n\t"
    "v_mfma_f32_16x16x32_bf16 v[208:211], a[32:35], a[20:23], 0\n\t"
    "ds_read_b64_tr_b16 a[124:125], v21 offset:12288\n\t"
    "v_mfma_f32_16x16x32_bf16 v[212:215], a[36:39], a[20:23], 0\n\t"
    "v_mfma_f32_16x16x32_bf16 v[216:219], a[40:43], a[20:23], 0\n\t"
    "ds_read_b64_tr_b16 a[126:127], v21 offset:14336\n\t"
    "v_mfma_f32_16x16x32_bf16 v[220:223], a[44:47], a[20:23], 0\n\t"
    "v_mfma_f32_16x16x32_bf16 v[224:227], a[48:51], a[20:23], 0\n\t"
    "buffer_load_dwordx4 v28, s[40:43], s52 offen offset:2048 lds\n\t"
    "v_mfma_f32_16x16x32_bf16 v[228:231], a[52:55], a[20:23], 0\n\t"
    "v_mfma_f32_16x16x32_bf16 v[232:235], a[56:59], a[20:23], 0\n\t"
    "v_add_u32 v16, s55, v45\n\t"
    "v_mfma_f32_16x16x32_bf16 v[236:239], a[60:63], a[20:23], 0\n\t"
    "v_mfma_f32_16x16x32_bf16 v[240:243], a[32:35], a[24:27], 0\n\t"
    "v_mfma_f32_16x16x32_bf16 v[244:247], a[36:39], a[24:27], 0\n\t"
    "v_xor_b32 v18, 0x8000, v18\n\t"
    "v_mfma_f32_16x16x32_bf16 v[248:251], a[40:43], a[24:27], 0\n\t"
    "v_mfma_f32_16x16x32_bf16 v[252:255], a[44:47], a[24:27], 0\n\t"
    "buffer_load_dwordx4 v29, s[40:43], s52 offen offset:3072 lds\n\t"
    "v_mfma_f32_16x16x32_bf16 a[128:131], a[48:51], a[24:27], 0\n\t"
    "v_xor_b32 v19, 0x8000, v19\n\t"
    "v_mfma_f32_16x16x32_bf16 a[132:135], a[52:55], a[24:27], 0\n\t"
    "v_mfma_f32_16x16x32_bf16 a[136:139], a[56:59], a[24:27], 0\n\t"
    "v_mfma_f32_16x16x32_bf16 a[140:143], a[60:63], a[24:27], 0\n\t"
    "v_xor_b32 v20, 0x8000, v20\n\t"
    "v_mfma_f32_16x16x32_bf16 a[144:147], a[32:35], a[28:31], 0\n\t"
    "v_mfma_f32_16x16x32_bf16 a[148:151], a[36:39], a[28:31], 0\n\t"
    "v_mfma_f32_16x16x32_bf16 a[152:155], a[40:43], a[28:31], 0\n\t"
    "v_xor_b32 v21, 0x8000, v21\n\t"
    "v_mfma_f32_16x16x32_bf16 a[156:159], a[44:47], a[28:31], 0\n\t"
    "v_mfma_f32_16x16x32_bf16 a[160:163], a[48:51], a[28:31], 0\n\t"
    "v_mfma_f32_16x16x32_bf16 a[164:167], a[52:55], a[28:31], 0\n\t"
    "v_mfma_f32_16x16x32_bf16 a[168:171], a[56:59], a[28:31], 0\n\t"
    "v_mfma_f32_16x16x32_bf16 a[172:175], a[60:63], a[28:31], 0\n\t"
    "s_waitcnt vmcnt(8) lgkmcnt(0)\n\t"
    "s_barrier\n\t"
    "v_mfma_f32_16x16x32_bf16 v[48:51], a[96:99], a[64:67], v[48:51]\n\t"
    "ds_read_b128 a[0:3], v16 offset:0\n\t"
    "v_mfma_f32_16x16x32_bf16 v[52:55], a[100:103], a[64:67], v[52:55]\n\t"
    "ds_read_b64_tr_b16 a[32:33], v18 offset:0\n\t"
    "s_add_u32 m0, s58, 0\n\t"
    "v_mfma_f32_16x16x32_bf16 v[56:59], a[104:107], a[64:67], v[56:59]\n\t"
    "buffer_load_dwordx4 v30, s[44:47], s53 offen offset:0 lds\n\t"
    "v_mfma_f32_16x16x32_bf16 v[60:63], a[108:111], a[64:67], v[60:63]\n\t"
    "ds_read_b64_tr_b16 a[34:35], v18 offset:2048\n\t"
    "v_mfma_f32_16x16x32_bf16 v[64:67], a[112:115], a[64:67], v[64:67]\n\t"
    "v_mfma_f32_16x16x32_bf16 v[68:71], a[116:119], a[64:67], v[68:71]\n\t"
    "ds_read_b128 a[4:7], v16 offset:2048\n\t"
    "v_mfma_f32_16x16x32_bf16 v[72:75], a[120:123], a[64:67], v[72:75]\n\t"
    "v_mfma_f32_16x16x32_bf16 v[76:79], a[124:127], a[64:67], v[76:79]\n\t"
    "ds_read_b64_tr_b16 a[36:37], v19 offset:0\n\t"
    "v_mfma_f32_16x16x32_bf16 v[80:83], a[96:99], a[68:71], v[80:83]\n\t"
    "buffer_load_dwordx4 v31, s[44:47], s53 offen offset:1024 lds\n\t"
    "v_mfma_f32_16x16x32_bf16 v[84:87], a[100:103], a[68:71], v[84:87]\n\t"
    "ds_read_b64_tr_b16 a[38:39], v19 offset:2048\n\t"
    "v_mfma_f32_16x16x32_bf16 v[88:91], a[104:107], a[68:71], v[88:91]\n\t"
    "v_mfma_f32_16x16x32_bf16 v[92:95], a[108:111], a[68:71], v[92:95]\n\t"
    "ds_read_b128 a[8:11], v16 offset:4096\n\t"
    "v_mfma_f32_16x16x32_bf16 v[96:99], a[112:115], a[68:71], v[96:99]\n\t"
    "ds_read_b64_tr_b16 a[40:41], v20 offset:0\n\t"
    "v_mfma_f32_16x16x32_bf16 v[100:103], a[116:119], a[68:71], v[100:103]\n\t"
    "v_mfma_f32_16x16x32_bf16 v[104:107], a[120:123], a[68:71], v[104:107]\n\t"
    "ds_read_b64_tr_b16 a[42:43], v20 offset:2048\n\t"
    "v_mfma_f32_16x16x32_bf16 v[108:111], a[124:127], a[68:71], v[108:111]\n\t"
    "buffer_load_dwordx4 v32, s[44:47], s53 offen offset:2048 lds\n\t"
    "v_mfma_f32_16x16x32_bf16 v[112:115], a[96:99], a[72:75], v[112:115]\n\t"
    "ds_read_b128 a[12:15], v16 offset:6144\n\t"
    "v_mfma_f32_16x16x32_bf16 v[116:119], a[100:103], a[72:75], v[116:119]\n\t"
    "v_mfma_f32_16x16x32_bf16 v[120:123], a[104:107], a[72:75], v[120:123]\n\t"
    "ds_read_b64_tr_b16 a[44:45], v21 offset:0\n\t"
    "v_mfma_f32_16x16x32_bf16 v[124:127], a[108:111], a[72:75], v[124:127]\n\t"
    "v_mfma_f32_16x16x32_bf16 v[128:131], a[112:115], a[72:75], v[128:131]\n\t"
    "ds_read_b64_tr_b16 a[46:47], v21 offset:2048\n\t"
    "v_mfma_f32_16x16x32_bf16 v[132:135], a[116:119], a[72:75], v[132:135]\n\t"
    "v_mfma_f32_16x16x32_bf16 v[136:139], a[120:123], a[72:75], v[136:139]\n\t"
    "ds_read_b128 a[16:19], v16 offset:8192\n\t"
    "buffer_load_dwordx4 v33, s[44:47], s53 offen offset:3072 lds\n\t"
    "v_mfma_f32_16x16x32_bf16 v[140:143], a[124:127], a[72:75], v[140:143]\n\t"
    "ds_read_b64_tr_b16 a[48:49], v18 offset:8192\n\t"
    "v_mfma_f32_16x16x32_bf16 v[144:147], a[96:99], a[76:79], v[144:147]\n\t"
    "v_mfma_f32_16x16x32_bf16 v[148:151], a[100:103], a[76:79], v[148:151]\n\t"
    "ds_read_b64_tr_b16 a[50:51], v18 offset:10240\n\t"
    "v_mfma_f32_16x16x32_bf16 v[152:155], a[104:107], a[76:79], v[152:155]\n\t"
    "v_mfma_f32_16x16x32_bf16 v[156:159], a[108:111], a[76:79], v[156:159]\n\t"
    "ds_read_b128 a[20:23], v16 offset:10240\n\t"
    "s_add_u32 m0, s58, 0x1000\n\t"
    "v_mfma_f32_16x16x32_bf16 v[160:163], a[112:115], a[76:79], v[160:163]\n\t"
    "buffer_load_dwordx4 v34, s[44:47], s53 offen offset:0 lds\n\t"
    "v_mfma_f32_16x16x32_bf16 v[164:167], a[116:119], a[76:79], v[164:167]\n\t"
    "ds_read_b64_tr_b16 a[52:53], v19 offset:8192\n\t"
    "v_mfma_f32_16x16x32_bf16 v[168:171], a[120:123], a[76:79], v[168:171]\n\t"
    "v_mfma_f32_16x16x32_bf16 v[172:175], a[124:127], a[76:79], v[172:175]\n\t"
    "ds_read_b64_tr_b16 a[54:55], v19 offset:10240\n\t"
    "v_mfma_f32_16x16x32_bf16 v[176:179], a[96:99], a[80:83], v[176:179]\n\t"
    "v_mfma_f32_16x16x32_bf16 v[180:183], a[100:103], a[80:83], v[180:183]\n\t"
    "ds_read_b128 a[24:27], v16 offset:12288\n\t"
    "v_mfma_f32_16x16x32_bf16 v[184:187], a[104:107], a[80:83], v[184:187]\n\t"
    "ds_read_b64_tr_b16 a[56:57], v20 offset:8192\n\t"
    "v_mfma_f32_16x16x32_bf16 v[188:191], a[108:111], a[80:83], v[188:191]\n\t"
    "buffer_load_dwordx4 v35, s[44:47], s53 offen offset:1024 lds\n\t"
    "v_mfma_f32_16x16x32_bf16 v[192:195], a[112:115], a[80:83], v[192:195]\n\t"
    "ds_read_b64_tr_b16 a[58:59], v20 offset:10240\n\t"
    "v_mfma_f32_16x16x32_bf16 v[196:199], a[116:119], a[80:83], v[196:199]\n\t"
    "v_mfma_f32_16x16x32_bf16 v[200:203], a[120:123], a[80:83], v[200:203]\n\t"
    "ds_read_b128 a[28:31], v16 offset:14336\n\t"
    "v_mfma_f32_16x16x32_bf16 v[204:207], a[124:127], a[80:83], v[204:207]\n\t"
    "v_mfma_f32_16x16x32_bf16 v[208:211], a[96:99], a[84:87], v[208:211]\n\t"
    "ds_read_b64_tr_b16 a[60:61], v21 offset:8192\n\t"
    "v_mfma_f32_16x16x32_bf16 v[212:215], a[100:103], a[84:87], v[212:215]\n\t"
    "v_mfma_f32_16x16x32_bf16 v[216:219], a[104:107], a[84:87], v[216:219]\n\t"
    "ds_read_b64_tr_b16 a[62:63], v21 offset:10240\n\t"
    "buffer_load_dwordx4 v36, s[44:47], s53 offen offset:2048 lds\n\t"
    "v_mfma_f32_16x16x32_bf16 v[220:223], a[108:111], a[84:87], v[220:223]\n\t"
    "v_mfma_f32_16x16x32_bf16 v[224:227], a[112:115], a[84:87], v[224:227]\n\t"
    "v_mfma_f32_16x16x32_bf16 v[228:231], a[116:119], a[84:87], v[228:231]\n\t"
    "v_mfma_f32_16x16x32_bf16 v[232:235], a[120:123], a[84:87], v[232:235]\n\t"
    "v_mfma_f32_16x16x32_bf16 v[236:239], a[124:127], a[84:87], v[236:239]\n\t"
    "v_mfma_f32_16x16x32_bf16 v[240:243], a[96:99], a[88:91], v[240:243]\n\t"
    "buffer_load_dwordx4 v37, s[44:47], s53 offen offset:3072 lds\n\t"
    "v_mfma_f32_16x16x32_bf16 v[244:247], a[100:103], a[88:91], v[244:247]\n\t"
    "v_mfma_f32_16x16x32_bf16 v[248:251], a[104:107], a[88:91], v[248:251]\n\t"
    "v_mfma_f32_16x16x32_bf16 v[252:255], a[108:111], a[88:91], v[252:255]\n\t"
    "v_mfma_f32_16x16x32_bf16 a[128:131], a[112:115], a[88:91], a[128:131]\n\t"
    "v_mfma_f32_16x16x32_bf16 a[132:135], a[116:119], a[88:91], a[132:135]\n\t"
    "v_mfma_f32_16x16x32_bf16 a[136:139], a[120:123], a[88:91], a[136:139]\n\t"
    "v_mfma_f32_16x16x32_bf16 a[140:143], a[124:127], a[88:91], a[140:143]\n\t"
    "buffer_load_dword a254, %12, s[68:71], s63 offen\n\t"
    "v_mfma_f32_16x16x32_bf16 a[144:147], a[96:99], a[92:95], a[144:147]\n\t"
    "v_mfma_f32_16x16x32_bf16 a[148:151], a[100:103], a[92:95], a[148:151]\n\t"
    "v_add_u32 v17, s55, v46\n\t"
    "v_mfma_f32_16x16x32_bf16 a[152:155], a[104:107], a[92:95], a[152:155]\n\t"
    "s_mov_b32 s60, s54\n\t"
    "s_mov_b32 s54, s55\n\t"
    "v_mfma_f32_16x16x32_bf16 a[156:159], a[108:111], a[92:95], a[156:159]\n\t"
    "s_mov_b32 s55, s56\n\t"
    "s_mov_b32 s56, s60\n\t"
    "v_mfma_f32_16x16x32_bf16 a[160:163], a[112:115], a[92:95], a[160:163]\n\t"
    "s_add_u32 s57, s62, s60\n\t"
    "s_add_u32 s52, s52, 0x80\n\t"
    "v_mfma_f32_16x16x32_bf16 a[164:167], a[116:119], a[92:95], a[164:167]\n\t"
    "s_xor_b32 s58, s58, 0x8000\n\t"
    "s_add_u32 s53, s53, 0x80000\n\t"
    "v_mfma_f32_16x16x32_bf16 a[168:171], a[120:123], a[92:95], a[168:171]\n\t"
    "s_add_u32 s63, s63, 0x1000\n\t"
    "v_mfma_f32_16x16x32_bf16 a[172:175], a[124:127], a[92:95], a[172:175]\n\t"
    "; ---- kvK_loop\n\t"
    "kvK_loop_%=:\n\t"
    "s_waitcnt lgkmcnt(6)\n\t"
    "v_mfma_f32_16x16x32_bf16 v[48:51], a[32:35], a[0:3], v[48:51]\n\t"
    "ds_read_b128 a[64:67], v17 offset:0\n\t"
    "v_mfma_f32_16x16x32_bf16 v[52:55], a[36:39], a[0:3], v[52:55]\n\t"
    "ds_read_b64_tr_b16 a[96:97], v18 offset:4096\n\t"
    "s_add_u32 m0, s57, 0\n\t"
    "v_mfma_f32_16x16x32_bf16 v[56:59], a[40:43], a[0:3], v[56:59]\n\t"
    "buffer_load_dwordx4 v22, s[40:43], s52 offen offset:0 lds\n\t"
    "v_mfma_f32_16x16x32_bf16 v[60:63], a[44:47], a[0:3], v[60:63]\n\t"
    "ds_read_b64_tr_b16 a[98:99], v18 offset:6144\n\t"
    "v_mfma_f32_16x16x32_bf16 v[64:67], a[48:51], a[0:3], v[64:67]\n\t"
    "v_mfma_f32_16x16x32_bf16 v[68:71], a[52:55], a[0:3], v[68:71]\n\t"
    "ds_read_b128 a[68:71], v17 offset:2048\n\t"
    "s_waitcnt lgkmcnt(4)\n\t"
    "v_mfma_f32_16x16x32_bf16 v[72:75], a[56:59], a[0:3], v[72:75]\n\t"
    "v_mfma_f32_16x16x32_bf16 v[76:79], a[60:63], a[0:3], v[76:79]\n\t"
    "ds_read_b64_tr_b16 a[100:101], v19 offset:4096\n\t"
    "v_mfma_f32_16x16x32_bf16 v[80:83], a[32:35], a[4:7], v[80:83]\n\t"
    "v_mfma_f32_16x16x32_bf16 v[84:87], a[36:39], a[4:7], v[84:87]\n\t"
    "ds_read_b64_tr_b16 a[102:103], v19 offset:6144\n\t"
    "buffer_load_dwordx4 v23, s[40:43], s52 offen offset:1024 lds\n\t"
    "v_mfma_f32_16x16x32_bf16 v[88:91], a[40:43], a[4:7], v[88:91]\n\t"
    "v_mfma_f32_16x16x32_bf16 v[92:95], a[44:47], a[4:7], v[92:95]\n\t"
    "ds_read_b128 a[72:75], v17 offset:4096\n\t"
    "v_mfma_f32_16x16x32_bf16 v[96:99], a[48:51], a[4:7], v[96:99]\n\t"
    "ds_read_b64_tr_b16 a[104:105], v20 offset:4096\n\t"
    "v_mfma_f32_16x16x32_bf16 v[100:103], a[52:55], a[4:7], v[100:103]\n\t"
    "v_mfma_f32_16x16x32_bf16 v[104:107], a[56:59], a[4:7], v[104:107]\n\t"
    "ds_read_b64_tr_b16 a[106:107], v20 offset:6144\n\t"
    "v_mfma_f32_16x16x32_bf16 v[108:111], a[60:63], a[4:7], v[108:111]\n\t"
    "v_mfma_f32_16x16x32_bf16 v[112:115], a[32:35], a[8:11], v[112:115]\n\t"
    "ds_read_b128 a[76:79], v17 offset:6144\n\t"
    "buffer_load_dwordx4 v24, s[40:43], s52 offen offset:2048 lds\n\t"
    "v_mfma_f32_16x16x32_bf16 v[116:119], a[36:39], a[8:11], v[116:119]\n\t"
    "v_mfma_f32_16x16x32_bf16 v[120:123], a[40:43], a[8:11], v[120:123]\n\t"
    "ds_read_b64_tr_b16 a[108:109], v21 offset:4096\n\t"
    "v_mfma_f32_16x16x32_bf16 v[124:127], a[44:47], a[8:11], v[124:127]\n\t"
    "v_mfma_f32_16x16x32_bf16 v[128:131], a[48:51], a[8:11], v[128:131]\n\t"
    "ds_read_b64_tr_b16 a[110:111], v21 offset:6144\n\t"
    "v_mfma_f32_16x16x32_bf16 v[132:135], a[52:55], a[8:11], v[132:135]\n\t"
    "v_mfma_f32_16x16x32_bf16 v[136:139], a[56:59], a[8:11], v[136:139]\n\t"
    "ds_read_b128 a[80:83], v17 offset:8192\n\t"
    "v_mfma_f32_16x16x32_bf16 v[140:143], a[60:63], a[8:11], v[140:143]\n\t"
    "ds_read_b64_tr_b16 a[112:113], v18 offset:12288\n\t"
    "buffer_load_dwordx4 v25, s[40:43], s52 offen offset:3072 lds\n\t"
    "v_mfma_f32_16x16x32_bf16 v[144:147], a[32:35], a[12:15], v[144:147]\n\t"
    "v_mfma_f32_16x16x32_bf16 v[148:151], a[36:39], a[12:15], v[148:151]\n\t"
    "ds_read_b64_tr_b16 a[114:115], v18 offset:14336\n\t"
    "v_mfma_f32_16x16x32_bf16 v[152:155], a[40:43], a[12:15], v[152:155]\n\t"
    "v_mfma_f32_16x16x32_bf16 v[156:159], a[44:47], a[12:15], v[156:159]\n\t"
    "ds_read_b128 a[84:87], v17 offset:10240\n\t"
    "v_mfma_f32_16x16x32_bf16 v[160:163], a[48:51], a[12:15], v[160:163]\n\t"
    "v_mfma_f32_16x16x32_bf16 v[164:167], a[52:55], a[12:15], v[164:167]\n\t"
    "ds_read_b64_tr_b16 a[116:117], v19 offset:12288\n\t"
    "s_add_u32 m0, s57, 0x1000\n\t"
    "v_mfma_f32_16x16x32_bf16 v[168:171], a[56:59], a[12:15], v[168:171]\n\t"
    "buffer_load_dwordx4 v26, s[40:43], s52 offen offset:0 lds\n\t"
    "v_mfma_f32_16x16x32_bf16 v[172:175], a[60:63], a[12:15], v[172:175]\n\t"
    "ds_read_b64_tr_b16 a[118:119], v19 offset:14336\n\t"
    "v_mfma_f32_16x16x32_bf16 v[176:179], a[32:35], a[16:19], v[176:179]\n\t"
    "v_mfma_f32_16x16x32_bf16 v[180:183], a[36:39], a[16:19], v[180:183]\n\t"
    "ds_read_b128 a[88:91], v17 offset:12288\n\t"
    "v_mfma_f32_16x16x32_bf16 v[184:187], a[40:43], a[16:19], v[184:187]\n\t"
    "ds_read_b64_tr_b16 a[120:121], v20 offset:12288\n\t"
    "v_mfma_f32_16x16x32_bf16 v[188:191], a[44:47], a[16:19], v[188:191]\n\t"
    "v_mfma_f32_16x16x32_bf16 v[192:195], a[48:51], a[16:19], v[192:195]\n\t"
    "ds_read_b64_tr_b16 a[122:123], v20 offset:14336\n\t"
    "v_mfma_f32_16x16x32_bf16 v[196:199], a[52:55], a[16:19], v[196:199]\n\t"
    "buffer_load_dwordx4 v27, s[40:43], s52 offen offset:1024 lds\n\t"
    "v_mfma_f32_16x16x32_bf16 v[200:203], a[56:59], a[16:19], v[200:203]\n\t"
    "ds_read_b128 a[92:95], v17 offset:14336\n\t"
    "v_mfma_f32_16x16x32_bf16 v[204:207], a[60:63], a[16:19], v[204:207]\n\t"
    "v_mfma_f32_16x16x32_bf16 v[208:211], a[32:35], a[20:23], v[208:211]\n\t"
    "ds_read_b64_tr_b16 a[124:125], v21 offset:12288\n\t"
    "v_mfma_f32_16x16x32_bf16 v[212:215], a[36:39], a[20:23], v[212:215]\n\t"
    "v_mfma_f32_16x16x32_bf16 v[216:219], a[40:43], a[20:23], v[216:219]\n\t"
    "ds_read_b64_tr_b16 a[126:127], v21 offset:14336\n\t"
    "v_mfma_f32_16x16x32_bf16 v[220:223], a[44:47], a[20:23], v[220:223]\n\t"
    "v_mfma_f32_16x16x32_bf16 v[224:227], a[48:51], a[20:23], v[224:227]\n\t"
    "buffer_load_dwordx4 v28, s[40:43], s52 offen offset:2048 lds\n\t"
    "v_mfma_f32_16x16x32_bf16 v[228:231], a[52:55], a[20:23], v[228:231]\n\t"
    "v_mfma_f32_16x16x32_bf16 v[232:235], a[56:59], a[20:23], v[232:235]\n\t"
    "v_add_u32 v16, s55, v45\n\t"
    "v_mfma_f32_16x16x32_bf16 v[236:239], a[60:63], a[20:23], v[236:239]\n\t"
    "v_mfma_f32_16x16x32_bf16 v[240:243], a[32:35], a[24:27], v[240:243]\n\t"
    "v_mfma_f32_16x16x32_bf16 v[244:247], a[36:39], a[24:27], v[244:247]\n\t"
    "v_xor_b32 v18, 0x8000, v18\n\t"
    "v_mfma_f32_16x16x32_bf16 v[248:251], a[40:43], a[24:27], v[248:251]\n\t"
    "v_mfma_f32_16x16x32_bf16 v[252:255], a[44:47], a[24:27], v[252:255]\n\t"
    "buffer_load_dwordx4 v29, s[40:43], s52 offen offset:3072 lds\n\t"
    "v_mfma_f32_16x16x32_bf16 a[128:131], a[48:51], a[24:27], a[128:131]\n\t"
    "v_xor_b32 v19, 0x8000, v19\n\t"
    "v_mfma_f32_16x16x32_bf16 a[132:135], a[52:55], a[24:27], a[132:135]\n\t"
    "v_mfma_f32_16x16x32_bf16 a[136:139], a[56:59], a[24:27], a[136:139]\n\t"
    "v_mfma_f32_16x16x32_bf16 a[140:143], a[60:63], a[24:27], a[140:143]\n\t"
    "v_xor_b32 v20, 0x8000, v20\n\t"
    "v_mfma_f32_16x16x32_bf16 a[144:147], a[32:35], a[28:31], a[144:147]\n\t"
    "v_mfma_f32_16x16x32_bf16 a[148:151], a[36:39], a[28:31], a[148:151]\n\t"
    "v_mfma_f32_16x16x32_bf16 a[152:155], a[40:43], a[28:31], a[152:155]\n\t"
    "v_xor_b32 v21, 0x8000, v21\n\t"
    "v_mfma_f32_16x16x32_bf16 a[156:159], a[44:47], a[28:31], a[156:159]\n\t"
    "v_mfma_f32_16x16x32_bf16 a[160:163], a[48:51], a[28:31], a[160:163]\n\t"
    "v_mfma_f32_16x16x32_bf16 a[164:167], a[52:55], a[28:31], a[164:167]\n\t"
    "v_mfma_f32_16x16x32_bf16 a[168:171], a[56:59], a[28:31], a[168:171]\n\t"
    "v_mfma_f32_16x16x32_bf16 a[172:175], a[60:63], a[28:31], a[172:175]\n\t"
    "s_waitcnt vmcnt(9) lgkmcnt(0)\n\t"
    "s_barrier\n\t"
    "v_mfma_f32_16x16x32_bf16 v[48:51], a[96:99], a[64:67], v[48:51]\n\t"
    "ds_read_b128 a[0:3], v16 offset:0\n\t"
    "v_mfma_f32_16x16x32_bf16 v[52:55], a[100:103], a[64:67], v[52:55]\n\t"
    "ds_read_b64_tr_b16 a[32:33], v18 offset:0\n\t"
    "s_add_u32 m0, s58, 0\n\t"
    "v_mfma_f32_16x16x32_bf16 v[56:59], a[104:107], a[64:67], v[56:59]\n\t"
    "buffer_load_dwordx4 v30, s[44:47], s53 offen offset:0 lds\n\t"
    "v_mfma_f32_16x16x32_bf16 v[60:63], a[108:111], a[64:67], v[60:63]\n\t"
    "ds_read_b64_tr_b16 a[34:35], v18 offset:2048\n\t"
    "v_mfma_f32_16x16x32_bf16 v[64:67], a[112:115], a[64:67], v[64:67]\n\t"
    "v_mfma_f32_16x16x32_bf16 v[68:71], a[116:119], a[64:67], v[68:71]\n\t"
    "ds_read_b128 a[4:7], v16 offset:2048\n\t"
    "v_mfma_f32_16x16x32_bf16 v[72:75], a[120:123], a[64:67], v[72:75]\n\t"
    "v_mfma_f32_16x16x32_bf16 v[76:79], a[124:127], a[64:67], v[76:79]\n\t"
    "ds_read_b64_tr_b16 a[36:37], v19 offset:0\n\t"
    "v_mfma_f32_16x16x32_bf16 v[80:83], a[96:99], a[68:71], v[80:83]\n\t"
    "buffer_load_dwordx4 v31, s[44:47], s53 offen offset:1024 lds\n\t"
    "v_mfma_f32_16x16x32_bf16 v[84:87], a[100:103], a[68:71], v[84:87]\n\t"
    "ds_read_b64_tr_b16 a[38:39], v19 offset:2048\n\t"
    "v_mfma_f32_16x16x32_bf16 v[88:91], a[104:107], a[68:71], v[88:91]\n\t"
    "v_mfma_f32_16x16x32_bf16 v[92:95], a[108:111], a[68:71], v[92:95]\n\t"
    "ds_read_b128 a[8:11], v16 offset:4096\n\t"
    "v_mfma_f32_16x16x32_bf16 v[96:99], a[112:115], a[68:71], v[96:99]\n\t"
    "ds_read_b64_tr_b16 a[40:41], v20 offset:0\n\t"
    "v_mfma_f32_16x16x32_bf16 v[100:103], a[116:119], a[68:71], v[100:103]\n\t"
    "v_mfma_f32_16x16x32_bf16 v[104:107], a[120:123], a[68:71], v[104:107]\n\t"
    "ds_read_b64_tr_b16 a[42:43], v20 offset:2048\n\t"
    "v_mfma_f32_16x16x32_bf16 v[108:111], a[124:127], a[68:71], v[108:111]\n\t"
    "buffer_load_dwordx4 v32, s[44:47], s53 offen offset:2048 lds\n\t"
    "v_mfma_f32_16x16x32_bf16 v[112:115], a[96:99], a[72:75], v[112:115]\n\t"
    "ds_read_b128 a[12:15], v16 offset:6144\n\t"
    "v_mfma_f32_16x16x32_bf16 v[116:119], a[100:103], a[72:75], v[116:119]\n\t"
    "v_mfma_f32_16x16x32_bf16 v[120:123], a[104:107], a[72:75], v[120:123]\n\t"
    "ds_read_b64_tr_b16 a[44:45], v21 offset:0\n\t"
    "v_mfma_f32_16x16x32_bf16 v[124:127], a[108:111], a[72:75], v[124:127]\n\t"
    "v_mfma_f32_16x16x32_bf16 v[128:131], a[112:115], a[72:75], v[128:131]\n\t"
    "ds_read_b64_tr_b16 a[46:47], v21 offset:2048\n\t"
    "v_mfma_f32_16x16x32_bf16 v[132:135], a[116:119], a[72:75], v[132:135]\n\t"
    "v_mfma_f32_16x16x32_bf16 v[136:139], a[120:123], a[72:75], v[136:139]\n\t"
    "ds_read_b128 a[16:19], v16 offset:8192\n\t"
    "buffer_load_dwordx4 v33, s[44:47], s53 offen offset:3072 lds\n\t"
    "v_mfma_f32_16x16x32_bf16 v[140:143], a[124:127], a[72:75], v[140:143]\n\t"
    "ds_read_b64_tr_b16 a[48:49], v18 offset:8192\n\t"
    "v_mfma_f32_16x16x32_bf16 v[144:147], a[96:99], a[76:79], v[144:147]\n\t"
    "v_mfma_f32_16x16x32_bf16 v[148:151], a[100:103], a[76:79], v[148:151]\n\t"
    "ds_read_b64_tr_b16 a[50:51], v18 offset:10240\n\t"
    "v_mfma_f32_16x16x32_bf16 v[152:155], a[104:107], a[76:79], v[152:155]\n\t"
    "v_mfma_f32_16x16x32_bf16 v[156:159], a[108:111], a[76:79], v[156:159]\n\t"
    "ds_read_b128 a[20:23], v16 offset:10240\n\t"
    "s_add_u32 m0, s58, 0x1000\n\t"
    "v_mfma_f32_16x16x32_bf16 v[160:163], a[112:115], a[76:79], v[160:163]\n\t"
    "buffer_load_dwordx4 v34, s[44:47], s53 offen offset:0 lds\n\t"
    "v_mfma_f32_16x16x32_bf16 v[164:167], a[116:119], a[76:79], v[164:167]\n\t"
    "ds_read_b64_tr_b16 a[52:53], v19 offset:8192\n\t"
    "v_mfma_f32_16x16x32_bf16 v[168:171], a[120:123], a[76:79], v[168:171]\n\t"
    "v_mfma_f32_16x16x32_bf16 v[172:175], a[124:127], a[76:79], v[172:175]\n\t"
    "ds_read_b64_tr_b16 a[54:55], v19 offset:10240\n\t"
    "v_mfma_f32_16x16x32_bf16 v[176:179], a[96:99], a[80:83], v[176:179]\n\t"
    "v_mfma_f32_16x16x32_bf16 v[180:183], a[100:103], a[80:83], v[180:183]\n\t"
    "ds_read_b128 a[24:27], v16 offset:12288\n\t"
    "v_mfma_f32_16x16x32_bf16 v[184:187], a[104:107], a[80:83], v[184:187]\n\t"
    "ds_read_b64_tr_b16 a[56:57], v20 offset:8192\n\t"
    "v_mfma_f32_16x16x32_bf16 v[188:191], a[108:111], a[80:83], v[188:191]\n\t"
    "buffer_load_dwordx4 v35, s[44:47], s53 offen offset:1024 lds\n\t"
    "v_mfma_f32_16x16x32_bf16 v[192:195], a[112:115], a[80:83], v[192:195]\n\t"
    "ds_read_b64_tr_b16 a[58:59], v20 offset:10240\n\t"
    "v_mfma_f32_16x16x32_bf16 v[196:199], a[116:119], a[80:83], v[196:199]\n\t"
    "v_mfma_f32_16x16x32_bf16 v[200:203], a[120:123], a[80:83], v[200:203]\n\t"
    "ds_read_b128 a[28:31], v16 offset:14336\n\t"
    "v_mfma_f32_16x16x32_bf16 v[204:207], a[124:127], a[80:83], v[204:207]\n\t"
    "v_mfma_f32_16x16x32_bf16 v[208:211], a[96:99], a[84:87], v[208:211]\n\t"
    "ds_read_b64_tr_b16 a[60:61], v21 offset:8192\n\t"
    "v_mfma_f32_16x16x32_bf16 v[212:215], a[100:103], a[84:87], v[212:215]\n\t"
    "v_mfma_f32_16x16x32_bf16 v[216:219], a[104:107], a[84:87], v[216:219]\n\t"
    "ds_read_b64_tr_b16 a[62:63], v21 offset:10240\n\t"
    "buffer_load_dwordx4 v36, s[44:47], s53 offen offset:2048 lds\n\t"
    "v_mfma_f32_16x16x32_bf16 v[220:223], a[108:111], a[84:87], v[220:223]\n\t"
    "v_mfma_f32_16x16x32_bf16 v[224:227], a[112:115], a[84:87], v[224:227]\n\t"
    "v_mfma_f32_16x16x32_bf16 v[228:231], a[116:119], a[84:87], v[228:231]\n\t"
    "v_mfma_f32_16x16x32_bf16 v[232:235], a[120:123], a[84:87], v[232:235]\n\t"
    "v_mfma_f32_16x16x32_bf16 v[236:239], a[124:127], a[84:87], v[236:239]\n\t"
    "v_mfma_f32_16x16x32_bf16 v[240:243], a[96:99], a[88:91], v[240:243]\n\t"
    "buffer_load_dwordx4 v37, s[44:47], s53 offen offset:3072 lds\n\t"
    "v_mfma_f32_16x16x32_bf16 v[244:247], a[100:103], a[88:91], v[244:247]\n\t"
    "v_mfma_f32_16x16x32_bf16 v[248:251], a[104:107], a[88:91], v[248:251]\n\t"
    "v_mfma_f32_16x16x32_bf16 v[252:255], a[108:111], a[88:91], v[252:255]\n\t"
    "v_mfma_f32_16x16x32_bf16 a[128:131], a[112:115], a[88:91], a[128:131]\n\t"
    "v_mfma_f32_16x16x32_bf16 a[132:135], a[116:119], a[88:91], a[132:135]\n\t"
    "v_mfma_f32_16x16x32_bf16 a[136:139], a[120:123], a[88:91], a[136:139]\n\t"
    "v_mfma_f32_16x16x32_bf16 a[140:143], a[124:127], a[88:91], a[140:143]\n\t"
    "buffer_load_dword a254, %12, s[68:71], s63 offen\n\t"
    "v_mfma_f32_16x16x32_bf16 a[144:147], a[96:99], a[92:95], a[144:147]\n\t"
    "v_mfma_f32_16x16x32_bf16 a[148:151], a[100:103], a[92:95], a[148:151]\n\t"
    "v_add_u32 v17, s55, v46\n\t"
    "v_mfma_f32_16x16x32_bf16 a[152:155], a[104:107], a[92:95], a[152:155]\n\t"
    "s_mov_b32 s60, s54\n\t"
    "s_mov_b32 s54, s55\n\t"
    "v_mfma_f32_16x16x32_bf16 a[156:159], a[108:111], a[92:95], a[156:159]\n\t"
    "s_mov_b32 s55, s56\n\t"
    "s_mov_b32 s56, s60\n\t"
    "v_mfma_f32_16x16x32_bf16 a[160:163], a[112:115], a[92:95], a[160:163]\n\t"
    "s_add_u32 s57, s62, s60\n\t"
    "s_add_u32 s52, s52, 0x80\n\t"
    "v_mfma_f32_16x16x32_bf16 a[164:167], a[116:119], a[92:95], a[164:167]\n\t"
    "s_xor_b32 s58, s58, 0x8000\n\t"
    "s_add_u32 s53, s53, 0x80000\n\t"
    "v_mfma_f32_16x16x32_bf16 a[168:171], a[120:123], a[92:95], a[168:171]\n\t"
    "s_add_u32 s63, s63, 0x1000\n\t"
    "v_mfma_f32_16x16x32_bf16 a[172:175], a[124:127], a[92:95], a[172:175]\n\t"
    "s_sub_u32 s59, s59, 1\n\t"
    "s_cmp_lg_u32 s59, 0\n\t"
    "s_cbranch_scc1 kvK_loop_%=\n\t"
    "; ---- kvK_s6\n\t"
    "s_waitcnt lgkmcnt(15)\n\t"
    "v_mfma_f32_16x16x32_bf16 v[48:51], a[32:35], a[0:3], v[48:51]\n\t"
    "s_mov_b64 s[40:41], %13\n\t"
    "s_mov_b32 s42, %14\n\t"
    "s_mov_b32 s43, 0x20000\n\t"
    "s_mov_b64 s[44:45], %15\n\t"
    "s_mov_b32 s46, %16\n\t"
    "s_mov_b32 s47, 0x20000\n\t"
    "ds_read_b128 a[64:67], v17 offset:0\n\t"
    "v_mfma_f32_16x16x32_bf16 v[52:55], a[36:39], a[0:3], v[52:55]\n\t"
    "v_and_b32 v47, 0x380, %12\n\t"
    "ds_read_b64_tr_b16 a[96:97], v18 offset:4096\n\t"
    "v_mfma_f32_16x16x32_bf16 v[56:59], a[40:43], a[0:3], v[56:59]\n\t"
    "v_add_u32 v22, v47, v22\n\t"
    "v_add_u32 v22, 0, v22\n\t"
    "s_waitcnt lgkmcnt(8)\n\t"
    "v_mfma_f32_16x16x32_bf16 v[60:63], a[44:47], a[0:3], v[60:63]\n\t"
    "v_add_u32 v23, v47, v23\n\t"
    "v_add_u32 v23, 0x400, v23\n\t"
    "v_add_u32 v24, v47, v24\n\t"
    "v_add_u32 v24, 0x800, v24\n\t"
    "ds_read_b64_tr_b16 a[98:99], v18 offset:6144\n\t"
    "v_mfma_f32_16x16x32_bf16 v[64:67], a[48:51], a[0:3], v[64:67]\n\t"
    "v_add_u32 v25, v47, v25\n\t"
    "v_add_u32 v25, 0xc00, v25\n\t"
    "v_mfma_f32_16x16x32_bf16 v[68:71], a[52:55], a[0:3], v[68:71]\n\t"
    "v_add_u32 v26, v47, v26\n\t"
    "v_add_u32 v26, 0x1000, v26\n\t"
    "ds_read_b128 a[68:71], v17 offset:2048\n\t"
    "s_waitcnt lgkmcnt(4)\n\t"
    "v_mfma_f32_16x16x32_bf16 v[72:75], a[56:59], a[0:3], v[72:75]\n\t"
    "v_add_u32 v27, v47, v27\n\t"
    "v_add_u32 v27, 0x1400, v27\n\t"
    "v_add_u32 v28, v47, v28\n\t"
    "v_add_u32 v28, 0x1800, v28\n\t"
    "v_mfma_f32_16x16x32_bf16 v[76:79], a[60:63], a[0:3], v[76:79]\n\t"
    "v_add_u32 v29, v47, v29\n\t"
    "v_add_u32 v29, 0x1c00, v29\n\t"
    "ds_read_b64_tr_b16 a[100:101], v19 offset:4096\n\t"
    "v_mfma_f32_16x16x32_bf16 v[80:83], a[32:35], a[4:7], v[80:83]\n\t"
    "v_add_u32 v30, 0, %11\n\t"
    "v_add_u32 v31, 0x1fc00, %11\n\t"
    "v_add_u32 v32, 0x7800, %11\n\t"
    "v_add_u32 v33, 0x27400, %11\n\t"
    "v_mfma_f32_16x16x32_bf16 v[84:87], a[36:39], a[4:7], v[84:87]\n\t"
    "s_and_b32 s60, %1, 1\n\t"
    "s_lshl_b32 s60, s60, 13\n\t"
    "s_lshr_b32 s64, %1, 1\n\t"
    "s_lshl_b32 s64, s64, 12\n\t"
    "s_add_u32 s64, s60, s64\n\t"
    "ds_read_b64_tr_b16 a[102:103], v19 offset:6144\n\t"
    "v_mfma_f32_16x16x32_bf16 v[88:91], a[40:43], a[4:7], v[88:91]\n\t"
    "v_mfma_f32_16x16x32_bf16 v[92:95], a[44:47], a[4:7], v[92:95]\n\t"
    "ds_read_b128 a[72:75], v17 offset:4096\n\t"
    "s_add_u32 s57, s62, 0x20000\n\t"
    "s_mov_b32 s52, 0\n\t"
    "s_add_u32 m0, s57, 0\n\t"
    "v_mfma_f32_16x16x32_bf16 v[96:99], a[48:51], a[4:7], v[96:99]\n\t"
    "ds_read_b64_tr_b16 a[104:105], v20 offset:4096\n\t"
    "buffer_load_dwordx4 v22, s[40:43], s52 offen offset:0 lds\n\t"
    "v_mfma_f32_16x16x32_bf16 v[100:103], a[52:55], a[4:7], v[100:103]\n\t"
    "v_mfma_f32_16x16x32_bf16 v[104:107], a[56:59], a[4:7], v[104:107]\n\t"
    "ds_read_b64_tr_b16 a[106:107], v20 offset:6144\n\t"
    "v_mfma_f32_16x16x32_bf16 v[108:111], a[60:63], a[4:7], v[108:111]\n\t"
    "v_mfma_f32_16x16x32_bf16 v[112:115], a[32:35], a[8:11], v[112:115]\n\t"
    "ds_read_b128 a[76:79], v17 offset:6144\n\t"
    "v_mfma_f32_16x16x32_bf16 v[116:119], a[36:39], a[8:11], v[116:119]\n\t"
    "buffer_load_dwordx4 v23, s[40:43], s52 offen offset:1024 lds\n\t"
    "v_mfma_f32_16x16x32_bf16 v[120:123], a[40:43], a[8:11], v[120:123]\n\t"
    "ds_read_b64_tr_b16 a[108:109], v21 offset:4096\n\t"
    "v_mfma_f32_16x16x32_bf16 v[124:127], a[44:47], a[8:11], v[124:127]\n\t"
    "v_mfma_f32_16x16x32_bf16 v[128:131], a[48:51], a[8:11], v[128:131]\n\t"
    "ds_read_b64_tr_b16 a[110:111], v21 offset:6144\n\t"
    "v_mfma_f32_16x16x32_bf16 v[132:135], a[52:55], a[8:11], v[132:135]\n\t"
    "v_mfma_f32_16x16x32_bf16 v[136:139], a[56:59], a[8:11], v[136:139]\n\t"
    "ds_read_b128 a[80:83], v17 offset:8192\n\t"
    "v_mfma_f32_16x16x32_bf16 v[140:143], a[60:63], a[8:11], v[140:143]\n\t"
    "ds_read_b64_tr_b16 a[112:113], v18 offset:12288\n\t"
    "buffer_load_dwordx4 v24, s[40:43], s52 offen offset:2048 lds\n\t"
    "v_mfma_f32_16x16x32_bf16 v[144:147], a[32:35], a[12:15], v[144:147]\n\t"
    "v_mfma_f32_16x16x32_bf16 v[148:151], a[36:39], a[12:15], v[148:151]\n\t"
    "ds_read_b64_tr_b16 a[114:115], v18 offset:14336\n\t"
    "v_mfma_f32_16x16x32_bf16 v[152:155], a[40:43], a[12:15], v[152:155]\n\t"
    "v_mfma_f32_16x16x32_bf16 v[156:159], a[44:47], a[12:15], v[156:159]\n\t"
    "ds_read_b128 a[84:87], v17 offset:10240\n\t"
    "v_mfma_f32_16x16x32_bf16 v[160:163], a[48:51], a[12:15], v[160:163]\n\t"
    "v_mfma_f32_16x16x32_bf16 v[164:167], a[52:55], a[12:15], v[164:167]\n\t"
    "ds_read_b64_tr_b16 a[116:117], v19 offset:12288\n\t"
    "buffer_load_dwordx4 v25, s[40:43], s52 offen offset:3072 lds\n\t"
    "v_mfma_f32_16x16x32_bf16 v[168:171], a[56:59], a[12:15], v[168:171]\n\t"
    "v_mfma_f32_16x16x32_bf16 v[172:175], a[60:63], a[12:15], v[172:175]\n\t"
    "ds_read_b64_tr_b16 a[118:119], v19 offset:14336\n\t"
    "v_mfma_f32_16x16x32_bf16 v[176:179], a[32:35], a[16:19], v[176:179]\n\t"
    "v_mfma_f32_16x16x32_bf16 v[180:183], a[36:39], a[16:19], v[180:183]\n\t"
    "ds_read_b128 a[88:91], v17 offset:12288\n\t"
    "v_mfma_f32_16x16x32_bf16 v[184:187], a[40:43], a[16:19], v[184:187]\n\t"
    "ds_read_b64_tr_b16 a[120:121], v20 offset:12288\n\t"
    "s_add_u32 m0, s57, 0x1000\n\t"
    "v_mfma_f32_16x16x32_bf16 v[188:191], a[44:47], a[16:19], v[188:191]\n\t"
    "buffer_load_dwordx4 v26, s[40:43], s52 offen offset:0 lds\n\t"
    "v_mfma_f32_16x16x32_bf16 v[192:195], a[48:51], a[16:19], v[192:195]\n\t"
    "ds_read_b64_tr_b16 a[122:123], v20 offset:14336\n\t"
    "v_mfma_f32_16x16x32_bf16 v[196:199], a[52:55], a[16:19], v[196:199]\n\t"
    "v_mfma_f32_16x16x32_bf16 v[200:203], a[56:59], a[16:19], v[200:203]\n\t"
    "ds_read_b128 a[92:95], v17 offset:14336\n\t"
    "v_mfma_f32_16x16x32_bf16 v[204:207], a[60:63], a[16:19], v[204:207]\n\t"
    "v_mfma_f32_16x16x32_bf16 v[208:211], a[32:35], a[20:23], v[208:211]\n\t"
    "ds_read_b64_tr_b16 a[124:125], v21 offset:12288\n\t"
    "buffer_load_dwordx4 v27, s[40:43], s52 offen offset:1024 lds\n\t"
    "v_mfma_f32_16x16x32_bf16 v[212:215], a[36:39], a[20:23], v[212:215]\n\t"
    "v_mfma_f32_16x16x32_bf16 v[216:219], a[40:43], a[20:23], v[216:219]\n\t"
    "ds_read_b64_tr_b16 a[126:127], v21 offset:14336\n\t"
    "v_mfma_f32_16x16x32_bf16 v[220:223], a[44:47], a[20:23], v[220:223]\n\t"
    "v_mfma_f32_16x16x32_bf16 v[224:227], a[48:51], a[20:23], v[224:227]\n\t"
    "v_mfma_f32_16x16x32_bf16 v[228:231], a[52:55], a[20:23], v[228:231]\n\t"
    "v_mfma_f32_16x16x32_bf16 v[232:235], a[56:59], a[20:23], v[232:235]\n\t"
    "buffer_load_dwordx4 v28, s[40:43], s52 offen offset:2048 lds\n\t"
    "v_add_u32 v16, s55, v45\n\t"
    "v_mfma_f32_16x16x32_bf16 v[236:239], a[60:63], a[20:23], v[236:239]\n\t"
    "v_mfma_f32_16x16x32_bf16 v[240:243], a[32:35], a[24:27], v[240:243]\n\t"
    "v_mfma_f32_16x16x32_bf16 v[244:247], a[36:39], a[24:27], v[244:247]\n\t"
    "v_xor_b32 v18, 0x8000, v18\n\t"
    "v_mfma_f32_16x16x32_bf16 v[248:251], a[40:43], a[24:27], v[248:251]\n\t"
    "v_mfma_f32_16x16x32_bf16 v[252:255], a[44:47], a[24:27], v[252:255]\n\t"
    "v_mfma_f32_16x16x32_bf16 a[128:131], a[48:51], a[24:27], a[128:131]\n\t"
    "buffer_load_dwordx4 v29, s[40:43], s52 offen offset:3072 lds\n\t"
    "v_xor_b32 v19, 0x8000, v19\n\t"
    "v_mfma_f32_16x16x32_bf16 a[132:135], a[52:55], a[24:27], a[132:135]\n\t"
    "v_mfma_f32_16x16x32_bf16 a[136:139], a[56:59], a[24:27], a[136:139]\n\t"
    "v_mfma_f32_16x16x32_bf16 a[140:143], a[60:63], a[24:27], a[140:143]\n\t"
    "v_xor_b32 v20, 0x8000, v20\n\t"
    "v_mfma_f32_16x16x32_bf16 a[144:147], a[32:35], a[28:31], a[144:147]\n\t"
    "v_mfma_f32_16x16x32_bf16 a[148:151], a[36:39], a[28:31], a[148:151]\n\t"
    "v_mfma_f32_16x16x32_bf16 a[152:155], a[40:43], a[28:31], a[152:155]\n\t"
    "v_xor_b32 v21, 0x8000, v21\n\t"
    "v_mfma_f32_16x16x32_bf16 a[156:159], a[44:47], a[28:31], a[156:159]\n\t"
    "v_mfma_f32_16x16x32_bf16 a[160:163], a[48:51], a[28:31], a[160:163]\n\t"
    "v_mfma_f32_16x16x32_bf16 a[164:167], a[52:55], a[28:31], a[164:167]\n\t"
    "v_mfma_f32_16x16x32_bf16 a[168:171], a[56:59], a[28:31], a[168:171]\n\t"
    "v_mfma_f32_16x16x32_bf16 a[172:175], a[60:63], a[28:31], a[172:175]\n\t"
    "s_waitcnt vmcnt(9) lgkmcnt(0)\n\t"
    "s_barrier\n\t"
    "v_mfma_f32_16x16x32_bf16 v[48:51], a[96:99], a[64:67], v[48:51]\n\t"
    "ds_read_b128 a[0:3], v16 offset:0\n\t"
    "v_mfma_f32_16x16x32_bf16 v[52:55], a[100:103], a[64:67], v[52:55]\n\t"
    "ds_read_b64_tr_b16 a[32:33], v18 offset:0\n\t"
    "s_add_u32 s58, s64, 0\n\t"
    "s_mov_b32 s53, 0\n\t"
    "s_add_u32 m0, s58, 0\n\t"
    "v_mfma_f32_16x16x32_bf16 v[56:59], a[104:107], a[64:67], v[56:59]\n\t"
    "buffer_load_dwordx4 v30, s[44:47], s53 offen offset:0 lds\n\t"
    "v_mfma_f32_16x16x32_bf16 v[60:63], a[108:111], a[64:67], v[60:63]\n\t"
    "ds_read_b64_tr_b16 a[34:35], v18 offset:2048\n\t"
    "v_mfma_f32_16x16x32_bf16 v[64:67], a[112:115], a[64:67], v[64:67]\n\t"
    "v_mfma_f32_16x16x32_bf16 v[68:71], a[116:119], a[64:67], v[68:71]\n\t"
    "ds_read_b128 a[4:7], v16 offset:2048\n\t"
    "v_mfma_f32_16x16x32_bf16 v[72:75], a[120:123], a[64:67], v[72:75]\n\t"
    "v_mfma_f32_16x16x32_bf16 v[76:79], a[124:127], a[64:67], v[76:79]\n\t"
    "ds_read_b64_tr_b16 a[36:37], v19 offset:0\n\t"
    "v_mfma_f32_16x16x32_bf16 v[80:83], a[96:99], a[68:71], v[80:83]\n\t"
    "buffer_load_dwordx4 v31, s[44:47], s53 offen offset:1024 lds\n\t"
    "v_mfma_f32_16x16x32_bf16 v[84:87], a[100:103], a[68:71], v[84:87]\n\t"
    "ds_read_b64_tr_b16 a[38:39], v19 offset:2048\n\t"
    "v_mfma_f32_16x16x32_bf16 v[88:91], a[104:107], a[68:71], v[88:91]\n\t"
    "v_mfma_f32_16x16x32_bf16 v[92:95], a[108:111], a[68:71], v[92:95]\n\t"
    "ds_read_b128 a[8:11], v16 offset:4096\n\t"
    "v_mfma_f32_16x16x32_bf16 v[96:99], a[112:115], a[68:71], v[96:99]\n\t"
    "ds_read_b64_tr_b16 a[40:41], v20 offset:0\n\t"
    "v_mfma_f32_16x16x32_bf16 v[100:103], a[116:119], a[68:71], v[100:103]\n\t"
    "v_mfma_f32_16x16x32_bf16 v[104:107], a[120:123], a[68:71], v[104:107]\n\t"
    "ds_read_b64_tr_b16 a[42:43], v20 offset:2048\n\t"
    "v_mfma_f32_16x16x32_bf16 v[108:111], a[124:127], a[68:71], v[108:111]\n\t"
    "buffer_load_dwordx4 v32, s[44:47], s53 offen offset:2048 lds\n\t"
    "v_mfma_f32_16x16x32_bf16 v[112:115], a[96:99], a[72:75], v[112:115]\n\t"
    "ds_read_b128 a[12:15], v16 offset:6144\n\t"
    "v_mfma_f32_16x16x32_bf16 v[116:119], a[100:103], a[72:75], v[116:119]\n\t"
    "v_mfma_f32_16x16x32_bf16 v[120:123], a[104:107], a[72:75], v[120:123]\n\t"
    "ds_read_b64_tr_b16 a[44:45], v21 offset:0\n\t"
    "v_mfma_f32_16x16x32_bf16 v[124:127], a[108:111], a[72:75], v[124:127]\n\t"
    "v_mfma_f32_16x16x32_bf16 v[128:131], a[112:115], a[72:75], v[128:131]\n\t"
    "ds_read_b64_tr_b16 a[46:47], v21 offset:2048\n\t"
    "v_mfma_f32_16x16x32_bf16 v[132:135], a[116:119], a[72:75], v[132:135]\n\t"
    "v_mfma_f32_16x16x32_bf16 v[136:139], a[120:123], a[72:75], v[136:139]\n\t"
    "ds_read_b128 a[16:19], v16 offset:8192\n\t"
    "buffer_load_dwordx4 v33, s[44:47], s53 offen offset:3072 lds\n\t"
    "v_mfma_f32_16x16x32_bf16 v[140:143], a[124:127], a[72:75], v[140:143]\n\t"
    "ds_read_b64_tr_b16 a[48:49], v18 offset:8192\n\t"
    "v_mfma_f32_16x16x32_bf16 v[144:147], a[96:99], a[76:79], v[144:147]\n\t"
    "v_mfma_f32_16x16x32_bf16 v[148:151], a[100:103], a[76:79], v[148:151]\n\t"
    "ds_read_b64_tr_b16 a[50:51], v18 offset:10240\n\t"
    "v_mfma_f32_16x16x32_bf16 v[152:155], a[104:107], a[76:79], v[152:155]\n\t"
    "v_mfma_f32_16x16x32_bf16 v[156:159], a[108:111], a[76:79], v[156:159]\n\t"
    "ds_read_b128 a[20:23], v16 offset:10240\n\t"
    "s_add_u32 s58, s64, 0x4000\n\t"
    "s_mov_b32 s53, 0x80000\n\t"
    "s_add_u32 m0, s58, 0\n\t"
    "v_mfma_f32_16x16x32_bf16 v[160:163], a[112:115], a[76:79], v[160:163]\n\t"
    "buffer_load_dwordx4 v30, s[44:47], s53 offen offset:0 lds\n\t"
    "v_mfma_f32_16x16x32_bf16 v[164:167], a[116:119], a[76:79], v[164:167]\n\t"
    "ds_read_b64_tr_b16 a[52:53], v19 offset:8192\n\t"
    "v_mfma_f32_16x16x32_bf16 v[168:171], a[120:123], a[76:79], v[168:171]\n\t"
    "v_mfma_f32_16x16x32_bf16 v[172:175], a[124:127], a[76:79], v[172:175]\n\t"
    "ds_read_b64_tr_b16 a[54:55], v19 offset:10240\n\t"
    "v_mfma_f32_16x16x32_bf16 v[176:179], a[96:99], a[80:83], v[176:179]\n\t"
    "v_mfma_f32_16x16x32_bf16 v[180:183], a[100:103], a[80:83], v[180:183]\n\t"
    "ds_read_b128 a[24:27], v16 offset:12288\n\t"
    "v_mfma_f32_16x16x32_bf16 v[184:187], a[104:107], a[80:83], v[184:187]\n\t"
    "ds_read_b64_tr_b16 a[56:57], v20 offset:8192\n\t"
    "v_mfma_f32_16x16x32_bf16 v[188:191], a[108:111], a[80:83], v[188:191]\n\t"
    "buffer_load_dwordx4 v31, s[44:47], s53 offen offset:1024 lds\n\t"
    "v_mfma_f32_16x16x32_bf16 v[192:195], a[112:115], a[80:83], v[192:195]\n\t"
    "ds_read_b64_tr_b16 a[58:59], v20 offset:10240\n\t"
    "v_mfma_f32_16x16x32_bf16 v[196:199], a[116:119], a[80:83], v[196:199]\n\t"
    "v_mfma_f32_16x16x32_bf16 v[200:203], a[120:123], a[80:83], v[200:203]\n\t"
    "ds_read_b128 a[28:31], v16 offset:14336\n\t"
    "v_mfma_f32_16x16x32_bf16 v[204:207], a[124:127], a[80:83], v[204:207]\n\t"
    "v_mfma_f32_16x16x32_bf16 v[208:211], a[96:99], a[84:87], v[208:211]\n\t"
    "ds_read_b64_tr_b16 a[60:61], v21 offset:8192\n\t"
    "v_mfma_f32_16x16x32_bf16 v[212:215], a[100:103], a[84:87], v[212:215]\n\t"
    "v_mfma_f32_16x16x32_bf16 v[216:219], a[104:107], a[84:87], v[216:219]\n\t"
    "ds_read_b64_tr_b16 a[62:63], v21 offset:10240\n\t"
    "buffer_load_dwordx4 v32, s[44:47], s53 offen offset:2048 lds\n\t"
    "v_mfma_f32_16x16x32_bf16 v[220:223], a[108:111], a[84:87], v[220:223]\n\t"
    "v_mfma_f32_16x16x32_bf16 v[224:227], a[112:115], a[84:87], v[224:227]\n\t"
    "v_mfma_f32_16x16x32_bf16 v[228:231], a[116:119], a[84:87], v[228:231]\n\t"
    "v_mfma_f32_16x16x32_bf16 v[232:235], a[120:123], a[84:87], v[232:235]\n\t"
    "v_mfma_f32_16x16x32_bf16 v[236:239], a[124:127], a[84:87], v[236:239]\n\t"
    "v_mfma_f32_16x16x32_bf16 v[240:243], a[96:99], a[88:91], v[240:243]\n\t"
    "buffer_load_dwordx4 v33, s[44:47], s53 offen offset:3072 lds\n\t"
    "v_mfma_f32_16x16x32_bf16 v[244:247], a[100:103], a[88:91], v[244:247]\n\t"
    "v_mfma_f32_16x16x32_bf16 v[248:251], a[104:107], a[88:91], v[248:251]\n\t"
    "v_mfma_f32_16x16x32_bf16 v[252:255], a[108:111], a[88:91], v[252:255]\n\t"
    "v_mfma_f32_16x16x32_bf16 a[128:131], a[112:115], a[88:91], a[128:131]\n\t"
    "v_mfma_f32_16x16x32_bf16 a[132:135], a[116:119], a[88:91], a[132:135]\n\t"
    "v_mfma_f32_16x16x32_bf16 a[136:139], a[120:123], a[88:91], a[136:139]\n\t"
    "v_mfma_f32_16x16x32_bf16 a[140:143], a[124:127], a[88:91], a[140:143]\n\t"
    "buffer_load_dword a254, %12, s[68:71], s63 offen\n\t"
    "v_mfma_f32_16x16x32_bf16 a[144:147], a[96:99], a[92:95], a[144:147]\n\t"
    "v_mfma_f32_16x16x32_bf16 a[148:151], a[100:103], a[92:95], a[148:151]\n\t"
    "v_add_u32 v17, s55, v46\n\t"
    "v_mfma_f32_16x16x32_bf16 a[152:155], a[104:107], a[92:95], a[152:155]\n\t"
    "v_mfma_f32_16x16x32_bf16 a[156:159], a[108:111], a[92:95], a[156:159]\n\t"
    "v_mfma_f32_16x16x32_bf16 a[160:163], a[112:115], a[92:95], a[160:163]\n\t"
    "v_mfma_f32_16x16x32_bf16 a[164:167], a[116:119], a[92:95], a[164:167]\n\t"
    "v_mfma_f32_16x16x32_bf16 a[168:171], a[120:123], a[92:95], a[168:171]\n\t"
    "v_mfma_f32_16x16x32_bf16 a[172:175], a[124:127], a[92:95], a[172:175]\n\t"
    "; ---- kvK_s7\n\t"
    "s_waitcnt lgkmcnt(12)\n\t"
    "v_mfma_f32_16x16x32_bf16 v[48:51], a[32:35], a[0:3], v[48:51]\n\t"
    "ds_read_b128 a[64:67], v17 offset:0\n\t"
    "v_mfma_f32_16x16x32_bf16 v[52:55], a[36:39], a[0:3], v[52:55]\n\t"
    "ds_read_b64_tr_b16 a[96:97], v18 offset:4096\n\t"
    "s_add_u32 s57, s62, 0x10000\n\t"
    "s_mov_b32 s52, 0x80\n\t"
    "s_add_u32 m0, s57, 0\n\t"
    "v_mfma_f32_16x16x32_bf16 v[56:59], a[40:43], a[0:3], v[56:59]\n\t"
    "buffer_load_dwordx4 v22, s[40:43], s52 offen offset:0 lds\n\t"
    "v_mfma_f32_16x16x32_bf16 v[60:63], a[44:47], a[0:3], v[60:63]\n\t"
    "ds_read_b64_tr_b16 a[98:99], v18 offset:6144\n\t"
    "s_waitcnt lgkmcnt(3)\n\t"
    "v_mfma_f32_16x16x32_bf16 v[64:67], a[48:51], a[0:3], v[64:67]\n\t"
    "v_mfma_f32_16x16x32_bf16 v[68:71], a[52:55], a[0:3], v[68:71]\n\t"
    "ds_read_b128 a[68:71], v17 offset:2048\n\t"
    "v_mfma_f32_16x16x32_bf16 v[72:75], a[56:59], a[0:3], v[72:75]\n\t"
    "v_mfma_f32_16x16x32_bf16 v[76:79], a[60:63], a[0:3], v[76:79]\n\t"
    "ds_read_b64_tr_b16 a[100:101], v19 offset:4096\n\t"
    "v_mfma_f32_16x16x32_bf16 v[80:83], a[32:35], a[4:7], v[80:83]\n\t"
    "v_mfma_f32_16x16x32_bf16 v[84:87], a[36:39], a[4:7], v[84:87]\n\t"
    "ds_read_b64_tr_b16 a[102:103], v19 offset:6144\n\t"
    "buffer_load_dwordx4 v23, s[40:43], s52 offen offset:1024 lds\n\t"
    "v_mfma_f32_16x16x32_bf16 v[88:91], a[40:43], a[4:7], v[88:91]\n\t"
    "v_mfma_f32_16x16x32_bf16 v[92:95], a[44:47], a[4:7], v[92:95]\n\t"
    "ds_read_b128 a[72:75], v17 offset:4096\n\t"
    "v_mfma_f32_16x16x32_bf16 v[96:99], a[48:51], a[4:7], v[96:99]\n\t"
    "ds_read_b64_tr_b16 a[104:105], v20 offset:4096\n\t"
    "v_mfma_f32_16x16x32_bf16 v[100:103], a[52:55], a[4:7], v[100:103]\n\t"
    "v_mfma_f32_16x16x32_bf16 v[104:107], a[56:59], a[4:7], v[104:107]\n\t"
    "ds_read_b64_tr_b16 a[106:107], v20 offset:6144\n\t"
    "v_mfma_f32_16x16x32_bf16 v[108:111], a[60:63], a[4:7], v[108:111]\n\t"
    "v_mfma_f32_16x16x32_bf16 v[112:115], a[32:35], a[8:11], v[112:115]\n\t"
    "ds_read_b128 a[76:79], v17 offset:6144\n\t"
    "buffer_load_dwordx4 v24, s[40:43], s52 offen offset:2048 lds\n\t"
    "v_mfma_f32_16x16x32_bf16 v[116:119], a[36:39], a[8:11], v[116:119]\n\t"
    "v_mfma_f32_16x16x32_bf16 v[120:123], a[40:43], a[8:11], v[120:123]\n\t"
    "ds_read_b64_tr_b16 a[108:109], v21 offset:4096\n\t"
    "v_mfma_f32_16x16x32_bf16 v[124:127], a[44:47], a[8:11], v[124:127]\n\t"
    "v_mfma_f32_16x16x32_bf16 v[128:131], a[48:51], a[8:11], v[128:131]\n\t"
    "ds_read_b64_tr_b16 a[110:111], v21 offset:6144\n\t"
    "v_mfma_f32_16x16x32_bf16 v[132:135], a[52:55], a[8:11], v[132:135]\n\t"
    "v_mfma_f32_16x16x32_bf16 v[136:139], a[56:59], a[8:11], v[136:139]\n\t"
    "ds_read_b128 a[80:83], v17 offset:8192\n\t"
    "v_mfma_f32_16x16x32_bf16 v[140:143], a[60:63], a[8:11], v[140:143]\n\t"
    "ds_read_b64_tr_b16 a[112:113], v18 offset:12288\n\t"
    "buffer_load_dwordx4 v25, s[40:43], s52 offen offset:3072 lds\n\t"
    "v_mfma_f32_16x16x32_bf16 v[144:147], a[32:35], a[12:15], v[144:147]\n\t"
    "v_mfma_f32_16x16x32_bf16 v[148:151], a[36:39], a[12:15], v[148:151]\n\t"
    "ds_read_b64_tr_b16 a[114:115], v18 offset:14336\n\t"
    "v_mfma_f32_16x16x32_bf16 v[152:155], a[40:43], a[12:15], v[152:155]\n\t"
    "v_mfma_f32_16x16x32_bf16 v[156:159], a[44:47], a[12:15], v[156:159]\n\t"
    "ds_read_b128 a[84:87], v17 offset:10240\n\t"
    "v_mfma_f32_16x16x32_bf16 v[160:163], a[48:51], a[12:15], v[160:163]\n\t"
    "v_mfma_f32_16x16x32_bf16 v[164:167], a[52:55], a[12:15], v[164:167]\n\t"
    "ds_read_b64_tr_b16 a[116:117], v19 offset:12288\n\t"
    "s_add_u32 m0, s57, 0x1000\n\t"
    "v_mfma_f32_16x16x32_bf16 v[168:171], a[56:59], a[12:15], v[168:171]\n\t"
    "buffer_load_dwordx4 v26, s[40:43], s52 offen offset:0 lds\n\t"
    "v_mfma_f32_16x16x32_bf16 v[172:175], a[60:63], a[12:15], v[172:175]\n\t"
    "ds_read_b64_tr_b16 a[118:119], v19 offset:14336\n\t"
    "v_mfma_f32_16x16x32_bf16 v[176:179], a[32:35], a[16:19], v[176:179]\n\t"
    "v_mfma_f32_16x16x32_bf16 v[180:183], a[36:39], a[16:19], v[180:183]\n\t"
    "ds_read_b128 a[88:91], v17 offset:12288\n\t"
    "v_mfma_f32_16x16x32_bf16 v[184:187], a[40:43], a[16:19], v[184:187]\n\t"
    "ds_read_b64_tr_b16 a[120:121], v20 offset:12288\n\t"
    "v_mfma_f32_16x16x32_bf16 v[188:191], a[44:47], a[16:19], v[188:191]\n\t"
    "v_mfma_f32_16x16x32_bf16 v[192:195], a[48:51], a[16:19], v[192:195]\n\t"
    "ds_read_b64_tr_b16 a[122:123], v20 offset:14336\n\t"
    "v_mfma_f32_16x16x32_bf16 v[196:199], a[52:55], a[16:19], v[196:199]\n\t"
    "buffer_load_dwordx4 v27, s[40:43], s52 offen offset:1024 lds\n\t"
    "v_mfma_f32_16x16x32_bf16 v[200:203], a[56:59], a[16:19], v[200:203]\n\t"
    "ds_read_b128 a[92:95], v17 offset:14336\n\t"
    "v_mfma_f32_16x16x32_bf16 v[204:207], a[60:63], a[16:19], v[204:207]\n\t"
    "v_mfma_f32_16x16x32_bf16 v[208:211], a[32:35], a[20:23], v[208:211]\n\t"
    "ds_read_b64_tr_b16 a[124:125], v21 offset:12288\n\t"
    "v_mfma_f32_16x16x32_bf16 v[212:215], a[36:39], a[20:23], v[212:215]\n\t"
    "v_mfma_f32_16x16x32_bf16 v[216:219], a[40:43], a[20:23], v[216:219]\n\t"
    "ds_read_b64_tr_b16 a[126:127], v21 offset:14336\n\t"
    "v_mfma_f32_16x16x32_bf16 v[220:223], a[44:47], a[20:23], v[220:223]\n\t"
    "v_mfma_f32_16x16x32_bf16 v[224:227], a[48:51], a[20:23], v[224:227]\n\t"
    "buffer_load_dwordx4 v28, s[40:43], s52 offen offset:2048 lds\n\t"
    "v_mfma_f32_16x16x32_bf16 v[228:231], a[52:55], a[20:23], v[228:231]\n\t"
    "v_mfma_f32_16x16x32_bf16 v[232:235], a[56:59], a[20:23], v[232:235]\n\t"
    "v_and_b32 v45, 0x3fff, %7\n\t"
    "v_mfma_f32_16x16x32_bf16 v[236:239], a[60:63], a[20:23], v[236:239]\n\t"
    "v_mfma_f32_16x16x32_bf16 v[240:243], a[32:35], a[24:27], v[240:243]\n\t"
    "v_add_u32 v45, s62, v45\n\t"
    "v_mfma_f32_16x16x32_bf16 v[244:247], a[36:39], a[24:27], v[244:247]\n\t"
    "v_mfma_f32_16x16x32_bf16 v[248:251], a[40:43], a[24:27], v[248:251]\n\t"
    "v_xor_b32 v46, 64, v45\n\t"
    "v_mfma_f32_16x16x32_bf16 v[252:255], a[44:47], a[24:27], v[252:255]\n\t"
    "buffer_load_dwordx4 v29, s[40:43], s52 offen offset:3072 lds\n\t"
    "v_mfma_f32_16x16x32_bf16 a[128:131], a[48:51], a[24:27], a[128:131]\n\t"
    "v_add_u32 v16, 0x20000, v45\n\t"
    "v_mfma_f32_16x16x32_bf16 a[132:135], a[52:55], a[24:27], a[132:135]\n\t"
    "v_mfma_f32_16x16x32_bf16 a[136:139], a[56:59], a[24:27], a[136:139]\n\t"
    "v_and_b32 v18, 0x3fff, v18\n\t"
    "v_mfma_f32_16x16x32_bf16 a[140:143], a[60:63], a[24:27], a[140:143]\n\t"
    "v_mfma_f32_16x16x32_bf16 a[144:147], a[32:35], a[28:31], a[144:147]\n\t"
    "v_and_b32 v19, 0x3fff, v19\n\t"
    "v_mfma_f32_16x16x32_bf16 a[148:151], a[36:39], a[28:31], a[148:151]\n\t"
    "v_mfma_f32_16x16x32_bf16 a[152:155], a[40:43], a[28:31], a[152:155]\n\t"
    "v_and_b32 v20, 0x3fff, v20\n\t"
    "v_mfma_f32_16x16x32_bf16 a[156:159], a[44:47], a[28:31], a[156:159]\n\t"
    "v_mfma_f32_16x16x32_bf16 a[160:163], a[48:51], a[28:31], a[160:163]\n\t"
    "v_and_b32 v21, 0x3fff, v21\n\t"
    "v_mfma_f32_16x16x32_bf16 a[164:167], a[52:55], a[28:31], a[164:167]\n\t"
    "v_mfma_f32_16x16x32_bf16 a[168:171], a[56:59], a[28:31], a[168:171]\n\t"
    "v_mfma_f32_16x16x32_bf16 a[172:175], a[60:63], a[28:31], a[172:175]\n\t"
    "s_waitcnt vmcnt(13) lgkmcnt(0)\n\t"
    "s_barrier\n\t"
    "v_mfma_f32_16x16x32_bf16 v[48:51], a[96:99], a[64:67], v[48:51]\n\t"
    "ds_read_b128 a[0:3], v16 offset:0\n\t"
    "s_add_u32 s57, s62, 0x18000\n\t"
    "s_mov_b32 s52, 0x100\n\t"
    "s_add_u32 m0, s57, 0\n\t"
    "v_mfma_f32_16x16x32_bf16 v[52:55], a[100:103], a[64:67], v[52:55]\n\t"
    "buffer_load_dwordx4 v22, s[40:43], s52 offen offset:0 lds\n\t"
    "v_mfma_f32_16x16x32_bf16 v[56:59], a[104:107], a[64:67], v[56:59]\n\t"
    "ds_read_b64_tr_b16 a[16:17], v18 offset:0\n\t"
    "v_mfma_f32_16x16x32_bf16 v[60:63], a[108:111], a[64:67], v[60:63]\n\t"
    "v_mfma_f32_16x16x32_bf16 v[64:67], a[112:115], a[64:67], v[64:67]\n\t"
    "ds_read_b64_tr_b16 a[18:19], v18 offset:2048\n\t"
    "v_mfma_f32_16x16x32_bf16 v[68:71], a[116:119], a[64:67], v[68:71]\n\t"
    "buffer_load_dwordx4 v23, s[40:43], s52 offen offset:1024 lds\n\t"
    "v_mfma_f32_16x16x32_bf16 v[72:75], a[120:123], a[64:67], v[72:75]\n\t"
    "ds_read_b128 a[4:7], v16 offset:2048\n\t"
    "v_mfma_f32_16x16x32_bf16 v[76:79], a[124:127], a[64:67], v[76:79]\n\t"
    "v_cvt_pk_bf16_f32 v48, v48, v49\n\t"
    "v_cvt_pk_bf16_f32 v49, v50, v51\n\t"
    "v_cvt_pk_bf16_f32 v50, v52, v53\n\t"
    "v_cvt_pk_bf16_f32 v51, v54, v55\n\t"
    "s_mov_b32 s61, 0\n\t"
    "buffer_store_dwordx4 v[48:51], %12, s[48:51], s61 offen offset:0 sc1\n\t"
    "v_mfma_f32_16x16x32_bf16 v[80:83], a[96:99], a[68:71], v[80:83]\n\t"
    "ds_read_b64_tr_b16 a[20:21], v19 offset:0\n\t"
    "v_mfma_f32_16x16x32_bf16 v[84:87], a[100:103], a[68:71], v[84:87]\n\t"
    "v_cvt_pk_bf16_f32 v56, v56, v57\n\t"
    "v_cvt_pk_bf16_f32 v57, v58, v59\n\t"
    "v_cvt_pk_bf16_f32 v58, v60, v61\n\t"
    "v_cvt_pk_bf16_f32 v59, v62, v63\n\t"
    "v_mfma_f32_16x16x32_bf16 v[88:91], a[104:107], a[68:71], v[88:91]\n\t"
    "ds_read_b64_tr_b16 a[22:23], v19 offset:2048\n\t"
    "buffer_load_dwordx4 v24, s[40:43], s52 offen offset:2048 lds\n\t"
    "v_mfma_f32_16x16x32_bf16 v[92:95], a[108:111], a[68:71], v[92:95]\n\t"
    "v_cvt_pk_bf16_f32 v64, v64, v65\n\t"
    "v_cvt_pk_bf16_f32 v65, v66, v67\n\t"
    "v_cvt_pk_bf16_f32 v66, v68, v69\n\t"
    "v_cvt_pk_bf16_f32 v67, v70, v71\n\t"
    "v_mfma_f32_16x16x32_bf16 v[96:99], a[112:115], a[68:71], v[96:99]\n\t"
    "ds_read_b128 a[8:11], v16 offset:4096\n\t"
    "v_mfma_f32_16x16x32_bf16 v[100:103], a[116:119], a[68:71], v[100:103]\n\t"
    "v_cvt_pk_bf16_f32 v72, v72, v73\n\t"
    "v_cvt_pk_bf16_f32 v73, v74, v75\n\t"
    "v_cvt_pk_bf16_f32 v74, v76, v77\n\t"
    "v_cvt_pk_bf16_f32 v75, v78, v79\n\t"
    "s_mov_b32 s61, 0\n\t"
    "buffer_store_dwordx4 v[72:75], %12, s[48:51], s61 offen offset:3072 sc1\n\t"
    "v_mfma_f32_16x16x32_bf16 v[104:107], a[120:123], a[68:71], v[104:107]\n\t"
    "ds_read_b64_tr_b16 a[24:25], v20 offset:0\n\t"
    "v_mfma_f32_16x16x32_bf16 v[108:111], a[124:127], a[68:71], v[108:111]\n\t"
    "buffer_load_dwordx4 v25, s[40:43], s52 offen offset:3072 lds\n\t"
    "v_cvt_pk_bf16_f32 v80, v80, v81\n\t"
    "v_cvt_pk_bf16_f32 v81, v82, v83\n\t"
    "v_cvt_pk_bf16_f32 v82, v84, v85\n\t"
    "v_cvt_pk_bf16_f32 v83, v86, v87\n\t"
    "v_mfma_f32_16x16x32_bf16 v[112:115], a[96:99], a[72:75], v[112:115]\n\t"
    "ds_read_b64_tr_b16 a[26:27], v20 offset:2048\n\t"
    "v_mfma_f32_16x16x32_bf16 v[116:119], a[100:103], a[72:75], v[116:119]\n\t"
    "v_cvt_pk_bf16_f32 v88, v88, v89\n\t"
    "v_cvt_pk_bf16_f32 v89, v90, v91\n\t"
    "v_cvt_pk_bf16_f32 v90, v92, v93\n\t"
    "v_cvt_pk_bf16_f32 v91, v94, v95\n\t"
    "v_mfma_f32_16x16x32_bf16 v[120:123], a[104:107], a[72:75], v[120:123]\n\t"
    "ds_read_b128 a[12:15], v16 offset:6144\n\t"
    "v_mfma_f32_16x16x32_bf16 v[124:127], a[108:111], a[72:75], v[124:127]\n\t"
    "s_add_u32 m0, s57, 0x1000\n\t"
    "v_cvt_pk_bf16_f32 v96, v96, v97\n\t"
    "v_cvt_pk_bf16_f32 v97, v98, v99\n\t"
    "v_cvt_pk_bf16_f32 v98, v100, v101\n\t"
    "v_cvt_pk_bf16_f32 v99, v102, v103\n\t"
    "s_mov_b32 s61, 0x1000\n\t"
    "buffer_store_dwordx4 v[96:99], %12, s[48:51], s61 offen offset:2048 sc1\n\t"
    "v_mfma_f32_16x16x32_bf16 v[128:131], a[112:115], a[72:75], v[128:131]\n\t"
    "ds_read_b64_tr_b16 a[28:29], v21 offset:0\n\t"
    "buffer_load_dwordx4 v26, s[40:43], s52 offen offset:0 lds\n\t"
    "v_mfma_f32_16x16x32_bf16 v[132:135], a[116:119], a[72:75], v[132:135]\n\t"
    "v_cvt_pk_bf16_f32 v104, v104, v105\n\t"
    "v_cvt_pk_bf16_f32 v105, v106, v107\n\t"
    "v_cvt_pk_bf16_f32 v106, v108, v109\n\t"
    "v_cvt_pk_bf16_f32 v107, v110, v111\n\t"
    "v_mfma_f32_16x16x32_bf16 v[136:139], a[120:123], a[72:75], v[136:139]\n\t"
    "ds_read_b64_tr_b16 a[30:31], v21 offset:2048\n\t"
    "v_mfma_f32_16x16x32_bf16 v[140:143], a[124:127], a[72:75], v[140:143]\n\t"
    "v_cvt_pk_bf16_f32 v112, v112, v113\n\t"
    "v_cvt_pk_bf16_f32 v113, v114, v115\n\t"
    "v_cvt_pk_bf16_f32 v114, v116, v117\n\t"
    "v_cvt_pk_bf16_f32 v115, v118, v119\n\t"
    "v_mfma_f32_16x16x32_bf16 v[144:147], a[96:99], a[76:79], v[144:147]\n\t"
    "ds_read_b64_tr_b16 a[32:33], v18 offset:8192\n\t"
    "v_mfma_f32_16x16x32_bf16 v[148:151], a[100:103], a[76:79], v[148:151]\n\t"
    "buffer_load_dwordx4 v27, s[40:43], s52 offen offset:1024 lds\n\t"
    "v_cvt_pk_bf16_f32 v120, v120, v121\n\t"
    "v_cvt_pk_bf16_f32 v121, v122, v123\n\t"
    "v_cvt_pk_bf16_f32 v122, v124, v125\n\t"
    "v_cvt_pk_bf16_f32 v123, v126, v127\n\t"
    "s_mov_b32 s61, 0x2000\n\t"
    "buffer_store_dwordx4 v[120:123], %12, s[48:51], s61 offen offset:1024 sc1\n\t"
    "v_mfma_f32_16x16x32_bf16 v[152:155], a[104:107], a[76:79], v[152:155]\n\t"
    "ds_read_b64_tr_b16 a[34:35], v18 offset:10240\n\t"
    "v_mfma_f32_16x16x32_bf16 v[156:159], a[108:111], a[76:79], v[156:159]\n\t"
    "v_cvt_pk_bf16_f32 v128, v128, v129\n\t"
    "v_cvt_pk_bf16_f32 v129, v130, v131\n\t"
    "v_cvt_pk_bf16_f32 v130, v132, v133\n\t"
    "v_cvt_pk_bf16_f32 v131, v134, v135\n\t"
    "v_mfma_f32_16x16x32_bf16 v[160:163], a[112:115], a[76:79], v[160:163]\n\t"
    "ds_read_b64_tr_b16 a[36:37], v19 offset:8192\n\t"
    "v_mfma_f32_16x16x32_bf16 v[164:167], a[116:119], a[76:79], v[164:167]\n\t"
    "v_cvt_pk_bf16_f32 v136, v136, v137\n\t"
    "v_cvt_pk_bf16_f32 v137, v138, v139\n\t"
    "v_cvt_pk_bf16_f32 v138, v140, v141\n\t"
    "v_cvt_pk_bf16_f32 v139, v142, v143\n\t"
    "v_mfma_f32_16x16x32_bf16 v[168:171], a[120:123], a[76:79], v[168:171]\n\t"
    "ds_read_b64_tr_b16 a[38:39], v19 offset:10240\n\t"
    "buffer_load_dwordx4 v28, s[40:43], s52 offen offset:2048 lds\n\t"
    "v_mfma_f32_16x16x32_bf16 v[172:175], a[124:127], a[76:79], v[172:175]\n\t"
    "v_cvt_pk_bf16_f32 v144, v144, v145\n\t"
    "v_cvt_pk_bf16_f32 v145, v146, v147\n\t"
    "v_cvt_pk_bf16_f32 v146, v148, v149\n\t"
    "v_cvt_pk_bf16_f32 v147, v150, v151\n\t"
    "s_mov_b32 s61, 0x3000\n\t"
    "buffer_store_dwordx4 v[144:147], %12, s[48:51], s61 offen offset:0 sc1\n\t"
    "v_mfma_f32_16x16x32_bf16 v[176:179], a[96:99], a[80:83], v[176:179]\n\t"
    "ds_read_b64_tr_b16 a[40:41], v20 offset:8192\n\t"
    "v_mfma_f32_16x16x32_bf16 v[180:183], a[100:103], a[80:83], v[180:183]\n\t"
    "v_cvt_pk_bf16_f32 v152, v152, v153\n\t"
    "v_cvt_pk_bf16_f32 v153, v154, v155\n\t"
    "v_cvt_pk_bf16_f32 v154, v156, v157\n\t"
    "v_cvt_pk_bf16_f32 v155, v158, v159\n\t"
    "v_mfma_f32_16x16x32_bf16 v[184:187], a[104:107], a[80:83], v[184:187]\n\t"
    "ds_read_b64_tr_b16 a[42:43], v20 offset:10240\n\t"
    "v_mfma_f32_16x16x32_bf16 v[188:191], a[108:111], a[80:83], v[188:191]\n\t"
    "buffer_load_dwordx4 v29, s[40:43], s52 offen offset:3072 lds\n\t"
    "v_cvt_pk_bf16_f32 v160, v160, v161\n\t"
    "v_cvt_pk_bf16_f32 v161, v162, v163\n\t"
    "v_cvt_pk_bf16_f32 v162, v164, v165\n\t"
    "v_cvt_pk_bf16_f32 v163, v166, v167\n\t"
    "v_mfma_f32_16x16x32_bf16 v[192:195], a[112:115], a[80:83], v[192:195]\n\t"
    "ds_read_b64_tr_b16 a[44:45], v21 offset:8192\n\t"
    "v_mfma_f32_16x16x32_bf16 v[196:199], a[116:119], a[80:83], v[196:199]\n\t"
    "v_cvt_pk_bf16_f32 v168, v168, v169\n\t"
    "v_cvt_pk_bf16_f32 v169, v170, v171\n\t"
    "v_cvt_pk_bf16_f32 v170, v172, v173\n\t"
    "v_cvt_pk_bf16_f32 v171, v174, v175\n\t"
    "s_mov_b32 s61, 0x3000\n\t"
    "buffer_store_dwordx4 v[168:171], %12, s[48:51], s61 offen offset:3072 sc1\n\t"
    "v_mfma_f32_16x16x32_bf16 v[200:203], a[120:123], a[80:83], v[200:203]\n\t"
    "ds_read_b64_tr_b16 a[46:47], v21 offset:10240\n\t"
    "v_mfma_f32_16x16x32_bf16 v[204:207], a[124:127], a[80:83], v[204:207]\n\t"
    "v_cvt_pk_bf16_f32 v176, v176, v177\n\t"
    "v_cvt_pk_bf16_f32 v177, v178, v179\n\t"
    "v_cvt_pk_bf16_f32 v178, v180, v181\n\t"
    "v_cvt_pk_bf16_f32 v179, v182, v183\n\t"
    "v_mfma_f32_16x16x32_bf16 v[208:211], a[96:99], a[84:87], v[208:211]\n\t"
    "v_mfma_f32_16x16x32_bf16 v[212:215], a[100:103], a[84:87], v[212:215]\n\t"
    "v_cvt_pk_bf16_f32 v184, v184, v185\n\t"
    "v_cvt_pk_bf16_f32 v185, v186, v187\n\t"
    "v_cvt_pk_bf16_f32 v186, v188, v189\n\t"
    "v_cvt_pk_bf16_f32 v187, v190, v191\n\t"
    "v_mfma_f32_16x16x32_bf16 v[216:219], a[104:107], a[84:87], v[216:219]\n\t"
    "v_mfma_f32_16x16x32_bf16 v[220:223], a[108:111], a[84:87], v[220:223]\n\t"
    "v_cvt_pk_bf16_f32 v192, v192, v193\n\t"
    "v_cvt_pk_bf16_f32 v193, v194, v195\n\t"
    "v_cvt_pk_bf16_f32 v194, v196, v197\n\t"
    "v_cvt_pk_bf16_f32 v195, v198, v199\n\t"
    "s_mov_b32 s61, 0x8000\n\t"
    "buffer_store_dwordx4 v[192:195], %12, s[48:51], s61 offen offset:2048 sc1\n\t"
    "v_mfma_f32_16x16x32_bf16 v[224:227], a[112:115], a[84:87], v[224:227]\n\t"
    "v_mfma_f32_16x16x32_bf16 v[228:231], a[116:119], a[84:87], v[228:231]\n\t"
    "v_cvt_pk_bf16_f32 v200, v200, v201\n\t"
    "v_cvt_pk_bf16_f32 v201, v202, v203\n\t"
    "v_cvt_pk_bf16_f32 v202, v204, v205\n\t"
    "v_cvt_pk_bf16_f32 v203, v206, v207\n\t"
    "v_mfma_f32_16x16x32_bf16 v[232:235], a[120:123], a[84:87], v[232:235]\n\t"
    "v_mfma_f32_16x16x32_bf16 v[236:239], a[124:127], a[84:87], v[236:239]\n\t"
    "v_cvt_pk_bf16_f32 v208, v208, v209\n\t"
    "v_cvt_pk_bf16_f32 v209, v210, v211\n\t"
    "v_cvt_pk_bf16_f32 v210, v212, v213\n\t"
    "v_cvt_pk_bf16_f32 v211, v214, v215\n\t"
    "v_mfma_f32_16x16x32_bf16 v[240:243], a[96:99], a[88:91], v[240:243]\n\t"
    "v_mfma_f32_16x16x32_bf16 v[244:247], a[100:103], a[88:91], v[244:247]\n\t"
    "v_cvt_pk_bf16_f32 v216, v216, v217\n\t"
    "v_cvt_pk_bf16_f32 v217, v218, v219\n\t"
    "v_cvt_pk_bf16_f32 v218, v220, v221\n\t"
    "v_cvt_pk_bf16_f32 v219, v222, v223\n\t"
    "s_mov_b32 s61, 0x9000\n\t"
    "buffer_store_dwordx4 v[216:219], %12, s[48:51], s61 offen offset:1024 sc1\n\t"
    "v_mfma_f32_16x16x32_bf16 v[248:251], a[104:107], a[88:91], v[248:251]\n\t"
    "v_mfma_f32_16x16x32_bf16 v[252:255], a[108:111], a[88:91], v[252:255]\n\t"
    "v_cvt_pk_bf16_f32 v224, v224, v225\n\t"
    "v_cvt_pk_bf16_f32 v225, v226, v227\n\t"
    "v_cvt_pk_bf16_f32 v226, v228, v229\n\t"
    "v_cvt_pk_bf16_f32 v227, v230, v231\n\t"
    "v_mfma_f32_16x16x32_bf16 a[128:131], a[112:115], a[88:91], a[128:131]\n\t"
    "v_mfma_f32_16x16x32_bf16 a[132:135], a[116:119], a[88:91], a[132:135]\n\t"
    "v_cvt_pk_bf16_f32 v232, v232, v233\n\t"
    "v_cvt_pk_bf16_f32 v233, v234, v235\n\t"
    "v_cvt_pk_bf16_f32 v234, v236, v237\n\t"
    "v_cvt_pk_bf16_f32 v235, v238, v239\n\t"
    "v_mfma_f32_16x16x32_bf16 a[136:139], a[120:123], a[88:91], a[136:139]\n\t"
    "v_mfma_f32_16x16x32_bf16 a[140:143], a[124:127], a[88:91], a[140:143]\n\t"
    "v_cvt_pk_bf16_f32 v240, v240, v241\n\t"
    "v_cvt_pk_bf16_f32 v241, v242, v243\n\t"
    "v_cvt_pk_bf16_f32 v242, v244, v245\n\t"
    "v_cvt_pk_bf16_f32 v243, v246, v247\n\t"
    "v_mfma_f32_16x16x32_bf16 a[144:147], a[96:99], a[92:95], a[144:147]\n\t"
    "v_mfma_f32_16x16x32_bf16 a[148:151], a[100:103], a[92:95], a[148:151]\n\t"
    "v_cvt_pk_bf16_f32 v248, v248, v249\n\t"
    "v_cvt_pk_bf16_f32 v249, v250, v251\n\t"
    "v_cvt_pk_bf16_f32 v250, v252, v253\n\t"
    "v_cvt_pk_bf16_f32 v251, v254, v255\n\t"
    "v_mfma_f32_16x16x32_bf16 a[152:155], a[104:107], a[92:95], a[152:155]\n\t"
    "v_mfma_f32_16x16x32_bf16 a[156:159], a[108:111], a[92:95], a[156:159]\n\t"
    "v_accvgpr_read_b32 v36, a128\n\t"
    "v_accvgpr_read_b32 v37, a129\n\t"
    "v_accvgpr_read_b32 v38, a130\n\t"
    "v_accvgpr_read_b32 v39, a131\n\t"
    "v_accvgpr_read_b32 v40, a132\n\t"
    "v_accvgpr_read_b32 v41, a133\n\t"
    "v_accvgpr_read_b32 v42, a134\n\t"
    "v_accvgpr_read_b32 v43, a135\n\t"
    "v_cvt_pk_bf16_f32 v36, v36, v37\n\t"
    "v_cvt_pk_bf16_f32 v37, v38, v39\n\t"
    "v_cvt_pk_bf16_f32 v38, v40, v41\n\t"
    "v_cvt_pk_bf16_f32 v39, v42, v43\n\t"
    "s_mov_b32 s61, 0xa000\n\t"
    "buffer_store_dwordx4 v[36:39], %12, s[48:51], s61 offen offset:2048 sc1\n\t"
    "v_mfma_f32_16x16x32_bf16 a[160:163], a[112:115], a[92:95], a[160:163]\n\t"
    "v_mfma_f32_16x16x32_bf16 a[164:167], a[116:119], a[92:95], a[164:167]\n\t"
    "v_accvgpr_read_b32 v36, a136\n\t"
    "v_accvgpr_read_b32 v37, a137\n\t"
    "v_accvgpr_read_b32 v38, a138\n\t"
    "v_accvgpr_read_b32 v39, a139\n\t"
    "v_accvgpr_read_b32 v40, a140\n\t"
    "v_accvgpr_read_b32 v41, a141\n\t"
    "v_accvgpr_read_b32 v42, a142\n\t"
    "v_accvgpr_read_b32 v43, a143\n\t"
    "v_cvt_pk_bf16_f32 v36, v36, v37\n\t"
    "v_cvt_pk_bf16_f32 v37, v38, v39\n\t"
    "v_cvt_pk_bf16_f32 v38, v40, v41\n\t"
    "v_cvt_pk_bf16_f32 v39, v42, v43\n\t"
    "s_mov_b32 s61, 0xa000\n\t"
    "buffer_store_dwordx4 v[36:39], %12, s[48:51], s61 offen offset:3072 sc1\n\t"
    "v_mfma_f32_16x16x32_bf16 a[168:171], a[120:123], a[92:95], a[168:171]\n\t"
    "v_mfma_f32_16x16x32_bf16 a[172:175], a[124:127], a[92:95], a[172:175]\n\t"
    "v_accvgpr_read_b32 v36, a144\n\t"
    "v_accvgpr_read_b32 v37, a145\n\t"
    "v_accvgpr_read_b32 v38, a146\n\t"
    "v_accvgpr_read_b32 v39, a147\n\t"
    "v_accvgpr_read_b32 v40, a148\n\t"
    "v_accvgpr_read_b32 v41, a149\n\t"
    "v_accvgpr_read_b32 v42, a150\n\t"
    "v_accvgpr_read_b32 v43, a151\n\t"
    "v_cvt_pk_bf16_f32 v36, v36, v37\n\t"
    "v_cvt_pk_bf16_f32 v37, v38, v39\n\t"
    "v_cvt_pk_bf16_f32 v38, v40, v41\n\t"
    "v_cvt_pk_bf16_f32 v39, v42, v43\n\t"
    "s_mov_b32 s61, 0xb000\n\t"
    "buffer_store_dwordx4 v[36:39], %12, s[48:51], s61 offen offset:0 sc1\n\t"
    "s_nop 1\n\t"
    "v_accvgpr_read_b32 v36, a152\n\t"
    "v_accvgpr_read_b32 v37, a153\n\t"
    "v_accvgpr_read_b32 v38, a154\n\t"
    "v_accvgpr_read_b32 v39, a155\n\t"
    "v_accvgpr_read_b32 v40, a156\n\t"
    "v_accvgpr_read_b32 v41, a157\n\t"
    "v_accvgpr_read_b32 v42, a158\n\t"
    "v_accvgpr_read_b32 v43, a159\n\t"
    "v_cvt_pk_bf16_f32 v36, v36, v37\n\t"
    "v_cvt_pk_bf16_f32 v37, v38, v39\n\t"
    "v_cvt_pk_bf16_f32 v38, v40, v41\n\t"
    "v_cvt_pk_bf16_f32 v39, v42, v43\n\t"
    "s_mov_b32 s61, 0xb000\n\t"
    "buffer_store_dwordx4 v[36:39], %12, s[48:51], s61 offen offset:1024 sc1\n\t"
    "s_nop 1\n\t"
    "v_accvgpr_read_b32 v36, a160\n\t"
    "v_accvgpr_read_b32 v37, a161\n\t"
    "v_accvgpr_read_b32 v38, a162\n\t"
    "v_accvgpr_read_b32 v39, a163\n\t"
    "v_accvgpr_read_b32 v40, a164\n\t"
    "v_accvgpr_read_b32 v41, a165\n\t"
    "v_accvgpr_read_b32 v42, a166\n\t"
    "v_accvgpr_read_b32 v43, a167\n\t"
    "v_cvt_pk_bf16_f32 v36, v36, v37\n\t"
    "v_cvt_pk_bf16_f32 v37, v38, v39\n\t"
    "v_cvt_pk_bf16_f32 v38, v40, v41\n\t"
    "v_cvt_pk_bf16_f32 v39, v42, v43\n\t"
    "s_mov_b32 s61, 0xb000\n\t"
    "buffer_store_dwordx4 v[36:39], %12, s[48:51], s61 offen offset:2048 sc1\n\t"
    "s_nop 1\n\t"
    "v_accvgpr_read_b32 v36, a168\n\t"
    "v_accvgpr_read_b32 v37, a169\n\t"
    "v_accvgpr_read_b32 v38, a170\n\t"
    "v_accvgpr_read_b32 v39, a171\n\t"
    "v_accvgpr_read_b32 v40, a172\n\t"
    "v_accvgpr_read_b32 v41, a173\n\t"
    "v_accvgpr_read_b32 v42, a174\n\t"
    "v_accvgpr_read_b32 v43, a175\n\t"
    "v_cvt_pk_bf16_f32 v36, v36, v37\n\t"
    "v_cvt_pk_bf16_f32 v37, v38, v39\n\t"
    "v_cvt_pk_bf16_f32 v38, v40, v41\n\t"
    "v_cvt_pk_bf16_f32 v39, v42, v43\n\t"
    "s_mov_b32 s61, 0xb000\n\t"
    "buffer_store_dwordx4 v[36:39], %12, s[48:51], s61 offen offset:3072 sc1\n\t"
    "s_mov_b32 s59, 0\n\t"
    "s_branch q_start_%=\n\t"
    "; ---- kvV_first\n\t"
    "kvV_first_%=:\n\t"
    "s_waitcnt lgkmcnt(14)\n\t"
    "v_mfma_f32_16x16x32_bf16 v[48:51], a[0:3], a[32:35], 0\n\t"
    "ds_read_b128 a[64:67], v17 offset:0\n\t"
    "v_mfma_f32_16x16x32_bf16 v[52:55], a[4:7], a[32:35], 0\n\t"
    "ds_read_b64_tr_b16 a[96:97], v18 offset:4096\n\t"
    "s_add_u32 m0, s57, 0\n\t"
    "v_mfma_f32_16x16x32_bf16 v[56:59], a[8:11], a[32:35], 0\n\t"
    "buffer_load_dwordx4 v22, s[40:43], s52 offen offset:0 lds\n\t"
    "v_mfma_f32_16x16x32_bf16 v[60:63], a[12:15], a[32:35], 0\n\t"
    "ds_read_b64_tr_b16 a[98:99], v18 offset:6144\n\t"
    "s_waitcnt lgkmcnt(8)\n\t"
    "v_mfma_f32_16x16x32_bf16 v[64:67], a[16:19], a[32:35], 0\n\t"
    "v_mfma_f32_16x16x32_bf16 v[68:71], a[20:23], a[32:35], 0\n\t"
    "ds_read_b128 a[68:71], v17 offset:2048\n\t"
    "v_mfma_f32_16x16x32_bf16 v[72:75], a[24:27], a[32:35], 0\n\t"
    "s_waitcnt lgkmcnt(6)\n\t"
    "v_mfma_f32_16x16x32_bf16 v[76:79], a[28:31], a[32:35], 0\n\t"
    "ds_read_b64_tr_b16 a[100:101], v19 offset:4096\n\t"
    "v_mfma_f32_16x16x32_bf16 v[80:83], a[0:3], a[36:39], 0\n\t"
    "v_mfma_f32_16x16x32_bf16 v[84:87], a[4:7], a[36:39], 0\n\t"
    "ds_read_b64_tr_b16 a[102:103], v19 offset:6144\n\t"
    "buffer_load_dwordx4 v23, s[40:43], s52 offen offset:1024 lds\n\t"
    "v_mfma_f32_16x16x32_bf16 v[88:91], a[8:11], a[36:39], 0\n\t"
    "v_mfma_f32_16x16x32_bf16 v[92:95], a[12:15], a[36:39], 0\n\t"
    "ds_read_b128 a[72:75], v17 offset:4096\n\t"
    "v_mfma_f32_16x16x32_bf16 v[96:99], a[16:19], a[36:39], 0\n\t"
    "ds_read_b64_tr_b16 a[104:105], v20 offset:4096\n\t"
    "v_mfma_f32_16x16x32_bf16 v[100:103], a[20:23], a[36:39], 0\n\t"
    "v_mfma_f32_16x16x32_bf16 v[104:107], a[24:27], a[36:39], 0\n\t"
    "ds_read_b64_tr_b16 a[106:107], v20 offset:6144\n\t"
    "v_mfma_f32_16x16x32_bf16 v[108:111], a[28:31], a[36:39], 0\n\t"
    "v_mfma_f32_16x16x32_bf16 v[112:115], a[0:3], a[40:43], 0\n\t"
    "ds_read_b128 a[76:79], v17 offset:6144\n\t"
    "buffer_load_dwordx4 v24, s[40:43], s52 offen offset:2048 lds\n\t"
    "v_mfma_f32_16x16x32_bf16 v[116:119], a[4:7], a[40:43], 0\n\t"
    "v_mfma_f32_16x16x32_bf16 v[120:123], a[8:11], a[40:43], 0\n\t"
    "ds_read_b64_tr_b16 a[108:109], v21 offset:4096\n\t"
    "v_mfma_f32_16x16x32_bf16 v[124:127], a[12:15], a[40:43], 0\n\t"
    "v_mfma_f32_16x16x32_bf16 v[128:131], a[16:19], a[40:43], 0\n\t"
    "ds_read_b64_tr_b16 a[110:111], v21 offset:6144\n\t"
    "v_mfma_f32_16x16x32_bf16 v[132:135], a[20:23], a[40:43], 0\n\t"
    "v_mfma_f32_16x16x32_bf16 v[136:139], a[24:27], a[40:43], 0\n\t"
    "ds_read_b128 a[80:83], v17 offset:8192\n\t"
    "v_mfma_f32_16x16x32_bf16 v[140:143], a[28:31], a[40:43], 0\n\t"
    "ds_read_b64_tr_b16 a[112:113], v18 offset:12288\n\t"
    "buffer_load_dwordx4 v25, s[40:43], s52 offen offset:3072 lds\n\t"
    "v_mfma_f32_16x16x32_bf16 v[144:147], a[0:3], a[44:47], 0\n\t"
    "v_mfma_f32_16x16x32_bf16 v[148:151], a[4:7], a[44:47], 0\n\t"
    "ds_read_b64_tr_b16 a[114:115], v18 offset:14336\n\t"
    "v_mfma_f32_16x16x32_bf16 v[152:155], a[8:11], a[44:47], 0\n\t"
    "v_mfma_f32_16x16x32_bf16 v[156:159], a[12:15], a[44:47], 0\n\t"
    "ds_read_b128 a[84:87], v17 offset:10240\n\t"
    "v_mfma_f32_16x16x32_bf16 v[160:163], a[16:19], a[44:47], 0\n\t"
    "v_mfma_f32_16x16x32_bf16 v[164:167], a[20:23], a[44:47], 0\n\t"
    "ds_read_b64_tr_b16 a[116:117], v19 offset:12288\n\t"
    "s_add_u32 m0, s57, 0x1000\n\t"
    "v_mfma_f32_16x16x32_bf16 v[168:171], a[24:27], a[44:47], 0\n\t"
    "buffer_load_dwordx4 v26, s[40:43], s52 offen offset:0 lds\n\t"
    "v_mfma_f32_16x16x32_bf16 v[172:175], a[28:31], a[44:47], 0\n\t"
    "ds_read_b64_tr_b16 a[118:119], v19 offset:14336\n\t"
    "v_mfma_f32_16x16x32_bf16 v[176:179], a[0:3], a[48:51], 0\n\t"
    "v_mfma_f32_16x16x32_bf16 v[180:183], a[4:7], a[48:51], 0\n\t"
    "ds_read_b128 a[88:91], v17 offset:12288\n\t"
    "v_mfma_f32_16x16x32_bf16 v[184:187], a[8:11], a[48:51], 0\n\t"
    "ds_read_b64_tr_b16 a[120:121], v20 offset:12288\n\t"
    "v_mfma_f32_16x16x32_bf16 v[188:191], a[12:15], a[48:51], 0\n\t"
    "v_mfma_f32_16x16x32_bf16 v[192:195], a[16:19], a[48:51], 0\n\t"
    "ds_read_b64_tr_b16 a[122:123], v20 offset:14336\n\t"
    "v_mfma_f32_16x16x32_bf16 v[196:199], a[20:23], a[48:51], 0\n\t"
    "buffer_load_dwordx4 v27, s[40:43], s52 offen offset:1024 lds\n\t"
    "v_mfma_f32_16x16x32_bf16 v[200:203], a[24:27], a[48:51], 0\n\t"
    "ds_read_b128 a[92:95], v17 offset:14336\n\t"
    "v_mfma_f32_16x16x32_bf16 v[204:207], a[28:31], a[48:51], 0\n\t"
    "v_mfma_f32_16x16x32_bf16 v[208:211], a[0:3], a[52:55], 0\n\t"
    "ds_read_b64_tr_b16 a[124:125], v21 offset:12288\n\t"
    "v_mfma_f32_16x16x32_bf16 v[212:215], a[4:7], a[52:55], 0\n\t"
    "v_mfma_f32_16x16x32_bf16 v[216:219], a[8:11], a[52:55], 0\n\t"
    "ds_read_b64_tr_b16 a[126:127], v21 offset:14336\n\t"
    "v_mfma_f32_16x16x32_bf16 v[220:223], a[12:15], a[52:55], 0\n\t"
    "v_mfma_f32_16x16x32_bf16 v[224:227], a[16:19], a[52:55], 0\n\t"
    "buffer_load_dwordx4 v28, s[40:43], s52 offen offset:2048 lds\n\t"
    "v_mfma_f32_16x16x32_bf16 v[228:231], a[20:23], a[52:55], 0\n\t"
    "v_mfma_f32_16x16x32_bf16 v[232:235], a[24:27], a[52:55], 0\n\t"
    "v_add_u32 v16, s55, v45\n\t"
    "v_mfma_f32_16x16x32_bf16 v[236:239], a[28:31], a[52:55], 0\n\t"
    "v_mfma_f32_16x16x32_bf16 v[240:243], a[0:3], a[56:59], 0\n\t"
    "v_mfma_f32_16x16x32_bf16 v[244:247], a[4:7], a[56:59], 0\n\t"
    "v_xor_b32 v18, 0x8000, v18\n\t"
    "v_mfma_f32_16x16x32_bf16 v[248:251], a[8:11], a[56:59], 0\n\t"
    "v_mfma_f32_16x16x32_bf16 v[252:255], a[12:15], a[56:59], 0\n\t"
    "buffer_load_dwordx4 v29, s[40:43], s52 offen offset:3072 lds\n\t"
    "v_mfma_f32_16x16x32_bf16 a[128:131], a[16:19], a[56:59], 0\n\t"
    "v_xor_b32 v19, 0x8000, v19\n\t"
    "v_mfma_f32_16x16x32_bf16 a[132:135], a[20:23], a[56:59], 0\n\t"
    "v_mfma_f32_16x16x32_bf16 a[136:139], a[24:27], a[56:59], 0\n\t"
    "v_mfma_f32_16x16x32_bf16 a[140:143], a[28:31], a[56:59], 0\n\t"
    "v_xor_b32 v20, 0x8000, v20\n\t"
    "s_waitcnt lgkmcnt(15)\n\t"
    "v_mfma_f32_16x16x32_bf16 a[144:147], a[0:3], a[60:63], 0\n\t"
    "v_mfma_f32_16x16x32_bf16 a[148:151], a[4:7], a[60:63], 0\n\t"
    "v_mfma_f32_16x16x32_bf16 a[152:155], a[8:11], a[60:63], 0\n\t"
    "v_xor_b32 v21, 0x8000, v21\n\t"
    "v_mfma_f32_16x16x32_bf16 a[156:159], a[12:15], a[60:63], 0\n\t"
    "v_mfma_f32_16x16x32_bf16 a[160:163], a[16:19], a[60:63], 0\n\t"
    "v_mfma_f32_16x16x32_bf16 a[164:167], a[20:23], a[60:63], 0\n\t"
    "v_mfma_f32_16x16x32_bf16 a[168:171], a[24:27], a[60:63], 0\n\t"
    "v_mfma_f32_16x16x32_bf16 a[172:175], a[28:31], a[60:63], 0\n\t"
    "s_waitcnt vmcnt(8) lgkmcnt(0)\n\t"
    "s_barrier\n\t"
    "v_mfma_f32_16x16x32_bf16 v[48:51], a[64:67], a[96:99], v[48:51]\n\t"
    "ds_read_b128 a[0:3], v16 offset:0\n\t"
    "v_mfma_f32_16x16x32_bf16 v[52:55], a[68:71], a[96:99], v[52:55]\n\t"
    "ds_read_b64_tr_b16 a[32:33], v18 offset:0\n\t"
    "s_add_u32 m0, s58, 0\n\t"
    "v_mfma_f32_16x16x32_bf16 v[56:59], a[72:75], a[96:99], v[56:59]\n\t"
    "buffer_load_dwordx4 v30, s[44:47], s53 offen offset:0 lds\n\t"
    "v_mfma_f32_16x16x32_bf16 v[60:63], a[76:79], a[96:99], v[60:63]\n\t"
    "ds_read_b64_tr_b16 a[34:35], v18 offset:2048\n\t"
    "v_mfma_f32_16x16x32_bf16 v[64:67], a[80:83], a[96:99], v[64:67]\n\t"
    "v_mfma_f32_16x16x32_bf16 v[68:71], a[84:87], a[96:99], v[68:71]\n\t"
    "ds_read_b128 a[4:7], v16 offset:2048\n\t"
    "v_mfma_f32_16x16x32_bf16 v[72:75], a[88:91], a[96:99], v[72:75]\n\t"
    "v_mfma_f32_16x16x32_bf16 v[76:79], a[92:95], a[96:99], v[76:79]\n\t"
    "ds_read_b64_tr_b16 a[36:37], v19 offset:0\n\t"
    "v_mfma_f32_16x16x32_bf16 v[80:83], a[64:67], a[100:103], v[80:83]\n\t"
    "buffer_load_dwordx4 v31, s[44:47], s53 offen offset:1024 lds\n\t"
    "v_mfma_f32_16x16x32_bf16 v[84:87], a[68:71], a[100:103], v[84:87]\n\t"
    "ds_read_b64_tr_b16 a[38:39], v19 offset:2048\n\t"
    "v_mfma_f32_16x16x32_bf16 v[88:91], a[72:75], a[100:103], v[88:91]\n\t"
    "v_mfma_f32_16x16x32_bf16 v[92:95], a[76:79], a[100:103], v[92:95]\n\t"
    "ds_read_b128 a[8:11], v16 offset:4096\n\t"
    "v_mfma_f32_16x16x32_bf16 v[96:99], a[80:83], a[100:103], v[96:99]\n\t"
    "ds_read_b64_tr_b16 a[40:41], v20 offset:0\n\t"
    "v_mfma_f32_16x16x32_bf16 v[100:103], a[84:87], a[100:103], v[100:103]\n\t"
    "v_mfma_f32_16x16x32_bf16 v[104:107], a[88:91], a[100:103], v[104:107]\n\t"
    "ds_read_b64_tr_b16 a[42:43], v20 offset:2048\n\t"
    "v_mfma_f32_16x16x32_bf16 v[108:111], a[92:95], a[100:103], v[108:111]\n\t"
    "buffer_load_dwordx4 v32, s[44:47], s53 offen offset:2048 lds\n\t"
    "v_mfma_f32_16x16x32_bf16 v[112:115], a[64:67], a[104:107], v[112:115]\n\t"
    "ds_read_b128 a[12:15], v16 offset:6144\n\t"
    "v_mfma_f32_16x16x32_bf16 v[116:119], a[68:71], a[104:107], v[116:119]\n\t"
    "v_mfma_f32_16x16x32_bf16 v[120:123], a[72:75], a[104:107], v[120:123]\n\t"
    "ds_read_b64_tr_b16 a[44:45], v21 offset:0\n\t"
    "v_mfma_f32_16x16x32_bf16 v[124:127], a[76:79], a[104:107], v[124:127]\n\t"
    "v_mfma_f32_16x16x32_bf16 v[128:131], a[80:83], a[104:107], v[128:131]\n\t"
    "ds_read_b64_tr_b16 a[46:47], v21 offset:2048\n\t"
    "v_mfma_f32_16x16x32_bf16 v[132:135], a[84:87], a[104:107], v[132:135]\n\t"
    "v_mfma_f32_16x16x32_bf16 v[136:139], a[88:91], a[104:107], v[136:139]\n\t"
    "ds_read_b128 a[16:19], v16 offset:8192\n\t"
    "buffer_load_dwordx4 v33, s[44:47], s53 offen offset:3072 lds\n\t"
    "v_mfma_f32_16x16x32_bf16 v[140:143], a[92:95], a[104:107], v[140:143]\n\t"
    "ds_read_b64_tr_b16 a[48:49], v18 offset:8192\n\t"
    "v_mfma_f32_16x16x32_bf16 v[144:147], a[64:67], a[108:111], v[144:147]\n\t"
    "v_mfma_f32_16x16x32_bf16 v[148:151], a[68:71], a[108:111], v[148:151]\n\t"
    "ds_read_b64_tr_b16 a[50:51], v18 offset:10240\n\t"
    "v_mfma_f32_16x16x32_bf16 v[152:155], a[72:75], a[108:111], v[152:155]\n\t"
    "v_mfma_f32_16x16x32_bf16 v[156:159], a[76:79], a[108:111], v[156:159]\n\t"
    "ds_read_b128 a[20:23], v16 offset:10240\n\t"
    "s_add_u32 m0, s58, 0x1000\n\t"
    "v_mfma_f32_16x16x32_bf16 v[160:163], a[80:83], a[108:111], v[160:163]\n\t"
    "buffer_load_dwordx4 v34, s[44:47], s53 offen offset:0 lds\n\t"
    "v_mfma_f32_16x16x32_bf16 v[164:167], a[84:87], a[108:111], v[164:167]\n\t"
    "ds_read_b64_tr_b16 a[52:53], v19 offset:8192\n\t"
    "v_mfma_f32_16x16x32_bf16 v[168:171], a[88:91], a[108:111], v[168:171]\n\t"
    "v_mfma_f32_16x16x32_bf16 v[172:175], a[92:95], a[108:111], v[172:175]\n\t"
    "ds_read_b64_tr_b16 a[54:55], v19 offset:10240\n\t"
    "v_mfma_f32_16x16x32_bf16 v[176:179], a[64:67], a[112:115], v[176:179]\n\t"
    "v_mfma_f32_16x16x32_bf16 v[180:183], a[68:71], a[112:115], v[180:183]\n\t"
    "ds_read_b128 a[24:27], v16 offset:12288\n\t"
    "v_mfma_f32_16x16x32_bf16 v[184:187], a[72:75], a[112:115], v[184:187]\n\t"
    "ds_read_b64_tr_b16 a[56:57], v20 offset:8192\n\t"
    "v_mfma_f32_16x16x32_bf16 v[188:191], a[76:79], a[112:115], v[188:191]\n\t"
    "buffer_load_dwordx4 v35, s[44:47], s53 offen offset:1024 lds\n\t"
    "v_mfma_f32_16x16x32_bf16 v[192:195], a[80:83], a[112:115], v[192:195]\n\t"
    "ds_read_b64_tr_b16 a[58:59], v20 offset:10240\n\t"
    "v_mfma_f32_16x16x32_bf16 v[196:199], a[84:87], a[112:115], v[196:199]\n\t"
    "v_mfma_f32_16x16x32_bf16 v[200:203], a[88:91], a[112:115], v[200:203]\n\t"
    "ds_read_b128 a[28:31], v16 offset:14336\n\t"
    "v_mfma_f32_16x16x32_bf16 v[204:207], a[92:95], a[112:115], v[204:207]\n\t"
    "v_mfma_f32_16x16x32_bf16 v[208:211], a[64:67], a[116:119], v[208:211]\n\t"
    "ds_read_b64_tr_b16 a[60:61], v21 offset:8192\n\t"
    "v_mfma_f32_16x16x32_bf16 v[212:215], a[68:71], a[116:119], v[212:215]\n\t"
    "v_mfma_f32_16x16x32_bf16 v[216:219], a[72:75], a[116:119], v[216:219]\n\t"
    "ds_read_b64_tr_b16 a[62:63], v21 offset:10240\n\t"
    "buffer_load_dwordx4 v36, s[44:47], s53 offen offset:2048 lds\n\t"
    "v_mfma_f32_16x16x32_bf16 v[220:223], a[76:79], a[116:119], v[220:223]\n\t"
    "v_mfma_f32_16x16x32_bf16 v[224:227], a[80:83], a[116:119], v[224:227]\n\t"
    "v_mfma_f32_16x16x32_bf16 v[228:231], a[84:87], a[116:119], v[228:231]\n\t"
    "v_mfma_f32_16x16x32_bf16 v[232:235], a[88:91], a[116:119], v[232:235]\n\t"
    "v_mfma_f32_16x16x32_bf16 v[236:239], a[92:95], a[116:119], v[236:239]\n\t"
    "v_mfma_f32_16x16x32_bf16 v[240:243], a[64:67], a[120:123], v[240:243]\n\t"
    "buffer_load_dwordx4 v37, s[44:47], s53 offen offset:3072 lds\n\t"
    "v_mfma_f32_16x16x32_bf16 v[244:247], a[68:71], a[120:123], v[244:247]\n\t"
    "v_mfma_f32_16x16x32_bf16 v[248:251], a[72:75], a[120:123], v[248:251]\n\t"
    "v_mfma_f32_16x16x32_bf16 v[252:255], a[76:79], a[120:123], v[252:255]\n\t"
    "v_mfma_f32_16x16x32_bf16 a[128:131], a[80:83], a[120:123], a[128:131]\n\t"
    "v_mfma_f32_16x16x32_bf16 a[132:135], a[84:87], a[120:123], a[132:135]\n\t"
    "v_mfma_f32_16x16x32_bf16 a[136:139], a[88:91], a[120:123], a[136:139]\n\t"
    "v_mfma_f32_16x16x32_bf16 a[140:143], a[92:95], a[120:123], a[140:143]\n\t"
    "buffer_load_dword a254, %12, s[68:71], s63 offen\n\t"
    "v_mfma_f32_16x16x32_bf16 a[144:147], a[64:67], a[124:127], a[144:147]\n\t"
    "v_mfma_f32_16x16x32_bf16 a[148:151], a[68:71], a[124:127], a[148:151]\n\t"
    "v_add_u32 v17, s55, v46\n\t"
    "v_mfma_f32_16x16x32_bf16 a[152:155], a[72:75], a[124:127], a[152:155]\n\t"
    "s_mov_b32 s60, s54\n\t"
    "s_mov_b32 s54, s55\n\t"
    "v_mfma_f32_16x16x32_bf16 a[156:159], a[76:79], a[124:127], a[156:159]\n\t"
    "s_mov_b32 s55, s56\n\t"
    "s_mov_b32 s56, s60\n\t"
    "v_mfma_f32_16x16x32_bf16 a[160:163], a[80:83], a[124:127], a[160:163]\n\t"
    "s_add_u32 s57, s62, s60\n\t"
    "s_add_u32 s52, s52, 0x80\n\t"
    "v_mfma_f32_16x16x32_bf16 a[164:167], a[84:87], a[124:127], a[164:167]\n\t"
    "s_xor_b32 s58, s58, 0x8000\n\t"
    "s_add_u32 s53, s53, 0x80000\n\t"
    "v_mfma_f32_16x16x32_bf16 a[168:171], a[88:91], a[124:127], a[168:171]\n\t"
    "s_add_u32 s63, s63, 0x1000\n\t"
    "v_mfma_f32_16x16x32_bf16 a[172:175], a[92:95], a[124:127], a[172:175]\n\t"
    "; ---- kvV_loop\n\t"
    "kvV_loop_%=:\n\t"
    "s_waitcnt lgkmcnt(8)\n\t"
    "v_mfma_f32_16x16x32_bf16 v[48:51], a[0:3], a[32:35], v[48:51]\n\t"
    "ds_read_b128 a[64:67], v17 offset:0\n\t"
    "v_mfma_f32_16x16x32_bf16 v[52:55], a[4:7], a[32:35], v[52:55]\n\t"
    "ds_read_b64_tr_b16 a[96:97], v18 offset:4096\n\t"
    "s_add_u32 m0, s57, 0\n\t"
    "v_mfma_f32_16x16x32_bf16 v[56:59], a[8:11], a[32:35], v[56:59]\n\t"
    "buffer_load_dwordx4 v22, s[40:43], s52 offen offset:0 lds\n\t"
    "v_mfma_f32_16x16x32_bf16 v[60:63], a[12:15], a[32:35], v[60:63]\n\t"
    "ds_read_b64_tr_b16 a[98:99], v18 offset:6144\n\t"
    "v_mfma_f32_16x16x32_bf16 v[64:67], a[16:19], a[32:35], v[64:67]\n\t"
    "v_mfma_f32_16x16x32_bf16 v[68:71], a[20:23], a[32:35], v[68:71]\n\t"
    "ds_read_b128 a[68:71], v17 offset:2048\n\t"
    "s_waitcnt lgkmcnt(6)\n\t"
    "v_mfma_f32_16x16x32_bf16 v[72:75], a[24:27], a[32:35], v[72:75]\n\t"
    "v_mfma_f32_16x16x32_bf16 v[76:79], a[28:31], a[32:35], v[76:79]\n\t"
    "ds_read_b64_tr_b16 a[100:101], v19 offset:4096\n\t"
    "v_mfma_f32_16x16x32_bf16 v[80:83], a[0:3], a[36:39], v[80:83]\n\t"
    "v_mfma_f32_16x16x32_bf16 v[84:87], a[4:7], a[36:39], v[84:87]\n\t"
    "ds_read_b64_tr_b16 a[102:103], v19 offset:6144\n\t"
    "buffer_load_dwordx4 v23, s[40:43], s52 offen offset:1024 lds\n\t"
    "v_mfma_f32_16x16x32_bf16 v[88:91], a[8:11], a[36:39], v[88:91]\n\t"
    "v_mfma_f32_16x16x32_bf16 v[92:95], a[12:15], a[36:39], v[92:95]\n\t"
    "ds_read_b128 a[72:75], v17 offset:4096\n\t"
    "v_mfma_f32_16x16x32_bf16 v[96:99], a[16:19], a[36:39], v[96:99]\n\t"
    "ds_read_b64_tr_b16 a[104:105], v20 offset:4096\n\t"
    "v_mfma_f32_16x16x32_bf16 v[100:103], a[20:23], a[36:39], v[100:103]\n\t"
    "v_mfma_f32_16x16x32_bf16 v[104:107], a[24:27], a[36:39], v[104:107]\n\t"
    "ds_read_b64_tr_b16 a[106:107], v20 offset:6144\n\t"
    "v_mfma_f32_16x16x32_bf16 v[108:111], a[28:31], a[36:39], v[108:111]\n\t"
    "v_mfma_f32_16x16x32_bf16 v[112:115], a[0:3], a[40:43], v[112:115]\n\t"
    "ds_read_b128 a[76:79], v17 offset:6144\n\t"
    "buffer_load_dwordx4 v24, s[40:43], s52 offen offset:2048 lds\n\t"
    "v_mfma_f32_16x16x32_bf16 v[116:119], a[4:7], a[40:43], v[116:119]\n\t"
    "v_mfma_f32_16x16x32_bf16 v[120:123], a[8:11], a[40:43], v[120:123]\n\t"
    "ds_read_b64_tr_b16 a[108:109], v21 offset:4096\n\t"
    "v_mfma_f32_16x16x32_bf16 v[124:127], a[12:15], a[40:43], v[124:127]\n\t"
    "v_mfma_f32_16x16x32_bf16 v[128:131], a[16:19], a[40:43], v[128:131]\n\t"
    "ds_read_b64_tr_b16 a[110:111], v21 offset:6144\n\t"
    "v_mfma_f32_16x16x32_bf16 v[132:135], a[20:23], a[40:43], v[132:135]\n\t"
    "v_mfma_f32_16x16x32_bf16 v[136:139], a[24:27], a[40:43], v[136:139]\n\t"
    "ds_read_b128 a[80:83], v17 offset:8192\n\t"
    "v_mfma_f32_16x16x32_bf16 v[140:143], a[28:31], a[40:43], v[140:143]\n\t"
    "ds_read_b64_tr_b16 a[112:113], v18 offset:12288\n\t"
    "buffer_load_dwordx4 v25, s[40:43], s52 offen offset:3072 lds\n\t"
    "v_mfma_f32_16x16x32_bf16 v[144:147], a[0:3], a[44:47], v[144:147]\n\t"
    "v_mfma_f32_16x16x32_bf16 v[148:151], a[4:7], a[44:47], v[148:151]\n\t"
    "ds_read_b64_tr_b16 a[114:115], v18 offset:14336\n\t"
    "v_mfma_f32_16x16x32_bf16 v[152:155], a[8:11], a[44:47], v[152:155]\n\t"
    "v_mfma_f32_16x16x32_bf16 v[156:159], a[12:15], a[44:47], v[156:159]\n\t"
    "ds_read_b128 a[84:87], v17 offset:10240\n\t"
    "v_mfma_f32_16x16x32_bf16 v[160:163], a[16:19], a[44:47], v[160:163]\n\t"
    "v_mfma_f32_16x16x32_bf16 v[164:167], a[20:23], a[44:47], v[164:167]\n\t"
    "ds_read_b64_tr_b16 a[116:117], v19 offset:12288\n\t"
    "s_add_u32 m0, s57, 0x1000\n\t"
    "v_mfma_f32_16x16x32_bf16 v[168:171], a[24:27], a[44:47], v[168:171]\n\t"
    "buffer_load_dwordx4 v26, s[40:43], s52 offen offset:0 lds\n\t"
    "v_mfma_f32_16x16x32_bf16 v[172:175], a[28:31], a[44:47], v[172:175]\n\t"
    "ds_read_b64_tr_b16 a[118:119], v19 offset:14336\n\t"
    "v_mfma_f32_16x16x32_bf16 v[176:179], a[0:3], a[48:51], v[176:179]\n\t"
    "v_mfma_f32_16x16x32_bf16 v[180:183], a[4:7], a[48:51], v[180:183]\n\t"
    "ds_read_b128 a[88:91], v17 offset:12288\n\t"
    "v_mfma_f32_16x16x32_bf16 v[184:187], a[8:11], a[48:51], v[184:187]\n\t"
    "ds_read_b64_tr_b16 a[120:121], v20 offset:12288\n\t"
    "v_mfma_f32_16x16x32_bf16 v[188:191], a[12:15], a[48:51], v[188:191]\n\t"
    "v_mfma_f32_16x16x32_bf16 v[192:195], a[16:19], a[48:51], v[192:195]\n\t"
    "ds_read_b64_tr_b16 a[122:123], v20 offset:14336\n\t"
    "v_mfma_f32_16x16x32_bf16 v[196:199], a[20:23], a[48:51], v[196:199]\n\t"
    "buffer_load_dwordx4 v27, s[40:43], s52 offen offset:1024 lds\n\t"
    "v_mfma_f32_16x16x32_bf16 v[200:203], a[24:27], a[48:51], v[200:203]\n\t"
    "ds_read_b128 a[92:95], v17 offset:14336\n\t"
    "v_mfma_f32_16x16x32_bf16 v[204:207], a[28:31], a[48:51], v[204:207]\n\t"
    "v_mfma_f32_16x16x32_bf16 v[208:211], a[0:3], a[52:55], v[208:211]\n\t"
    "ds_read_b64_tr_b16 a[124:125], v21 offset:12288\n\t"
    "v_mfma_f32_16x16x32_bf16 v[212:215], a[4:7], a[52:55], v[212:215]\n\t"
    "v_mfma_f32_16x16x32_bf16 v[216:219], a[8:11], a[52:55], v[216:219]\n\t"
    "ds_read_b64_tr_b16 a[126:127], v21 offset:14336\n\t"
    "v_mfma_f32_16x16x32_bf16 v[220:223], a[12:15], a[52:55], v[220:223]\n\t"
    "v_mfma_f32_16x16x32_bf16 v[224:227], a[16:19], a[52:55], v[224:227]\n\t"
    "buffer_load_dwordx4 v28, s[40:43], s52 offen offset:2048 lds\n\t"
    "v_mfma_f32_16x16x32_bf16 v[228:231], a[20:23], a[52:55], v[228:231]\n\t"
    "v_mfma_f32_16x16x32_bf16 v[232:235], a[24:27], a[52:55], v[232:235]\n\t"
    "v_add_u32 v16, s55, v45\n\t"
    "v_mfma_f32_16x16x32_bf16 v[236:239], a[28:31], a[52:55], v[236:239]\n\t"
    "v_mfma_f32_16x16x32_bf16 v[240:243], a[0:3], a[56:59], v[240:243]\n\t"
    "v_mfma_f32_16x16x32_bf16 v[244:247], a[4:7], a[56:59], v[244:247]\n\t"
    "v_xor_b32 v18, 0x8000, v18\n\t"
    "v_mfma_f32_16x16x32_bf16 v[248:251], a[8:11], a[56:59], v[248:251]\n\t"
    "v_mfma_f32_16x16x32_bf16 v[252:255], a[12:15], a[56:59], v[252:255]\n\t"
    "buffer_load_dwordx4 v29, s[40:43], s52 offen offset:3072 lds\n\t"
    "v_mfma_f32_16x16x32_bf16 a[128:131], a[16:19], a[56:59], a[128:131]\n\t"
    "v_xor_b32 v19, 0x8000, v19\n\t"
    "v_mfma_f32_16x16x32_bf16 a[132:135], a[20:23], a[56:59], a[132:135]\n\t"
    "v_mfma_f32_16x16x32_bf16 a[136:139], a[24:27], a[56:59], a[136:139]\n\t"
    "v_mfma_f32_16x16x32_bf16 a[140:143], a[28:31], a[56:59], a[140:143]\n\t"
    "v_xor_b32 v20, 0x8000, v20\n\t"
    "s_waitcnt lgkmcnt(15)\n\t"
    "v_mfma_f32_16x16x32_bf16 a[144:147], a[0:3], a[60:63], a[144:147]\n\t"
    "v_mfma_f32_16x16x32_bf16 a[148:151], a[4:7], a[60:63], a[148:151]\n\t"
    "v_mfma_f32_16x16x32_bf16 a[152:155], a[8:11], a[60:63], a[152:155]\n\t"
    "v_xor_b32 v21, 0x8000, v21\n\t"
    "v_mfma_f32_16x16x32_bf16 a[156:159], a[12:15], a[60:63], a[156:159]\n\t"
    "v_mfma_f32_16x16x32_bf16 a[160:163], a[16:19], a[60:63], a[160:163]\n\t"
    "v_mfma_f32_16x16x32_bf16 a[164:167], a[20:23], a[60:63], a[164:167]\n\t"
    "v_mfma_f32_16x16x32_bf16 a[168:171], a[24:27], a[60:63], a[168:171]\n\t"
    "v_mfma_f32_16x16x32_bf16 a[172:175], a[28:31], a[60:63], a[172:175]\n\t"
    "s_waitcnt vmcnt(9) lgkmcnt(0)\n\t"
    "s_barrier\n\t"
    "v_mfma_f32_16x16x32_bf16 v[48:51], a[64:67], a[96:99], v[48:51]\n\t"
    "ds_read_b128 a[0:3], v16 offset:0\n\t"
    "v_mfma_f32_16x16x32_bf16 v[52:55], a[68:71], a[96:99], v[52:55]\n\t"
    "ds_read_b64_tr_b16 a[32:33], v18 offset:0\n\t"
    "s_add_u32 m0, s58, 0\n\t"
    "v_mfma_f32_16x16x32_bf16 v[56:59], a[72:75], a[96:99], v[56:59]\n\t"
    "buffer_load_dwordx4 v30, s[44:47], s53 offen offset:0 lds\n\t"
    "v_mfma_f32_16x16x32_bf16 v[60:63], a[76:79], a[96:99], v[60:63]\n\t"
    "ds_read_b64_tr_b16 a[34:35], v18 offset:2048\n\t"
    "v_mfma_f32_16x16x32_bf16 v[64:67], a[80:83], a[96:99], v[64:67]\n\t"
    "v_mfma_f32_16x16x32_bf16 v[68:71], a[84:87], a[96:99], v[68:71]\n\t"
    "ds_read_b128 a[4:7], v16 offset:2048\n\t"
    "v_mfma_f32_16x16x32_bf16 v[72:75], a[88:91], a[96:99], v[72:75]\n\t"
    "v_mfma_f32_16x16x32_bf16 v[76:79], a[92:95], a[96:99], v[76:79]\n\t"
    "ds_read_b64_tr_b16 a[36:37], v19 offset:0\n\t"
    "v_mfma_f32_16x16x32_bf16 v[80:83], a[64:67], a[100:103], v[80:83]\n\t"
    "buffer_load_dwordx4 v31, s[44:47], s53 offen offset:1024 lds\n\t"
    "v_mfma_f32_16x16x32_bf16 v[84:87], a[68:71], a[100:103], v[84:87]\n\t"
    "ds_read_b64_tr_b16 a[38:39], v19 offset:2048\n\t"
    "v_mfma_f32_16x16x32_bf16 v[88:91], a[72:75], a[100:103], v[88:91]\n\t"
    "v_mfma_f32_16x16x32_bf16 v[92:95], a[76:79], a[100:103], v[92:95]\n\t"
    "ds_read_b128 a[8:11], v16 offset:4096\n\t"
    "v_mfma_f32_16x16x32_bf16 v[96:99], a[80:83], a[100:103], v[96:99]\n\t"
    "ds_read_b64_tr_b16 a[40:41], v20 offset:0\n\t"
    "v_mfma_f32_16x16x32_bf16 v[100:103], a[84:87], a[100:103], v[100:103]\n\t"
    "v_mfma_f32_16x16x32_bf16 v[104:107], a[88:91], a[100:103], v[104:107]\n\t"
    "ds_read_b64_tr_b16 a[42:43], v20 offset:2048\n\t"
    "v_mfma_f32_16x16x32_bf16 v[108:111], a[92:95], a[100:103], v[108:111]\n\t"
    "buffer_load_dwordx4 v32, s[44:47], s53 offen offset:2048 lds\n\t"
    "v_mfma_f32_16x16x32_bf16 v[112:115], a[64:67], a[104:107], v[112:115]\n\t"
    "ds_read_b128 a[12:15], v16 offset:6144\n\t"
    "v_mfma_f32_16x16x32_bf16 v[116:119], a[68:71], a[104:107], v[116:119]\n\t"
    "v_mfma_f32_16x16x32_bf16 v[120:123], a[72:75], a[104:107], v[120:123]\n\t"
    "ds_read_b64_tr_b16 a[44:45], v21 offset:0\n\t"
    "v_mfma_f32_16x16x32_bf16 v[124:127], a[76:79], a[104:107], v[124:127]\n\t"
    "v_mfma_f32_16x16x32_bf16 v[128:131], a[80:83], a[104:107], v[128:131]\n\t"
    "ds_read_b64_tr_b16 a[46:47], v21 offset:2048\n\t"
    "v_mfma_f32_16x16x32_bf16 v[132:135], a[84:87], a[104:107], v[132:135]\n\t"
    "v_mfma_f32_16x16x32_bf16 v[136:139], a[88:91], a[104:107], v[136:139]\n\t"
    "ds_read_b128 a[16:19], v16 offset:8192\n\t"
    "buffer_load_dwordx4 v33, s[44:47], s53 offen offset:3072 lds\n\t"
    "v_mfma_f32_16x16x32_bf16 v[140:143], a[92:95], a[104:107], v[140:143]\n\t"
    "ds_read_b64_tr_b16 a[48:49], v18 offset:8192\n\t"
    "v_mfma_f32_16x16x32_bf16 v[144:147], a[64:67], a[108:111], v[144:147]\n\t"
    "v_mfma_f32_16x16x32_bf16 v[148:151], a[68:71], a[108:111], v[148:151]\n\t"
    "ds_read_b64_tr_b16 a[50:51], v18 offset:10240\n\t"
    "v_mfma_f32_16x16x32_bf16 v[152:155], a[72:75], a[108:111], v[152:155]\n\t"
    "v_mfma_f32_16x16x32_bf16 v[156:159], a[76:79], a[108:111], v[156:159]\n\t"
    "ds_read_b128 a[20:23], v16 offset:10240\n\t"
    "s_add_u32 m0, s58, 0x1000\n\t"
    "v_mfma_f32_16x16x32_bf16 v[160:163], a[80:83], a[108:111], v[160:163]\n\t"
    "buffer_load_dwordx4 v34, s[44:47], s53 offen offset:0 lds\n\t"
    "v_mfma_f32_16x16x32_bf16 v[164:167], a[84:87], a[108:111], v[164:167]\n\t"
    "ds_read_b64_tr_b16 a[52:53], v19 offset:8192\n\t"
    "v_mfma_f32_16x16x32_bf16 v[168:171], a[88:91], a[108:111], v[168:171]\n\t"
    "v_mfma_f32_16x16x32_bf16 v[172:175], a[92:95], a[108:111], v[172:175]\n\t"
    "ds_read_b64_tr_b16 a[54:55], v19 offset:10240\n\t"
    "v_mfma_f32_16x16x32_bf16 v[176:179], a[64:67], a[112:115], v[176:179]\n\t"
    "v_mfma_f32_16x16x32_bf16 v[180:183], a[68:71], a[112:115], v[180:183]\n\t"
    "ds_read_b128 a[24:27], v16 offset:12288\n\t"
    "v_mfma_f32_16x16x32_bf16 v[184:187], a[72:75], a[112:115], v[184:187]\n\t"
    "ds_read_b64_tr_b16 a[56:57], v20 offset:8192\n\t"
    "v_mfma_f32_16x16x32_bf16 v[188:191], a[76:79], a[112:115], v[188:191]\n\t"
    "buffer_load_dwordx4 v35, s[44:47], s53 offen offset:1024 lds\n\t"
    "v_mfma_f32_16x16x32_bf16 v[192:195], a[80:83], a[112:115], v[192:195]\n\t"
    "ds_read_b64_tr_b16 a[58:59], v20 offset:10240\n\t"
    "v_mfma_f32_16x16x32_bf16 v[196:199], a[84:87], a[112:115], v[196:199]\n\t"
    "v_mfma_f32_16x16x32_bf16 v[200:203], a[88:91], a[112:115], v[200:203]\n\t"
    "ds_read_b128 a[28:31], v16 offset:14336\n\t"
    "v_mfma_f32_16x16x32_bf16 v[204:207], a[92:95], a[112:115], v[204:207]\n\t"
    "v_mfma_f32_16x16x32_bf16 v[208:211], a[64:67], a[116:119], v[208:211]\n\t"
    "ds_read_b64_tr_b16 a[60:61], v21 offset:8192\n\t"
    "v_mfma_f32_16x16x32_bf16 v[212:215], a[68:71], a[116:119], v[212:215]\n\t"
    "v_mfma_f32_16x16x32_bf16 v[216:219], a[72:75], a[116:119], v[216:219]\n\t"
    "ds_read_b64_tr_b16 a[62:63], v21 offset:10240\n\t"
    "buffer_load_dwordx4 v36, s[44:47], s53 offen offset:2048 lds\n\t"
    "v_mfma_f32_16x16x32_bf16 v[220:223], a[76:79], a[116:119], v[220:223]\n\t"
    "v_mfma_f32_16x16x32_bf16 v[224:227], a[80:83], a[116:119], v[224:227]\n\t"
    "v_mfma_f32_16x16x32_bf16 v[228:231], a[84:87], a[116:119], v[228:231]\n\t"
    "v_mfma_f32_16x16x32_bf16 v[232:235], a[88:91], a[116:119], v[232:235]\n\t"
    "v_mfma_f32_16x16x32_bf16 v[236:239], a[92:95], a[116:119], v[236:239]\n\t"
    "v_mfma_f32_16x16x32_bf16 v[240:243], a[64:67], a[120:123], v[240:243]\n\t"
    "buffer_load_dwordx4 v37, s[44:47], s53 offen offset:3072 lds\n\t"
    "v_mfma_f32_16x16x32_bf16 v[244:247], a[68:71], a[120:123], v[244:247]\n\t"
    "v_mfma_f32_16x16x32_bf16 v[248:251], a[72:75], a[120:123], v[248:251]\n\t"
    "v_mfma_f32_16x16x32_bf16 v[252:255], a[76:79], a[120:123], v[252:255]\n\t"
    "v_mfma_f32_16x16x32_bf16 a[128:131], a[80:83], a[120:123], a[128:131]\n\t"
    "v_mfma_f32_16x16x32_bf16 a[132:135], a[84:87], a[120:123], a[132:135]\n\t"
    "v_mfma_f32_16x16x32_bf16 a[136:139], a[88:91], a[120:123], a[136:139]\n\t"
    "v_mfma_f32_16x16x32_bf16 a[140:143], a[92:95], a[120:123], a[140:143]\n\t"
    "buffer_load_dword a254, %12, s[68:71], s63 offen\n\t"
    "v_mfma_f32_16x16x32_bf16 a[144:147], a[64:67], a[124:127], a[144:147]\n\t"
    "v_mfma_f32_16x16x32_bf16 a[148:151], a[68:71], a[124:127], a[148:151]\n\t"
    "v_add_u32 v17, s55, v46\n\t"
    "v_mfma_f32_16x16x32_bf16 a[152:155], a[72:75], a[124:127], a[152:155]\n\t"
    "s_mov_b32 s60, s54\n\t"
    "s_mov_b32 s54, s55\n\t"
    "v_mfma_f32_16x16x32_bf16 a[156:159], a[76:79], a[124:127], a[156:159]\n\t"
    "s_mov_b32 s55, s56\n\t"
    "s_mov_b32 s56, s60\n\t"
    "v_mfma_f32_16x16x32_bf16 a[160:163], a[80:83], a[124:127], a[160:163]\n\t"
    "s_add_u32 s57, s62, s60\n\t"
    "s_add_u32 s52, s52, 0x80\n\t"
    "v_mfma_f32_16x16x32_bf16 a[164:167], a[84:87], a[124:127], a[164:167]\n\t"
    "s_xor_b32 s58, s58, 0x8000\n\t"
    "s_add_u32 s53, s53, 0x80000\n\t"
    "v_mfma_f32_16x16x32_bf16 a[168:171], a[88:91], a[124:127], a[168:171]\n\t"
    "s_add_u32 s63, s63, 0x1000\n\t"
    "v_mfma_f32_16x16x32_bf16 a[172:175], a[92:95], a[124:127], a[172:175]\n\t"
    "s_sub_u32 s59, s59, 1\n\t"
    "s_cmp_lg_u32 s59, 0\n\t"
    "s_cbranch_scc1 kvV_loop_%=\n\t"
    "; ---- kvV_s6\n\t"
    "s_waitcnt lgkmcnt(15)\n\t"
    "v_mfma_f32_16x16x32_bf16 v[48:51], a[0:3], a[32:35], v[48:51]\n\t"
    "s_mov_b64 s[40:41], %13\n\t"
    "s_mov_b32 s42, %14\n\t"
    "s_mov_b32 s43, 0x20000\n\t"
    "s_mov_b64 s[44:45], %15\n\t"
    "s_mov_b32 s46, %16\n\t"
    "s_mov_b32 s47, 0x20000\n\t"
    "ds_read_b128 a[64:67], v17 offset:0\n\t"
    "v_mfma_f32_16x16x32_bf16 v[52:55], a[4:7], a[32:35], v[52:55]\n\t"
    "v_and_b32 v47, 0x380, %12\n\t"
    "ds_read_b64_tr_b16 a[96:97], v18 offset:4096\n\t"
    "v_mfma_f32_16x16x32_bf16 v[56:59], a[8:11], a[32:35], v[56:59]\n\t"
    "v_add_u32 v22, v47, v22\n\t"
    "v_add_u32 v22, 0, v22\n\t"
    "s_waitcnt lgkmcnt(10)\n\t"
    "v_mfma_f32_16x16x32_bf16 v[60:63], a[12:15], a[32:35], v[60:63]\n\t"
    "v_add_u32 v23, v47, v23\n\t"
    "v_add_u32 v23, 0x400, v23\n\t"
    "v_add_u32 v24, v47, v24\n\t"
    "v_add_u32 v24, 0x800, v24\n\t"
    "ds_read_b64_tr_b16 a[98:99], v18 offset:6144\n\t"
    "v_mfma_f32_16x16x32_bf16 v[64:67], a[16:19], a[32:35], v[64:67]\n\t"
    "v_add_u32 v25, v47, v25\n\t"
    "v_add_u32 v25, 0xc00, v25\n\t"
    "v_mfma_f32_16x16x32_bf16 v[68:71], a[20:23], a[32:35], v[68:71]\n\t"
    "v_add_u32 v26, v47, v26\n\t"
    "v_add_u32 v26, 0x1000, v26\n\t"
    "ds_read_b128 a[68:71], v17 offset:2048\n\t"
    "s_waitcnt lgkmcnt(6)\n\t"
    "v_mfma_f32_16x16x32_bf16 v[72:75], a[24:27], a[32:35], v[72:75]\n\t"
    "v_add_u32 v27, v47, v27\n\t"
    "v_add_u32 v27, 0x1400, v27\n\t"
    "v_add_u32 v28, v47, v28\n\t"
    "v_add_u32 v28, 0x1800, v28\n\t"
    "v_mfma_f32_16x16x32_bf16 v[76:79], a[28:31], a[32:35], v[76:79]\n\t"
    "v_add_u32 v29, v47, v29\n\t"
    "v_add_u32 v29, 0x1c00, v29\n\t"
    "ds_read_b64_tr_b16 a[100:101], v19 offset:4096\n\t"
    "v_mfma_f32_16x16x32_bf16 v[80:83], a[0:3], a[36:39], v[80:83]\n\t"
    "v_add_u32 v30, 0, %11\n\t"
    "v_add_u32 v31, 0x1fc00, %11\n\t"
    "v_add_u32 v32, 0x7800, %11\n\t"
    "v_add_u32 v33, 0x27400, %11\n\t"
    "v_mfma_f32_16x16x32_bf16 v[84:87], a[4:7], a[36:39], v[84:87]\n\t"
    "s_and_b32 s60, %1, 1\n\t"
    "s_lshl_b32 s60, s60, 13\n\t"
    "s_lshr_b32 s64, %1, 1\n\t"
    "s_lshl_b32 s64, s64, 12\n\t"
    "s_add_u32 s64, s60, s64\n\t"
    "ds_read_b64_tr_b16 a[102:103], v19 offset:6144\n\t"
    "v_mfma_f32_16x16x32_bf16 v[88:91], a[8:11], a[36:39], v[88:91]\n\t"
    "v_mfma_f32_16x16x32_bf16 v[92:95], a[12:15], a[36:39], v[92:95]\n\t"
    "ds_read_b128 a[72:75], v17 offset:4096\n\t"
    "s_add_u32 s57, s62, 0x20000\n\t"
    "s_mov_b32 s52, 0\n\t"
    "s_add_u32 m0, s57, 0\n\t"
    "v_mfma_f32_16x16x32_bf16 v[96:99], a[16:19], a[36:39], v[96:99]\n\t"
    "ds_read_b64_tr_b16 a[104:105], v20 offset:4096\n\t"
    "buffer_load_dwordx4 v22, s[40:43], s52 offen offset:0 lds\n\t"
    "v_mfma_f32_16x16x32_bf16 v[100:103], a[20:23], a[36:39], v[100:103]\n\t"
    "v_mfma_f32_16x16x32_bf16 v[104:107], a[24:27], a[36:39], v[104:107]\n\t"
    "ds_read_b64_tr_b16 a[106:107], v20 offset:6144\n\t"
    "v_mfma_f32_16x16x32_bf16 v[108:111], a[28:31], a[36:39], v[108:111]\n\t"
    "v_mfma_f32_16x16x32_bf16 v[112:115], a[0:3], a[40:43], v[112:115]\n\t"
    "ds_read_b128 a[76:79], v17 offset:6144\n\t"
    "v_mfma_f32_16x16x32_bf16 v[116:119], a[4:7], a[40:43], v[116:119]\n\t"
    "buffer_load_dwordx4 v23, s[40:43], s52 offen offset:1024 lds\n\t"
    "v_mfma_f32_16x16x32_bf16 v[120:123], a[8:11], a[40:43], v[120:123]\n\t"
    "ds_read_b64_tr_b16 a[108:109], v21 offset:4096\n\t"
    "v_mfma_f32_16x16x32_bf16 v[124:127], a[12:15], a[40:43], v[124:127]\n\t"
    "v_mfma_f32_16x16x32_bf16 v[128:131], a[16:19], a[40:43], v[128:131]\n\t"
    "ds_read_b64_tr_b16 a[110:111], v21 offset:6144\n\t"
    "v_mfma_f32_16x16x32_bf16 v[132:135], a[20:23], a[40:43], v[132:135]\n\t"
    "v_mfma_f32_16x16x32_bf16 v[136:139], a[24:27], a[40:43], v[136:139]\n\t"
    "ds_read_b128 a[80:83], v17 offset:8192\n\t"
    "v_mfma_f32_16x16x32_bf16 v[140:143], a[28:31], a[40:43], v[140:143]\n\t"
    "ds_read_b64_tr_b16 a[112:113], v18 offset:12288\n\t"
    "buffer_load_dwordx4 v24, s[40:43], s52 offen offset:2048 lds\n\t"
    "v_mfma_f32_16x16x32_bf16 v[144:147], a[0:3], a[44:47], v[144:147]\n\t"
    "v_mfma_f32_16x16x32_bf16 v[148:151], a[4:7], a[44:47], v[148:151]\n\t"
    "ds_read_b64_tr_b16 a[114:115], v18 offset:14336\n\t"
    "v_mfma_f32_16x16x32_bf16 v[152:155], a[8:11], a[44:47], v[152:155]\n\t"
    "v_mfma_f32_16x16x32_bf16 v[156:159], a[12:15], a[44:47], v[156:159]\n\t"
    "ds_read_b128 a[84:87], v17 offset:10240\n\t"
    "v_mfma_f32_16x16x32_bf16 v[160:163], a[16:19], a[44:47], v[160:163]\n\t"
    "v_mfma_f32_16x16x32_bf16 v[164:167], a[20:23], a[44:47], v[164:167]\n\t"
    "ds_read_b64_tr_b16 a[116:117], v19 offset:12288\n\t"
    "buffer_load_dwordx4 v25, s[40:43], s52 offen offset:3072 lds\n\t"
    "v_mfma_f32_16x16x32_bf16 v[168:171], a[24:27], a[44:47], v[168:171]\n\t"
    "v_mfma_f32_16x16x32_bf16 v[172:175], a[28:31], a[44:47], v[172:175]\n\t"
    "ds_read_b64_tr_b16 a[118:119], v19 offset:14336\n\t"
    "v_mfma_f32_16x16x32_bf16 v[176:179], a[0:3], a[48:51], v[176:179]\n\t"
    "v_mfma_f32_16x16x32_bf16 v[180:183], a[4:7], a[48:51], v[180:183]\n\t"
    "ds_read_b128 a[88:91], v17 offset:12288\n\t"
    "v_mfma_f32_16x16x32_bf16 v[184:187], a[8:11], a[48:51], v[184:187]\n\t"
    "ds_read_b64_tr_b16 a[120:121], v20 offset:12288\n\t"
    "s_add_u32 m0, s57, 0x1000\n\t"
    "v_mfma_f32_16x16x32_bf16 v[188:191], a[12:15], a[48:51], v[188:191]\n\t"
    "buffer_load_dwordx4 v26, s[40:43], s52 offen offset:0 lds\n\t"
    "v_mfma_f32_16x16x32_bf16 v[192:195], a[16:19], a[48:51], v[192:195]\n\t"
    "ds_read_b64_tr_b16 a[122:123], v20 offset:14336\n\t"
    "v_mfma_f32_16x16x32_bf16 v[196:199], a[20:23], a[48:51], v[196:199]\n\t"
    "v_mfma_f32_16x16x32_bf16 v[200:203], a[24:27], a[48:51], v[200:203]\n\t"
    "ds_read_b128 a[92:95], v17 offset:14336\n\t"
    "v_mfma_f32_16x16x32_bf16 v[204:207], a[28:31], a[48:51], v[204:207]\n\t"
    "v_mfma_f32_16x16x32_bf16 v[208:211], a[0:3], a[52:55], v[208:211]\n\t"
    "ds_read_b64_tr_b16 a[124:125], v21 offset:12288\n\t"
    "buffer_load_dwordx4 v27, s[40:43], s52 offen offset:1024 lds\n\t"
    "v_mfma_f32_16x16x32_bf16 v[212:215], a[4:7], a[52:55], v[212:215]\n\t"
    "v_mfma_f32_16x16x32_bf16 v[216:219], a[8:11], a[52:55], v[216:219]\n\t"
    "ds_read_b64_tr_b16 a[126:127], v21 offset:14336\n\t"
    "v_mfma_f32_16x16x32_bf16 v[220:223], a[12:15], a[52:55], v[220:223]\n\t"
    "v_mfma_f32_16x16x32_bf16 v[224:227], a[16:19], a[52:55], v[224:227]\n\t"
    "v_mfma_f32_16x16x32_bf16 v[228:231], a[20:23], a[52:55], v[228:231]\n\t"
    "v_mfma_f32_16x16x32_bf16 v[232:235], a[24:27], a[52:55], v[232:235]\n\t"
    "buffer_load_dwordx4 v28, s[40:43], s52 offen offset:2048 lds\n\t"
    "v_add_u32 v16, s55, v45\n\t"
    "v_mfma_f32_16x16x32_bf16 v[236:239], a[28:31], a[52:55], v[236:239]\n\t"
    "v_mfma_f32_16x16x32_bf16 v[240:243], a[0:3], a[56:59], v[240:243]\n\t"
    "v_mfma_f32_16x16x32_bf16 v[244:247], a[4:7], a[56:59], v[244:247]\n\t"
    "v_xor_b32 v18, 0x8000, v18\n\t"
    "v_mfma_f32_16x16x32_bf16 v[248:251], a[8:11], a[56:59], v[248:251]\n\t"
    "v_mfma_f32_16x16x32_bf16 v[252:255], a[12:15], a[56:59], v[252:255]\n\t"
    "v_mfma_f32_16x16x32_bf16 a[128:131], a[16:19], a[56:59], a[128:131]\n\t"
    "buffer_load_dwordx4 v29, s[40:43], s52 offen offset:3072 lds\n\t"
    "v_xor_b32 v19, 0x8000, v19\n\t"
    "v_mfma_f32_16x16x32_bf16 a[132:135], a[20:23], a[56:59], a[132:135]\n\t"
    "v_mfma_f32_16x16x32_bf16 a[136:139], a[24:27], a[56:59], a[136:139]\n\t"
    "v_mfma_f32_16x16x32_bf16 a[140:143], a[28:31], a[56:59], a[140:143]\n\t"
    "v_xor_b32 v20, 0x8000, v20\n\t"
    "s_waitcnt lgkmcnt(15)\n\t"
    "v_mfma_f32_16x16x32_bf16 a[144:147], a[0:3], a[60:63], a[144:147]\n\t"
    "v_mfma_f32_16x16x32_bf16 a[148:151], a[4:7], a[60:63], a[148:151]\n\t"
    "v_mfma_f32_16x16x32_bf16 a[152:155], a[8:11], a[60:63], a[152:155]\n\t"
    "v_xor_b32 v21, 0x8000, v21\n\t"
    "v_mfma_f32_16x16x32_bf16 a[156:159], a[12:15], a[60:63], a[156:159]\n\t"
    "v_mfma_f32_16x16x32_bf16 a[160:163], a[16:19], a[60:63], a[160:163]\n\t"
    "v_mfma_f32_16x16x32_bf16 a[164:167], a[20:23], a[60:63], a[164:167]\n\t"
    "v_mfma_f32_16x16x32_bf16 a[168:171], a[24:27], a[60:63], a[168:171]\n\t"
    "v_mfma_f32_16x16x32_bf16 a[172:175], a[28:31], a[60:63], a[172:175]\n\t"
    "s_waitcnt vmcnt(9) lgkmcnt(0)\n\t"
    "s_barrier\n\t"
    "v_mfma_f32_16x16x32_bf16 v[48:51], a[64:67], a[96:99], v[48:51]\n\t"
    "ds_read_b128 a[0:3], v16 offset:0\n\t"
    "v_mfma_f32_16x16x32_bf16 v[52:55], a[68:71], a[96:99], v[52:55]\n\t"
    "ds_read_b64_tr_b16 a[32:33], v18 offset:0\n\t"
    "s_add_u32 s58, s64, 0\n\t"
    "s_mov_b32 s53, 0\n\t"
    "s_add_u32 m0, s58, 0\n\t"
    "v_mfma_f32_16x16x32_bf16 v[56:59], a[72:75], a[96:99], v[56:59]\n\t"
    "buffer_load_dwordx4 v30, s[44:47], s53 offen offset:0 lds\n\t"
    "v_mfma_f32_16x16x32_bf16 v[60:63], a[76:79], a[96:99], v[60:63]\n\t"
    "ds_read_b64_tr_b16 a[34:35], v18 offset:2048\n\t"
    "v_mfma_f32_16x16x32_bf16 v[64:67], a[80:83], a[96:99], v[64:67]\n\t"
    "v_mfma_f32_16x16x32_bf16 v[68:71], a[84:87], a[96:99], v[68:71]\n\t"
    "ds_read_b128 a[4:7], v16 offset:2048\n\t"
    "v_mfma_f32_16x16x32_bf16 v[72:75], a[88:91], a[96:99], v[72:75]\n\t"
    "v_mfma_f32_16x16x32_bf16 v[76:79], a[92:95], a[96:99], v[76:79]\n\t"
    "ds_read_b64_tr_b16 a[36:37], v19 offset:0\n\t"
    "v_mfma_f32_16x16x32_bf16 v[80:83], a[64:67], a[100:103], v[80:83]\n\t"
    "buffer_load_dwordx4 v31, s[44:47], s53 offen offset:1024 lds\n\t"
    "v_mfma_f32_16x16x32_bf16 v[84:87], a[68:71], a[100:103], v[84:87]\n\t"
    "ds_read_b64_tr_b16 a[38:39], v19 offset:2048\n\t"
    "v_mfma_f32_16x16x32_bf16 v[88:91], a[72:75], a[100:103], v[88:91]\n\t"
    "v_mfma_f32_16x16x32_bf16 v[92:95], a[76:79], a[100:103], v[92:95]\n\t"
    "ds_read_b128 a[8:11], v16 offset:4096\n\t"
    "v_mfma_f32_16x16x32_bf16 v[96:99], a[80:83], a[100:103], v[96:99]\n\t"
    "ds_read_b64_tr_b16 a[40:41], v20 offset:0\n\t"
    "v_mfma_f32_16x16x32_bf16 v[100:103], a[84:87], a[100:103], v[100:103]\n\t"
    "v_mfma_f32_16x16x32_bf16 v[104:107], a[88:91], a[100:103], v[104:107]\n\t"
    "ds_read_b64_tr_b16 a[42:43], v20 offset:2048\n\t"
    "v_mfma_f32_16x16x32_bf16 v[108:111], a[92:95], a[100:103], v[108:111]\n\t"
    "buffer_load_dwordx4 v32, s[44:47], s53 offen offset:2048 lds\n\t"
    "v_mfma_f32_16x16x32_bf16 v[112:115], a[64:67], a[104:107], v[112:115]\n\t"
    "ds_read_b128 a[12:15], v16 offset:6144\n\t"
    "v_mfma_f32_16x16x32_bf16 v[116:119], a[68:71], a[104:107], v[116:119]\n\t"
    "v_mfma_f32_16x16x32_bf16 v[120:123], a[72:75], a[104:107], v[120:123]\n\t"
    "ds_read_b64_tr_b16 a[44:45], v21 offset:0\n\t"
    "v_mfma_f32_16x16x32_bf16 v[124:127], a[76:79], a[104:107], v[124:127]\n\t"
    "v_mfma_f32_16x16x32_bf16 v[128:131], a[80:83], a[104:107], v[128:131]\n\t"
    "ds_read_b64_tr_b16 a[46:47], v21 offset:2048\n\t"
    "v_mfma_f32_16x16x32_bf16 v[132:135], a[84:87], a[104:107], v[132:135]\n\t"
    "v_mfma_f32_16x16x32_bf16 v[136:139], a[88:91], a[104:107], v[136:139]\n\t"
    "ds_read_b128 a[16:19], v16 offset:8192\n\t"
    "buffer_load_dwordx4 v33, s[44:47], s53 offen offset:3072 lds\n\t"
    "v_mfma_f32_16x16x32_bf16 v[140:143], a[92:95], a[104:107], v[140:143]\n\t"
    "ds_read_b64_tr_b16 a[48:49], v18 offset:8192\n\t"
    "v_mfma_f32_16x16x32_bf16 v[144:147], a[64:67], a[108:111], v[144:147]\n\t"
    "v_mfma_f32_16x16x32_bf16 v[148:151], a[68:71], a[108:111], v[148:151]\n\t"
    "ds_read_b64_tr_b16 a[50:51], v18 offset:10240\n\t"
    "v_mfma_f32_16x16x32_bf16 v[152:155], a[72:75], a[108:111], v[152:155]\n\t"
    "v_mfma_f32_16x16x32_bf16 v[156:159], a[76:79], a[108:111], v[156:159]\n\t"
    "ds_read_b128 a[20:23], v16 offset:10240\n\t"
    "s_add_u32 s58, s64, 0x4000\n\t"
    "s_mov_b32 s53, 0x80000\n\t"
    "s_add_u32 m0, s58, 0\n\t"
    "v_mfma_f32_16x16x32_bf16 v[160:163], a[80:83], a[108:111], v[160:163]\n\t"
    "buffer_load_dwordx4 v30, s[44:47], s53 offen offset:0 lds\n\t"
    "v_mfma_f32_16x16x32_bf16 v[164:167], a[84:87], a[108:111], v[164:167]\n\t"
    "ds_read_b64_tr_b16 a[52:53], v19 offset:8192\n\t"
    "v_mfma_f32_16x16x32_bf16 v[168:171], a[88:91], a[108:111], v[168:171]\n\t"
    "v_mfma_f32_16x16x32_bf16 v[172:175], a[92:95], a[108:111], v[172:175]\n\t"
    "ds_read_b64_tr_b16 a[54:55], v19 offset:10240\n\t"
    "v_mfma_f32_16x16x32_bf16 v[176:179], a[64:67], a[112:115], v[176:179]\n\t"
    "v_mfma_f32_16x16x32_bf16 v[180:183], a[68:71], a[112:115], v[180:183]\n\t"
    "ds_read_b128 a[24:27], v16 offset:12288\n\t"
    "v_mfma_f32_16x16x32_bf16 v[184:187], a[72:75], a[112:115], v[184:187]\n\t"
    "ds_read_b64_tr_b16 a[56:57], v20 offset:8192\n\t"
    "v_mfma_f32_16x16x32_bf16 v[188:191], a[76:79], a[112:115], v[188:191]\n\t"
    "buffer_load_dwordx4 v31, s[44:47], s53 offen offset:1024 lds\n\t"
    "v_mfma_f32_16x16x32_bf16 v[192:195], a[80:83], a[112:115], v[192:195]\n\t"
    "ds_read_b64_tr_b16 a[58:59], v20 offset:10240\n\t"
    "v_mfma_f32_16x16x32_bf16 v[196:199], a[84:87], a[112:115], v[196:199]\n\t"
    "v_mfma_f32_16x16x32_bf16 v[200:203], a[88:91], a[112:115], v[200:203]\n\t"
    "ds_read_b128 a[28:31], v16 offset:14336\n\t"
    "v_mfma_f32_16x16x32_bf16 v[204:207], a[92:95], a[112:115], v[204:207]\n\t"
    "v_mfma_f32_16x16x32_bf16 v[208:211], a[64:67], a[116:119], v[208:211]\n\t"
    "ds_read_b64_tr_b16 a[60:61], v21 offset:8192\n\t"
    "v_mfma_f32_16x16x32_bf16 v[212:215], a[68:71], a[116:119], v[212:215]\n\t"
    "v_mfma_f32_16x16x32_bf16 v[216:219], a[72:75], a[116:119], v[216:219]\n\t"
    "ds_read_b64_tr_b16 a[62:63], v21 offset:10240\n\t"
    "buffer_load_dwordx4 v32, s[44:47], s53 offen offset:2048 lds\n\t"
    "v_mfma_f32_16x16x32_bf16 v[220:223], a[76:79], a[116:119], v[220:223]\n\t"
    "v_mfma_f32_16x16x32_bf16 v[224:227], a[80:83], a[116:119], v[224:227]\n\t"
    "v_mfma_f32_16x16x32_bf16 v[228:231], a[84:87], a[116:119], v[228:231]\n\t"
    "v_mfma_f32_16x16x32_bf16 v[232:235], a[88:91], a[116:119], v[232:235]\n\t"
    "v_mfma_f32_16x16x32_bf16 v[236:239], a[92:95], a[116:119], v[236:239]\n\t"
    "v_mfma_f32_16x16x32_bf16 v[240:243], a[64:67], a[120:123], v[240:243]\n\t"
    "buffer_load_dwordx4 v33, s[44:47], s53 offen offset:3072 lds\n\t"
    "v_mfma_f32_16x16x32_bf16 v[244:247], a[68:71], a[120:123], v[244:247]\n\t"
    "v_mfma_f32_16x16x32_bf16 v[248:251], a[72:75], a[120:123], v[248:251]\n\t"
    "v_mfma_f32_16x16x32_bf16 v[252:255], a[76:79], a[120:123], v[252:255]\n\t"
    "v_mfma_f32_16x16x32_bf16 a[128:131], a[80:83], a[120:123], a[128:131]\n\t"
    "v_mfma_f32_16x16x32_bf16 a[132:135], a[84:87], a[120:123], a[132:135]\n\t"
    "v_mfma_f32_16x16x32_bf16 a[136:139], a[88:91], a[120:123], a[136:139]\n\t"
    "v_mfma_f32_16x16x32_bf16 a[140:143], a[92:95], a[120:123], a[140:143]\n\t"
    "buffer_load_dword a254, %12, s[68:71], s63 offen\n\t"
    "v_mfma_f32_16x16x32_bf16 a[144:147], a[64:67], a[124:127], a[144:147]\n\t"
    "v_mfma_f32_16x16x32_bf16 a[148:151], a[68:71], a[124:127], a[148:151]\n\t"
    "v_add_u32 v17, s55, v46\n\t"
    "v_mfma_f32_16x16x32_bf16 a[152:155], a[72:75], a[124:127], a[152:155]\n\t"
    "v_mfma_f32_16x16x32_bf16 a[156:159], a[76:79], a[124:127], a[156:159]\n\t"
    "v_mfma_f32_16x16x32_bf16 a[160:163], a[80:83], a[124:127], a[160:163]\n\t"
    "v_mfma_f32_16x16x32_bf16 a[164:167], a[84:87], a[124:127], a[164:167]\n\t"
    "v_mfma_f32_16x16x32_bf16 a[168:171], a[88:91], a[124:127], a[168:171]\n\t"
    "v_mfma_f32_16x16x32_bf16 a[172:175], a[92:95], a[124:127], a[172:175]\n\t"
    "; ---- kvV_s7\n\t"
    "s_waitcnt lgkmcnt(14)\n\t"
    "v_mfma_f32_16x16x32_bf16 v[48:51], a[0:3], a[32:35], v[48:51]\n\t"
    "ds_read_b128 a[64:67], v17 offset:0\n\t"
    "v_mfma_f32_16x16x32_bf16 v[52:55], a[4:7], a[32:35], v[52:55]\n\t"
    "ds_read_b64_tr_b16 a[96:97], v18 offset:4096\n\t"
    "s_add_u32 s57, s62, 0x10000\n\t"
    "s_mov_b32 s52, 0x80\n\t"
    "s_add_u32 m0, s57, 0\n\t"
    "v_mfma_f32_16x16x32_bf16 v[56:59], a[8:11], a[32:35], v[56:59]\n\t"
    "buffer_load_dwordx4 v22, s[40:43], s52 offen offset:0 lds\n\t"
    "v_mfma_f32_16x16x32_bf16 v[60:63], a[12:15], a[32:35], v[60:63]\n\t"
    "ds_read_b64_tr_b16 a[98:99], v18 offset:6144\n\t"
    "s_waitcnt lgkmcnt(5)\n\t"
    "v_mfma_f32_16x16x32_bf16 v[64:67], a[16:19], a[32:35], v[64:67]\n\t"
    "v_mfma_f32_16x16x32_bf16 v[68:71], a[20:23], a[32:35], v[68:71]\n\t"
    "ds_read_b128 a[68:71], v17 offset:2048\n\t"
    "v_mfma_f32_16x16x32_bf16 v[72:75], a[24:27], a[32:35], v[72:75]\n\t"
    "v_mfma_f32_16x16x32_bf16 v[76:79], a[28:31], a[32:35], v[76:79]\n\t"
    "ds_read_b64_tr_b16 a[100:101], v19 offset:4096\n\t"
    "v_mfma_f32_16x16x32_bf16 v[80:83], a[0:3], a[36:39], v[80:83]\n\t"
    "v_mfma_f32_16x16x32_bf16 v[84:87], a[4:7], a[36:39], v[84:87]\n\t"
    "ds_read_b64_tr_b16 a[102:103], v19 offset:6144\n\t"
    "buffer_load_dwordx4 v23, s[40:43], s52 offen offset:1024 lds\n\t"
    "v_mfma_f32_16x16x32_bf16 v[88:91], a[8:11], a[36:39], v[88:91]\n\t"
    "v_mfma_f32_16x16x32_bf16 v[92:95], a[12:15], a[36:39], v[92:95]\n\t"
    "ds_read_b128 a[72:75], v17 offset:4096\n\t"
    "v_mfma_f32_16x16x32_bf16 v[96:99], a[16:19], a[36:39], v[96:99]\n\t"
    "ds_read_b64_tr_b16 a[104:105], v20 offset:4096\n\t"
    "v_mfma_f32_16x16x32_bf16 v[100:103], a[20:23], a[36:39], v[100:103]\n\t"
    "v_mfma_f32_16x16x32_bf16 v[104:107], a[24:27], a[36:39], v[104:107]\n\t"
    "ds_read_b64_tr_b16 a[106:107], v20 offset:6144\n\t"
    "v_mfma_f32_16x16x32_bf16 v[108:111], a[28:31], a[36:39], v[108:111]\n\t"
    "v_mfma_f32_16x16x32_bf16 v[112:115], a[0:3], a[40:43], v[112:115]\n\t"
    "ds_read_b128 a[76:79], v17 offset:6144\n\t"
    "buffer_load_dwordx4 v24, s[40:43], s52 offen offset:2048 lds\n\t"
    "v_mfma_f32_16x16x32_bf16 v[116:119], a[4:7], a[40:43], v[116:119]\n\t"
    "v_mfma_f32_16x16x32_bf16 v[120:123], a[8:11], a[40:43], v[120:123]\n\t"
    "ds_read_b64_tr_b16 a[108:109], v21 offset:4096\n\t"
    "v_mfma_f32_16x16x32_bf16 v[124:127], a[12:15], a[40:43], v[124:127]\n\t"
    "v_mfma_f32_16x16x32_bf16 v[128:131], a[16:19], a[40:43], v[128:131]\n\t"
    "ds_read_b64_tr_b16 a[110:111], v21 offset:6144\n\t"
    "v_mfma_f32_16x16x32_bf16 v[132:135], a[20:23], a[40:43], v[132:135]\n\t"
    "v_mfma_f32_16x16x32_bf16 v[136:139], a[24:27], a[40:43], v[136:139]\n\t"
    "ds_read_b128 a[80:83], v17 offset:8192\n\t"
    "v_mfma_f32_16x16x32_bf16 v[140:143], a[28:31], a[40:43], v[140:143]\n\t"
    "ds_read_b64_tr_b16 a[112:113], v18 offset:12288\n\t"
    "buffer_load_dwordx4 v25, s[40:43], s52 offen offset:3072 lds\n\t"
    "v_mfma_f32_16x16x32_bf16 v[144:147], a[0:3], a[44:47], v[144:147]\n\t"
    "v_mfma_f32_16x16x32_bf16 v[148:151], a[4:7], a[44:47], v[148:151]\n\t"
    "ds_read_b64_tr_b16 a[114:115], v18 offset:14336\n\t"
    "v_mfma_f32_16x16x32_bf16 v[152:155], a[8:11], a[44:47], v[152:155]\n\t"
    "v_mfma_f32_16x16x32_bf16 v[156:159], a[12:15], a[44:47], v[156:159]\n\t"
    "ds_read_b128 a[84:87], v17 offset:10240\n\t"
    "v_mfma_f32_16x16x32_bf16 v[160:163], a[16:19], a[44:47], v[160:163]\n\t"
    "v_mfma_f32_16x16x32_bf16 v[164:167], a[20:23], a[44:47], v[164:167]\n\t"
    "ds_read_b64_tr_b16 a[116:117], v19 offset:12288\n\t"
    "s_add_u32 m0, s57, 0x1000\n\t"
    "v_mfma_f32_16x16x32_bf16 v[168:171], a[24:27], a[44:47], v[168:171]\n\t"
    "buffer_load_dwordx4 v26, s[40:43], s52 offen offset:0 lds\n\t"
    "v_mfma_f32_16x16x32_bf16 v[172:175], a[28:31], a[44:47], v[172:175]\n\t"
    "ds_read_b64_tr_b16 a[118:119], v19 offset:14336\n\t"
    "v_mfma_f32_16x16x32_bf16 v[176:179], a[0:3], a[48:51], v[176:179]\n\t"
    "v_mfma_f32_16x16x32_bf16 v[180:183], a[4:7], a[48:51], v[180:183]\n\t"
    "ds_read_b128 a[88:91], v17 offset:12288\n\t"
    "v_mfma_f32_16x16x32_bf16 v[184:187], a[8:11], a[48:51], v[184:187]\n\t"
    "ds_read_b64_tr_b16 a[120:121], v20 offset:12288\n\t"
    "v_mfma_f32_16x16x32_bf16 v[188:191], a[12:15], a[48:51], v[188:191]\n\t"
    "v_mfma_f32_16x16x32_bf16 v[192:195], a[16:19], a[48:51], v[192:195]\n\t"
    "ds_read_b64_tr_b16 a[122:123], v20 offset:14336\n\t"
    "v_mfma_f32_16x16x32_bf16 v[196:199], a[20:23], a[48:51], v[196:199]\n\t"
    "buffer_load_dwordx4 v27, s[40:43], s52 offen offset:1024 lds\n\t"
    "v_mfma_f32_16x16x32_bf16 v[200:203], a[24:27], a[48:51], v[200:203]\n\t"
    "ds_read_b128 a[92:95], v17 offset:14336\n\t"
    "v_mfma_f32_16x16x32_bf16 v[204:207], a[28:31], a[48:51], v[204:207]\n\t"
    "v_mfma_f32_16x16x32_bf16 v[208:211], a[0:3], a[52:55], v[208:211]\n\t"
    "ds_read_b64_tr_b16 a[124:125], v21 offset:12288\n\t"
    "v_mfma_f32_16x16x32_bf16 v[212:215], a[4:7], a[52:55], v[212:215]\n\t"
    "v_mfma_f32_16x16x32_bf16 v[216:219], a[8:11], a[52:55], v[216:219]\n\t"
    "ds_read_b64_tr_b16 a[126:127], v21 offset:14336\n\t"
    "v_mfma_f32_16x16x32_bf16 v[220:223], a[12:15], a[52:55], v[220:223]\n\t"
    "v_mfma_f32_16x16x32_bf16 v[224:227], a[16:19], a[52:55], v[224:227]\n\t"
    "buffer_load_dwordx4 v28, s[40:43], s52 offen offset:2048 lds\n\t"
    "v_mfma_f32_16x16x32_bf16 v[228:231], a[20:23], a[52:55], v[228:231]\n\t"
    "v_mfma_f32_16x16x32_bf16 v[232:235], a[24:27], a[52:55], v[232:235]\n\t"
    "v_and_b32 v45, 0x3fff, %7\n\t"
    "v_mfma_f32_16x16x32_bf16 v[236:239], a[28:31], a[52:55], v[236:239]\n\t"
    "v_mfma_f32_16x16x32_bf16 v[240:243], a[0:3], a[56:59], v[240:243]\n\t"
    "v_add_u32 v45, s62, v45\n\t"
    "v_mfma_f32_16x16x32_bf16 v[244:247], a[4:7], a[56:59], v[244:247]\n\t"
    "v_mfma_f32_16x16x32_bf16 v[248:251], a[8:11], a[56:59], v[248:251]\n\t"
    "v_xor_b32 v46, 64, v45\n\t"
    "v_mfma_f32_16x16x32_bf16 v[252:255], a[12:15], a[56:59], v[252:255]\n\t"
    "buffer_load_dwordx4 v29, s[40:43], s52 offen offset:3072 lds\n\t"
    "v_mfma_f32_16x16x32_bf16 a[128:131], a[16:19], a[56:59], a[128:131]\n\t"
    "v_add_u32 v16, 0x20000, v45\n\t"
    "v_mfma_f32_16x16x32_bf16 a[132:135], a[20:23], a[56:59], a[132:135]\n\t"
    "v_mfma_f32_16x16x32_bf16 a[136:139], a[24:27], a[56:59], a[136:139]\n\t"
    "v_and_b32 v18, 0x3fff, v18\n\t"
    "v_mfma_f32_16x16x32_bf16 a[140:143], a[28:31], a[56:59], a[140:143]\n\t"
    "s_waitcnt lgkmcnt(15)\n\t"
    "v_mfma_f32_16x16x32_bf16 a[144:147], a[0:3], a[60:63], a[144:147]\n\t"
    "v_and_b32 v19, 0x3fff, v19\n\t"
    "v_mfma_f32_16x16x32_bf16 a[148:151], a[4:7], a[60:63], a[148:151]\n\t"
    "v_mfma_f32_16x16x32_bf16 a[152:155], a[8:11], a[60:63], a[152:155]\n\t"
    "v_and_b32 v20, 0x3fff, v20\n\t"
    "v_mfma_f32_16x16x32_bf16 a[156:159], a[12:15], a[60:63], a[156:159]\n\t"
    "v_mfma_f32_16x16x32_bf16 a[160:163], a[16:19], a[60:63], a[160:163]\n\t"
    "v_and_b32 v21, 0x3fff, v21\n\t"
    "v_mfma_f32_16x16x32_bf16 a[164:167], a[20:23], a[60:63], a[164:167]\n\t"
    "v_mfma_f32_16x16x32_bf16 a[168:171], a[24:27], a[60:63], a[168:171]\n\t"
    "v_mfma_f32_16x16x32_bf16 a[172:175], a[28:31], a[60:63], a[172:175]\n\t"
    "s_waitcnt vmcnt(13) lgkmcnt(0)\n\t"
    "s_barrier\n\t"
    "v_mfma_f32_16x16x32_bf16 v[48:51], a[64:67], a[96:99], v[48:51]\n\t"
    "ds_read_b128 a[0:3], v16 offset:0\n\t"
    "s_add_u32 s57, s62, 0x18000\n\t"
    "s_mov_b32 s52, 0x100\n\t"
    "s_add_u32 m0, s57, 0\n\t"
    "v_mfma_f32_16x16x32_bf16 v[52:55], a[68:71], a[96:99], v[52:55]\n\t"
    "buffer_load_dwordx4 v22, s[40:43], s52 offen offset:0 lds\n\t"
    "v_mfma_f32_16x16x32_bf16 v[56:59], a[72:75], a[96:99], v[56:59]\n\t"
    "ds_read_b64_tr_b16 a[16:17], v18 offset:0\n\t"
    "v_mfma_f32_16x16x32_bf16 v[60:63], a[76:79], a[96:99], v[60:63]\n\t"
    "v_mfma_f32_16x16x32_bf16 v[64:67], a[80:83], a[96:99], v[64:67]\n\t"
    "ds_read_b64_tr_b16 a[18:19], v18 offset:2048\n\t"
    "v_mfma_f32_16x16x32_bf16 v[68:71], a[84:87], a[96:99], v[68:71]\n\t"
    "buffer_load_dwordx4 v23, s[40:43], s52 offen offset:1024 lds\n\t"
    "v_mfma_f32_16x16x32_bf16 v[72:75], a[88:91], a[96:99], v[72:75]\n\t"
    "ds_read_b128 a[4:7], v16 offset:2048\n\t"
    "v_mfma_f32_16x16x32_bf16 v[76:79], a[92:95], a[96:99], v[76:79]\n\t"
    "v_cvt_pk_bf16_f32 v48, v48, v49\n\t"
    "v_cvt_pk_bf16_f32 v49, v50, v51\n\t"
    "v_cvt_pk_bf16_f32 v50, v52, v53\n\t"
    "v_cvt_pk_bf16_f32 v51, v54, v55\n\t"
    "s_mov_b32 s61, 0\n\t"
    "buffer_store_dwordx4 v[48:51], %12, s[48:51], s61 offen offset:0 sc1\n\t"
    "v_mfma_f32_16x16x32_bf16 v[80:83], a[64:67], a[100:103], v[80:83]\n\t"
    "ds_read_b64_tr_b16 a[20:21], v19 offset:0\n\t"
    "v_mfma_f32_16x16x32_bf16 v[84:87], a[68:71], a[100:103], v[84:87]\n\t"
    "v_cvt_pk_bf16_f32 v56, v56, v57\n\t"
    "v_cvt_pk_bf16_f32 v57, v58, v59\n\t"
    "v_cvt_pk_bf16_f32 v58, v60, v61\n\t"
    "v_cvt_pk_bf16_f32 v59, v62, v63\n\t"
    "v_mfma_f32_16x16x32_bf16 v[88:91], a[72:75], a[100:103], v[88:91]\n\t"
    "ds_read_b64_tr_b16 a[22:23], v19 offset:2048\n\t"
    "buffer_load_dwordx4 v24, s[40:43], s52 offen offset:2048 lds\n\t"
    "v_mfma_f32_16x16x32_bf16 v[92:95], a[76:79], a[100:103], v[92:95]\n\t"
    "v_cvt_pk_bf16_f32 v64, v64, v65\n\t"
    "v_cvt_pk_bf16_f32 v65, v66, v67\n\t"
    "v_cvt_pk_bf16_f32 v66, v68, v69\n\t"
    "v_cvt_pk_bf16_f32 v67, v70, v71\n\t"
    "v_mfma_f32_16x16x32_bf16 v[96:99], a[80:83], a[100:103], v[96:99]\n\t"
    "ds_read_b128 a[8:11], v16 offset:4096\n\t"
    "v_mfma_f32_16x16x32_bf16 v[100:103], a[84:87], a[100:103], v[100:103]\n\t"
    "v_cvt_pk_bf16_f32 v72, v72, v73\n\t"
    "v_cvt_pk_bf16_f32 v73, v74, v75\n\t"
    "v_cvt_pk_bf16_f32 v74, v76, v77\n\t"
    "v_cvt_pk_bf16_f32 v75, v78, v79\n\t"
    "s_mov_b32 s61, 0xa000\n\t"
    "buffer_store_dwordx4 v[72:75], %12, s[48:51], s61 offen offset:0 sc1\n\t"
    "v_mfma_f32_16x16x32_bf16 v[104:107], a[88:91], a[100:103], v[104:107]\n\t"
    "ds_read_b64_tr_b16 a[24:25], v20 offset:0\n\t"
    "v_mfma_f32_16x16x32_bf16 v[108:111], a[92:95], a[100:103], v[108:111]\n\t"
    "buffer_load_dwordx4 v25, s[40:43], s52 offen offset:3072 lds\n\t"
    "v_cvt_pk_bf16_f32 v80, v80, v81\n\t"
    "v_cvt_pk_bf16_f32 v81, v82, v83\n\t"
    "v_cvt_pk_bf16_f32 v82, v84, v85\n\t"
    "v_cvt_pk_bf16_f32 v83, v86, v87\n\t"
    "v_mfma_f32_16x16x32_bf16 v[112:115], a[64:67], a[104:107], v[112:115]\n\t"
    "ds_read_b64_tr_b16 a[26:27], v20 offset:2048\n\t"
    "v_mfma_f32_16x16x32_bf16 v[116:119], a[68:71], a[104:107], v[116:119]\n\t"
    "v_cvt_pk_bf16_f32 v88, v88, v89\n\t"
    "v_cvt_pk_bf16_f32 v89, v90, v91\n\t"
    "v_cvt_pk_bf16_f32 v90, v92, v93\n\t"
    "v_cvt_pk_bf16_f32 v91, v94, v95\n\t"
    "v_mfma_f32_16x16x32_bf16 v[120:123], a[72:75], a[104:107], v[120:123]\n\t"
    "ds_read_b128 a[12:15], v16 offset:6144\n\t"
    "v_mfma_f32_16x16x32_bf16 v[124:127], a[76:79], a[104:107], v[124:127]\n\t"
    "s_add_u32 m0, s57, 0x1000\n\t"
    "v_cvt_pk_bf16_f32 v96, v96, v97\n\t"
    "v_cvt_pk_bf16_f32 v97, v98, v99\n\t"
    "v_cvt_pk_bf16_f32 v98, v100, v101\n\t"
    "v_cvt_pk_bf16_f32 v99, v102, v103\n\t"
    "s_mov_b32 s61, 0x8000\n\t"
    "buffer_store_dwordx4 v[96:99], %12, s[48:51], s61 offen offset:1024 sc1\n\t"
    "v_mfma_f32_16x16x32_bf16 v[128:131], a[80:83], a[104:107], v[128:131]\n\t"
    "ds_read_b64_tr_b16 a[28:29], v21 offset:0\n\t"
    "buffer_load_dwordx4 v26, s[40:43], s52 offen offset:0 lds\n\t"
    "v_mfma_f32_16x16x32_bf16 v[132:135], a[84:87], a[104:107], v[132:135]\n\t"
    "v_cvt_pk_bf16_f32 v104, v104, v105\n\t"
    "v_cvt_pk_bf16_f32 v105, v106, v107\n\t"
    "v_cvt_pk_bf16_f32 v106, v108, v109\n\t"
    "v_cvt_pk_bf16_f32 v107, v110, v111\n\t"
    "v_mfma_f32_16x16x32_bf16 v[136:139], a[88:91], a[104:107], v[136:139]\n\t"
    "ds_read_b64_tr_b16 a[30:31], v21 offset:2048\n\t"
    "v_mfma_f32_16x16x32_bf16 v[140:143], a[92:95], a[104:107], v[140:143]\n\t"
    "v_cvt_pk_bf16_f32 v112, v112, v113\n\t"
    "v_cvt_pk_bf16_f32 v113, v114, v115\n\t"
    "v_cvt_pk_bf16_f32 v114, v116, v117\n\t"
    "v_cvt_pk_bf16_f32 v115, v118, v119\n\t"
    "v_mfma_f32_16x16x32_bf16 v[144:147], a[64:67], a[108:111], v[144:147]\n\t"
    "ds_read_b64_tr_b16 a[32:33], v18 offset:8192\n\t"
    "v_mfma_f32_16x16x32_bf16 v[148:151], a[68:71], a[108:111], v[148:151]\n\t"
    "buffer_load_dwordx4 v27, s[40:43], s52 offen offset:1024 lds\n\t"
    "v_cvt_pk_bf16_f32 v120, v120, v121\n\t"
    "v_cvt_pk_bf16_f32 v121, v122, v123\n\t"
    "v_cvt_pk_bf16_f32 v122, v124, v125\n\t"
    "v_cvt_pk_bf16_f32 v123, v126, v127\n\t"
    "s_mov_b32 s61, 0x2000\n\t"
    "buffer_store_dwordx4 v[120:123], %12, s[48:51], s61 offen offset:2048 sc1\n\t"
    "v_mfma_f32_16x16x32_bf16 v[152:155], a[72:75], a[108:111], v[152:155]\n\t"
    "ds_read_b64_tr_b16 a[34:35], v18 offset:10240\n\t"
    "v_mfma_f32_16x16x32_bf16 v[156:159], a[76:79], a[108:111], v[156:159]\n\t"
    "v_cvt_pk_bf16_f32 v128, v128, v129\n\t"
    "v_cvt_pk_bf16_f32 v129, v130, v131\n\t"
    "v_cvt_pk_bf16_f32 v130, v132, v133\n\t"
    "v_cvt_pk_bf16_f32 v131, v134, v135\n\t"
    "v_mfma_f32_16x16x32_bf16 v[160:163], a[80:83], a[108:111], v[160:163]\n\t"
    "ds_read_b64_tr_b16 a[36:37], v19 offset:8192\n\t"
    "v_mfma_f32_16x16x32_bf16 v[164:167], a[84:87], a[108:111], v[164:167]\n\t"
    "v_cvt_pk_bf16_f32 v136, v136, v137\n\t"
    "v_cvt_pk_bf16_f32 v137, v138, v139\n\t"
    "v_cvt_pk_bf16_f32 v138, v140, v141\n\t"
    "v_cvt_pk_bf16_f32 v139, v142, v143\n\t"
    "v_mfma_f32_16x16x32_bf16 v[168:171], a[88:91], a[108:111], v[168:171]\n\t"
    "ds_read_b64_tr_b16 a[38:39], v19 offset:10240\n\t"
    "buffer_load_dwordx4 v28, s[40:43], s52 offen offset:2048 lds\n\t"
    "v_mfma_f32_16x16x32_bf16 v[172:175], a[92:95], a[108:111], v[172:175]\n\t"
    "v_cvt_pk_bf16_f32 v144, v144, v145\n\t"
    "v_cvt_pk_bf16_f32 v145, v146, v147\n\t"
    "v_cvt_pk_bf16_f32 v146, v148, v149\n\t"
    "v_cvt_pk_bf16_f32 v147, v150, v151\n\t"
    "s_mov_b32 s61, 0\n\t"
    "buffer_store_dwordx4 v[144:147], %12, s[48:51], s61 offen offset:3072 sc1\n\t"
    "v_mfma_f32_16x16x32_bf16 v[176:179], a[64:67], a[112:115], v[176:179]\n\t"
    "ds_read_b64_tr_b16 a[40:41], v20 offset:8192\n\t"
    "v_mfma_f32_16x16x32_bf16 v[180:183], a[68:71], a[112:115], v[180:183]\n\t"
    "v_cvt_pk_bf16_f32 v152, v152, v153\n\t"
    "v_cvt_pk_bf16_f32 v153, v154, v155\n\t"
    "v_cvt_pk_bf16_f32 v154, v156, v157\n\t"
    "v_cvt_pk_bf16_f32 v155, v158, v159\n\t"
    "v_mfma_f32_16x16x32_bf16 v[184:187], a[72:75], a[112:115], v[184:187]\n\t"
    "ds_read_b64_tr_b16 a[42:43], v20 offset:10240\n\t"
    "v_mfma_f32_16x16x32_bf16 v[188:191], a[76:79], a[112:115], v[188:191]\n\t"
    "buffer_load_dwordx4 v29, s[40:43], s52 offen offset:3072 lds\n\t"
    "v_cvt_pk_bf16_f32 v160, v160, v161\n\t"
    "v_cvt_pk_bf16_f32 v161, v162, v163\n\t"
    "v_cvt_pk_bf16_f32 v162, v164, v165\n\t"
    "v_cvt_pk_bf16_f32 v163, v166, v167\n\t"
    "v_mfma_f32_16x16x32_bf16 v[192:195], a[80:83], a[112:115], v[192:195]\n\t"
    "ds_read_b64_tr_b16 a[44:45], v21 offset:8192\n\t"
    "v_mfma_f32_16x16x32_bf16 v[196:199], a[84:87], a[112:115], v[196:199]\n\t"
    "v_cvt_pk_bf16_f32 v168, v168, v169\n\t"
    "v_cvt_pk_bf16_f32 v169, v170, v171\n\t"
    "v_cvt_pk_bf16_f32 v170, v172, v173\n\t"
    "v_cvt_pk_bf16_f32 v171, v174, v175\n\t"
    "s_mov_b32 s61, 0xa000\n\t"
    "buffer_store_dwordx4 v[168:171], %12, s[48:51], s61 offen offset:3072 sc1\n\t"
    "v_mfma_f32_16x16x32_bf16 v[200:203], a[88:91], a[112:115], v[200:203]\n\t"
    "ds_read_b64_tr_b16 a[46:47], v21 offset:10240\n\t"
    "v_mfma_f32_16x16x32_bf16 v[204:207], a[92:95], a[112:115], v[204:207]\n\t"
    "v_cvt_pk_bf16_f32 v176, v176, v177\n\t"
    "v_cvt_pk_bf16_f32 v177, v178, v179\n\t"
    "v_cvt_pk_bf16_f32 v178, v180, v181\n\t"
    "v_cvt_pk_bf16_f32 v179, v182, v183\n\t"
    "v_mfma_f32_16x16x32_bf16 v[208:211], a[64:67], a[116:119], v[208:211]\n\t"
    "v_mfma_f32_16x16x32_bf16 v[212:215], a[68:71], a[116:119], v[212:215]\n\t"
    "v_cvt_pk_bf16_f32 v184, v184, v185\n\t"
    "v_cvt_pk_bf16_f32 v185, v186, v187\n\t"
    "v_cvt_pk_bf16_f32 v186, v188, v189\n\t"
    "v_cvt_pk_bf16_f32 v187, v190, v191\n\t"
    "v_mfma_f32_16x16x32_bf16 v[216:219], a[72:75], a[116:119], v[216:219]\n\t"
    "v_mfma_f32_16x16x32_bf16 v[220:223], a[76:79], a[116:119], v[220:223]\n\t"
    "v_cvt_pk_bf16_f32 v192, v192, v193\n\t"
    "v_cvt_pk_bf16_f32 v193, v194, v195\n\t"
    "v_cvt_pk_bf16_f32 v194, v196, v197\n\t"
    "v_cvt_pk_bf16_f32 v195, v198, v199\n\t"
    "s_mov_b32 s61, 0x9000\n\t"
    "buffer_store_dwordx4 v[192:195], %12, s[48:51], s61 offen offset:0 sc1\n\t"
    "v_mfma_f32_16x16x32_bf16 v[224:227], a[80:83], a[116:119], v[224:227]\n\t"
    "v_mfma_f32_16x16x32_bf16 v[228:231], a[84:87], a[116:119], v[228:231]\n\t"
    "v_cvt_pk_bf16_f32 v200, v200, v201\n\t"
    "v_cvt_pk_bf16_f32 v201, v202, v203\n\t"
    "v_cvt_pk_bf16_f32 v202, v204, v205\n\t"
    "v_cvt_pk_bf16_f32 v203, v206, v207\n\t"
    "v_mfma_f32_16x16x32_bf16 v[232:235], a[88:91], a[116:119], v[232:235]\n\t"
    "v_mfma_f32_16x16x32_bf16 v[236:239], a[92:95], a[116:119], v[236:239]\n\t"
    "v_cvt_pk_bf16_f32 v208, v208, v209\n\t"
    "v_cvt_pk_bf16_f32 v209, v210, v211\n\t"
    "v_cvt_pk_bf16_f32 v210, v212, v213\n\t"
    "v_cvt_pk_bf16_f32 v211, v214, v215\n\t"
    "v_mfma_f32_16x16x32_bf16 v[240:243], a[64:67], a[120:123], v[240:243]\n\t"
    "v_mfma_f32_16x16x32_bf16 v[244:247], a[68:71], a[120:123], v[244:247]\n\t"
    "v_cvt_pk_bf16_f32 v216, v216, v217\n\t"
    "v_cvt_pk_bf16_f32 v217, v218, v219\n\t"
    "v_cvt_pk_bf16_f32 v218, v220, v221\n\t"
    "v_cvt_pk_bf16_f32 v219, v222, v223\n\t"
    "s_mov_b32 s61, 0x3000\n\t"
    "buffer_store_dwordx4 v[216:219], %12, s[48:51], s61 offen offset:1024 sc1\n\t"
    "v_mfma_f32_16x16x32_bf16 v[248:251], a[72:75], a[120:123], v[248:251]\n\t"
    "v_mfma_f32_16x16x32_bf16 v[252:255], a[76:79], a[120:123], v[252:255]\n\t"
    "v_cvt_pk_bf16_f32 v224, v224, v225\n\t"
    "v_cvt_pk_bf16_f32 v225, v226, v227\n\t"
    "v_cvt_pk_bf16_f32 v226, v228, v229\n\t"
    "v_cvt_pk_bf16_f32 v227, v230, v231\n\t"
    "v_mfma_f32_16x16x32_bf16 a[128:131], a[80:83], a[120:123], a[128:131]\n\t"
    "v_mfma_f32_16x16x32_bf16 a[132:135], a[84:87], a[120:123], a[132:135]\n\t"
    "v_cvt_pk_bf16_f32 v232, v232, v233\n\t"
    "v_cvt_pk_bf16_f32 v233, v234, v235\n\t"
    "v_cvt_pk_bf16_f32 v234, v236, v237\n\t"
    "v_cvt_pk_bf16_f32 v235, v238, v239\n\t"
    "v_mfma_f32_16x16x32_bf16 a[136:139], a[88:91], a[120:123], a[136:139]\n\t"
    "v_mfma_f32_16x16x32_bf16 a[140:143], a[92:95], a[120:123], a[140:143]\n\t"
    "v_cvt_pk_bf16_f32 v240, v240, v241\n\t"
    "v_cvt_pk_bf16_f32 v241, v242, v243\n\t"
    "v_cvt_pk_bf16_f32 v242, v244, v245\n\t"
    "v_cvt_pk_bf16_f32 v243, v246, v247\n\t"
    "v_mfma_f32_16x16x32_bf16 a[144:147], a[64:67], a[124:127], a[144:147]\n\t"
    "v_mfma_f32_16x16x32_bf16 a[148:151], a[68:71], a[124:127], a[148:151]\n\t"
    "v_cvt_pk_bf16_f32 v248, v248, v249\n\t"
    "v_cvt_pk_bf16_f32 v249, v250, v251\n\t"
    "v_cvt_pk_bf16_f32 v250, v252, v253\n\t"
    "v_cvt_pk_bf16_f32 v251, v254, v255\n\t"
    "v_mfma_f32_16x16x32_bf16 a[152:155], a[72:75], a[124:127], a[152:155]\n\t"
    "v_mfma_f32_16x16x32_bf16 a[156:159], a[76:79], a[124:127], a[156:159]\n\t"
    "v_accvgpr_read_b32 v36, a128\n\t"
    "v_accvgpr_read_b32 v37, a129\n\t"
    "v_accvgpr_read_b32 v38, a130\n\t"
    "v_accvgpr_read_b32 v39, a131\n\t"
    "v_accvgpr_read_b32 v40, a132\n\t"
    "v_accvgpr_read_b32 v41, a133\n\t"
    "v_accvgpr_read_b32 v42, a134\n\t"
    "v_accvgpr_read_b32 v43, a135\n\t"
    "v_cvt_pk_bf16_f32 v36, v36, v37\n\t"
    "v_cvt_pk_bf16_f32 v37, v38, v39\n\t"
    "v_cvt_pk_bf16_f32 v38, v40, v41\n\t"
    "v_cvt_pk_bf16_f32 v39, v42, v43\n\t"
    "s_mov_b32 s61, 0x9000\n\t"
    "buffer_store_dwordx4 v[36:39], %12, s[48:51], s61 offen offset:2048 sc1\n\t"
    "v_mfma_f32_16x16x32_bf16 a[160:163], a[80:83], a[124:127], a[160:163]\n\t"
    "v_mfma_f32_16x16x32_bf16 a[164:167], a[84:87], a[124:127], a[164:167]\n\t"
    "v_accvgpr_read_b32 v36, a136\n\t"
    "v_accvgpr_read_b32 v37, a137\n\t"
    "v_accvgpr_read_b32 v38, a138\n\t"
    "v_accvgpr_read_b32 v39, a139\n\t"
    "v_accvgpr_read_b32 v40, a140\n\t"
    "v_accvgpr_read_b32 v41, a141\n\t"
    "v_accvgpr_read_b32 v42, a142\n\t"
    "v_accvgpr_read_b32 v43, a143\n\t"
    "v_cvt_pk_bf16_f32 v36, v36, v37\n\t"
    "v_cvt_pk_bf16_f32 v37, v38, v39\n\t"
    "v_cvt_pk_bf16_f32 v38, v40, v41\n\t"
    "v_cvt_pk_bf16_f32 v39, v42, v43\n\t"
    "s_mov_b32 s61, 0xb000\n\t"
    "buffer_store_dwordx4 v[36:39], %12, s[48:51], s61 offen offset:2048 sc1\n\t"
    "v_mfma_f32_16x16x32_bf16 a[168:171], a[88:91], a[124:127], a[168:171]\n\t"
    "v_mfma_f32_16x16x32_bf16 a[172:175], a[92:95], a[124:127], a[172:175]\n\t"
    "v_accvgpr_read_b32 v36, a144\n\t"
    "v_accvgpr_read_b32 v37, a145\n\t"
    "v_accvgpr_read_b32 v38, a146\n\t"
    "v_accvgpr_read_b32 v39, a147\n\t"
    "v_accvgpr_read_b32 v40, a148\n\t"
    "v_accvgpr_read_b32 v41, a149\n\t"
    "v_accvgpr_read_b32 v42, a150\n\t"
    "v_accvgpr_read_b32 v43, a151\n\t"
    "v_cvt_pk_bf16_f32 v36, v36, v37\n\t"
    "v_cvt_pk_bf16_f32 v37, v38, v39\n\t"
    "v_cvt_pk_bf16_f32 v38, v40, v41\n\t"
    "v_cvt_pk_bf16_f32 v39, v42, v43\n\t"
    "s_mov_b32 s61, 0x1000\n\t"
    "buffer_store_dwordx4 v[36:39], %12, s[48:51], s61 offen offset:3072 sc1\n\t"
    "s_nop 1\n\t"
    "v_accvgpr_read_b32 v36, a152\n\t"
    "v_accvgpr_read_b32 v37, a153\n\t"
    "v_accvgpr_read_b32 v38, a154\n\t"
    "v_accvgpr_read_b32 v39, a155\n\t"
    "v_accvgpr_read_b32 v40, a156\n\t"
    "v_accvgpr_read_b32 v41, a157\n\t"
    "v_accvgpr_read_b32 v42, a158\n\t"
    "v_accvgpr_read_b32 v43, a159\n\t"
    "v_cvt_pk_bf16_f32 v36, v36, v37\n\t"
    "v_cvt_pk_bf16_f32 v37, v38, v39\n\t"
    "v_cvt_pk_bf16_f32 v38, v40, v41\n\t"
    "v_cvt_pk_bf16_f32 v39, v42, v43\n\t"
    "s_mov_b32 s61, 0x3000\n\t"
    "buffer_store_dwordx4 v[36:39], %12, s[48:51], s61 offen offset:3072 sc1\n\t"
    "s_nop 1\n\t"
    "v_accvgpr_read_b32 v36, a160\n\t"
    "v_accvgpr_read_b32 v37, a161\n\t"
    "v_accvgpr_read_b32 v38, a162\n\t"
    "v_accvgpr_read_b32 v39, a163\n\t"
    "v_accvgpr_read_b32 v40, a164\n\t"
    "v_accvgpr_read_b32 v41, a165\n\t"
    "v_accvgpr_read_b32 v42, a166\n\t"
    "v_accvgpr_read_b32 v43, a167\n\t"
    "v_cvt_pk_bf16_f32 v36, v36, v37\n\t"
    "v_cvt_pk_bf16_f32 v37, v38, v39\n\t"
    "v_cvt_pk_bf16_f32 v38, v40, v41\n\t"
    "v_cvt_pk_bf16_f32 v39, v42, v43\n\t"
    "s_mov_b32 s61, 0x9000\n\t"
    "buffer_store_dwordx4 v[36:39], %12, s[48:51], s61 offen offset:3072 sc1\n\t"
    "s_nop 1\n\t"
    "v_accvgpr_read_b32 v36, a168\n\t"
    "v_accvgpr_read_b32 v37, a169\n\t"
    "v_accvgpr_read_b32 v38, a170\n\t"
    "v_accvgpr_read_b32 v39, a171\n\t"
    "v_accvgpr_read_b32 v40, a172\n\t"
    "v_accvgpr_read_b32 v41, a173\n\t"
    "v_accvgpr_read_b32 v42, a174\n\t"
    "v_accvgpr_read_b32 v43, a175\n\t"
    "v_cvt_pk_bf16_f32 v36, v36, v37\n\t"
    "v_cvt_pk_bf16_f32 v37, v38, v39\n\t"
    "v_cvt_pk_bf16_f32 v38, v40, v41\n\t"
    "v_cvt_pk_bf16_f32 v39, v42, v43\n\t"
    "s_mov_b32 s61, 0xb000\n\t"
    "buffer_store_dwordx4 v[36:39], %12, s[48:51], s61 offen offset:3072 sc1\n\t"
    "s_mov_b32 s59, 1\n\t"
    "; ---- q\n\t"
    "q_start_%=:\n\t"
    "v_add_u32 v17, 0x20000, v46\n\t"
    "s_and_b32 s67, %1, 1\n\t"
    "s_lshl_b32 s60, s67, 15\n\t"
    "s_add_u32 s60, s60, 0x14000\n\t"
    "s_lshl_b32 s67, s67, 14\n\t"
    "s_add_u32 s67, s67, 0x20000\n\t"
    "s_cmp_lt_u32 %1, 2\n\t"
    "s_cselect_b32 s60, s60, s67\n\t"
    "v_add_u32 v34, s60, %12\n\t"
    "s_waitcnt lgkmcnt(4)\n\t"
    "v_mfma_f32_16x16x32_bf16 v[52:55], a[16:19], a[0:3], 0\n\t"
    "ds_read_b128 a[48:51], v17 offset:0\n\t"
    "v_mfma_f32_16x16x32_bf16 v[60:63], a[20:23], a[0:3], 0\n\t"
    "ds_read_b64_tr_b16 a[64:65], v18 offset:4096\n\t"
    "v_mfma_f32_16x16x32_bf16 v[68:71], a[24:27], a[0:3], 0\n\t"
    "ds_read_b64_tr_b16 a[66:67], v18 offset:6144\n\t"
    "v_mfma_f32_16x16x32_bf16 v[76:79], a[28:31], a[0:3], 0\n\t"
    "ds_read_b128 a[52:55], v17 offset:2048\n\t"
    "v_mfma_f32_16x16x32_bf16 v[84:87], a[32:35], a[0:3], 0\n\t"
    "ds_read_b64_tr_b16 a[68:69], v19 offset:4096\n\t"
    "v_mfma_f32_16x16x32_bf16 v[92:95], a[36:39], a[0:3], 0\n\t"
    "ds_read_b64_tr_b16 a[70:71], v19 offset:6144\n\t"
    "s_waitcnt lgkmcnt(6)\n\t"
    "v_mfma_f32_16x16x32_bf16 v[100:103], a[40:43], a[0:3], 0\n\t"
    "ds_read_b128 a[56:59], v17 offset:4096\n\t"
    "v_mfma_f32_16x16x32_bf16 v[108:111], a[44:47], a[0:3], 0\n\t"
    "ds_read_b64_tr_b16 a[72:73], v20 offset:4096\n\t"
    "v_mfma_f32_16x16x32_bf16 v[116:119], a[16:19], a[4:7], 0\n\t"
    "ds_read_b64_tr_b16 a[74:75], v20 offset:6144\n\t"
    "v_mfma_f32_16x16x32_bf16 v[124:127], a[20:23], a[4:7], 0\n\t"
    "ds_read_b128 a[60:63], v17 offset:6144\n\t"
    "v_mfma_f32_16x16x32_bf16 v[132:135], a[24:27], a[4:7], 0\n\t"
    "v_mfma_f32_16x16x32_bf16 v[140:143], a[28:31], a[4:7], 0\n\t"
    "ds_read_b64_tr_b16 a[76:77], v21 offset:4096\n\t"
    "v_mfma_f32_16x16x32_bf16 v[148:151], a[32:35], a[4:7], 0\n\t"
    "ds_read_b64_tr_b16 a[78:79], v21 offset:6144\n\t"
    "v_mfma_f32_16x16x32_bf16 v[156:159], a[36:39], a[4:7], 0\n\t"
    "ds_read_b64_tr_b16 a[80:81], v18 offset:12288\n\t"
    "v_mfma_f32_16x16x32_bf16 v[164:167], a[40:43], a[4:7], 0\n\t"
    "ds_read_b64_tr_b16 a[82:83], v18 offset:14336\n\t"
    "v_mfma_f32_16x16x32_bf16 v[172:175], a[44:47], a[4:7], 0\n\t"
    "ds_read_b64_tr_b16 a[84:85], v19 offset:12288\n\t"
    "v_mfma_f32_16x16x32_bf16 v[180:183], a[16:19], a[8:11], 0\n\t"
    "ds_read_b64_tr_b16 a[86:87], v19 offset:14336\n\t"
    "v_mfma_f32_16x16x32_bf16 v[188:191], a[20:23], a[8:11], 0\n\t"
    "ds_read_b64_tr_b16 a[88:89], v20 offset:12288\n\t"
    "v_mfma_f32_16x16x32_bf16 v[196:199], a[24:27], a[8:11], 0\n\t"
    "ds_read_b64_tr_b16 a[90:91], v20 offset:14336\n\t"
    "v_mfma_f32_16x16x32_bf16 v[204:207], a[28:31], a[8:11], 0\n\t"
    "ds_read_b64_tr_b16 a[92:93], v21 offset:12288\n\t"
    "v_mfma_f32_16x16x32_bf16 v[212:215], a[32:35], a[8:11], 0\n\t"
    "ds_read_b64_tr_b16 a[94:95], v21 offset:14336\n\t"
    "v_mfma_f32_16x16x32_bf16 v[220:223], a[36:39], a[8:11], 0\n\t"
    "v_mfma_f32_16x16x32_bf16 v[228:231], a[40:43], a[8:11], 0\n\t"
    "v_mfma_f32_16x16x32_bf16 v[236:239], a[44:47], a[8:11], 0\n\t"
    "v_mfma_f32_16x16x32_bf16 v[244:247], a[16:19], a[12:15], 0\n\t"
    "v_add_u32 v16, 0x10000, v45\n\t"
    "v_mfma_f32_16x16x32_bf16 v[252:255], a[20:23], a[12:15], 0\n\t"
    "v_mfma_f32_16x16x32_bf16 a[176:179], a[24:27], a[12:15], 0\n\t"
    "v_mfma_f32_16x16x32_bf16 a[180:183], a[28:31], a[12:15], 0\n\t"
    "v_mfma_f32_16x16x32_bf16 a[184:187], a[32:35], a[12:15], 0\n\t"
    "v_mfma_f32_16x16x32_bf16 a[188:191], a[36:39], a[12:15], 0\n\t"
    "v_mfma_f32_16x16x32_bf16 a[192:195], a[40:43], a[12:15], 0\n\t"
    "v_mfma_f32_16x16x32_bf16 a[196:199], a[44:47], a[12:15], 0\n\t"
    "s_waitcnt vmcnt(22) lgkmcnt(0)\n\t"
    "s_barrier\n\t"
    "v_mfma_f32_16x16x32_bf16 v[52:55], a[64:67], a[48:51], v[52:55]\n\t"
    "ds_read_b128 a[0:3], v16 offset:0\n\t"
    "s_add_u32 s58, s64, 0\n\t"
    "s_mov_b32 s53, 0x100000\n\t"
    "s_add_u32 m0, s58, 0\n\t"
    "v_mfma_f32_16x16x32_bf16 v[60:63], a[68:71], a[48:51], v[60:63]\n\t"
    "ds_read_b64_tr_b16 a[16:17], v18 offset:16384\n\t"
    "buffer_load_dwordx4 v30, s[44:47], s53 offen offset:0 lds\n\t"
    "v_mfma_f32_16x16x32_bf16 v[68:71], a[72:75], a[48:51], v[68:71]\n\t"
    "ds_read_b64_tr_b16 a[18:19], v18 offset:18432\n\t"
    "v_mfma_f32_16x16x32_bf16 v[76:79], a[76:79], a[48:51], v[76:79]\n\t"
    "ds_read_b128 a[4:7], v16 offset:2048\n\t"
    "v_mfma_f32_16x16x32_bf16 v[84:87], a[80:83], a[48:51], v[84:87]\n\t"
    "ds_read_b64_tr_b16 a[20:21], v19 offset:16384\n\t"
    "v_mfma_f32_16x16x32_bf16 v[92:95], a[84:87], a[48:51], v[92:95]\n\t"
    "ds_read_b64_tr_b16 a[22:23], v19 offset:18432\n\t"
    "buffer_load_dwordx4 v31, s[44:47], s53 offen offset:1024 lds\n\t"
    "v_mfma_f32_16x16x32_bf16 v[100:103], a[88:91], a[48:51], v[100:103]\n\t"
    "ds_read_b128 a[8:11], v16 offset:4096\n\t"
    "v_mfma_f32_16x16x32_bf16 v[108:111], a[92:95], a[48:51], v[108:111]\n\t"
    "ds_read_b64_tr_b16 a[24:25], v20 offset:16384\n\t"
    "v_mfma_f32_16x16x32_bf16 v[116:119], a[64:67], a[52:55], v[116:119]\n\t"
    "ds_read_b64_tr_b16 a[26:27], v20 offset:18432\n\t"
    "v_mfma_f32_16x16x32_bf16 v[124:127], a[68:71], a[52:55], v[124:127]\n\t"
    "ds_read_b128 a[12:15], v16 offset:6144\n\t"
    "v_mfma_f32_16x16x32_bf16 v[132:135], a[72:75], a[52:55], v[132:135]\n\t"
    "buffer_load_dwordx4 v32, s[44:47], s53 offen offset:2048 lds\n\t"
    "v_mfma_f32_16x16x32_bf16 v[140:143], a[76:79], a[52:55], v[140:143]\n\t"
    "ds_read_b64_tr_b16 a[28:29], v21 offset:16384\n\t"
    "v_mfma_f32_16x16x32_bf16 v[148:151], a[80:83], a[52:55], v[148:151]\n\t"
    "ds_read_b64_tr_b16 a[30:31], v21 offset:18432\n\t"
    "v_mfma_f32_16x16x32_bf16 v[156:159], a[84:87], a[52:55], v[156:159]\n\t"
    "ds_read_b64_tr_b16 a[32:33], v18 offset:24576\n\t"
    "v_mfma_f32_16x16x32_bf16 v[164:167], a[88:91], a[52:55], v[164:167]\n\t"
    "ds_read_b64_tr_b16 a[34:35], v18 offset:26624\n\t"
    "buffer_load_dwordx4 v33, s[44:47], s53 offen offset:3072 lds\n\t"
    "v_mfma_f32_16x16x32_bf16 v[172:175], a[92:95], a[52:55], v[172:175]\n\t"
    "ds_read_b64_tr_b16 a[36:37], v19 offset:24576\n\t"
    "v_mfma_f32_16x16x32_bf16 v[180:183], a[64:67], a[56:59], v[180:183]\n\t"
    "ds_read_b64_tr_b16 a[38:39], v19 offset:26624\n\t"
    "v_mfma_f32_16x16x32_bf16 v[188:191], a[68:71], a[56:59], v[188:191]\n\t"
    "ds_read_b64_tr_b16 a[40:41], v20 offset:24576\n\t"
    "v_mfma_f32_16x16x32_bf16 v[196:199], a[72:75], a[56:59], v[196:199]\n\t"
    "ds_read_b64_tr_b16 a[42:43], v20 offset:26624\n\t"
    "s_cmp_eq_u32 s59, 0\n\t"
    "s_mov_b32 s61, 0x400\n\t"
    "s_cselect_b32 s61, s61, 0x2000\n\t"
    "v_mfma_f32_16x16x32_bf16 v[204:207], a[76:79], a[56:59], v[204:207]\n\t"
    "ds_read_b64_tr_b16 a[44:45], v21 offset:24576\n\t"
    "buffer_store_dwordx4 v[56:59], %12, s[48:51], s61 offen offset:0 sc1\n\t"
    "v_mfma_f32_16x16x32_bf16 v[212:215], a[80:83], a[56:59], v[212:215]\n\t"
    "ds_read_b64_tr_b16 a[46:47], v21 offset:26624\n\t"
    "v_mfma_f32_16x16x32_bf16 v[220:223], a[84:87], a[56:59], v[220:223]\n\t"
    "v_mfma_f32_16x16x32_bf16 v[228:231], a[88:91], a[56:59], v[228:231]\n\t"
    "s_mov_b64 s[68:69], %26\n\t"
    "s_mov_b32 s70, %27\n\t"
    "s_mov_b32 s71, 0x20000\n\t"
    "s_lshl_b32 s63, %1, 10\n\t"
    "s_add_u32 s63, s63, 0\n\t"
    "v_mfma_f32_16x16x32_bf16 v[236:239], a[92:95], a[56:59], v[236:239]\n\t"
    "buffer_load_dword a255, %12, s[68:71], s63 offen\n\t"
    "v_mfma_f32_16x16x32_bf16 v[244:247], a[64:67], a[60:63], v[244:247]\n\t"
    "v_mfma_f32_16x16x32_bf16 v[252:255], a[68:71], a[60:63], v[252:255]\n\t"
    "v_mfma_f32_16x16x32_bf16 a[176:179], a[72:75], a[60:63], a[176:179]\n\t"
    "v_mfma_f32_16x16x32_bf16 a[180:183], a[76:79], a[60:63], a[180:183]\n\t"
    "v_mfma_f32_16x16x32_bf16 a[184:187], a[80:83], a[60:63], a[184:187]\n\t"
    "v_add_u32 v17, 0x10000, v46\n\t"
    "v_mfma_f32_16x16x32_bf16 a[188:191], a[84:87], a[60:63], a[188:191]\n\t"
    "v_mfma_f32_16x16x32_bf16 a[192:195], a[88:91], a[60:63], a[192:195]\n\t"
    "v_mfma_f32_16x16x32_bf16 a[196:199], a[92:95], a[60:63], a[196:199]\n\t"
    "s_waitcnt lgkmcnt(8)\n\t"
    "v_mfma_f32_16x16x32_bf16 v[52:55], a[16:19], a[0:3], v[52:55]\n\t"
    "ds_read_b128 a[48:51], v17 offset:0\n\t"
    "s_add_u32 s57, s62, 0x20000\n\t"
    "s_mov_b32 s52, 0x180\n\t"
    "s_add_u32 m0, s57, 0\n\t"
    "v_mfma_f32_16x16x32_bf16 v[60:63], a[20:23], a[0:3], v[60:63]\n\t"
    "ds_read_b64_tr_b16 a[64:65], v18 offset:20480\n\t"
    "buffer_load_dwordx4 v22, s[40:43], s52 offen offset:0 lds\n\t"
    "v_mfma_f32_16x16x32_bf16 v[68:71], a[24:27], a[0:3], v[68:71]\n\t"
    "ds_read_b64_tr_b16 a[66:67], v18 offset:22528\n\t"
    "v_mfma_f32_16x16x32_bf16 v[76:79], a[28:31], a[0:3], v[76:79]\n\t"
    "ds_read_b128 a[52:55], v17 offset:2048\n\t"
    "s_waitcnt lgkmcnt(4)\n\t"
    "v_mfma_f32_16x16x32_bf16 v[84:87], a[32:35], a[0:3], v[84:87]\n\t"
    "ds_read_b64_tr_b16 a[68:69], v19 offset:20480\n\t"
    "buffer_load_dwordx4 v23, s[40:43], s52 offen offset:1024 lds\n\t"
    "v_mfma_f32_16x16x32_bf16 v[92:95], a[36:39], a[0:3], v[92:95]\n\t"
    "ds_read_b64_tr_b16 a[70:71], v19 offset:22528\n\t"
    "v_mfma_f32_16x16x32_bf16 v[100:103], a[40:43], a[0:3], v[100:103]\n\t"
    "ds_read_b128 a[56:59], v17 offset:4096\n\t"
    "v_mfma_f32_16x16x32_bf16 v[108:111], a[44:47], a[0:3], v[108:111]\n\t"
    "ds_read_b64_tr_b16 a[72:73], v20 offset:20480\n\t"
    "v_mfma_f32_16x16x32_bf16 v[116:119], a[16:19], a[4:7], v[116:119]\n\t"
    "ds_read_b64_tr_b16 a[74:75], v20 offset:22528\n\t"
    "buffer_load_dwordx4 v24, s[40:43], s52 offen offset:2048 lds\n\t"
    "v_mfma_f32_16x16x32_bf16 v[124:127], a[20:23], a[4:7], v[124:127]\n\t"
    "ds_read_b128 a[60:63], v17 offset:6144\n\t"
    "v_mfma_f32_16x16x32_bf16 v[132:135], a[24:27], a[4:7], v[132:135]\n\t"
    "v_mfma_f32_16x16x32_bf16 v[140:143], a[28:31], a[4:7], v[140:143]\n\t"
    "ds_read_b64_tr_b16 a[76:77], v21 offset:20480\n\t"
    "buffer_load_dwordx4 v25, s[40:43], s52 offen offset:3072 lds\n\t"
    "v_mfma_f32_16x16x32_bf16 v[148:151], a[32:35], a[4:7], v[148:151]\n\t"
    "ds_read_b64_tr_b16 a[78:79], v21 offset:22528\n\t"
    "v_mfma_f32_16x16x32_bf16 v[156:159], a[36:39], a[4:7], v[156:159]\n\t"
    "ds_read_b64_tr_b16 a[80:81], v18 offset:28672\n\t"
    "v_mfma_f32_16x16x32_bf16 v[164:167], a[40:43], a[4:7], v[164:167]\n\t"
    "ds_read_b64_tr_b16 a[82:83], v18 offset:30720\n\t"
    "s_add_u32 m0, s57, 0x1000\n\t"
    "v_mfma_f32_16x16x32_bf16 v[172:175], a[44:47], a[4:7], v[172:175]\n\t"
    "ds_read_b64_tr_b16 a[84:85], v19 offset:28672\n\t"
    "buffer_load_dwordx4 v26, s[40:43], s52 offen offset:0 lds\n\t"
    "v_mfma_f32_16x16x32_bf16 v[180:183], a[16:19], a[8:11], v[180:183]\n\t"
    "ds_read_b64_tr_b16 a[86:87], v19 offset:30720\n\t"
    "v_mfma_f32_16x16x32_bf16 v[188:191], a[20:23], a[8:11], v[188:191]\n\t"
    "ds_read_b64_tr_b16 a[88:89], v20 offset:28672\n\t"
    "v_mfma_f32_16x16x32_bf16 v[196:199], a[24:27], a[8:11], v[196:199]\n\t"
    "ds_read_b64_tr_b16 a[90:91], v20 offset:30720\n\t"
    "v_mfma_f32_16x16x32_bf16 v[204:207], a[28:31], a[8:11], v[204:207]\n\t"
    "ds_read_b64_tr_b16 a[92:93], v21 offset:28672\n\t"
    "buffer_load_dwordx4 v27, s[40:43], s52 offen offset:1024 lds\n\t"
    "v_mfma_f32_16x16x32_bf16 v[212:215], a[32:35], a[8:11], v[212:215]\n\t"
    "ds_read_b64_tr_b16 a[94:95], v21 offset:30720\n\t"
    "v_mfma_f32_16x16x32_bf16 v[220:223], a[36:39], a[8:11], v[220:223]\n\t"
    "v_mfma_f32_16x16x32_bf16 v[228:231], a[40:43], a[8:11], v[228:231]\n\t"
    "buffer_load_dwordx4 v28, s[40:43], s52 offen offset:2048 lds\n\t"
    "v_mfma_f32_16x16x32_bf16 v[236:239], a[44:47], a[8:11], v[236:239]\n\t"
    "v_mfma_f32_16x16x32_bf16 v[244:247], a[16:19], a[12:15], v[244:247]\n\t"
    "v_add_u32 v16, 0x18000, v45\n\t"
    "v_mfma_f32_16x16x32_bf16 v[252:255], a[20:23], a[12:15], v[252:255]\n\t"
    "v_mfma_f32_16x16x32_bf16 a[176:179], a[24:27], a[12:15], a[176:179]\n\t"
    "buffer_load_dwordx4 v29, s[40:43], s52 offen offset:3072 lds\n\t"
    "v_mfma_f32_16x16x32_bf16 a[180:183], a[28:31], a[12:15], a[180:183]\n\t"
    "v_mfma_f32_16x16x32_bf16 a[184:187], a[32:35], a[12:15], a[184:187]\n\t"
    "v_mfma_f32_16x16x32_bf16 a[188:191], a[36:39], a[12:15], a[188:191]\n\t"
    "v_mfma_f32_16x16x32_bf16 a[192:195], a[40:43], a[12:15], a[192:195]\n\t"
    "v_mfma_f32_16x16x32_bf16 a[196:199], a[44:47], a[12:15], a[196:199]\n\t"
    "s_waitcnt vmcnt(10) lgkmcnt(0)\n\t"
    "s_barrier\n\t"
    "v_mfma_f32_16x16x32_bf16 v[52:55], a[64:67], a[48:51], v[52:55]\n\t"
    "ds_read_b128 a[0:3], v16 offset:0\n\t"
    "s_add_u32 s58, s64, 0x4000\n\t"
    "s_mov_b32 s53, 0x180000\n\t"
    "s_add_u32 m0, s58, 0\n\t"
    "v_mfma_f32_16x16x32_bf16 v[60:63], a[68:71], a[48:51], v[60:63]\n\t"
    "ds_read_b64_tr_b16 a[16:17], v18 offset:0\n\t"
    "buffer_load_dwordx4 v30, s[44:47], s53 offen offset:0 lds\n\t"
    "v_mfma_f32_16x16x32_bf16 v[68:71], a[72:75], a[48:51], v[68:71]\n\t"
    "ds_read_b64_tr_b16 a[18:19], v18 offset:2048\n\t"
    "v_mfma_f32_16x16x32_bf16 v[76:79], a[76:79], a[48:51], v[76:79]\n\t"
    "ds_read_b128 a[4:7], v16 offset:2048\n\t"
    "v_mfma_f32_16x16x32_bf16 v[84:87], a[80:83], a[48:51], v[84:87]\n\t"
    "ds_read_b64_tr_b16 a[20:21], v19 offset:0\n\t"
    "v_mfma_f32_16x16x32_bf16 v[92:95], a[84:87], a[48:51], v[92:95]\n\t"
    "ds_read_b64_tr_b16 a[22:23], v19 offset:2048\n\t"
    "buffer_load_dwordx4 v31, s[44:47], s53 offen offset:1024 lds\n\t"
    "v_mfma_f32_16x16x32_bf16 v[100:103], a[88:91], a[48:51], v[100:103]\n\t"
    "ds_read_b128 a[8:11], v16 offset:4096\n\t"
    "v_mfma_f32_16x16x32_bf16 v[108:111], a[92:95], a[48:51], v[108:111]\n\t"
    "ds_read_b64_tr_b16 a[24:25], v20 offset:0\n\t"
    "v_mfma_f32_16x16x32_bf16 v[116:119], a[64:67], a[52:55], v[116:119]\n\t"
    "ds_read_b64_tr_b16 a[26:27], v20 offset:2048\n\t"
    "v_mfma_f32_16x16x32_bf16 v[124:127], a[68:71], a[52:55], v[124:127]\n\t"
    "ds_read_b128 a[12:15], v16 offset:6144\n\t"
    "v_mfma_f32_16x16x32_bf16 v[132:135], a[72:75], a[52:55], v[132:135]\n\t"
    "buffer_load_dwordx4 v32, s[44:47], s53 offen offset:2048 lds\n\t"
    "v_mfma_f32_16x16x32_bf16 v[140:143], a[76:79], a[52:55], v[140:143]\n\t"
    "ds_read_b64_tr_b16 a[28:29], v21 offset:0\n\t"
    "v_mfma_f32_16x16x32_bf16 v[148:151], a[80:83], a[52:55], v[148:151]\n\t"
    "ds_read_b64_tr_b16 a[30:31], v21 offset:2048\n\t"
    "v_mfma_f32_16x16x32_bf16 v[156:159], a[84:87], a[52:55], v[156:159]\n\t"
    "ds_read_b64_tr_b16 a[32:33], v18 offset:8192\n\t"
    "v_mfma_f32_16x16x32_bf16 v[164:167], a[88:91], a[52:55], v[164:167]\n\t"
    "ds_read_b64_tr_b16 a[34:35], v18 offset:10240\n\t"
    "buffer_load_dwordx4 v33, s[44:47], s53 offen offset:3072 lds\n\t"
    "v_mfma_f32_16x16x32_bf16 v[172:175], a[92:95], a[52:55], v[172:175]\n\t"
    "ds_read_b64_tr_b16 a[36:37], v19 offset:8192\n\t"
    "v_mfma_f32_16x16x32_bf16 v[180:183], a[64:67], a[56:59], v[180:183]\n\t"
    "ds_read_b64_tr_b16 a[38:39], v19 offset:10240\n\t"
    "v_mfma_f32_16x16x32_bf16 v[188:191], a[68:71], a[56:59], v[188:191]\n\t"
    "ds_read_b64_tr_b16 a[40:41], v20 offset:8192\n\t"
    "v_mfma_f32_16x16x32_bf16 v[196:199], a[72:75], a[56:59], v[196:199]\n\t"
    "ds_read_b64_tr_b16 a[42:43], v20 offset:10240\n\t"
    "s_cmp_eq_u32 s59, 0\n\t"
    "s_mov_b32 s61, 0x800\n\t"
    "s_cselect_b32 s61, s61, 0x8000\n\t"
    "v_mfma_f32_16x16x32_bf16 v[204:207], a[76:79], a[56:59], v[204:207]\n\t"
    "ds_read_b64_tr_b16 a[44:45], v21 offset:8192\n\t"
    "buffer_store_dwordx4 v[64:67], %12, s[48:51], s61 offen offset:0 sc1\n\t"
    "v_mfma_f32_16x16x32_bf16 v[212:215], a[80:83], a[56:59], v[212:215]\n\t"
    "ds_read_b64_tr_b16 a[46:47], v21 offset:10240\n\t"
    "v_mfma_f32_16x16x32_bf16 v[220:223], a[84:87], a[56:59], v[220:223]\n\t"
    "v_mfma_f32_16x16x32_bf16 v[228:231], a[88:91], a[56:59], v[228:231]\n\t"
    "s_lshl_b32 s63, %1, 10\n\t"
    "s_add_u32 s63, s63, 0x1000\n\t"
    "v_mfma_f32_16x16x32_bf16 v[236:239], a[92:95], a[56:59], v[236:239]\n\t"
    "buffer_load_dword a255, %12, s[68:71], s63 offen\n\t"
    "v_mfma_f32_16x16x32_bf16 v[244:247], a[64:67], a[60:63], v[244:247]\n\t"
    "v_mfma_f32_16x16x32_bf16 v[252:255], a[68:71], a[60:63], v[252:255]\n\t"
    "v_mfma_f32_16x16x32_bf16 a[176:179], a[72:75], a[60:63], a[176:179]\n\t"
    "v_mfma_f32_16x16x32_bf16 a[180:183], a[76:79], a[60:63], a[180:183]\n\t"
    "v_mfma_f32_16x16x32_bf16 a[184:187], a[80:83], a[60:63], a[184:187]\n\t"
    "v_add_u32 v17, 0x18000, v46\n\t"
    "v_mfma_f32_16x16x32_bf16 a[188:191], a[84:87], a[60:63], a[188:191]\n\t"
    "v_mfma_f32_16x16x32_bf16 a[192:195], a[88:91], a[60:63], a[192:195]\n\t"
    "v_mfma_f32_16x16x32_bf16 a[196:199], a[92:95], a[60:63], a[196:199]\n\t"
    "s_waitcnt lgkmcnt(8)\n\t"
    "v_mfma_f32_16x16x32_bf16 v[52:55], a[16:19], a[0:3], v[52:55]\n\t"
    "ds_read_b128 a[48:51], v17 offset:0\n\t"
    "s_add_u32 s57, s62, 0x10000\n\t"
    "s_mov_b32 s52, 0x200\n\t"
    "s_add_u32 m0, s57, 0\n\t"
    "v_mfma_f32_16x16x32_bf16 v[60:63], a[20:23], a[0:3], v[60:63]\n\t"
    "ds_read_b64_tr_b16 a[64:65], v18 offset:4096\n\t"
    "buffer_load_dwordx4 v22, s[40:43], s52 offen offset:0 lds\n\t"
    "v_mfma_f32_16x16x32_bf16 v[68:71], a[24:27], a[0:3], v[68:71]\n\t"
    "ds_read_b64_tr_b16 a[66:67], v18 offset:6144\n\t"
    "v_mfma_f32_16x16x32_bf16 v[76:79], a[28:31], a[0:3], v[76:79]\n\t"
    "ds_read_b128 a[52:55], v17 offset:2048\n\t"
    "s_waitcnt lgkmcnt(4)\n\t"
    "v_mfma_f32_16x16x32_bf16 v[84:87], a[32:35], a[0:3], v[84:87]\n\t"
    "ds_read_b64_tr_b16 a[68:69], v19 offset:4096\n\t"
    "buffer_load_dwordx4 v23, s[40:43], s52 offen offset:1024 lds\n\t"
    "v_mfma_f32_16x16x32_bf16 v[92:95], a[36:39], a[0:3], v[92:95]\n\t"
    "ds_read_b64_tr_b16 a[70:71], v19 offset:6144\n\t"
    "v_mfma_f32_16x16x32_bf16 v[100:103], a[40:43], a[0:3], v[100:103]\n\t"
    "ds_read_b128 a[56:59], v17 offset:4096\n\t"
    "v_mfma_f32_16x16x32_bf16 v[108:111], a[44:47], a[0:3], v[108:111]\n\t"
    "ds_read_b64_tr_b16 a[72:73], v20 offset:4096\n\t"
    "v_mfma_f32_16x16x32_bf16 v[116:119], a[16:19], a[4:7], v[116:119]\n\t"
    "ds_read_b64_tr_b16 a[74:75], v20 offset:6144\n\t"
    "buffer_load_dwordx4 v24, s[40:43], s52 offen offset:2048 lds\n\t"
    "v_mfma_f32_16x16x32_bf16 v[124:127], a[20:23], a[4:7], v[124:127]\n\t"
    "ds_read_b128 a[60:63], v17 offset:6144\n\t"
    "v_mfma_f32_16x16x32_bf16 v[132:135], a[24:27], a[4:7], v[132:135]\n\t"
    "v_mfma_f32_16x16x32_bf16 v[140:143], a[28:31], a[4:7], v[140:143]\n\t"
    "ds_read_b64_tr_b16 a[76:77], v21 offset:4096\n\t"
    "buffer_load_dwordx4 v25, s[40:43], s52 offen offset:3072 lds\n\t"
    "v_mfma_f32_16x16x32_bf16 v[148:151], a[32:35], a[4:7], v[148:151]\n\t"
    "ds_read_b64_tr_b16 a[78:79], v21 offset:6144\n\t"
    "v_mfma_f32_16x16x32_bf16 v[156:159], a[36:39], a[4:7], v[156:159]\n\t"
    "ds_read_b64_tr_b16 a[80:81], v18 offset:12288\n\t"
    "v_mfma_f32_16x16x32_bf16 v[164:167], a[40:43], a[4:7], v[164:167]\n\t"
    "ds_read_b64_tr_b16 a[82:83], v18 offset:14336\n\t"
    "s_add_u32 m0, s57, 0x1000\n\t"
    "v_mfma_f32_16x16x32_bf16 v[172:175], a[44:47], a[4:7], v[172:175]\n\t"
    "ds_read_b64_tr_b16 a[84:85], v19 offset:12288\n\t"
    "buffer_load_dwordx4 v26, s[40:43], s52 offen offset:0 lds\n\t"
    "v_mfma_f32_16x16x32_bf16 v[180:183], a[16:19], a[8:11], v[180:183]\n\t"
    "ds_read_b64_tr_b16 a[86:87], v19 offset:14336\n\t"
    "v_mfma_f32_16x16x32_bf16 v[188:191], a[20:23], a[8:11], v[188:191]\n\t"
    "ds_read_b64_tr_b16 a[88:89], v20 offset:12288\n\t"
    "v_mfma_f32_16x16x32_bf16 v[196:199], a[24:27], a[8:11], v[196:199]\n\t"
    "ds_read_b64_tr_b16 a[90:91], v20 offset:14336\n\t"
    "v_mfma_f32_16x16x32_bf16 v[204:207], a[28:31], a[8:11], v[204:207]\n\t"
    "ds_read_b64_tr_b16 a[92:93], v21 offset:12288\n\t"
    "buffer_load_dwordx4 v27, s[40:43], s52 offen offset:1024 lds\n\t"
    "v_mfma_f32_16x16x32_bf16 v[212:215], a[32:35], a[8:11], v[212:215]\n\t"
    "ds_read_b64_tr_b16 a[94:95], v21 offset:14336\n\t"
    "v_mfma_f32_16x16x32_bf16 v[220:223], a[36:39], a[8:11], v[220:223]\n\t"
    "v_mfma_f32_16x16x32_bf16 v[228:231], a[40:43], a[8:11], v[228:231]\n\t"
    "buffer_load_dwordx4 v28, s[40:43], s52 offen offset:2048 lds\n\t"
    "v_mfma_f32_16x16x32_bf16 v[236:239], a[44:47], a[8:11], v[236:239]\n\t"
    "v_mfma_f32_16x16x32_bf16 v[244:247], a[16:19], a[12:15], v[244:247]\n\t"
    "v_add_u32 v16, 0x20000, v45\n\t"
    "v_mfma_f32_16x16x32_bf16 v[252:255], a[20:23], a[12:15], v[252:255]\n\t"
    "v_mfma_f32_16x16x32_bf16 a[176:179], a[24:27], a[12:15], a[176:179]\n\t"
    "buffer_load_dwordx4 v29, s[40:43], s52 offen offset:3072 lds\n\t"
    "v_mfma_f32_16x16x32_bf16 a[180:183], a[28:31], a[12:15], a[180:183]\n\t"
    "v_mfma_f32_16x16x32_bf16 a[184:187], a[32:35], a[12:15], a[184:187]\n\t"
    "v_mfma_f32_16x16x32_bf16 a[188:191], a[36:39], a[12:15], a[188:191]\n\t"
    "v_mfma_f32_16x16x32_bf16 a[192:195], a[40:43], a[12:15], a[192:195]\n\t"
    "v_mfma_f32_16x16x32_bf16 a[196:199], a[44:47], a[12:15], a[196:199]\n\t"
    "s_waitcnt vmcnt(10) lgkmcnt(0)\n\t"
    "s_barrier\n\t"
    "v_mfma_f32_16x16x32_bf16 v[52:55], a[64:67], a[48:51], v[52:55]\n\t"
    "ds_read_b128 a[0:3], v16 offset:0\n\t"
    "s_add_u32 s58, s64, 0\n\t"
    "s_mov_b32 s53, 0x200000\n\t"
    "s_add_u32 m0, s58, 0\n\t"
    "v_mfma_f32_16x16x32_bf16 v[60:63], a[68:71], a[48:51], v[60:63]\n\t"
    "ds_read_b64_tr_b16 a[16:17], v18 offset:16384\n\t"
    "buffer_load_dwordx4 v30, s[44:47], s53 offen offset:0 lds\n\t"
    "v_mfma_f32_16x16x32_bf16 v[68:71], a[72:75], a[48:51], v[68:71]\n\t"
    "ds_read_b64_tr_b16 a[18:19], v18 offset:18432\n\t"
    "v_mfma_f32_16x16x32_bf16 v[76:79], a[76:79], a[48:51], v[76:79]\n\t"
    "ds_read_b128 a[4:7], v16 offset:2048\n\t"
    "v_mfma_f32_16x16x32_bf16 v[84:87], a[80:83], a[48:51], v[84:87]\n\t"
    "ds_read_b64_tr_b16 a[20:21], v19 offset:16384\n\t"
    "v_mfma_f32_16x16x32_bf16 v[92:95], a[84:87], a[48:51], v[92:95]\n\t"
    "ds_read_b64_tr_b16 a[22:23], v19 offset:18432\n\t"
    "buffer_load_dwordx4 v31, s[44:47], s53 offen offset:1024 lds\n\t"
    "v_mfma_f32_16x16x32_bf16 v[100:103], a[88:91], a[48:51], v[100:103]\n\t"
    "ds_read_b128 a[8:11], v16 offset:4096\n\t"
    "v_mfma_f32_16x16x32_bf16 v[108:111], a[92:95], a[48:51], v[108:111]\n\t"
    "ds_read_b64_tr_b16 a[24:25], v20 offset:16384\n\t"
    "v_mfma_f32_16x16x32_bf16 v[116:119], a[64:67], a[52:55], v[116:119]\n\t"
    "ds_read_b64_tr_b16 a[26:27], v20 offset:18432\n\t"
    "v_mfma_f32_16x16x32_bf16 v[124:127], a[68:71], a[52:55], v[124:127]\n\t"
    "ds_read_b128 a[12:15], v16 offset:6144\n\t"
    "v_mfma_f32_16x16x32_bf16 v[132:135], a[72:75], a[52:55], v[132:135]\n\t"
    "buffer_load_dwordx4 v32, s[44:47], s53 offen offset:2048 lds\n\t"
    "v_mfma_f32_16x16x32_bf16 v[140:143], a[76:79], a[52:55], v[140:143]\n\t"
    "ds_read_b64_tr_b16 a[28:29], v21 offset:16384\n\t"
    "v_mfma_f32_16x16x32_bf16 v[148:151], a[80:83], a[52:55], v[148:151]\n\t"
    "ds_read_b64_tr_b16 a[30:31], v21 offset:18432\n\t"
    "v_mfma_f32_16x16x32_bf16 v[156:159], a[84:87], a[52:55], v[156:159]\n\t"
    "ds_read_b64_tr_b16 a[32:33], v18 offset:24576\n\t"
    "v_mfma_f32_16x16x32_bf16 v[164:167], a[88:91], a[52:55], v[164:167]\n\t"
    "ds_read_b64_tr_b16 a[34:35], v18 offset:26624\n\t"
    "buffer_load_dwordx4 v33, s[44:47], s53 offen offset:3072 lds\n\t"
    "v_mfma_f32_16x16x32_bf16 v[172:175], a[92:95], a[52:55], v[172:175]\n\t"
    "ds_read_b64_tr_b16 a[36:37], v19 offset:24576\n\t"
    "v_mfma_f32_16x16x32_bf16 v[180:183], a[64:67], a[56:59], v[180:183]\n\t"
    "ds_read_b64_tr_b16 a[38:39], v19 offset:26624\n\t"
    "v_mfma_f32_16x16x32_bf16 v[188:191], a[68:71], a[56:59], v[188:191]\n\t"
    "ds_read_b64_tr_b16 a[40:41], v20 offset:24576\n\t"
    "v_mfma_f32_16x16x32_bf16 v[196:199], a[72:75], a[56:59], v[196:199]\n\t"
    "ds_read_b64_tr_b16 a[42:43], v20 offset:26624\n\t"
    "s_cmp_eq_u32 s59, 0\n\t"
    "s_mov_b32 s61, 0x1000\n\t"
    "s_cselect_b32 s61, s61, 0x400\n\t"
    "v_mfma_f32_16x16x32_bf16 v[204:207], a[76:79], a[56:59], v[204:207]\n\t"
    "ds_read_b64_tr_b16 a[44:45], v21 offset:24576\n\t"
    "buffer_store_dwordx4 v[80:83], %12, s[48:51], s61 offen offset:0 sc1\n\t"
    "v_mfma_f32_16x16x32_bf16 v[212:215], a[80:83], a[56:59], v[212:215]\n\t"
    "ds_read_b64_tr_b16 a[46:47], v21 offset:26624\n\t"
    "v_mfma_f32_16x16x32_bf16 v[220:223], a[84:87], a[56:59], v[220:223]\n\t"
    "v_mfma_f32_16x16x32_bf16 v[228:231], a[88:91], a[56:59], v[228:231]\n\t"
    "s_lshl_b32 s63, %1, 10\n\t"
    "s_add_u32 s63, s63, 0x2000\n\t"
    "v_mfma_f32_16x16x32_bf16 v[236:239], a[92:95], a[56:59], v[236:239]\n\t"
    "buffer_load_dword a255, %12, s[68:71], s63 offen\n\t"
    "v_mfma_f32_16x16x32_bf16 v[244:247], a[64:67], a[60:63], v[244:247]\n\t"
    "v_mfma_f32_16x16x32_bf16 v[252:255], a[68:71], a[60:63], v[252:255]\n\t"
    "v_mfma_f32_16x16x32_bf16 a[176:179], a[72:75], a[60:63], a[176:179]\n\t"
    "v_mfma_f32_16x16x32_bf16 a[180:183], a[76:79], a[60:63], a[180:183]\n\t"
    "v_mfma_f32_16x16x32_bf16 a[184:187], a[80:83], a[60:63], a[184:187]\n\t"
    "v_add_u32 v17, 0x20000, v46\n\t"
    "v_mfma_f32_16x16x32_bf16 a[188:191], a[84:87], a[60:63], a[188:191]\n\t"
    "v_mfma_f32_16x16x32_bf16 a[192:195], a[88:91], a[60:63], a[192:195]\n\t"
    "v_mfma_f32_16x16x32_bf16 a[196:199], a[92:95], a[60:63], a[196:199]\n\t"
    "s_waitcnt lgkmcnt(8)\n\t"
    "v_mfma_f32_16x16x32_bf16 v[52:55], a[16:19], a[0:3], v[52:55]\n\t"
    "ds_read_b128 a[48:51], v17 offset:0\n\t"
    "s_add_u32 s57, s62, 0x18000\n\t"
    "s_mov_b32 s52, 0x280\n\t"
    "s_add_u32 m0, s57, 0\n\t"
    "v_mfma_f32_16x16x32_bf16 v[60:63], a[20:23], a[0:3], v[60:63]\n\t"
    "ds_read_b64_tr_b16 a[64:65], v18 offset:20480\n\t"
    "buffer_load_dwordx4 v22, s[40:43], s52 offen offset:0 lds\n\t"
    "v_mfma_f32_16x16x32_bf16 v[68:71], a[24:27], a[0:3], v[68:71]\n\t"
    "ds_read_b64_tr_b16 a[66:67], v18 offset:22528\n\t"
    "v_mfma_f32_16x16x32_bf16 v[76:79], a[28:31], a[0:3], v[76:79]\n\t"
    "ds_read_b128 a[52:55], v17 offset:2048\n\t"
    "s_waitcnt lgkmcnt(4)\n\t"
    "v_mfma_f32_16x16x32_bf16 v[84:87], a[32:35], a[0:3], v[84:87]\n\t"
    "ds_read_b64_tr_b16 a[68:69], v19 offset:20480\n\t"
    "buffer_load_dwordx4 v23, s[40:43], s52 offen offset:1024 lds\n\t"
    "v_mfma_f32_16x16x32_bf16 v[92:95], a[36:39], a[0:3], v[92:95]\n\t"
    "ds_read_b64_tr_b16 a[70:71], v19 offset:22528\n\t"
    "v_mfma_f32_16x16x32_bf16 v[100:103], a[40:43], a[0:3], v[100:103]\n\t"
    "ds_read_b128 a[56:59], v17 offset:4096\n\t"
    "v_mfma_f32_16x16x32_bf16 v[108:111], a[44:47], a[0:3], v[108:111]\n\t"
    "ds_read_b64_tr_b16 a[72:73], v20 offset:20480\n\t"
    "v_mfma_f32_16x16x32_bf16 v[116:119], a[16:19], a[4:7], v[116:119]\n\t"
    "ds_read_b64_tr_b16 a[74:75], v20 offset:22528\n\t"
    "buffer_load_dwordx4 v24, s[40:43], s52 offen offset:2048 lds\n\t"
    "v_mfma_f32_16x16x32_bf16 v[124:127], a[20:23], a[4:7], v[124:127]\n\t"
    "ds_read_b128 a[60:63], v17 offset:6144\n\t"
    "v_mfma_f32_16x16x32_bf16 v[132:135], a[24:27], a[4:7], v[132:135]\n\t"
    "v_mfma_f32_16x16x32_bf16 v[140:143], a[28:31], a[4:7], v[140:143]\n\t"
    "ds_read_b64_tr_b16 a[76:77], v21 offset:20480\n\t"
    "buffer_load_dwordx4 v25, s[40:43], s52 offen offset:3072 lds\n\t"
    "v_mfma_f32_16x16x32_bf16 v[148:151], a[32:35], a[4:7], v[148:151]\n\t"
    "ds_read_b64_tr_b16 a[78:79], v21 offset:22528\n\t"
    "v_mfma_f32_16x16x32_bf16 v[156:159], a[36:39], a[4:7], v[156:159]\n\t"
    "ds_read_b64_tr_b16 a[80:81], v18 offset:28672\n\t"
    "v_mfma_f32_16x16x32_bf16 v[164:167], a[40:43], a[4:7], v[164:167]\n\t"
    "ds_read_b64_tr_b16 a[82:83], v18 offset:30720\n\t"
    "s_add_u32 m0, s57, 0x1000\n\t"
    "v_mfma_f32_16x16x32_bf16 v[172:175], a[44:47], a[4:7], v[172:175]\n\t"
    "ds_read_b64_tr_b16 a[84:85], v19 offset:28672\n\t"
    "buffer_load_dwordx4 v26, s[40:43], s52 offen offset:0 lds\n\t"
    "v_mfma_f32_16x16x32_bf16 v[180:183], a[16:19], a[8:11], v[180:183]\n\t"
    "ds_read_b64_tr_b16 a[86:87], v19 offset:30720\n\t"
    "v_mfma_f32_16x16x32_bf16 v[188:191], a[20:23], a[8:11], v[188:191]\n\t"
    "ds_read_b64_tr_b16 a[88:89], v20 offset:28672\n\t"
    "v_mfma_f32_16x16x32_bf16 v[196:199], a[24:27], a[8:11], v[196:199]\n\t"
    "ds_read_b64_tr_b16 a[90:91], v20 offset:30720\n\t"
    "v_mfma_f32_16x16x32_bf16 v[204:207], a[28:31], a[8:11], v[204:207]\n\t"
    "ds_read_b64_tr_b16 a[92:93], v21 offset:28672\n\t"
    "buffer_load_dwordx4 v27, s[40:43], s52 offen offset:1024 lds\n\t"
    "v_mfma_f32_16x16x32_bf16 v[212:215], a[32:35], a[8:11], v[212:215]\n\t"
    "ds_read_b64_tr_b16 a[94:95], v21 offset:30720\n\t"
    "v_mfma_f32_16x16x32_bf16 v[220:223], a[36:39], a[8:11], v[220:223]\n\t"
    "v_mfma_f32_16x16x32_bf16 v[228:231], a[40:43], a[8:11], v[228:231]\n\t"
    "buffer_load_dwordx4 v28, s[40:43], s52 offen offset:2048 lds\n\t"
    "v_mfma_f32_16x16x32_bf16 v[236:239], a[44:47], a[8:11], v[236:239]\n\t"
    "v_mfma_f32_16x16x32_bf16 v[244:247], a[16:19], a[12:15], v[244:247]\n\t"
    "v_add_u32 v16, 0x10000, v45\n\t"
    "v_mfma_f32_16x16x32_bf16 v[252:255], a[20:23], a[12:15], v[252:255]\n\t"
    "v_mfma_f32_16x16x32_bf16 a[176:179], a[24:27], a[12:15], a[176:179]\n\t"
    "buffer_load_dwordx4 v29, s[40:43], s52 offen offset:3072 lds\n\t"
    "v_mfma_f32_16x16x32_bf16 a[180:183], a[28:31], a[12:15], a[180:183]\n\t"
    "v_mfma_f32_16x16x32_bf16 a[184:187], a[32:35], a[12:15], a[184:187]\n\t"
    "v_mfma_f32_16x16x32_bf16 a[188:191], a[36:39], a[12:15], a[188:191]\n\t"
    "v_mfma_f32_16x16x32_bf16 a[192:195], a[40:43], a[12:15], a[192:195]\n\t"
    "v_mfma_f32_16x16x32_bf16 a[196:199], a[44:47], a[12:15], a[196:199]\n\t"
    "s_waitcnt vmcnt(10) lgkmcnt(0)\n\t"
    "s_barrier\n\t"
    "v_mfma_f32_16x16x32_bf16 v[52:55], a[64:67], a[48:51], v[52:55]\n\t"
    "ds_read_b128 a[0:3], v16 offset:0\n\t"
    "s_add_u32 s58, s64, 0x4000\n\t"
    "s_mov_b32 s53, 0x280000\n\t"
    "s_add_u32 m0, s58, 0\n\t"
    "v_mfma_f32_16x16x32_bf16 v[60:63], a[68:71], a[48:51], v[60:63]\n\t"
    "ds_read_b64_tr_b16 a[16:17], v18 offset:0\n\t"
    "buffer_load_dwordx4 v30, s[44:47], s53 offen offset:0 lds\n\t"
    "v_mfma_f32_16x16x32_bf16 v[68:71], a[72:75], a[48:51], v[68:71]\n\t"
    "ds_read_b64_tr_b16 a[18:19], v18 offset:2048\n\t"
    "v_mfma_f32_16x16x32_bf16 v[76:79], a[76:79], a[48:51], v[76:79]\n\t"
    "ds_read_b128 a[4:7], v16 offset:2048\n\t"
    "v_mfma_f32_16x16x32_bf16 v[84:87], a[80:83], a[48:51], v[84:87]\n\t"
    "ds_read_b64_tr_b16 a[20:21], v19 offset:0\n\t"
    "v_mfma_f32_16x16x32_bf16 v[92:95], a[84:87], a[48:51], v[92:95]\n\t"
    "ds_read_b64_tr_b16 a[22:23], v19 offset:2048\n\t"
    "buffer_load_dwordx4 v31, s[44:47], s53 offen offset:1024 lds\n\t"
    "v_mfma_f32_16x16x32_bf16 v[100:103], a[88:91], a[48:51], v[100:103]\n\t"
    "ds_read_b128 a[8:11], v16 offset:4096\n\t"
    "v_mfma_f32_16x16x32_bf16 v[108:111], a[92:95], a[48:51], v[108:111]\n\t"
    "ds_read_b64_tr_b16 a[24:25], v20 offset:0\n\t"
    "v_mfma_f32_16x16x32_bf16 v[116:119], a[64:67], a[52:55], v[116:119]\n\t"
    "ds_read_b64_tr_b16 a[26:27], v20 offset:2048\n\t"
    "v_mfma_f32_16x16x32_bf16 v[124:127], a[68:71], a[52:55], v[124:127]\n\t"
    "ds_read_b128 a[12:15], v16 offset:6144\n\t"
    "v_mfma_f32_16x16x32_bf16 v[132:135], a[72:75], a[52:55], v[132:135]\n\t"
    "buffer_load_dwordx4 v32, s[44:47], s53 offen offset:2048 lds\n\t"
    "v_mfma_f32_16x16x32_bf16 v[140:143], a[76:79], a[52:55], v[140:143]\n\t"
    "ds_read_b64_tr_b16 a[28:29], v21 offset:0\n\t"
    "v_mfma_f32_16x16x32_bf16 v[148:151], a[80:83], a[52:55], v[148:151]\n\t"
    "ds_read_b64_tr_b16 a[30:31], v21 offset:2048\n\t"
    "v_mfma_f32_16x16x32_bf16 v[156:159], a[84:87], a[52:55], v[156:159]\n\t"
    "ds_read_b64_tr_b16 a[32:33], v18 offset:8192\n\t"
    "v_mfma_f32_16x16x32_bf16 v[164:167], a[88:91], a[52:55], v[164:167]\n\t"
    "ds_read_b64_tr_b16 a[34:35], v18 offset:10240\n\t"
    "buffer_load_dwordx4 v33, s[44:47], s53 offen offset:3072 lds\n\t"
    "v_mfma_f32_16x16x32_bf16 v[172:175], a[92:95], a[52:55], v[172:175]\n\t"
    "ds_read_b64_tr_b16 a[36:37], v19 offset:8192\n\t"
    "v_mfma_f32_16x16x32_bf16 v[180:183], a[64:67], a[56:59], v[180:183]\n\t"
    "ds_read_b64_tr_b16 a[38:39], v19 offset:10240\n\t"
    "v_mfma_f32_16x16x32_bf16 v[188:191], a[68:71], a[56:59], v[188:191]\n\t"
    "ds_read_b64_tr_b16 a[40:41], v20 offset:8192\n\t"
    "v_mfma_f32_16x16x32_bf16 v[196:199], a[72:75], a[56:59], v[196:199]\n\t"
    "ds_read_b64_tr_b16 a[42:43], v20 offset:10240\n\t"
    "s_cmp_eq_u32 s59, 0\n\t"
    "s_mov_b32 s61, 0x1400\n\t"
    "s_cselect_b32 s61, s61, 0x2400\n\t"
    "v_mfma_f32_16x16x32_bf16 v[204:207], a[76:79], a[56:59], v[204:207]\n\t"
    "ds_read_b64_tr_b16 a[44:45], v21 offset:8192\n\t"
    "buffer_store_dwordx4 v[88:91], %12, s[48:51], s61 offen offset:0 sc1\n\t"
    "v_mfma_f32_16x16x32_bf16 v[212:215], a[80:83], a[56:59], v[212:215]\n\t"
    "ds_read_b64_tr_b16 a[46:47], v21 offset:10240\n\t"
    "v_mfma_f32_16x16x32_bf16 v[220:223], a[84:87], a[56:59], v[220:223]\n\t"
    "v_mfma_f32_16x16x32_bf16 v[228:231], a[88:91], a[56:59], v[228:231]\n\t"
    "s_lshl_b32 s63, %1, 10\n\t"
    "s_add_u32 s63, s63, 0x3000\n\t"
    "v_mfma_f32_16x16x32_bf16 v[236:239], a[92:95], a[56:59], v[236:239]\n\t"
    "buffer_load_dword a255, %12, s[68:71], s63 offen\n\t"
    "v_mfma_f32_16x16x32_bf16 v[244:247], a[64:67], a[60:63], v[244:247]\n\t"
    "v_mfma_f32_16x16x32_bf16 v[252:255], a[68:71], a[60:63], v[252:255]\n\t"
    "v_mfma_f32_16x16x32_bf16 a[176:179], a[72:75], a[60:63], a[176:179]\n\t"
    "v_mfma_f32_16x16x32_bf16 a[180:183], a[76:79], a[60:63], a[180:183]\n\t"
    "v_mfma_f32_16x16x32_bf16 a[184:187], a[80:83], a[60:63], a[184:187]\n\t"
    "v_add_u32 v17, 0x10000, v46\n\t"
    "v_mfma_f32_16x16x32_bf16 a[188:191], a[84:87], a[60:63], a[188:191]\n\t"
    "v_mfma_f32_16x16x32_bf16 a[192:195], a[88:91], a[60:63], a[192:195]\n\t"
    "v_mfma_f32_16x16x32_bf16 a[196:199], a[92:95], a[60:63], a[196:199]\n\t"
    "s_waitcnt lgkmcnt(8)\n\t"
    "v_mfma_f32_16x16x32_bf16 v[52:55], a[16:19], a[0:3], v[52:55]\n\t"
    "ds_read_b128 a[48:51], v17 offset:0\n\t"
    "s_add_u32 s57, s62, 0x20000\n\t"
    "s_mov_b32 s52, 0x300\n\t"
    "s_add_u32 m0, s57, 0\n\t"
    "v_mfma_f32_16x16x32_bf16 v[60:63], a[20:23], a[0:3], v[60:63]\n\t"
    "ds_read_b64_tr_b16 a[64:65], v18 offset:4096\n\t"
    "buffer_load_dwordx4 v22, s[40:43], s52 offen offset:0 lds\n\t"
    "v_mfma_f32_16x16x32_bf16 v[68:71], a[24:27], a[0:3], v[68:71]\n\t"
    "ds_read_b64_tr_b16 a[66:67], v18 offset:6144\n\t"
    "v_mfma_f32_16x16x32_bf16 v[76:79], a[28:31], a[0:3], v[76:79]\n\t"
    "ds_read_b128 a[52:55], v17 offset:2048\n\t"
    "s_waitcnt lgkmcnt(4)\n\t"
    "v_mfma_f32_16x16x32_bf16 v[84:87], a[32:35], a[0:3], v[84:87]\n\t"
    "ds_read_b64_tr_b16 a[68:69], v19 offset:4096\n\t"
    "buffer_load_dwordx4 v23, s[40:43], s52 offen offset:1024 lds\n\t"
    "v_mfma_f32_16x16x32_bf16 v[92:95], a[36:39], a[0:3], v[92:95]\n\t"
    "ds_read_b64_tr_b16 a[70:71], v19 offset:6144\n\t"
    "v_mfma_f32_16x16x32_bf16 v[100:103], a[40:43], a[0:3], v[100:103]\n\t"
    "ds_read_b128 a[56:59], v17 offset:4096\n\t"
    "v_mfma_f32_16x16x32_bf16 v[108:111], a[44:47], a[0:3], v[108:111]\n\t"
    "ds_read_b64_tr_b16 a[72:73], v20 offset:4096\n\t"
    "v_mfma_f32_16x16x32_bf16 v[116:119], a[16:19], a[4:7], v[116:119]\n\t"
    "ds_read_b64_tr_b16 a[74:75], v20 offset:6144\n\t"
    "buffer_load_dwordx4 v24, s[40:43], s52 offen offset:2048 lds\n\t"
    "v_mfma_f32_16x16x32_bf16 v[124:127], a[20:23], a[4:7], v[124:127]\n\t"
    "ds_read_b128 a[60:63], v17 offset:6144\n\t"
    "v_mfma_f32_16x16x32_bf16 v[132:135], a[24:27], a[4:7], v[132:135]\n\t"
    "v_mfma_f32_16x16x32_bf16 v[140:143], a[28:31], a[4:7], v[140:143]\n\t"
    "ds_read_b64_tr_b16 a[76:77], v21 offset:4096\n\t"
    "buffer_load_dwordx4 v25, s[40:43], s52 offen offset:3072 lds\n\t"
    "v_mfma_f32_16x16x32_bf16 v[148:151], a[32:35], a[4:7], v[148:151]\n\t"
    "ds_read_b64_tr_b16 a[78:79], v21 offset:6144\n\t"
    "v_mfma_f32_16x16x32_bf16 v[156:159], a[36:39], a[4:7], v[156:159]\n\t"
    "ds_read_b64_tr_b16 a[80:81], v18 offset:12288\n\t"
    "v_mfma_f32_16x16x32_bf16 v[164:167], a[40:43], a[4:7], v[164:167]\n\t"
    "ds_read_b64_tr_b16 a[82:83], v18 offset:14336\n\t"
    "s_add_u32 m0, s57, 0x1000\n\t"
    "v_mfma_f32_16x16x32_bf16 v[172:175], a[44:47], a[4:7], v[172:175]\n\t"
    "ds_read_b64_tr_b16 a[84:85], v19 offset:12288\n\t"
    "buffer_load_dwordx4 v26, s[40:43], s52 offen offset:0 lds\n\t"
    "v_mfma_f32_16x16x32_bf16 v[180:183], a[16:19], a[8:11], v[180:183]\n\t"
    "ds_read_b64_tr_b16 a[86:87], v19 offset:14336\n\t"
    "v_mfma_f32_16x16x32_bf16 v[188:191], a[20:23], a[8:11], v[188:191]\n\t"
    "ds_read_b64_tr_b16 a[88:89], v20 offset:12288\n\t"
    "v_mfma_f32_16x16x32_bf16 v[196:199], a[24:27], a[8:11], v[196:199]\n\t"
    "ds_read_b64_tr_b16 a[90:91], v20 offset:14336\n\t"
    "v_mfma_f32_16x16x32_bf16 v[204:207], a[28:31], a[8:11], v[204:207]\n\t"
    "ds_read_b64_tr_b16 a[92:93], v21 offset:12288\n\t"
    "buffer_load_dwordx4 v27, s[40:43], s52 offen offset:1024 lds\n\t"
    "v_mfma_f32_16x16x32_bf16 v[212:215], a[32:35], a[8:11], v[212:215]\n\t"
    "ds_read_b64_tr_b16 a[94:95], v21 offset:14336\n\t"
    "v_mfma_f32_16x16x32_bf16 v[220:223], a[36:39], a[8:11], v[220:223]\n\t"
    "v_mfma_f32_16x16x32_bf16 v[228:231], a[40:43], a[8:11], v[228:231]\n\t"
    "buffer_load_dwordx4 v28, s[40:43], s52 offen offset:2048 lds\n\t"
    "v_mfma_f32_16x16x32_bf16 v[236:239], a[44:47], a[8:11], v[236:239]\n\t"
    "v_mfma_f32_16x16x32_bf16 v[244:247], a[16:19], a[12:15], v[244:247]\n\t"
    "v_add_u32 v16, 0x18000, v45\n\t"
    "v_mfma_f32_16x16x32_bf16 v[252:255], a[20:23], a[12:15], v[252:255]\n\t"
    "v_mfma_f32_16x16x32_bf16 a[176:179], a[24:27], a[12:15], a[176:179]\n\t"
    "buffer_load_dwordx4 v29, s[40:43], s52 offen offset:3072 lds\n\t"
    "v_mfma_f32_16x16x32_bf16 a[180:183], a[28:31], a[12:15], a[180:183]\n\t"
    "v_mfma_f32_16x16x32_bf16 a[184:187], a[32:35], a[12:15], a[184:187]\n\t"
    "v_mfma_f32_16x16x32_bf16 a[188:191], a[36:39], a[12:15], a[188:191]\n\t"
    "v_mfma_f32_16x16x32_bf16 a[192:195], a[40:43], a[12:15], a[192:195]\n\t"
    "v_mfma_f32_16x16x32_bf16 a[196:199], a[44:47], a[12:15], a[196:199]\n\t"
    "s_waitcnt vmcnt(10) lgkmcnt(0)\n\t"
    "s_barrier\n\t"
    "v_mfma_f32_16x16x32_bf16 v[52:55], a[64:67], a[48:51], v[52:55]\n\t"
    "ds_read_b128 a[0:3], v16 offset:0\n\t"
    "s_add_u32 s58, s64, 0\n\t"
    "s_mov_b32 s53, 0x300000\n\t"
    "s_add_u32 m0, s58, 0\n\t"
    "v_mfma_f32_16x16x32_bf16 v[60:63], a[68:71], a[48:51], v[60:63]\n\t"
    "ds_read_b64_tr_b16 a[16:17], v18 offset:16384\n\t"
    "buffer_load_dwordx4 v30, s[44:47], s53 offen offset:0 lds\n\t"
    "v_mfma_f32_16x16x32_bf16 v[68:71], a[72:75], a[48:51], v[68:71]\n\t"
    "ds_read_b64_tr_b16 a[18:19], v18 offset:18432\n\t"
    "v_mfma_f32_16x16x32_bf16 v[76:79], a[76:79], a[48:51], v[76:79]\n\t"
    "ds_read_b128 a[4:7], v16 offset:2048\n\t"
    "v_mfma_f32_16x16x32_bf16 v[84:87], a[80:83], a[48:51], v[84:87]\n\t"
    "ds_read_b64_tr_b16 a[20:21], v19 offset:16384\n\t"
    "v_mfma_f32_16x16x32_bf16 v[92:95], a[84:87], a[48:51], v[92:95]\n\t"
    "ds_read_b64_tr_b16 a[22:23], v19 offset:18432\n\t"
    "buffer_load_dwordx4 v31, s[44:47], s53 offen offset:1024 lds\n\t"
    "v_mfma_f32_16x16x32_bf16 v[100:103], a[88:91], a[48:51], v[100:103]\n\t"
    "ds_read_b128 a[8:11], v16 offset:4096\n\t"
    "v_mfma_f32_16x16x32_bf16 v[108:111], a[92:95], a[48:51], v[108:111]\n\t"
    "ds_read_b64_tr_b16 a[24:25], v20 offset:16384\n\t"
    "v_mfma_f32_16x16x32_bf16 v[116:119], a[64:67], a[52:55], v[116:119]\n\t"
    "ds_read_b64_tr_b16 a[26:27], v20 offset:18432\n\t"
    "v_mfma_f32_16x16x32_bf16 v[124:127], a[68:71], a[52:55], v[124:127]\n\t"
    "ds_read_b128 a[12:15], v16 offset:6144\n\t"
    "v_mfma_f32_16x16x32_bf16 v[132:135], a[72:75], a[52:55], v[132:135]\n\t"
    "buffer_load_dwordx4 v32, s[44:47], s53 offen offset:2048 lds\n\t"
    "v_mfma_f32_16x16x32_bf16 v[140:143], a[76:79], a[52:55], v[140:143]\n\t"
    "ds_read_b64_tr_b16 a[28:29], v21 offset:16384\n\t"
    "v_mfma_f32_16x16x32_bf16 v[148:151], a[80:83], a[52:55], v[148:151]\n\t"
    "ds_read_b64_tr_b16 a[30:31], v21 offset:18432\n\t"
    "v_mfma_f32_16x16x32_bf16 v[156:159], a[84:87], a[52:55], v[156:159]\n\t"
    "ds_read_b64_tr_b16 a[32:33], v18 offset:24576\n\t"
    "v_mfma_f32_16x16x32_bf16 v[164:167], a[88:91], a[52:55], v[164:167]\n\t"
    "ds_read_b64_tr_b16 a[34:35], v18 offset:26624\n\t"
    "buffer_load_dwordx4 v33, s[44:47], s53 offen offset:3072 lds\n\t"
    "v_mfma_f32_16x16x32_bf16 v[172:175], a[92:95], a[52:55], v[172:175]\n\t"
    "ds_read_b64_tr_b16 a[36:37], v19 offset:24576\n\t"
    "v_mfma_f32_16x16x32_bf16 v[180:183], a[64:67], a[56:59], v[180:183]\n\t"
    "ds_read_b64_tr_b16 a[38:39], v19 offset:26624\n\t"
    "v_mfma_f32_16x16x32_bf16 v[188:191], a[68:71], a[56:59], v[188:191]\n\t"
    "ds_read_b64_tr_b16 a[40:41], v20 offset:24576\n\t"
    "v_mfma_f32_16x16x32_bf16 v[196:199], a[72:75], a[56:59], v[196:199]\n\t"
    "ds_read_b64_tr_b16 a[42:43], v20 offset:26624\n\t"
    "s_cmp_eq_u32 s59, 0\n\t"
    "s_mov_b32 s61, 0x1c00\n\t"
    "s_cselect_b32 s61, s61, 0xa400\n\t"
    "v_mfma_f32_16x16x32_bf16 v[204:207], a[76:79], a[56:59], v[204:207]\n\t"
    "ds_read_b64_tr_b16 a[44:45], v21 offset:24576\n\t"
    "buffer_store_dwordx4 v[104:107], %12, s[48:51], s61 offen offset:0 sc1\n\t"
    "v_mfma_f32_16x16x32_bf16 v[212:215], a[80:83], a[56:59], v[212:215]\n\t"
    "ds_read_b64_tr_b16 a[46:47], v21 offset:26624\n\t"
    "v_mfma_f32_16x16x32_bf16 v[220:223], a[84:87], a[56:59], v[220:223]\n\t"
    "v_mfma_f32_16x16x32_bf16 v[228:231], a[88:91], a[56:59], v[228:231]\n\t"
    "s_lshl_b32 s63, %1, 10\n\t"
    "s_add_u32 s63, s63, 0x4000\n\t"
    "v_mfma_f32_16x16x32_bf16 v[236:239], a[92:95], a[56:59], v[236:239]\n\t"
    "buffer_load_dword a255, %12, s[68:71], s63 offen\n\t"
    "v_mfma_f32_16x16x32_bf16 v[244:247], a[64:67], a[60:63], v[244:247]\n\t"
    "v_mfma_f32_16x16x32_bf16 v[252:255], a[68:71], a[60:63], v[252:255]\n\t"
    "v_mfma_f32_16x16x32_bf16 a[176:179], a[72:75], a[60:63], a[176:179]\n\t"
    "v_mfma_f32_16x16x32_bf16 a[180:183], a[76:79], a[60:63], a[180:183]\n\t"
    "v_mfma_f32_16x16x32_bf16 a[184:187], a[80:83], a[60:63], a[184:187]\n\t"
    "v_add_u32 v17, 0x18000, v46\n\t"
    "v_mfma_f32_16x16x32_bf16 a[188:191], a[84:87], a[60:63], a[188:191]\n\t"
    "v_mfma_f32_16x16x32_bf16 a[192:195], a[88:91], a[60:63], a[192:195]\n\t"
    "v_mfma_f32_16x16x32_bf16 a[196:199], a[92:95], a[60:63], a[196:199]\n\t"
    "s_waitcnt lgkmcnt(8)\n\t"
    "v_mfma_f32_16x16x32_bf16 v[52:55], a[16:19], a[0:3], v[52:55]\n\t"
    "ds_read_b128 a[48:51], v17 offset:0\n\t"
    "s_add_u32 s57, s62, 0x10000\n\t"
    "s_mov_b32 s52, 0x380\n\t"
    "s_add_u32 m0, s57, 0\n\t"
    "v_mfma_f32_16x16x32_bf16 v[60:63], a[20:23], a[0:3], v[60:63]\n\t"
    "ds_read_b64_tr_b16 a[64:65], v18 offset:20480\n\t"
    "buffer_load_dwordx4 v22, s[40:43], s52 offen offset:0 lds\n\t"
    "v_mfma_f32_16x16x32_bf16 v[68:71], a[24:27], a[0:3], v[68:71]\n\t"
    "ds_read_b64_tr_b16 a[66:67], v18 offset:22528\n\t"
    "v_mfma_f32_16x16x32_bf16 v[76:79], a[28:31], a[0:3], v[76:79]\n\t"
    "ds_read_b128 a[52:55], v17 offset:2048\n\t"
    "s_waitcnt lgkmcnt(4)\n\t"
    "v_mfma_f32_16x16x32_bf16 v[84:87], a[32:35], a[0:3], v[84:87]\n\t"
    "ds_read_b64_tr_b16 a[68:69], v19 offset:20480\n\t"
    "buffer_load_dwordx4 v23, s[40:43], s52 offen offset:1024 lds\n\t"
    "v_mfma_f32_16x16x32_bf16 v[92:95], a[36:39], a[0:3], v[92:95]\n\t"
    "ds_read_b64_tr_b16 a[70:71], v19 offset:22528\n\t"
    "v_mfma_f32_16x16x32_bf16 v[100:103], a[40:43], a[0:3], v[100:103]\n\t"
    "ds_read_b128 a[56:59], v17 offset:4096\n\t"
    "v_mfma_f32_16x16x32_bf16 v[108:111], a[44:47], a[0:3], v[108:111]\n\t"
    "ds_read_b64_tr_b16 a[72:73], v20 offset:20480\n\t"
    "v_mfma_f32_16x16x32_bf16 v[116:119], a[16:19], a[4:7], v[116:119]\n\t"
    "ds_read_b64_tr_b16 a[74:75], v20 offset:22528\n\t"
    "buffer_load_dwordx4 v24, s[40:43], s52 offen offset:2048 lds\n\t"
    "v_mfma_f32_16x16x32_bf16 v[124:127], a[20:23], a[4:7], v[124:127]\n\t"
    "ds_read_b128 a[60:63], v17 offset:6144\n\t"
    "v_mfma_f32_16x16x32_bf16 v[132:135], a[24:27], a[4:7], v[132:135]\n\t"
    "v_mfma_f32_16x16x32_bf16 v[140:143], a[28:31], a[4:7], v[140:143]\n\t"
    "ds_read_b64_tr_b16 a[76:77], v21 offset:20480\n\t"
    "buffer_load_dwordx4 v25, s[40:43], s52 offen offset:3072 lds\n\t"
    "v_mfma_f32_16x16x32_bf16 v[148:151], a[32:35], a[4:7], v[148:151]\n\t"
    "ds_read_b64_tr_b16 a[78:79], v21 offset:22528\n\t"
    "v_mfma_f32_16x16x32_bf16 v[156:159], a[36:39], a[4:7], v[156:159]\n\t"
    "ds_read_b64_tr_b16 a[80:81], v18 offset:28672\n\t"
    "v_mfma_f32_16x16x32_bf16 v[164:167], a[40:43], a[4:7], v[164:167]\n\t"
    "ds_read_b64_tr_b16 a[82:83], v18 offset:30720\n\t"
    "s_add_u32 m0, s57, 0x1000\n\t"
    "v_mfma_f32_16x16x32_bf16 v[172:175], a[44:47], a[4:7], v[172:175]\n\t"
    "ds_read_b64_tr_b16 a[84:85], v19 offset:28672\n\t"
    "buffer_load_dwordx4 v26, s[40:43], s52 offen offset:0 lds\n\t"
    "v_mfma_f32_16x16x32_bf16 v[180:183], a[16:19], a[8:11], v[180:183]\n\t"
    "ds_read_b64_tr_b16 a[86:87], v19 offset:30720\n\t"
    "v_mfma_f32_16x16x32_bf16 v[188:191], a[20:23], a[8:11], v[188:191]\n\t"
    "ds_read_b64_tr_b16 a[88:89], v20 offset:28672\n\t"
    "v_mfma_f32_16x16x32_bf16 v[196:199], a[24:27], a[8:11], v[196:199]\n\t"
    "ds_read_b64_tr_b16 a[90:91], v20 offset:30720\n\t"
    "v_mfma_f32_16x16x32_bf16 v[204:207], a[28:31], a[8:11], v[204:207]\n\t"
    "ds_read_b64_tr_b16 a[92:93], v21 offset:28672\n\t"
    "buffer_load_dwordx4 v27, s[40:43], s52 offen offset:1024 lds\n\t"
    "v_mfma_f32_16x16x32_bf16 v[212:215], a[32:35], a[8:11], v[212:215]\n\t"
    "ds_read_b64_tr_b16 a[94:95], v21 offset:30720\n\t"
    "v_mfma_f32_16x16x32_bf16 v[220:223], a[36:39], a[8:11], v[220:223]\n\t"
    "v_mfma_f32_16x16x32_bf16 v[228:231], a[40:43], a[8:11], v[228:231]\n\t"
    "buffer_load_dwordx4 v28, s[40:43], s52 offen offset:2048 lds\n\t"
    "v_mfma_f32_16x16x32_bf16 v[236:239], a[44:47], a[8:11], v[236:239]\n\t"
    "v_mfma_f32_16x16x32_bf16 v[244:247], a[16:19], a[12:15], v[244:247]\n\t"
    "v_add_u32 v16, 0x20000, v45\n\t"
    "v_mfma_f32_16x16x32_bf16 v[252:255], a[20:23], a[12:15], v[252:255]\n\t"
    "v_mfma_f32_16x16x32_bf16 a[176:179], a[24:27], a[12:15], a[176:179]\n\t"
    "buffer_load_dwordx4 v29, s[40:43], s52 offen offset:3072 lds\n\t"
    "v_mfma_f32_16x16x32_bf16 a[180:183], a[28:31], a[12:15], a[180:183]\n\t"
    "v_mfma_f32_16x16x32_bf16 a[184:187], a[32:35], a[12:15], a[184:187]\n\t"
    "v_mfma_f32_16x16x32_bf16 a[188:191], a[36:39], a[12:15], a[188:191]\n\t"
    "v_mfma_f32_16x16x32_bf16 a[192:195], a[40:43], a[12:15], a[192:195]\n\t"
    "v_mfma_f32_16x16x32_bf16 a[196:199], a[44:47], a[12:15], a[196:199]\n\t"
    "s_waitcnt vmcnt(10) lgkmcnt(0)\n\t"
    "s_barrier\n\t"
    "v_mfma_f32_16x16x32_bf16 v[52:55], a[64:67], a[48:51], v[52:55]\n\t"
    "ds_read_b128 a[0:3], v16 offset:0\n\t"
    "s_add_u32 s58, s64, 0x4000\n\t"
    "s_mov_b32 s53, 0x380000\n\t"
    "s_add_u32 m0, s58, 0\n\t"
    "v_mfma_f32_16x16x32_bf16 v[60:63], a[68:71], a[48:51], v[60:63]\n\t"
    "ds_read_b64_tr_b16 a[16:17], v18 offset:0\n\t"
    "buffer_load_dwordx4 v30, s[44:47], s53 offen offset:0 lds\n\t"
    "v_mfma_f32_16x16x32_bf16 v[68:71], a[72:75], a[48:51], v[68:71]\n\t"
    "ds_read_b64_tr_b16 a[18:19], v18 offset:2048\n\t"
    "v_mfma_f32_16x16x32_bf16 v[76:79], a[76:79], a[48:51], v[76:79]\n\t"
    "ds_read_b128 a[4:7], v16 offset:2048\n\t"
    "v_mfma_f32_16x16x32_bf16 v[84:87], a[80:83], a[48:51], v[84:87]\n\t"
    "ds_read_b64_tr_b16 a[20:21], v19 offset:0\n\t"
    "v_mfma_f32_16x16x32_bf16 v[92:95], a[84:87], a[48:51], v[92:95]\n\t"
    "ds_read_b64_tr_b16 a[22:23], v19 offset:2048\n\t"
    "buffer_load_dwordx4 v31, s[44:47], s53 offen offset:1024 lds\n\t"
    "v_mfma_f32_16x16x32_bf16 v[100:103], a[88:91], a[48:51], v[100:103]\n\t"
    "ds_read_b128 a[8:11], v16 offset:4096\n\t"
    "v_mfma_f32_16x16x32_bf16 v[108:111], a[92:95], a[48:51], v[108:111]\n\t"
    "ds_read_b64_tr_b16 a[24:25], v20 offset:0\n\t"
    "v_mfma_f32_16x16x32_bf16 v[116:119], a[64:67], a[52:55], v[116:119]\n\t"
    "ds_read_b64_tr_b16 a[26:27], v20 offset:2048\n\t"
    "v_mfma_f32_16x16x32_bf16 v[124:127], a[68:71], a[52:55], v[124:127]\n\t"
    "ds_read_b128 a[12:15], v16 offset:6144\n\t"
    "v_mfma_f32_16x16x32_bf16 v[132:135], a[72:75], a[52:55], v[132:135]\n\t"
    "buffer_load_dwordx4 v32, s[44:47], s53 offen offset:2048 lds\n\t"
    "v_mfma_f32_16x16x32_bf16 v[140:143], a[76:79], a[52:55], v[140:143]\n\t"
    "ds_read_b64_tr_b16 a[28:29], v21 offset:0\n\t"
    "v_mfma_f32_16x16x32_bf16 v[148:151], a[80:83], a[52:55], v[148:151]\n\t"
    "ds_read_b64_tr_b16 a[30:31], v21 offset:2048\n\t"
    "v_mfma_f32_16x16x32_bf16 v[156:159], a[84:87], a[52:55], v[156:159]\n\t"
    "ds_read_b64_tr_b16 a[32:33], v18 offset:8192\n\t"
    "v_mfma_f32_16x16x32_bf16 v[164:167], a[88:91], a[52:55], v[164:167]\n\t"
    "ds_read_b64_tr_b16 a[34:35], v18 offset:10240\n\t"
    "buffer_load_dwordx4 v33, s[44:47], s53 offen offset:3072 lds\n\t"
    "v_mfma_f32_16x16x32_bf16 v[172:175], a[92:95], a[52:55], v[172:175]\n\t"
    "ds_read_b64_tr_b16 a[36:37], v19 offset:8192\n\t"
    "v_mfma_f32_16x16x32_bf16 v[180:183], a[64:67], a[56:59], v[180:183]\n\t"
    "ds_read_b64_tr_b16 a[38:39], v19 offset:10240\n\t"
    "v_mfma_f32_16x16x32_bf16 v[188:191], a[68:71], a[56:59], v[188:191]\n\t"
    "ds_read_b64_tr_b16 a[40:41], v20 offset:8192\n\t"
    "v_mfma_f32_16x16x32_bf16 v[196:199], a[72:75], a[56:59], v[196:199]\n\t"
    "ds_read_b64_tr_b16 a[42:43], v20 offset:10240\n\t"
    "s_cmp_eq_u32 s59, 0\n\t"
    "s_mov_b32 s61, 0x2000\n\t"
    "s_cselect_b32 s61, s61, 0x800\n\t"
    "v_mfma_f32_16x16x32_bf16 v[204:207], a[76:79], a[56:59], v[204:207]\n\t"
    "ds_read_b64_tr_b16 a[44:45], v21 offset:8192\n\t"
    "buffer_store_dwordx4 v[112:115], %12, s[48:51], s61 offen offset:0 sc1\n\t"
    "v_mfma_f32_16x16x32_bf16 v[212:215], a[80:83], a[56:59], v[212:215]\n\t"
    "ds_read_b64_tr_b16 a[46:47], v21 offset:10240\n\t"
    "v_mfma_f32_16x16x32_bf16 v[220:223], a[84:87], a[56:59], v[220:223]\n\t"
    "v_mfma_f32_16x16x32_bf16 v[228:231], a[88:91], a[56:59], v[228:231]\n\t"
    "s_cmp_eq_u32 s59, 0\n\t"
    "s_mov_b32 s61, 0x2800\n\t"
    "s_cselect_b32 s61, s61, 0x8800\n\t"
    "v_mfma_f32_16x16x32_bf16 v[236:239], a[92:95], a[56:59], v[236:239]\n\t"
    "buffer_store_dwordx4 v[128:131], %12, s[48:51], s61 offen offset:0 sc1\n\t"
    "v_mfma_f32_16x16x32_bf16 v[244:247], a[64:67], a[60:63], v[244:247]\n\t"
    "v_mfma_f32_16x16x32_bf16 v[252:255], a[68:71], a[60:63], v[252:255]\n\t"
    "v_mfma_f32_16x16x32_bf16 a[176:179], a[72:75], a[60:63], a[176:179]\n\t"
    "v_mfma_f32_16x16x32_bf16 a[180:183], a[76:79], a[60:63], a[180:183]\n\t"
    "v_mfma_f32_16x16x32_bf16 a[184:187], a[80:83], a[60:63], a[184:187]\n\t"
    "v_add_u32 v17, 0x20000, v46\n\t"
    "v_mfma_f32_16x16x32_bf16 a[188:191], a[84:87], a[60:63], a[188:191]\n\t"
    "v_mfma_f32_16x16x32_bf16 a[192:195], a[88:91], a[60:63], a[192:195]\n\t"
    "v_mfma_f32_16x16x32_bf16 a[196:199], a[92:95], a[60:63], a[196:199]\n\t"
    "s_waitcnt lgkmcnt(8)\n\t"
    "v_mfma_f32_16x16x32_bf16 v[52:55], a[16:19], a[0:3], v[52:55]\n\t"
    "ds_read_b128 a[48:51], v17 offset:0\n\t"
    "s_add_u32 s57, s62, 0x18000\n\t"
    "s_mov_b32 s52, 0x400\n\t"
    "s_add_u32 m0, s57, 0\n\t"
    "v_mfma_f32_16x16x32_bf16 v[60:63], a[20:23], a[0:3], v[60:63]\n\t"
    "ds_read_b64_tr_b16 a[64:65], v18 offset:4096\n\t"
    "buffer_load_dwordx4 v22, s[40:43], s52 offen offset:0 lds\n\t"
    "v_mfma_f32_16x16x32_bf16 v[68:71], a[24:27], a[0:3], v[68:71]\n\t"
    "ds_read_b64_tr_b16 a[66:67], v18 offset:6144\n\t"
    "v_mfma_f32_16x16x32_bf16 v[76:79], a[28:31], a[0:3], v[76:79]\n\t"
    "ds_read_b128 a[52:55], v17 offset:2048\n\t"
    "s_waitcnt lgkmcnt(4)\n\t"
    "v_mfma_f32_16x16x32_bf16 v[84:87], a[32:35], a[0:3], v[84:87]\n\t"
    "ds_read_b64_tr_b16 a[68:69], v19 offset:4096\n\t"
    "buffer_load_dwordx4 v23, s[40:43], s52 offen offset:1024 lds\n\t"
    "v_mfma_f32_16x16x32_bf16 v[92:95], a[36:39], a[0:3], v[92:95]\n\t"
    "ds_read_b64_tr_b16 a[70:71], v19 offset:6144\n\t"
    "v_mfma_f32_16x16x32_bf16 v[100:103], a[40:43], a[0:3], v[100:103]\n\t"
    "ds_read_b128 a[56:59], v17 offset:4096\n\t"
    "v_mfma_f32_16x16x32_bf16 v[108:111], a[44:47], a[0:3], v[108:111]\n\t"
    "ds_read_b64_tr_b16 a[72:73], v20 offset:4096\n\t"
    "v_mfma_f32_16x16x32_bf16 v[116:119], a[16:19], a[4:7], v[116:119]\n\t"
    "ds_read_b64_tr_b16 a[74:75], v20 offset:6144\n\t"
    "buffer_load_dwordx4 v24, s[40:43], s52 offen offset:2048 lds\n\t"
    "v_mfma_f32_16x16x32_bf16 v[124:127], a[20:23], a[4:7], v[124:127]\n\t"
    "ds_read_b128 a[60:63], v17 offset:6144\n\t"
    "v_mfma_f32_16x16x32_bf16 v[132:135], a[24:27], a[4:7], v[132:135]\n\t"
    "v_mfma_f32_16x16x32_bf16 v[140:143], a[28:31], a[4:7], v[140:143]\n\t"
    "ds_read_b64_tr_b16 a[76:77], v21 offset:4096\n\t"
    "buffer_load_dwordx4 v25, s[40:43], s52 offen offset:3072 lds\n\t"
    "v_mfma_f32_16x16x32_bf16 v[148:151], a[32:35], a[4:7], v[148:151]\n\t"
    "ds_read_b64_tr_b16 a[78:79], v21 offset:6144\n\t"
    "v_mfma_f32_16x16x32_bf16 v[156:159], a[36:39], a[4:7], v[156:159]\n\t"
    "ds_read_b64_tr_b16 a[80:81], v18 offset:12288\n\t"
    "v_mfma_f32_16x16x32_bf16 v[164:167], a[40:43], a[4:7], v[164:167]\n\t"
    "ds_read_b64_tr_b16 a[82:83], v18 offset:14336\n\t"
    "s_add_u32 m0, s57, 0x1000\n\t"
    "v_mfma_f32_16x16x32_bf16 v[172:175], a[44:47], a[4:7], v[172:175]\n\t"
    "ds_read_b64_tr_b16 a[84:85], v19 offset:12288\n\t"
    "buffer_load_dwordx4 v26, s[40:43], s52 offen offset:0 lds\n\t"
    "v_mfma_f32_16x16x32_bf16 v[180:183], a[16:19], a[8:11], v[180:183]\n\t"
    "ds_read_b64_tr_b16 a[86:87], v19 offset:14336\n\t"
    "v_mfma_f32_16x16x32_bf16 v[188:191], a[20:23], a[8:11], v[188:191]\n\t"
    "ds_read_b64_tr_b16 a[88:89], v20 offset:12288\n\t"
    "v_mfma_f32_16x16x32_bf16 v[196:199], a[24:27], a[8:11], v[196:199]\n\t"
    "ds_read_b64_tr_b16 a[90:91], v20 offset:14336\n\t"
    "v_mfma_f32_16x16x32_bf16 v[204:207], a[28:31], a[8:11], v[204:207]\n\t"
    "ds_read_b64_tr_b16 a[92:93], v21 offset:12288\n\t"
    "buffer_load_dwordx4 v27, s[40:43], s52 offen offset:1024 lds\n\t"
    "v_mfma_f32_16x16x32_bf16 v[212:215], a[32:35], a[8:11], v[212:215]\n\t"
    "ds_read_b64_tr_b16 a[94:95], v21 offset:14336\n\t"
    "v_mfma_f32_16x16x32_bf16 v[220:223], a[36:39], a[8:11], v[220:223]\n\t"
    "v_mfma_f32_16x16x32_bf16 v[228:231], a[40:43], a[8:11], v[228:231]\n\t"
    "buffer_load_dwordx4 v28, s[40:43], s52 offen offset:2048 lds\n\t"
    "v_mfma_f32_16x16x32_bf16 v[236:239], a[44:47], a[8:11], v[236:239]\n\t"
    "v_mfma_f32_16x16x32_bf16 v[244:247], a[16:19], a[12:15], v[244:247]\n\t"
    "v_add_u32 v16, 0x10000, v45\n\t"
    "v_mfma_f32_16x16x32_bf16 v[252:255], a[20:23], a[12:15], v[252:255]\n\t"
    "v_mfma_f32_16x16x32_bf16 a[176:179], a[24:27], a[12:15], a[176:179]\n\t"
    "buffer_load_dwordx4 v29, s[40:43], s52 offen offset:3072 lds\n\t"
    "v_mfma_f32_16x16x32_bf16 a[180:183], a[28:31], a[12:15], a[180:183]\n\t"
    "v_mfma_f32_16x16x32_bf16 a[184:187], a[32:35], a[12:15], a[184:187]\n\t"
    "v_mfma_f32_16x16x32_bf16 a[188:191], a[36:39], a[12:15], a[188:191]\n\t"
    "v_mfma_f32_16x16x32_bf16 a[192:195], a[40:43], a[12:15], a[192:195]\n\t"
    "v_mfma_f32_16x16x32_bf16 a[196:199], a[44:47], a[12:15], a[196:199]\n\t"
    "s_waitcnt vmcnt(10) lgkmcnt(0)\n\t"
    "s_barrier\n\t"
    "v_mfma_f32_16x16x32_bf16 v[52:55], a[64:67], a[48:51], v[52:55]\n\t"
    "ds_read_b128 a[0:3], v16 offset:0\n\t"
    "s_add_u32 s58, s64, 0\n\t"
    "s_mov_b32 s53, 0x400000\n\t"
    "s_add_u32 m0, s58, 0\n\t"
    "v_mfma_f32_16x16x32_bf16 v[60:63], a[68:71], a[48:51], v[60:63]\n\t"
    "ds_read_b64_tr_b16 a[16:17], v18 offset:16384\n\t"
    "buffer_load_dwordx4 v30, s[44:47], s53 offen offset:0 lds\n\t"
    "v_mfma_f32_16x16x32_bf16 v[68:71], a[72:75], a[48:51], v[68:71]\n\t"
    "ds_read_b64_tr_b16 a[18:19], v18 offset:18432\n\t"
    "v_mfma_f32_16x16x32_bf16 v[76:79], a[76:79], a[48:51], v[76:79]\n\t"
    "ds_read_b128 a[4:7], v16 offset:2048\n\t"
    "v_mfma_f32_16x16x32_bf16 v[84:87], a[80:83], a[48:51], v[84:87]\n\t"
    "ds_read_b64_tr_b16 a[20:21], v19 offset:16384\n\t"
    "buffer_load_dwordx4 v31, s[44:47], s53 offen offset:1024 lds\n\t"
    "v_mfma_f32_16x16x32_bf16 v[92:95], a[84:87], a[48:51], v[92:95]\n\t"
    "ds_read_b64_tr_b16 a[22:23], v19 offset:18432\n\t"
    "v_mfma_f32_16x16x32_bf16 v[100:103], a[88:91], a[48:51], v[100:103]\n\t"
    "ds_read_b128 a[8:11], v16 offset:4096\n\t"
    "v_mfma_f32_16x16x32_bf16 v[108:111], a[92:95], a[48:51], v[108:111]\n\t"
    "ds_read_b64_tr_b16 a[24:25], v20 offset:16384\n\t"
    "v_mfma_f32_16x16x32_bf16 v[116:119], a[64:67], a[52:55], v[116:119]\n\t"
    "ds_read_b64_tr_b16 a[26:27], v20 offset:18432\n\t"
    "buffer_load_dwordx4 v32, s[44:47], s53 offen offset:2048 lds\n\t"
    "v_mfma_f32_16x16x32_bf16 v[124:127], a[68:71], a[52:55], v[124:127]\n\t"
    "ds_read_b128 a[12:15], v16 offset:6144\n\t"
    "v_mfma_f32_16x16x32_bf16 v[132:135], a[72:75], a[52:55], v[132:135]\n\t"
    "v_mfma_f32_16x16x32_bf16 v[140:143], a[76:79], a[52:55], v[140:143]\n\t"
    "ds_read_b64_tr_b16 a[28:29], v21 offset:16384\n\t"
    "v_mfma_f32_16x16x32_bf16 v[148:151], a[80:83], a[52:55], v[148:151]\n\t"
    "ds_read_b64_tr_b16 a[30:31], v21 offset:18432\n\t"
    "buffer_load_dwordx4 v33, s[44:47], s53 offen offset:3072 lds\n\t"
    "v_mfma_f32_16x16x32_bf16 v[156:159], a[84:87], a[52:55], v[156:159]\n\t"
    "ds_read_b64_tr_b16 a[32:33], v18 offset:24576\n\t"
    "v_mfma_f32_16x16x32_bf16 v[164:167], a[88:91], a[52:55], v[164:167]\n\t"
    "ds_read_b64_tr_b16 a[34:35], v18 offset:26624\n\t"
    "v_mfma_f32_16x16x32_bf16 v[172:175], a[92:95], a[52:55], v[172:175]\n\t"
    "ds_read_b64_tr_b16 a[36:37], v19 offset:24576\n\t"
    "s_cmp_eq_u32 s59, 0\n\t"
    "s_mov_b32 s61, 0x2c00\n\t"
    "s_cselect_b32 s61, s61, 0xa800\n\t"
    "v_mfma_f32_16x16x32_bf16 v[180:183], a[64:67], a[56:59], v[180:183]\n\t"
    "ds_read_b64_tr_b16 a[38:39], v19 offset:26624\n\t"
    "buffer_store_dwordx4 v[136:139], %12, s[48:51], s61 offen offset:0 sc1\n\t"
    "v_mfma_f32_16x16x32_bf16 v[188:191], a[68:71], a[56:59], v[188:191]\n\t"
    "ds_read_b64_tr_b16 a[40:41], v20 offset:24576\n\t"
    "v_mfma_f32_16x16x32_bf16 v[196:199], a[72:75], a[56:59], v[196:199]\n\t"
    "ds_read_b64_tr_b16 a[42:43], v20 offset:26624\n\t"
    "v_mfma_f32_16x16x32_bf16 v[204:207], a[76:79], a[56:59], v[204:207]\n\t"
    "ds_read_b64_tr_b16 a[44:45], v21 offset:24576\n\t"
    "s_cmp_eq_u32 s59, 0\n\t"
    "s_mov_b32 s61, 0x3400\n\t"
    "s_cselect_b32 s61, s61, 0x2c00\n\t"
    "v_mfma_f32_16x16x32_bf16 v[212:215], a[80:83], a[56:59], v[212:215]\n\t"
    "ds_read_b64_tr_b16 a[46:47], v21 offset:26624\n\t"
    "buffer_store_dwordx4 v[152:155], %12, s[48:51], s61 offen offset:0 sc1\n\t"
    "v_mfma_f32_16x16x32_bf16 v[220:223], a[84:87], a[56:59], v[220:223]\n\t"
    "v_mfma_f32_16x16x32_bf16 v[228:231], a[88:91], a[56:59], v[228:231]\n\t"
    "v_mfma_f32_16x16x32_bf16 v[236:239], a[92:95], a[56:59], v[236:239]\n\t"
    "s_cmp_eq_u32 s59, 0\n\t"
    "s_mov_b32 s61, 0x3800\n\t"
    "s_cselect_b32 s61, s61, 0x8c00\n\t"
    "v_mfma_f32_16x16x32_bf16 v[244:247], a[64:67], a[60:63], v[244:247]\n\t"
    "buffer_store_dwordx4 v[160:163], %12, s[48:51], s61 offen offset:0 sc1\n\t"
    "v_mfma_f32_16x16x32_bf16 v[252:255], a[68:71], a[60:63], v[252:255]\n\t"
    "v_mfma_f32_16x16x32_bf16 a[176:179], a[72:75], a[60:63], a[176:179]\n\t"
    "v_mfma_f32_16x16x32_bf16 a[180:183], a[76:79], a[60:63], a[180:183]\n\t"
    "v_mfma_f32_16x16x32_bf16 a[184:187], a[80:83], a[60:63], a[184:187]\n\t"
    "v_add_u32 v17, 0x10000, v46\n\t"
    "v_mfma_f32_16x16x32_bf16 a[188:191], a[84:87], a[60:63], a[188:191]\n\t"
    "v_mfma_f32_16x16x32_bf16 a[192:195], a[88:91], a[60:63], a[192:195]\n\t"
    "v_mfma_f32_16x16x32_bf16 a[196:199], a[92:95], a[60:63], a[196:199]\n\t"
    "s_waitcnt lgkmcnt(11)\n\t"
    "v_mfma_f32_16x16x32_bf16 v[52:55], a[16:19], a[0:3], v[52:55]\n\t"
    "s_mov_b64 s[40:41], %17\n\t"
    "s_mov_b32 s42, %18\n\t"
    "s_mov_b32 s43, 0x20000\n\t"
    "ds_read_b128 a[48:51], v17 offset:0\n\t"
    "v_mfma_f32_16x16x32_bf16 v[60:63], a[20:23], a[0:3], v[60:63]\n\t"
    "s_mov_b64 s[44:45], %19\n\t"
    "s_mov_b32 s46, %20\n\t"
    "s_mov_b32 s47, 0x20000\n\t"
    "ds_read_b64_tr_b16 a[64:65], v18 offset:20480\n\t"
    "v_mfma_f32_16x16x32_bf16 v[68:71], a[24:27], a[0:3], v[68:71]\n\t"
    "ds_read_b64_tr_b16 a[66:67], v18 offset:22528\n\t"
    "s_waitcnt lgkmcnt(5)\n\t"
    "v_mfma_f32_16x16x32_bf16 v[76:79], a[28:31], a[0:3], v[76:79]\n\t"
    "ds_read_b128 a[52:55], v17 offset:2048\n\t"
    "s_add_u32 s57, s62, 0x8000\n\t"
    "s_mov_b32 s52, 0\n\t"
    "s_add_u32 m0, s57, 0\n\t"
    "v_mfma_f32_16x16x32_bf16 v[84:87], a[32:35], a[0:3], v[84:87]\n\t"
    "ds_read_b64_tr_b16 a[68:69], v19 offset:20480\n\t"
    "buffer_load_dwordx4 v22, s[40:43], s52 offen offset:0 lds\n\t"
    "v_mfma_f32_16x16x32_bf16 v[92:95], a[36:39], a[0:3], v[92:95]\n\t"
    "ds_read_b64_tr_b16 a[70:71], v19 offset:22528\n\t"
    "v_mfma_f32_16x16x32_bf16 v[100:103], a[40:43], a[0:3], v[100:103]\n\t"
    "ds_read_b128 a[56:59], v17 offset:4096\n\t"
    "s_waitcnt lgkmcnt(7)\n\t"
    "v_mfma_f32_16x16x32_bf16 v[108:111], a[44:47], a[0:3], v[108:111]\n\t"
    "ds_read_b64_tr_b16 a[72:73], v20 offset:20480\n\t"
    "buffer_load_dwordx4 v23, s[40:43], s52 offen offset:1024 lds\n\t"
    "v_mfma_f32_16x16x32_bf16 v[116:119], a[16:19], a[4:7], v[116:119]\n\t"
    "ds_read_b64_tr_b16 a[74:75], v20 offset:22528\n\t"
    "v_mfma_f32_16x16x32_bf16 v[124:127], a[20:23], a[4:7], v[124:127]\n\t"
    "ds_read_b128 a[60:63], v17 offset:6144\n\t"
    "v_mfma_f32_16x16x32_bf16 v[132:135], a[24:27], a[4:7], v[132:135]\n\t"
    "buffer_load_dwordx4 v24, s[40:43], s52 offen offset:2048 lds\n\t"
    "v_mfma_f32_16x16x32_bf16 v[140:143], a[28:31], a[4:7], v[140:143]\n\t"
    "ds_read_b64_tr_b16 a[76:77], v21 offset:20480\n\t"
    "v_mfma_f32_16x16x32_bf16 v[148:151], a[32:35], a[4:7], v[148:151]\n\t"
    "ds_read_b64_tr_b16 a[78:79], v21 offset:22528\n\t"
    "v_mfma_f32_16x16x32_bf16 v[156:159], a[36:39], a[4:7], v[156:159]\n\t"
    "ds_read_b64_tr_b16 a[80:81], v18 offset:28672\n\t"
    "buffer_load_dwordx4 v25, s[40:43], s52 offen offset:3072 lds\n\t"
    "v_mfma_f32_16x16x32_bf16 v[164:167], a[40:43], a[4:7], v[164:167]\n\t"
    "ds_read_b64_tr_b16 a[82:83], v18 offset:30720\n\t"
    "v_mfma_f32_16x16x32_bf16 v[172:175], a[44:47], a[4:7], v[172:175]\n\t"
    "ds_read_b64_tr_b16 a[84:85], v19 offset:28672\n\t"
    "v_mfma_f32_16x16x32_bf16 v[180:183], a[16:19], a[8:11], v[180:183]\n\t"
    "ds_read_b64_tr_b16 a[86:87], v19 offset:30720\n\t"
    "s_add_u32 m0, s57, 0x1000\n\t"
    "v_mfma_f32_16x16x32_bf16 v[188:191], a[20:23], a[8:11], v[188:191]\n\t"
    "ds_read_b64_tr_b16 a[88:89], v20 offset:28672\n\t"
    "buffer_load_dwordx4 v26, s[40:43], s52 offen offset:0 lds\n\t"
    "v_mfma_f32_16x16x32_bf16 v[196:199], a[24:27], a[8:11], v[196:199]\n\t"
    "ds_read_b64_tr_b16 a[90:91], v20 offset:30720\n\t"
    "v_mfma_f32_16x16x32_bf16 v[204:207], a[28:31], a[8:11], v[204:207]\n\t"
    "ds_read_b64_tr_b16 a[92:93], v21 offset:28672\n\t"
    "v_mfma_f32_16x16x32_bf16 v[212:215], a[32:35], a[8:11], v[212:215]\n\t"
    "ds_read_b64_tr_b16 a[94:95], v21 offset:30720\n\t"
    "buffer_load_dwordx4 v27, s[40:43], s52 offen offset:1024 lds\n\t"
    "v_mfma_f32_16x16x32_bf16 v[220:223], a[36:39], a[8:11], v[220:223]\n\t"
    "v_mfma_f32_16x16x32_bf16 v[228:231], a[40:43], a[8:11], v[228:231]\n\t"
    "v_mfma_f32_16x16x32_bf16 v[236:239], a[44:47], a[8:11], v[236:239]\n\t"
    "buffer_load_dwordx4 v28, s[40:43], s52 offen offset:2048 lds\n\t"
    "v_mfma_f32_16x16x32_bf16 v[244:247], a[16:19], a[12:15], v[244:247]\n\t"
    "v_add_u32 v16, 0x18000, v45\n\t"
    "v_mfma_f32_16x16x32_bf16 v[252:255], a[20:23], a[12:15], v[252:255]\n\t"
    "v_mfma_f32_16x16x32_bf16 a[176:179], a[24:27], a[12:15], a[176:179]\n\t"
    "buffer_load_dwordx4 v29, s[40:43], s52 offen offset:3072 lds\n\t"
    "v_mfma_f32_16x16x32_bf16 a[180:183], a[28:31], a[12:15], a[180:183]\n\t"
    "v_mfma_f32_16x16x32_bf16 a[184:187], a[32:35], a[12:15], a[184:187]\n\t"
    "v_mfma_f32_16x16x32_bf16 a[188:191], a[36:39], a[12:15], a[188:191]\n\t"
    "v_mfma_f32_16x16x32_bf16 a[192:195], a[40:43], a[12:15], a[192:195]\n\t"
    "v_mfma_f32_16x16x32_bf16 a[196:199], a[44:47], a[12:15], a[196:199]\n\t"
    "s_waitcnt vmcnt(11) lgkmcnt(0)\n\t"
    "s_barrier\n\t"
    "v_mfma_f32_16x16x32_bf16 v[52:55], a[64:67], a[48:51], v[52:55]\n\t"
    "ds_read_b128 a[0:3], v16 offset:0\n\t"
    "s_add_u32 m0, s66, 0x10000\n\t"
    "s_mov_b32 s67, 0\n\t"
    "v_mfma_f32_16x16x32_bf16 v[60:63], a[68:71], a[48:51], v[60:63]\n\t"
    "ds_read_b64_tr_b16 a[16:17], v18 offset:0\n\t"
    "buffer_load_dwordx4 %21, s[44:47], s67 offen offset:0 lds\n\t"
    "v_mfma_f32_16x16x32_bf16 v[68:71], a[72:75], a[48:51], v[68:71]\n\t"
    "ds_read_b64_tr_b16 a[18:19], v18 offset:2048\n\t"
    "v_mfma_f32_16x16x32_bf16 v[76:79], a[76:79], a[48:51], v[76:79]\n\t"
    "ds_read_b128 a[4:7], v16 offset:2048\n\t"
    "s_mov_b32 s67, 0x80\n\t"
    "v_mfma_f32_16x16x32_bf16 v[84:87], a[80:83], a[48:51], v[84:87]\n\t"
    "ds_read_b64_tr_b16 a[20:21], v19 offset:0\n\t"
    "buffer_load_dwordx4 %21, s[44:47], s67 offen offset:1024 lds\n\t"
    "v_mfma_f32_16x16x32_bf16 v[92:95], a[84:87], a[48:51], v[92:95]\n\t"
    "ds_read_b64_tr_b16 a[22:23], v19 offset:2048\n\t"
    "v_mfma_f32_16x16x32_bf16 v[100:103], a[88:91], a[48:51], v[100:103]\n\t"
    "ds_read_b128 a[8:11], v16 offset:4096\n\t"
    "s_mov_b32 s67, 0x100\n\t"
    "v_mfma_f32_16x16x32_bf16 v[108:111], a[92:95], a[48:51], v[108:111]\n\t"
    "ds_read_b64_tr_b16 a[24:25], v20 offset:0\n\t"
    "buffer_load_dwordx4 %21, s[44:47], s67 offen offset:2048 lds\n\t"
    "v_mfma_f32_16x16x32_bf16 v[116:119], a[64:67], a[52:55], v[116:119]\n\t"
    "ds_read_b64_tr_b16 a[26:27], v20 offset:2048\n\t"
    "v_mfma_f32_16x16x32_bf16 v[124:127], a[68:71], a[52:55], v[124:127]\n\t"
    "ds_read_b128 a[12:15], v16 offset:6144\n\t"
    "v_mfma_f32_16x16x32_bf16 v[132:135], a[72:75], a[52:55], v[132:135]\n\t"
    "s_mov_b64 s[68:69], %22\n\t"
    "s_mov_b32 s70, 0x288\n\t"
    "s_mov_b32 s71, 0x20000\n\t"
    "s_lshl_b32 s67, %1, 10\n\t"
    "s_add_u32 m0, s67, 0x13000\n\t"
    "v_mfma_f32_16x16x32_bf16 v[140:143], a[76:79], a[52:55], v[140:143]\n\t"
    "ds_read_b64_tr_b16 a[28:29], v21 offset:0\n\t"
    "buffer_load_dwordx4 %12, s[68:71], 0 offen offset:0 lds\n\t"
    "v_mfma_f32_16x16x32_bf16 v[148:151], a[80:83], a[52:55], v[148:151]\n\t"
    "ds_read_b64_tr_b16 a[30:31], v21 offset:2048\n\t"
    "v_mfma_f32_16x16x32_bf16 v[156:159], a[84:87], a[52:55], v[156:159]\n\t"
    "ds_read_b64_tr_b16 a[32:33], v18 offset:8192\n\t"
    "s_cmp_eq_u32 s59, 0\n\t"
    "s_mov_b32 s61, 0x8000\n\t"
    "s_cselect_b32 s61, s61, 0x1000\n\t"
    "v_mfma_f32_16x16x32_bf16 v[164:167], a[88:91], a[52:55], v[164:167]\n\t"
    "ds_read_b64_tr_b16 a[34:35], v18 offset:10240\n\t"
    "buffer_store_dwordx4 v[176:179], %12, s[48:51], s61 offen offset:0 sc1\n\t"
    "v_mfma_f32_16x16x32_bf16 v[172:175], a[92:95], a[52:55], v[172:175]\n\t"
    "ds_read_b64_tr_b16 a[36:37], v19 offset:8192\n\t"
    "v_mfma_f32_16x16x32_bf16 v[180:183], a[64:67], a[56:59], v[180:183]\n\t"
    "ds_read_b64_tr_b16 a[38:39], v19 offset:10240\n\t"
    "s_cmp_eq_u32 s59, 0\n\t"
    "s_mov_b32 s61, 0x8400\n\t"
    "s_cselect_b32 s61, s61, 0x3000\n\t"
    "v_mfma_f32_16x16x32_bf16 v[188:191], a[68:71], a[56:59], v[188:191]\n\t"
    "ds_read_b64_tr_b16 a[40:41], v20 offset:8192\n\t"
    "buffer_store_dwordx4 v[184:187], %12, s[48:51], s61 offen offset:0 sc1\n\t"
    "v_mfma_f32_16x16x32_bf16 v[196:199], a[72:75], a[56:59], v[196:199]\n\t"
    "ds_read_b64_tr_b16 a[42:43], v20 offset:10240\n\t"
    "v_mfma_f32_16x16x32_bf16 v[204:207], a[76:79], a[56:59], v[204:207]\n\t"
    "ds_read_b64_tr_b16 a[44:45], v21 offset:8192\n\t"
    "v_mfma_f32_16x16x32_bf16 v[212:215], a[80:83], a[56:59], v[212:215]\n\t"
    "ds_read_b64_tr_b16 a[46:47], v21 offset:10240\n\t"
    "s_cmp_eq_u32 s59, 0\n\t"
    "s_mov_b32 s61, 0x8c00\n\t"
    "s_cselect_b32 s61, s61, 0xb000\n\t"
    "v_mfma_f32_16x16x32_bf16 v[220:223], a[84:87], a[56:59], v[220:223]\n\t"
    "buffer_store_dwordx4 v[200:203], %12, s[48:51], s61 offen offset:0 sc1\n\t"
    "v_mfma_f32_16x16x32_bf16 v[228:231], a[88:91], a[56:59], v[228:231]\n\t"
    "v_mfma_f32_16x16x32_bf16 v[236:239], a[92:95], a[56:59], v[236:239]\n\t"
    "s_cmp_eq_u32 s59, 0\n\t"
    "s_mov_b32 s61, 0x9000\n\t"
    "s_cselect_b32 s61, s61, 0x1400\n\t"
    "v_mfma_f32_16x16x32_bf16 v[244:247], a[64:67], a[60:63], v[244:247]\n\t"
    "buffer_store_dwordx4 v[208:211], %12, s[48:51], s61 offen offset:0 sc1\n\t"
    "v_mfma_f32_16x16x32_bf16 v[252:255], a[68:71], a[60:63], v[252:255]\n\t"
    "v_mfma_f32_16x16x32_bf16 a[176:179], a[72:75], a[60:63], a[176:179]\n\t"
    "v_mfma_f32_16x16x32_bf16 a[180:183], a[76:79], a[60:63], a[180:183]\n\t"
    "v_mfma_f32_16x16x32_bf16 a[184:187], a[80:83], a[60:63], a[184:187]\n\t"
    "v_add_u32 v17, 0x18000, v46\n\t"
    "v_mfma_f32_16x16x32_bf16 a[188:191], a[84:87], a[60:63], a[188:191]\n\t"
    "v_mfma_f32_16x16x32_bf16 a[192:195], a[88:91], a[60:63], a[192:195]\n\t"
    "v_mfma_f32_16x16x32_bf16 a[196:199], a[92:95], a[60:63], a[196:199]\n\t"
    "s_waitcnt lgkmcnt(8)\n\t"
    "v_mfma_f32_16x16x32_bf16 v[52:55], a[16:19], a[0:3], v[52:55]\n\t"
    "ds_read_b128 a[48:51], v17 offset:0\n\t"
    "s_cmp_eq_u32 s59, 0\n\t"
    "s_mov_b32 s61, 0x9800\n\t"
    "s_cselect_b32 s61, s61, 0x9400\n\t"
    "v_mfma_f32_16x16x32_bf16 v[60:63], a[20:23], a[0:3], v[60:63]\n\t"
    "ds_read_b64_tr_b16 a[64:65], v18 offset:4096\n\t"
    "buffer_store_dwordx4 v[224:227], %12, s[48:51], s61 offen offset:0 sc1\n\t"
    "v_mfma_f32_16x16x32_bf16 v[68:71], a[24:27], a[0:3], v[68:71]\n\t"
    "ds_read_b64_tr_b16 a[66:67], v18 offset:6144\n\t"
    "v_mfma_f32_16x16x32_bf16 v[76:79], a[28:31], a[0:3], v[76:79]\n\t"
    "ds_read_b128 a[52:55], v17 offset:2048\n\t"
    "s_waitcnt lgkmcnt(4)\n\t"
    "v_mfma_f32_16x16x32_bf16 v[84:87], a[32:35], a[0:3], v[84:87]\n\t"
    "ds_read_b64_tr_b16 a[68:69], v19 offset:4096\n\t"
    "v_mfma_f32_16x16x32_bf16 v[92:95], a[36:39], a[0:3], v[92:95]\n\t"
    "ds_read_b64_tr_b16 a[70:71], v19 offset:6144\n\t"
    "v_mfma_f32_16x16x32_bf16 v[100:103], a[40:43], a[0:3], v[100:103]\n\t"
    "ds_read_b128 a[56:59], v17 offset:4096\n\t"
    "v_mfma_f32_16x16x32_bf16 v[108:111], a[44:47], a[0:3], v[108:111]\n\t"
    "ds_read_b64_tr_b16 a[72:73], v20 offset:4096\n\t"
    "v_mfma_f32_16x16x32_bf16 v[116:119], a[16:19], a[4:7], v[116:119]\n\t"
    "ds_read_b64_tr_b16 a[74:75], v20 offset:6144\n\t"
    "v_mfma_f32_16x16x32_bf16 v[124:127], a[20:23], a[4:7], v[124:127]\n\t"
    "ds_read_b128 a[60:63], v17 offset:6144\n\t"
    "v_mfma_f32_16x16x32_bf16 v[132:135], a[24:27], a[4:7], v[132:135]\n\t"
    "v_mfma_f32_16x16x32_bf16 v[140:143], a[28:31], a[4:7], v[140:143]\n\t"
    "ds_read_b64_tr_b16 a[76:77], v21 offset:4096\n\t"
    "v_mfma_f32_16x16x32_bf16 v[148:151], a[32:35], a[4:7], v[148:151]\n\t"
    "ds_read_b64_tr_b16 a[78:79], v21 offset:6144\n\t"
    "v_mfma_f32_16x16x32_bf16 v[156:159], a[36:39], a[4:7], v[156:159]\n\t"
    "ds_read_b64_tr_b16 a[80:81], v18 offset:12288\n\t"
    "v_mfma_f32_16x16x32_bf16 v[164:167], a[40:43], a[4:7], v[164:167]\n\t"
    "ds_read_b64_tr_b16 a[82:83], v18 offset:14336\n\t"
    "s_cmp_eq_u32 s59, 0\n\t"
    "s_mov_b32 s61, 0x9c00\n\t"
    "s_cselect_b32 s61, s61, 0xb400\n\t"
    "v_mfma_f32_16x16x32_bf16 v[172:175], a[44:47], a[4:7], v[172:175]\n\t"
    "ds_read_b64_tr_b16 a[84:85], v19 offset:12288\n\t"
    "buffer_store_dwordx4 v[232:235], %12, s[48:51], s61 offen offset:0 sc1\n\t"
    "v_mfma_f32_16x16x32_bf16 v[180:183], a[16:19], a[8:11], v[180:183]\n\t"
    "ds_read_b64_tr_b16 a[86:87], v19 offset:14336\n\t"
    "v_mfma_f32_16x16x32_bf16 v[188:191], a[20:23], a[8:11], v[188:191]\n\t"
    "ds_read_b64_tr_b16 a[88:89], v20 offset:12288\n\t"
    "v_mfma_f32_16x16x32_bf16 v[196:199], a[24:27], a[8:11], v[196:199]\n\t"
    "ds_read_b64_tr_b16 a[90:91], v20 offset:14336\n\t"
    "v_mfma_f32_16x16x32_bf16 v[204:207], a[28:31], a[8:11], v[204:207]\n\t"
    "ds_read_b64_tr_b16 a[92:93], v21 offset:12288\n\t"
    "v_mfma_f32_16x16x32_bf16 v[212:215], a[32:35], a[8:11], v[212:215]\n\t"
    "ds_read_b64_tr_b16 a[94:95], v21 offset:14336\n\t"
    "v_mfma_f32_16x16x32_bf16 v[220:223], a[36:39], a[8:11], v[220:223]\n\t"
    "v_mfma_f32_16x16x32_bf16 v[228:231], a[40:43], a[8:11], v[228:231]\n\t"
    "v_mfma_f32_16x16x32_bf16 v[236:239], a[44:47], a[8:11], v[236:239]\n\t"
    "v_mfma_f32_16x16x32_bf16 v[244:247], a[16:19], a[12:15], v[244:247]\n\t"
    "v_mfma_f32_16x16x32_bf16 v[252:255], a[20:23], a[12:15], v[252:255]\n\t"
    "v_mfma_f32_16x16x32_bf16 a[176:179], a[24:27], a[12:15], a[176:179]\n\t"
    "v_mfma_f32_16x16x32_bf16 a[180:183], a[28:31], a[12:15], a[180:183]\n\t"
    "v_mfma_f32_16x16x32_bf16 a[184:187], a[32:35], a[12:15], a[184:187]\n\t"
    "v_mfma_f32_16x16x32_bf16 a[188:191], a[36:39], a[12:15], a[188:191]\n\t"
    "v_mfma_f32_16x16x32_bf16 a[192:195], a[40:43], a[12:15], a[192:195]\n\t"
    "v_mfma_f32_16x16x32_bf16 a[196:199], a[44:47], a[12:15], a[196:199]\n\t"
    "s_waitcnt lgkmcnt(0)\n\t"
    "s_barrier\n\t"
    "v_mfma_f32_16x16x32_bf16 v[52:55], a[64:67], a[48:51], v[52:55]\n\t"
    "s_add_u32 m0, s66, 0x18000\n\t"
    "s_mov_b32 s67, 0x36000\n\t"
    "v_mfma_f32_16x16x32_bf16 v[60:63], a[68:71], a[48:51], v[60:63]\n\t"
    "buffer_load_dwordx4 %21, s[44:47], s67 offen offset:0 lds\n\t"
    "v_mfma_f32_16x16x32_bf16 v[68:71], a[72:75], a[48:51], v[68:71]\n\t"
    "s_mov_b32 s67, 0x36080\n\t"
    "v_mfma_f32_16x16x32_bf16 v[76:79], a[76:79], a[48:51], v[76:79]\n\t"
    "buffer_load_dwordx4 %21, s[44:47], s67 offen offset:1024 lds\n\t"
    "v_mfma_f32_16x16x32_bf16 v[84:87], a[80:83], a[48:51], v[84:87]\n\t"
    "s_mov_b32 s67, 0x36100\n\t"
    "v_mfma_f32_16x16x32_bf16 v[92:95], a[84:87], a[48:51], v[92:95]\n\t"
    "buffer_load_dwordx4 %21, s[44:47], s67 offen offset:2048 lds\n\t"
    "v_mfma_f32_16x16x32_bf16 v[100:103], a[88:91], a[48:51], v[100:103]\n\t"
    "s_add_u32 s57, s62, 0\n\t"
    "s_mov_b32 s52, 0x80\n\t"
    "s_add_u32 m0, s57, 0\n\t"
    "v_pk_mul_f32 v[52:53], v[52:53], s[76:77] op_sel_hi:[1,0]\n\t"
    "v_pk_mul_f32 v[54:55], v[54:55], s[76:77] op_sel_hi:[1,0]\n\t"
    "v_cvt_pk_bf16_f32 v36, v52, v53\n\t"
    "v_cvt_pk_bf16_f32 v37, v54, v55\n\t"
    "v_pk_mul_f32 v[60:61], v[60:61], s[76:77] op_sel_hi:[1,0]\n\t"
    "v_pk_mul_f32 v[62:63], v[62:63], s[76:77] op_sel_hi:[1,0]\n\t"
    "v_cvt_pk_bf16_f32 v38, v60, v61\n\t"
    "v_cvt_pk_bf16_f32 v39, v62, v63\n\t"
    "ds_write_b128 v34, v[36:39] offset:0\n\t"
    "v_mfma_f32_16x16x32_bf16 v[108:111], a[92:95], a[48:51], v[108:111]\n\t"
    "buffer_load_dwordx4 v22, s[40:43], s52 offen offset:0 lds\n\t"
    "v_mfma_f32_16x16x32_bf16 v[116:119], a[64:67], a[52:55], v[116:119]\n\t"
    "v_pk_mul_f32 v[68:69], v[68:69], s[76:77] op_sel_hi:[1,0]\n\t"
    "v_pk_mul_f32 v[70:71], v[70:71], s[76:77] op_sel_hi:[1,0]\n\t"
    "v_cvt_pk_bf16_f32 v40, v68, v69\n\t"
    "v_cvt_pk_bf16_f32 v41, v70, v71\n\t"
    "v_pk_mul_f32 v[76:77], v[76:77], s[76:77] op_sel_hi:[1,0]\n\t"
    "v_pk_mul_f32 v[78:79], v[78:79], s[76:77] op_sel_hi:[1,0]\n\t"
    "v_cvt_pk_bf16_f32 v42, v76, v77\n\t"
    "v_cvt_pk_bf16_f32 v43, v78, v79\n\t"
    "ds_write_b128 v34, v[40:43] offset:1024\n\t"
    "v_mfma_f32_16x16x32_bf16 v[124:127], a[68:71], a[52:55], v[124:127]\n\t"
    "buffer_load_dwordx4 v23, s[40:43], s52 offen offset:1024 lds\n\t"
    "v_mfma_f32_16x16x32_bf16 v[132:135], a[72:75], a[52:55], v[132:135]\n\t"
    "v_pk_mul_f32 v[84:85], v[84:85], s[76:77] op_sel_hi:[1,0]\n\t"
    "v_pk_mul_f32 v[86:87], v[86:87], s[76:77] op_sel_hi:[1,0]\n\t"
    "v_cvt_pk_bf16_f32 v36, v84, v85\n\t"
    "v_cvt_pk_bf16_f32 v37, v86, v87\n\t"
    "v_pk_mul_f32 v[92:93], v[92:93], s[76:77] op_sel_hi:[1,0]\n\t"
    "v_pk_mul_f32 v[94:95], v[94:95], s[76:77] op_sel_hi:[1,0]\n\t"
    "v_cvt_pk_bf16_f32 v38, v92, v93\n\t"
    "v_cvt_pk_bf16_f32 v39, v94, v95\n\t"
    "ds_write_b128 v34, v[36:39] offset:2048\n\t"
    "v_mfma_f32_16x16x32_bf16 v[140:143], a[76:79], a[52:55], v[140:143]\n\t"
    "v_mfma_f32_16x16x32_bf16 v[148:151], a[80:83], a[52:55], v[148:151]\n\t"
    "buffer_load_dwordx4 v24, s[40:43], s52 offen offset:2048 lds\n\t"
    "v_pk_mul_f32 v[100:101], v[100:101], s[76:77] op_sel_hi:[1,0]\n\t"
    "v_pk_mul_f32 v[102:103], v[102:103], s[76:77] op_sel_hi:[1,0]\n\t"
    "v_cvt_pk_bf16_f32 v40, v100, v101\n\t"
    "v_cvt_pk_bf16_f32 v41, v102, v103\n\t"
    "v_pk_mul_f32 v[108:109], v[108:109], s[76:77] op_sel_hi:[1,0]\n\t"
    "v_pk_mul_f32 v[110:111], v[110:111], s[76:77] op_sel_hi:[1,0]\n\t"
    "v_cvt_pk_bf16_f32 v42, v108, v109\n\t"
    "v_cvt_pk_bf16_f32 v43, v110, v111\n\t"
    "ds_write_b128 v34, v[40:43] offset:3072\n\t"
    "v_mfma_f32_16x16x32_bf16 v[156:159], a[84:87], a[52:55], v[156:159]\n\t"
    "v_mfma_f32_16x16x32_bf16 v[164:167], a[88:91], a[52:55], v[164:167]\n\t"
    "buffer_load_dwordx4 v25, s[40:43], s52 offen offset:3072 lds\n\t"
    "v_pk_mul_f32 v[116:117], v[116:117], s[76:77] op_sel_hi:[1,0]\n\t"
    "v_pk_mul_f32 v[118:119], v[118:119], s[76:77] op_sel_hi:[1,0]\n\t"
    "v_cvt_pk_bf16_f32 v36, v116, v117\n\t"
    "v_cvt_pk_bf16_f32 v37, v118, v119\n\t"
    "v_pk_mul_f32 v[124:125], v[124:125], s[76:77] op_sel_hi:[1,0]\n\t"
    "v_pk_mul_f32 v[126:127], v[126:127], s[76:77] op_sel_hi:[1,0]\n\t"
    "v_cvt_pk_bf16_f32 v38, v124, v125\n\t"
    "v_cvt_pk_bf16_f32 v39, v126, v127\n\t"
    "ds_write_b128 v34, v[36:39] offset:4096\n\t"
    "v_mfma_f32_16x16x32_bf16 v[172:175], a[92:95], a[52:55], v[172:175]\n\t"
    "s_add_u32 m0, s57, 0x1000\n\t"
    "v_mfma_f32_16x16x32_bf16 v[180:183], a[64:67], a[56:59], v[180:183]\n\t"
    "buffer_load_dwordx4 v26, s[40:43], s52 offen offset:0 lds\n\t"
    "v_pk_mul_f32 v[132:133], v[132:133], s[76:77] op_sel_hi:[1,0]\n\t"
    "v_pk_mul_f32 v[134:135], v[134:135], s[76:77] op_sel_hi:[1,0]\n\t"
    "v_cvt_pk_bf16_f32 v40, v132, v133\n\t"
    "v_cvt_pk_bf16_f32 v41, v134, v135\n\t"
    "v_pk_mul_f32 v[140:141], v[140:141], s[76:77] op_sel_hi:[1,0]\n\t"
    "v_pk_mul_f32 v[142:143], v[142:143], s[76:77] op_sel_hi:[1,0]\n\t"
    "v_cvt_pk_bf16_f32 v42, v140, v141\n\t"
    "v_cvt_pk_bf16_f32 v43, v142, v143\n\t"
    "ds_write_b128 v34, v[40:43] offset:5120\n\t"
    "v_mfma_f32_16x16x32_bf16 v[188:191], a[68:71], a[56:59], v[188:191]\n\t"
    "v_mfma_f32_16x16x32_bf16 v[196:199], a[72:75], a[56:59], v[196:199]\n\t"
    "buffer_load_dwordx4 v27, s[40:43], s52 offen offset:1024 lds\n\t"
    "v_pk_mul_f32 v[148:149], v[148:149], s[76:77] op_sel_hi:[1,0]\n\t"
    "v_pk_mul_f32 v[150:151], v[150:151], s[76:77] op_sel_hi:[1,0]\n\t"
    "v_cvt_pk_bf16_f32 v36, v148, v149\n\t"
    "v_cvt_pk_bf16_f32 v37, v150, v151\n\t"
    "v_pk_mul_f32 v[156:157], v[156:157], s[76:77] op_sel_hi:[1,0]\n\t"
    "v_pk_mul_f32 v[158:159], v[158:159], s[76:77] op_sel_hi:[1,0]\n\t"
    "v_cvt_pk_bf16_f32 v38, v156, v157\n\t"
    "v_cvt_pk_bf16_f32 v39, v158, v159\n\t"
    "ds_write_b128 v34, v[36:39] offset:6144\n\t"
    "v_mfma_f32_16x16x32_bf16 v[204:207], a[76:79], a[56:59], v[204:207]\n\t"
    "v_mfma_f32_16x16x32_bf16 v[212:215], a[80:83], a[56:59], v[212:215]\n\t"
    "v_pk_mul_f32 v[164:165], v[164:165], s[76:77] op_sel_hi:[1,0]\n\t"
    "v_pk_mul_f32 v[166:167], v[166:167], s[76:77] op_sel_hi:[1,0]\n\t"
    "v_cvt_pk_bf16_f32 v40, v164, v165\n\t"
    "v_cvt_pk_bf16_f32 v41, v166, v167\n\t"
    "v_pk_mul_f32 v[172:173], v[172:173], s[76:77] op_sel_hi:[1,0]\n\t"
    "v_pk_mul_f32 v[174:175], v[174:175], s[76:77] op_sel_hi:[1,0]\n\t"
    "v_cvt_pk_bf16_f32 v42, v172, v173\n\t"
    "v_cvt_pk_bf16_f32 v43, v174, v175\n\t"
    "ds_write_b128 v34, v[40:43] offset:7168\n\t"
    "v_mfma_f32_16x16x32_bf16 v[220:223], a[84:87], a[56:59], v[220:223]\n\t"
    "buffer_load_dwordx4 v28, s[40:43], s52 offen offset:2048 lds\n\t"
    "v_mfma_f32_16x16x32_bf16 v[228:231], a[88:91], a[56:59], v[228:231]\n\t"
    "v_pk_mul_f32 v[180:181], v[180:181], s[76:77] op_sel_hi:[1,0]\n\t"
    "v_pk_mul_f32 v[182:183], v[182:183], s[76:77] op_sel_hi:[1,0]\n\t"
    "v_cvt_pk_bf16_f32 v36, v180, v181\n\t"
    "v_cvt_pk_bf16_f32 v37, v182, v183\n\t"
    "v_pk_mul_f32 v[188:189], v[188:189], s[76:77] op_sel_hi:[1,0]\n\t"
    "v_pk_mul_f32 v[190:191], v[190:191], s[76:77] op_sel_hi:[1,0]\n\t"
    "v_cvt_pk_bf16_f32 v38, v188, v189\n\t"
    "v_cvt_pk_bf16_f32 v39, v190, v191\n\t"
    "ds_write_b128 v34, v[36:39] offset:8192\n\t"
    "v_mfma_f32_16x16x32_bf16 v[236:239], a[92:95], a[56:59], v[236:239]\n\t"
    "buffer_load_dwordx4 v29, s[40:43], s52 offen offset:3072 lds\n\t"
    "v_mfma_f32_16x16x32_bf16 v[244:247], a[64:67], a[60:63], v[244:247]\n\t"
    "s_cmp_eq_u32 s59, 0\n\t"
    "s_mov_b32 s61, 0xa000\n\t"
    "s_cselect_b32 s61, s61, 0x1800\n\t"
    "v_pk_mul_f32 v[196:197], v[196:197], s[76:77] op_sel_hi:[1,0]\n\t"
    "v_pk_mul_f32 v[198:199], v[198:199], s[76:77] op_sel_hi:[1,0]\n\t"
    "v_cvt_pk_bf16_f32 v40, v196, v197\n\t"
    "v_cvt_pk_bf16_f32 v41, v198, v199\n\t"
    "v_pk_mul_f32 v[204:205], v[204:205], s[76:77] op_sel_hi:[1,0]\n\t"
    "v_pk_mul_f32 v[206:207], v[206:207], s[76:77] op_sel_hi:[1,0]\n\t"
    "v_cvt_pk_bf16_f32 v42, v204, v205\n\t"
    "v_cvt_pk_bf16_f32 v43, v206, v207\n\t"
    "ds_write_b128 v34, v[40:43] offset:9216\n\t"
    "v_mfma_f32_16x16x32_bf16 v[252:255], a[68:71], a[60:63], v[252:255]\n\t"
    "buffer_store_dwordx4 v[240:243], %12, s[48:51], s61 offen offset:0 sc1\n\t"
    "v_mfma_f32_16x16x32_bf16 a[176:179], a[72:75], a[60:63], a[176:179]\n\t"
    "s_cmp_eq_u32 s59, 0\n\t"
    "s_mov_b32 s61, 0xa400\n\t"
    "s_cselect_b32 s61, s61, 0x3800\n\t"
    "v_pk_mul_f32 v[212:213], v[212:213], s[76:77] op_sel_hi:[1,0]\n\t"
    "v_pk_mul_f32 v[214:215], v[214:215], s[76:77] op_sel_hi:[1,0]\n\t"
    "v_cvt_pk_bf16_f32 v36, v212, v213\n\t"
    "v_cvt_pk_bf16_f32 v37, v214, v215\n\t"
    "v_pk_mul_f32 v[220:221], v[220:221], s[76:77] op_sel_hi:[1,0]\n\t"
    "v_pk_mul_f32 v[222:223], v[222:223], s[76:77] op_sel_hi:[1,0]\n\t"
    "v_cvt_pk_bf16_f32 v38, v220, v221\n\t"
    "v_cvt_pk_bf16_f32 v39, v222, v223\n\t"
    "ds_write_b128 v34, v[36:39] offset:10240\n\t"
    "v_mfma_f32_16x16x32_bf16 a[180:183], a[76:79], a[60:63], a[180:183]\n\t"
    "buffer_store_dwordx4 v[248:251], %12, s[48:51], s61 offen offset:0 sc1\n\t"
    "v_mfma_f32_16x16x32_bf16 a[184:187], a[80:83], a[60:63], a[184:187]\n\t"
    "v_pk_mul_f32 v[228:229], v[228:229], s[76:77] op_sel_hi:[1,0]\n\t"
    "v_pk_mul_f32 v[230:231], v[230:231], s[76:77] op_sel_hi:[1,0]\n\t"
    "v_cvt_pk_bf16_f32 v40, v228, v229\n\t"
    "v_cvt_pk_bf16_f32 v41, v230, v231\n\t"
    "v_pk_mul_f32 v[236:237], v[236:237], s[76:77] op_sel_hi:[1,0]\n\t"
    "v_pk_mul_f32 v[238:239], v[238:239], s[76:77] op_sel_hi:[1,0]\n\t"
    "v_cvt_pk_bf16_f32 v42, v236, v237\n\t"
    "v_cvt_pk_bf16_f32 v43, v238, v239\n\t"
    "ds_write_b128 v34, v[40:43] offset:11264\n\t"
    "v_mfma_f32_16x16x32_bf16 a[188:191], a[84:87], a[60:63], a[188:191]\n\t"
    "v_mfma_f32_16x16x32_bf16 a[192:195], a[88:91], a[60:63], a[192:195]\n\t"
    "v_pk_mul_f32 v[244:245], v[244:245], s[76:77] op_sel_hi:[1,0]\n\t"
    "v_pk_mul_f32 v[246:247], v[246:247], s[76:77] op_sel_hi:[1,0]\n\t"
    "v_cvt_pk_bf16_f32 v36, v244, v245\n\t"
    "v_cvt_pk_bf16_f32 v37, v246, v247\n\t"
    "v_pk_mul_f32 v[252:253], v[252:253], s[76:77] op_sel_hi:[1,0]\n\t"
    "v_pk_mul_f32 v[254:255], v[254:255], s[76:77] op_sel_hi:[1,0]\n\t"
    "v_cvt_pk_bf16_f32 v38, v252, v253\n\t"
    "v_cvt_pk_bf16_f32 v39, v254, v255\n\t"
    "ds_write_b128 v34, v[36:39] offset:12288\n\t"
    "v_mfma_f32_16x16x32_bf16 a[196:199], a[92:95], a[60:63], a[196:199]\n\t"
    "v_accvgpr_read_b32 v16, a176\n\t"
    "v_accvgpr_read_b32 v17, a177\n\t"
    "v_accvgpr_read_b32 v18, a178\n\t"
    "v_accvgpr_read_b32 v19, a179\n\t"
    "v_pk_mul_f32 v[16:17], v[16:17], s[76:77] op_sel_hi:[1,0]\n\t"
    "v_pk_mul_f32 v[18:19], v[18:19], s[76:77] op_sel_hi:[1,0]\n\t"
    "v_cvt_pk_bf16_f32 v40, v16, v17\n\t"
    "v_cvt_pk_bf16_f32 v41, v18, v19\n\t"
    "v_accvgpr_read_b32 v30, a180\n\t"
    "v_accvgpr_read_b32 v31, a181\n\t"
    "v_accvgpr_read_b32 v32, a182\n\t"
    "v_accvgpr_read_b32 v33, a183\n\t"
    "v_pk_mul_f32 v[30:31], v[30:31], s[76:77] op_sel_hi:[1,0]\n\t"
    "v_pk_mul_f32 v[32:33], v[32:33], s[76:77] op_sel_hi:[1,0]\n\t"
    "v_cvt_pk_bf16_f32 v42, v30, v31\n\t"
    "v_cvt_pk_bf16_f32 v43, v32, v33\n\t"
    "ds_write_b128 v34, v[40:43] offset:13312\n\t"
    "v_accvgpr_read_b32 v16, a184\n\t"
    "v_accvgpr_read_b32 v17, a185\n\t"
    "v_accvgpr_read_b32 v18, a186\n\t"
    "v_accvgpr_read_b32 v19, a187\n\t"
    "v_pk_mul_f32 v[16:17], v[16:17], s[76:77] op_sel_hi:[1,0]\n\t"
    "v_pk_mul_f32 v[18:19], v[18:19], s[76:77] op_sel_hi:[1,0]\n\t"
    "v_cvt_pk_bf16_f32 v36, v16, v17\n\t"
    "v_cvt_pk_bf16_f32 v37, v18, v19\n\t"
    "v_accvgpr_read_b32 v30, a188\n\t"
    "v_accvgpr_read_b32 v31, a189\n\t"
    "v_accvgpr_read_b32 v32, a190\n\t"
    "v_accvgpr_read_b32 v33, a191\n\t"
    "v_pk_mul_f32 v[30:31], v[30:31], s[76:77] op_sel_hi:[1,0]\n\t"
    "v_pk_mul_f32 v[32:33], v[32:33], s[76:77] op_sel_hi:[1,0]\n\t"
    "v_cvt_pk_bf16_f32 v38, v30, v31\n\t"
    "v_cvt_pk_bf16_f32 v39, v32, v33\n\t"
    "ds_write_b128 v34, v[36:39] offset:14336\n\t"
    "v_accvgpr_read_b32 v16, a192\n\t"
    "v_accvgpr_read_b32 v17, a193\n\t"
    "v_accvgpr_read_b32 v18, a194\n\t"
    "v_accvgpr_read_b32 v19, a195\n\t"
    "v_pk_mul_f32 v[16:17], v[16:17], s[76:77] op_sel_hi:[1,0]\n\t"
    "v_pk_mul_f32 v[18:19], v[18:19], s[76:77] op_sel_hi:[1,0]\n\t"
    "v_cvt_pk_bf16_f32 v40, v16, v17\n\t"
    "v_cvt_pk_bf16_f32 v41, v18, v19\n\t"
    "v_accvgpr_read_b32 v30, a196\n\t"
    "v_accvgpr_read_b32 v31, a197\n\t"
    "v_accvgpr_read_b32 v32, a198\n\t"
    "v_accvgpr_read_b32 v33, a199\n\t"
    "v_pk_mul_f32 v[30:31], v[30:31], s[76:77] op_sel_hi:[1,0]\n\t"
    "v_pk_mul_f32 v[32:33], v[32:33], s[76:77] op_sel_hi:[1,0]\n\t"
    "v_cvt_pk_bf16_f32 v42, v30, v31\n\t"
    "v_cvt_pk_bf16_f32 v43, v32, v33\n\t"
    "ds_write_b128 v34, v[40:43] offset:15360\n\t"
    "s_waitcnt lgkmcnt(0)\n\t"
    "s_nop 1\n\t"
      : "=&s"(tag)
      : "s"(w), "s"(uni64(ckv + offR)), "s"(2048u * 1024u - offR), "s"(uni64(((w >> 1) ? wuv : wuk) + offT)), "s"(512u * 8192u - offT),
        "s"(uni64(ws + WS_KV + (size_t)h * (1u << 20) + (4 * xb + 2 * (w >> 1)) * 32768u + (w & 1) * 16384u)),
        "v"((w >> 1) * 16384 + c15 * 128 + ((g4 ^ ((c15 >> 1) & 7)) * 16)), "v"((w & 1) * 16384 + (l >> 5) * 1024 + r8 * 128 + (r8 >> 1) * 32 + 8 * (l & 3)),
        "v"(dr * 1024 + ((dc ^ (dr >> 1)) * 16)), "v"(dr * 1024 + ((dc ^ (4 + (dr >> 1))) * 16)),
        "v"((8 * (dr >> 2) + (dr & 3)) * 8192 + (((((dc >> 1) ^ ((dr >> 1) & 3)) << 1) | (dc & 1)) * 16)), "v"(l * 16),
        "s"(uni64(x + offRq)), "s"(2048u * 1152u - offRq), "s"(uni64(wq + offTq)), "s"(576u * 8192u - offTq),
        "s"(pRb), "s"(2048u * 1152u - offRb), "s"(pTb), "s"(576u * 3456u - offTb), "v"(bt_voff(l)), "s"(uni64(bwphi + (bid >> 3) * 648)),
        "s"(uni64((bid < 83 ? x : wq) + (po < pn ? po : 0u))), "s"(RFL(po < pn ? pn - po : 0u)), "s"(uni64(ws + WS_FLAG + FL_SEED)),
        "s"(uni64((bid < 116 ? bx : bwqkv) + (po2 < pn2 ? po2 : 0u))), "s"(RFL(po2 < pn2 ? pn2 - po2 : 0u)) DBGOP(0)
      : "v14", "v15", "v16", "v17", "v18", "v19", "v20", "v21", "v22", "v23", "v24", "v25", "v26", "v27", "v28", "v29", "v30", "v31", "v32", "v33", "v34", "v35", "v36", "v37", "v38", "v39", "v40", "v41", "v42", "v43", "v44", "v45", "v46", "v47", "v48", "v49", "v50", "v51", "v52", "v53", "v54", "v55", "v56", "v57", "v58", "v59", "v60", "v61", "v62", "v63", "v64", "v65", "v66", "v67", "v68", "v69", "v70", "v71", "v72", "v73", "v74", "v75", "v76", "v77", "v78", "v79", "v80", "v81", "v82", "v83", "v84", "v85", "v86", "v87", "v88", "v89", "v90", "v91", "v92", "v93", "v94", "v95", "v96", "v97", "v98", "v99", "v100", "v101", "v102", "v103", "v104", "v105", "v106", "v107", "v108", "v109", "v110", "v111", "v112", "v113", "v114", "v115", "v116", "v117", "v118", "v119", "v120", "v121", "v122", "v123", "v124", "v125", "v126", "v127", "v128", "v129", "v130", "v131", "v132", "v133", "v134", "v135", "v136", "v137", "v138", "v139", "v140", "v141", "v142", "v143", "v144", "v145", "v146", "v147", "v148", "v149", "v150", "v151", "v152", "v153", "v154", "v155", "v156", "v157", "v158", "v159", "v160", "v161", "v162", "v163", "v164", "v165", "v166", "v167", "v168", "v169", "v170", "v171", "v172", "v173", "v174", "v175", "v176", "v177", "v178", "v179", "v180", "v181", "v182", "v183", "v184", "v185", "v186", "v187", "v188", "v189", "v190", "v191", "v192", "v193", "v194", "v195", "v196", "v197", "v198", "v199", "v200", "v201", "v202", "v203", "v204", "v205", "v206", "v207", "v208", "v209", "v210", "v211", "v212", "v213", "v214", "v215", "v216", "v217", "v218", "v219", "v220", "v221", "v222", "v223", "v224", "v225", "v226", "v227", "v228", "v229", "v230", "v231", "v232", "v233", "v234", "v235", "v236", "v237", "v238", "v239", "v240", "v241", "v242", "v243", "v244", "v245", "v246", "v247", "v248", "v249", "v250", "v251", "v252", "v253", "v254", "v255", "a0", "a1", "a2", "a3", "a4", "a5", "a6", "a7", "a8", "a9", "a10", "a11", "a12", "a13", "a14", "a15", "a16", "a17", "a18", "a19", "a20", "a21", "a22", "a23", "a24", "a25", "a26", "a27", "a28", "a29", "a30", "a31", "a32", "a33", "a34", "a35", "a36", "a37", "a38", "a39", "a40", "a41", "a42", "a43", "a44", "a45", "a46", "a47", "a48", "a49", "a50", "a51", "a52", "a53", "a54", "a55", "a56", "a57", "a58", "a59", "a60", "a61", "a62", "a63", "a64", "a65", "a66", "a67", "a68", "a69", "a70", "a71", "a72", "a73", "a74", "a75", "a76", "a77", "a78", "a79", "a80", "a81", "a82", "a83", "a84", "a85", "a86", "a87", "a88", "a89", "a90", "a91", "a92", "a93", "a94", "a95", "a96", "a97", "a98", "a99", "a100", "a101", "a102", "a103", "a104", "a105", "a106", "a107", "a108", "a109", "a110", "a111", "a112", "a113", "a114", "a115", "a116", "a117", "a118", "a119", "a120", "a121", "a122", "a123", "a124", "a125", "a126", "a127", "a128", "a129", "a130", "a131", "a132", "a133", "a134", "a135", "a136", "a137", "a138", "a139", "a140", "a141", "a142", "a143", "a144", "a145", "a146", "a147", "a148", "a149", "a150", "a151", "a152", "a153", "a154", "a155", "a156", "a157", "a158", "a159", "a160", "a161", "a162", "a163", "a164", "a165", "a166", "a167", "a168", "a169", "a170", "a171", "a172", "a173", "a174", "a175", "a176", "a177", "a178", "a179", "a180", "a181", "a182", "a183", "a184", "a185", "a186", "a187", "a188", "a189", "a190", "a191", "a192", "a193", "a194", "a195", "a196", "a197", "a198", "a199", "a200", "a201", "a202", "a203", "a204", "a205", "a206", "a207", "a208", "a209", "a210", "a211", "a212", "a213", "a214", "a215", "a216", "a217", "a218", "a219", "a220", "a221", "a222", "a223", "a224", "a225", "a226", "a227", "a228", "a229", "a230", "a231", "a232", "a233", "a234", "a235", "a236", "a237", "a238", "a239", "a240", "a241", "a242", "a243", "a244", "a245", "a246", "a247", "a248", "a249", "a250", "a251", "a252", "a253", "a254", "a255", "s40", "s41", "s42", "s43", "s44", "s45", "s46", "s47", "s48", "s49", "s50", "s51", "s52", "s53", "s54", "s55", "s56", "s57", "s58", "s59", "s60", "s61", "s62", "s63", "s64", "s65", "s66", "s67", "s68", "s69", "s70", "s71", "s72", "s73", "s74", "s75", "s76", "s77", "s78", "s79", "s80", "s81", "vcc", "scc", "memory");
  }
  tag += 1;
  const u64 pKV = uni64(ws + WS_KV + (size_t)h * (1u << 20) + g * 32768u + qh * 8192u);
  {
    const int l = fresh_lane(), hb = bid >> 3;
    const unsigned c15 = l & 15, g4 = l >> 4, r8 = ((l >> 4) & 1) * 4 + ((l >> 2) & 3), p4 = l & 3, o = ((36u * hb) & 15u) - 4 * (hb & 1);
    CODE_WARM(1, "a255", (bid >> 3) * 4096 + w * 1024 + l * 16);
    asm volatile(
    "; ---- b\n\t"
    "s_mov_b64 s[40:41], %1\n\t"
    "s_mov_b32 s42, %2\n\t"
    "s_mov_b32 s43, 0x20000\n\t"
    "s_mov_b64 s[44:45], %3\n\t"
    "s_mov_b32 s46, %4\n\t"
    "s_mov_b32 s47, 0x20000\n\t"
    "s_mov_b64 s[48:49], %9\n\t"
    "s_mov_b32 s50, 0x40000\n\t"
    "s_mov_b32 s51, 0x20000\n\t"
    "s_mov_b64 s[52:53], %13\n\t"
    "s_mov_b32 s54, 0x100000\n\t"
    "s_mov_b32 s55, 0x20000\n\t"
    "s_mul_i32 s57, %0, 0xc00\n\t"
    "s_lshl_b32 s62, %0, 13\n\t"
    "v_mbcnt_lo_u32_b32 v244, -1, 0\n\t"
    "v_mbcnt_hi_u32_b32 v244, -1, v244\n\t"
    "v_lshrrev_b32 v241, 3, v244\n\t"
    "v_lshrrev_b32 v242, 4, v244\n\t"
    "v_xor_b32 v242, v242, v244\n\t"
    "v_and_b32 v242, 7, v242\n\t"
    "v_lshlrev_b32 v242, 4, v242\n\t"
    "v_mul_u32_u24 v241, 0x480, v241\n\t"
    "v_add_u32 v232, v241, v242\n\t"
    "v_xor_b32 v242, 64, v242\n\t"
    "v_add_u32 v233, v241, v242\n\t"
    "v_add_u32 v239, 0xf000, v233\n\t"
    "v_add_u32 v238, 0xd000, v232\n\t"
    "v_add_u32 v237, 0xb000, v233\n\t"
    "v_add_u32 v236, 0x9000, v232\n\t"
    "v_add_u32 v235, 0x6000, v233\n\t"
    "v_add_u32 v234, 0x4000, v232\n\t"
    "v_add_u32 v233, 0x2000, v233\n\t"
    "v_lshlrev_b32 v244, 4, v244\n\t"
    "v_add_u32 v245, 0x1000, v244\n\t"
    "v_xor_b32 v240, 64, %5\n\t"
    "s_waitcnt vmcnt(2)\n\t"
    "s_barrier\n\t"
    "ds_read_b128 a[32:35], %5 offset:32768\n\t"
    "ds_read_b128 a[36:39], %5 offset:34816\n\t"
    "ds_read_b128 a[40:43], %5 offset:36864\n\t"
    "ds_read_b128 a[44:47], %5 offset:38912\n\t"
    "ds_read_b128 a[48:51], v240 offset:32768\n\t"
    "ds_read_b128 a[52:55], v240 offset:34816\n\t"
    "ds_read_b128 a[56:59], v240 offset:36864\n\t"
    "ds_read_b128 a[60:63], v240 offset:38912\n\t"
    "ds_read_b64_tr_b16 a[0:1], %6 offset:0\n\t"
    "ds_read_b64_tr_b16 a[2:3], %6 offset:3072\n\t"
    "ds_read_b64_tr_b16 a[4:5], %6 offset:1024\n\t"
    "ds_read_b64_tr_b16 a[6:7], %6 offset:4096\n\t"
    "ds_read_b64_tr_b16 a[8:9], %6 offset:2048\n\t"
    "ds_read_b64_tr_b16 a[10:11], %6 offset:5120\n\t"
    "ds_read_b64_tr_b16 a[12:13], %7 offset:0\n\t"
    "ds_read_b64_tr_b16 a[14:15], %7 offset:3072\n\t"
    "s_waitcnt lgkmcnt(8)\n\t"
    "s_mov_b32 s58, 0x100\n\t"
    "s_add_u32 m0, s62, 0x8000\n\t"
    "s_nop 0\n\t"
    "buffer_load_dwordx4 v232, s[40:43], s58 offen offset:0 lds\n\t"
    "buffer_load_dwordx4 v233, s[40:43], s58 offen offset:1024 lds\n\t"
    "buffer_load_dwordx4 v234, s[40:43], s58 offen offset:2048 lds\n\t"
    "buffer_load_dwordx4 v235, s[40:43], s58 offen offset:3072 lds\n\t"
    "s_add_u32 m0, s62, 0x9000\n\t"
    "s_nop 0\n\t"
    "buffer_load_dwordx4 v236, s[40:43], s58 offen offset:0 lds\n\t"
    "buffer_load_dwordx4 v237, s[40:43], s58 offen offset:1024 lds\n\t"
    "buffer_load_dwordx4 v238, s[40:43], s58 offen offset:2048 lds\n\t"
    "buffer_load_dwordx4 v239, s[40:43], s58 offen offset:3072 lds\n\t"
    "s_waitcnt lgkmcnt(4)\n\t"
    "v_mfma_f32_16x16x32_bf16 v[32:35], a[0:3], a[32:35], 0\n\t"
    "ds_read_b64_tr_b16 a[16:17], %6 offset:6144\n\t"
    "v_mfma_f32_16x16x32_bf16 v[36:39], a[4:7], a[32:35], 0\n\t"
    "ds_read_b64_tr_b16 a[18:19], %6 offset:9216\n\t"
    "s_waitcnt lgkmcnt(2)\n\t"
    "v_mfma_f32_16x16x32_bf16 v[40:43], a[8:11], a[32:35], 0\n\t"
    "ds_read_b64_tr_b16 a[20:21], %6 offset:7168\n\t"
    "v_mfma_f32_16x16x32_bf16 v[44:47], a[12:15], a[32:35], 0\n\t"
    "ds_read_b64_tr_b16 a[22:23], %6 offset:10240\n\t"
    "v_mfma_f32_16x16x32_bf16 v[48:51], a[0:3], a[36:39], 0\n\t"
    "ds_read_b64_tr_b16 a[24:25], %6 offset:8192\n\t"
    "v_mfma_f32_16x16x32_bf16 v[52:55], a[4:7], a[36:39], 0\n\t"
    "ds_read_b64_tr_b16 a[26:27], %6 offset:11264\n\t"
    "v_mfma_f32_16x16x32_bf16 v[56:59], a[8:11], a[36:39], 0\n\t"
    "ds_read_b64_tr_b16 a[28:29], %7 offset:6144\n\t"
    "v_mfma_f32_16x16x32_bf16 v[60:63], a[12:15], a[36:39], 0\n\t"
    "ds_read_b64_tr_b16 a[30:31], %7 offset:9216\n\t"
    "v_mfma_f32_16x16x32_bf16 v[64:67], a[0:3], a[40:43], 0\n\t"
    "v_mfma_f32_16x16x32_bf16 v[68:71], a[4:7], a[40:43], 0\n\t"
    "v_mfma_f32_16x16x32_bf16 v[72:75], a[8:11], a[40:43], 0\n\t"
    "v_mfma_f32_16x16x32_bf16 v[76:79], a[12:15], a[40:43], 0\n\t"
    "v_mfma_f32_16x16x32_bf16 v[80:83], a[0:3], a[44:47], 0\n\t"
    "v_mfma_f32_16x16x32_bf16 v[84:87], a[4:7], a[44:47], 0\n\t"
    "v_mfma_f32_16x16x32_bf16 v[88:91], a[8:11], a[44:47], 0\n\t"
    "v_mfma_f32_16x16x32_bf16 v[92:95], a[12:15], a[44:47], 0\n\t"
    "s_waitcnt lgkmcnt(0)\n\t"
    "s_barrier\n\t"
    "v_mfma_f32_16x16x32_bf16 v[32:35], a[16:19], a[48:51], v[32:35]\n\t"
    "ds_read_b128 a[64:67], %5 offset:0\n\t"
    "ds_read_b128 a[68:71], %5 offset:2048\n\t"
    "ds_read_b128 a[72:75], %5 offset:4096\n\t"
    "s_add_u32 m0, s57, 0x10000\n\t"
    "s_mov_b32 s56, 0x6c000\n\t"
    "v_mfma_f32_16x16x32_bf16 v[36:39], a[20:23], a[48:51], v[36:39]\n\t"
    "ds_read_b128 a[76:79], %5 offset:6144\n\t"
    "ds_read_b128 a[80:83], v240 offset:0\n\t"
    "ds_read_b128 a[84:87], v240 offset:2048\n\t"
    "buffer_load_dwordx4 %8, s[44:47], s56 offen offset:0 lds\n\t"
    "s_mov_b32 s56, 0x6c080\n\t"
    "v_mfma_f32_16x16x32_bf16 v[40:43], a[24:27], a[48:51], v[40:43]\n\t"
    "ds_read_b128 a[88:91], v240 offset:4096\n\t"
    "ds_read_b128 a[92:95], v240 offset:6144\n\t"
    "buffer_load_dwordx4 %8, s[44:47], s56 offen offset:1024 lds\n\t"
    "v_mfma_f32_16x16x32_bf16 v[44:47], a[28:31], a[48:51], v[44:47]\n\t"
    "ds_read_b64_tr_b16 a[0:1], %6 offset:32768\n\t"
    "ds_read_b64_tr_b16 a[2:3], %6 offset:35840\n\t"
    "ds_read_b64_tr_b16 a[4:5], %6 offset:33792\n\t"
    "s_mov_b32 s56, 0x6c100\n\t"
    "v_mfma_f32_16x16x32_bf16 v[48:51], a[16:19], a[52:55], v[48:51]\n\t"
    "ds_read_b64_tr_b16 a[6:7], %6 offset:36864\n\t"
    "ds_read_b64_tr_b16 a[8:9], %6 offset:34816\n\t"
    "ds_read_b64_tr_b16 a[10:11], %6 offset:37888\n\t"
    "buffer_load_dwordx4 %8, s[44:47], s56 offen offset:2048 lds\n\t"
    "v_mfma_f32_16x16x32_bf16 v[52:55], a[20:23], a[52:55], v[52:55]\n\t"
    "ds_read_b64_tr_b16 a[12:13], %7 offset:32768\n\t"
    "ds_read_b64_tr_b16 a[14:15], %7 offset:35840\n\t"
    "v_mfma_f32_16x16x32_bf16 v[56:59], a[24:27], a[52:55], v[56:59]\n\t"
    "v_mfma_f32_16x16x32_bf16 v[60:63], a[28:31], a[52:55], v[60:63]\n\t"
    "s_waitcnt lgkmcnt(8)\n\t"
    "s_mov_b32 s58, 0x180\n\t"
    "s_add_u32 m0, s62, 0\n\t"
    "v_mfma_f32_16x16x32_bf16 v[64:67], a[16:19], a[56:59], v[64:67]\n\t"
    "buffer_load_dwordx4 v232, s[40:43], s58 offen offset:0 lds\n\t"
    "v_mfma_f32_16x16x32_bf16 v[68:71], a[20:23], a[56:59], v[68:71]\n\t"
    "buffer_load_dwordx4 v233, s[40:43], s58 offen offset:1024 lds\n\t"
    "v_mfma_f32_16x16x32_bf16 v[72:75], a[24:27], a[56:59], v[72:75]\n\t"
    "buffer_load_dwordx4 v234, s[40:43], s58 offen offset:2048 lds\n\t"
    "v_mfma_f32_16x16x32_bf16 v[76:79], a[28:31], a[56:59], v[76:79]\n\t"
    "buffer_load_dwordx4 v235, s[40:43], s58 offen offset:3072 lds\n\t"
    "s_add_u32 m0, s62, 0x1000\n\t"
    "v_mfma_f32_16x16x32_bf16 v[80:83], a[16:19], a[60:63], v[80:83]\n\t"
    "buffer_load_dwordx4 v236, s[40:43], s58 offen offset:0 lds\n\t"
    "v_mfma_f32_16x16x32_bf16 v[84:87], a[20:23], a[60:63], v[84:87]\n\t"
    "buffer_load_dwordx4 v237, s[40:43], s58 offen offset:1024 lds\n\t"
    "v_mfma_f32_16x16x32_bf16 v[88:91], a[24:27], a[60:63], v[88:91]\n\t"
    "buffer_load_dwordx4 v238, s[40:43], s58 offen offset:2048 lds\n\t"
    "v_mfma_f32_16x16x32_bf16 v[92:95], a[28:31], a[60:63], v[92:95]\n\t"
    "buffer_load_dwordx4 v239, s[40:43], s58 offen offset:3072 lds\n\t"
    "s_waitcnt lgkmcnt(0)\n\t"
    "v_mfma_f32_16x16x32_bf16 v[32:35], a[0:3], a[64:67], v[32:35]\n\t"
    "ds_read_b64_tr_b16 a[16:17], %6 offset:38912\n\t"
    "v_mfma_f32_16x16x32_bf16 v[36:39], a[4:7], a[64:67], v[36:39]\n\t"
    "ds_read_b64_tr_b16 a[18:19], %6 offset:41984\n\t"
    "v_mfma_f32_16x16x32_bf16 v[40:43], a[8:11], a[64:67], v[40:43]\n\t"
    "ds_read_b64_tr_b16 a[20:21], %6 offset:39936\n\t"
    "v_mfma_f32_16x16x32_bf16 v[44:47], a[12:15], a[64:67], v[44:47]\n\t"
    "ds_read_b64_tr_b16 a[22:23], %6 offset:43008\n\t"
    "v_mfma_f32_16x16x32_bf16 v[48:51], a[0:3], a[68:71], v[48:51]\n\t"
    "ds_read_b64_tr_b16 a[24:25], %6 offset:40960\n\t"
    "v_mfma_f32_16x16x32_bf16 v[52:55], a[4:7], a[68:71], v[52:55]\n\t"
    "ds_read_b64_tr_b16 a[26:27], %6 offset:44032\n\t"
    "v_mfma_f32_16x16x32_bf16 v[56:59], a[8:11], a[68:71], v[56:59]\n\t"
    "ds_read_b64_tr_b16 a[28:29], %7 offset:38912\n\t"
    "v_mfma_f32_16x16x32_bf16 v[60:63], a[12:15], a[68:71], v[60:63]\n\t"
    "ds_read_b64_tr_b16 a[30:31], %7 offset:41984\n\t"
    "v_mfma_f32_16x16x32_bf16 v[64:67], a[0:3], a[72:75], v[64:67]\n\t"
    "v_mfma_f32_16x16x32_bf16 v[68:71], a[4:7], a[72:75], v[68:71]\n\t"
    "v_mfma_f32_16x16x32_bf16 v[72:75], a[8:11], a[72:75], v[72:75]\n\t"
    "v_mfma_f32_16x16x32_bf16 v[76:79], a[12:15], a[72:75], v[76:79]\n\t"
    "v_mfma_f32_16x16x32_bf16 v[80:83], a[0:3], a[76:79], v[80:83]\n\t"
    "v_mfma_f32_16x16x32_bf16 v[84:87], a[4:7], a[76:79], v[84:87]\n\t"
    "v_mfma_f32_16x16x32_bf16 v[88:91], a[8:11], a[76:79], v[88:91]\n\t"
    "v_mfma_f32_16x16x32_bf16 v[92:95], a[12:15], a[76:79], v[92:95]\n\t"
    "v_bfe_u32 v112, v244, 4, 4\n\t"
    "v_lshrrev_b32 v113, 8, v244\n\t"
    "s_lshr_b32 s63, %10, 3\n\t"
    "s_and_b32 s63, s63, 1\n\t"
    "s_lshl_b32 s63, s63, 1\n\t"
    "s_lshl_b32 s64, %0, 10\n\t"
    "s_add_u32 s64, s64, 0x13000\n\t"
    "s_add_u32 s59, s64, 0x320\n\t"
    "v_mov_b32 v122, s59\n\t"
    "v_lshlrev_b32 v241, 2, v113\n\t"
    "v_subrev_u32 v241, s63, v241\n\t"
    "v_mul_i32_i24 v242, 36, v241\n\t"
    "v_lshl_add_u32 v114, v112, 1, v242\n\t"
    "v_add_u32 v114, s64, v114\n\t"
    "v_cmp_le_i32 vcc, 0, v241\n\t"
    "s_nop 1\n\t"
    "v_cndmask_b32 v115, v122, v114, vcc\n\t"
    "v_cmp_gt_u32 vcc, 2, v112\n\t"
    "v_add_u32 v242, 32, v114\n\t"
    "s_nop 0\n\t"
    "v_cndmask_b32 v116, v122, v242, vcc\n\t"
    "v_add_u32 v242, 32, v115\n\t"
    "v_cndmask_b32 v117, v122, v242, vcc\n\t"
    "v_add_u32 v241, -2, v112\n\t"
    "v_cmp_gt_u32 vcc, 2, v241\n\t"
    "v_add_u32 v242, 28, v114\n\t"
    "s_nop 0\n\t"
    "v_cndmask_b32 v118, v122, v242, vcc\n\t"
    "v_add_u32 v242, 28, v115\n\t"
    "v_cndmask_b32 v119, v122, v242, vcc\n\t"
    "s_waitcnt vmcnt(8) lgkmcnt(0)\n\t"
    "s_barrier\n\t"
    "v_mfma_f32_16x16x32_bf16 v[32:35], a[16:19], a[80:83], v[32:35]\n\t"
    "ds_read_b128 a[32:35], %5 offset:32768\n\t"
    "ds_read_b128 a[36:39], %5 offset:34816\n\t"
    "ds_read_b128 a[40:43], %5 offset:36864\n\t"
    "s_add_u32 m0, s57, 0x18000\n\t"
    "s_mov_b32 s56, 0xa2000\n\t"
    "v_mfma_f32_16x16x32_bf16 v[36:39], a[20:23], a[80:83], v[36:39]\n\t"
    "ds_read_b128 a[44:47], %5 offset:38912\n\t"
    "ds_read_b128 a[48:51], v240 offset:32768\n\t"
    "ds_read_b128 a[52:55], v240 offset:34816\n\t"
    "buffer_load_dwordx4 %8, s[44:47], s56 offen offset:0 lds\n\t"
    "s_mov_b32 s56, 0xa2080\n\t"
    "v_mfma_f32_16x16x32_bf16 v[40:43], a[24:27], a[80:83], v[40:43]\n\t"
    "ds_read_b128 a[56:59], v240 offset:36864\n\t"
    "ds_read_b128 a[60:63], v240 offset:38912\n\t"
    "buffer_load_dwordx4 %8, s[44:47], s56 offen offset:1024 lds\n\t"
    "v_mfma_f32_16x16x32_bf16 v[44:47], a[28:31], a[80:83], v[44:47]\n\t"
    "ds_read_b64_tr_b16 a[0:1], %6 offset:0\n\t"
    "ds_read_b64_tr_b16 a[2:3], %6 offset:3072\n\t"
    "ds_read_b64_tr_b16 a[4:5], %6 offset:1024\n\t"
    "s_mov_b32 s56, 0xa2100\n\t"
    "v_mfma_f32_16x16x32_bf16 v[48:51], a[16:19], a[84:87], v[48:51]\n\t"
    "ds_read_b64_tr_b16 a[6:7], %6 offset:4096\n\t"
    "ds_read_b64_tr_b16 a[8:9], %6 offset:2048\n\t"
    "ds_read_b64_tr_b16 a[10:11], %6 offset:5120\n\t"
    "buffer_load_dwordx4 %8, s[44:47], s56 offen offset:2048 lds\n\t"
    "v_mfma_f32_16x16x32_bf16 v[52:55], a[20:23], a[84:87], v[52:55]\n\t"
    "ds_read_b64_tr_b16 a[12:13], %7 offset:0\n\t"
    "ds_read_b64_tr_b16 a[14:15], %7 offset:3072\n\t"
    "v_mfma_f32_16x16x32_bf16 v[56:59], a[24:27], a[84:87], v[56:59]\n\t"
    "v_mfma_f32_16x16x32_bf16 v[60:63], a[28:31], a[84:87], v[60:63]\n\t"
    "s_waitcnt lgkmcnt(8)\n\t"
    "s_mov_b32 s58, 0x200\n\t"
    "s_add_u32 m0, s62, 0x8000\n\t"
    "v_mfma_f32_16x16x32_bf16 v[64:67], a[16:19], a[88:91], v[64:67]\n\t"
    "buffer_load_dwordx4 v232, s[40:43], s58 offen offset:0 lds\n\t"
    "v_mfma_f32_16x16x32_bf16 v[68:71], a[20:23], a[88:91], v[68:71]\n\t"
    "buffer_load_dwordx4 v233, s[40:43], s58 offen offset:1024 lds\n\t"
    "v_mfma_f32_16x16x32_bf16 v[72:75], a[24:27], a[88:91], v[72:75]\n\t"
    "buffer_load_dwordx4 v234, s[40:43], s58 offen offset:2048 lds\n\t"
    "v_mfma_f32_16x16x32_bf16 v[76:79], a[28:31], a[88:91], v[76:79]\n\t"
    "buffer_load_dwordx4 v235, s[40:43], s58 offen offset:3072 lds\n\t"
    "s_add_u32 m0, s62, 0x9000\n\t"
    "v_mfma_f32_16x16x32_bf16 v[80:83], a[16:19], a[92:95], v[80:83]\n\t"
    "buffer_load_dwordx4 v236, s[40:43], s58 offen offset:0 lds\n\t"
    "v_mfma_f32_16x16x32_bf16 v[84:87], a[20:23], a[92:95], v[84:87]\n\t"
    "buffer_load_dwordx4 v237, s[40:43], s58 offen offset:1024 lds\n\t"
    "v_mfma_f32_16x16x32_bf16 v[88:91], a[24:27], a[92:95], v[88:91]\n\t"
    "buffer_load_dwordx4 v238, s[40:43], s58 offen offset:2048 lds\n\t"
    "v_mfma_f32_16x16x32_bf16 v[92:95], a[28:31], a[92:95], v[92:95]\n\t"
    "buffer_load_dwordx4 v239, s[40:43], s58 offen offset:3072 lds\n\t"
    "s_waitcnt lgkmcnt(0)\n\t"
    "v_mfma_f32_16x16x32_bf16 v[32:35], a[0:3], a[32:35], v[32:35]\n\t"
    "ds_read_b64_tr_b16 a[16:17], %6 offset:6144\n\t"
    "v_mfma_f32_16x16x32_bf16 v[36:39], a[4:7], a[32:35], v[36:39]\n\t"
    "ds_read_b64_tr_b16 a[18:19], %6 offset:9216\n\t"
    "v_mfma_f32_16x16x32_bf16 v[40:43], a[8:11], a[32:35], v[40:43]\n\t"
    "ds_read_b64_tr_b16 a[20:21], %6 offset:7168\n\t"
    "v_mfma_f32_16x16x32_bf16 v[44:47], a[12:15], a[32:35], v[44:47]\n\t"
    "ds_read_b64_tr_b16 a[22:23], %6 offset:10240\n\t"
    "v_mfma_f32_16x16x32_bf16 v[48:51], a[0:3], a[36:39], v[48:51]\n\t"
    "ds_read_b64_tr_b16 a[24:25], %6 offset:8192\n\t"
    "v_mfma_f32_16x16x32_bf16 v[52:55], a[4:7], a[36:39], v[52:55]\n\t"
    "ds_read_b64_tr_b16 a[26:27], %6 offset:11264\n\t"
    "v_mfma_f32_16x16x32_bf16 v[56:59], a[8:11], a[36:39], v[56:59]\n\t"
    "ds_read_b64_tr_b16 a[28:29], %7 offset:6144\n\t"
    "v_mfma_f32_16x16x32_bf16 v[60:63], a[12:15], a[36:39], v[60:63]\n\t"
    "ds_read_b64_tr_b16 a[30:31], %7 offset:9216\n\t"
    "v_mfma_f32_16x16x32_bf16 v[64:67], a[0:3], a[40:43], v[64:67]\n\t"
    "v_mfma_f32_16x16x32_bf16 v[68:71], a[4:7], a[40:43], v[68:71]\n\t"
    "v_mfma_f32_16x16x32_bf16 v[72:75], a[8:11], a[40:43], v[72:75]\n\t"
    "v_mfma_f32_16x16x32_bf16 v[76:79], a[12:15], a[40:43], v[76:79]\n\t"
    "v_mfma_f32_16x16x32_bf16 v[80:83], a[0:3], a[44:47], v[80:83]\n\t"
    "v_mfma_f32_16x16x32_bf16 v[84:87], a[4:7], a[44:47], v[84:87]\n\t"
    "v_mfma_f32_16x16x32_bf16 v[88:91], a[8:11], a[44:47], v[88:91]\n\t"
    "v_mfma_f32_16x16x32_bf16 v[92:95], a[12:15], a[44:47], v[92:95]\n\t"
    "ds_read_u16 v96, v115 offset:0\n\t"
    "ds_read_u16 v200, v115 offset:36\n\t"
    "ds_read_u16 v97, v114 offset:72\n\t"
    "ds_read_u16 v201, v114 offset:108\n\t"
    "ds_read_u16 v104, v115 offset:0\n\t"
    "ds_read_u16 v208, v115 offset:36\n\t"
    "ds_read_u16 v105, v114 offset:72\n\t"
    "ds_read_u16 v209, v114 offset:108\n\t"
    "ds_read_u16 v100, v117 offset:0\n\t"
    "ds_read_u16 v204, v117 offset:36\n\t"
    "ds_read_u16 v101, v116 offset:72\n\t"
    "ds_read_u16 v205, v116 offset:108\n\t"
    "ds_read_u16 v108, v119 offset:0\n\t"
    "ds_read_u16 v212, v119 offset:36\n\t"
    "ds_read_u16 v109, v118 offset:72\n\t"
    "ds_read_u16 v213, v118 offset:108\n\t"
    "s_sub_u32 s59, 16, s63\n\t"
    "s_mul_i32 s59, s59, 36\n\t"
    "s_add_u32 s59, s59, s64\n\t"
    "v_lshl_add_u32 v114, v112, 1, s59\n\t"
    "s_cmp_lg_u32 s63, 0\n\t"
    "s_cselect_b64 s[66:67], -1, 0\n\t"
    "v_cmp_eq_u32 vcc, 0, v113\n\t"
    "s_nop 1\n\t"
    "v_cndmask_b32 v115, v122, v114, vcc\n\t"
    "v_cndmask_b32 v116, v122, v115, s[66:67]\n\t"
    "v_add_u32 v241, 0, v112\n\t"
    "v_cmp_gt_u32 vcc, 2, v241\n\t"
    "s_waitcnt vmcnt(0) lgkmcnt(0)\n\t"
    "s_barrier\n\t"
    "s_lshl_b32 s76, %10, 7\n\t"
    "s_cmp_lg_u32 %0, 0\n\t"
    "s_cbranch_scc1 pubkv_%=\n\t"
    "v_mov_b64 v[248:249], %12\n\t"
    "v_mov_b32 v250, 0\n\t"
    "s_mov_b64 s[60:61], exec\n\t"
    "s_mov_b64 exec, 1\n\t"
    "buffer_store_dwordx2 v[248:249], v250, s[48:51], s76 offen offset:0 sc1\n\t"
    "s_mov_b64 exec, s[60:61]\n\t"
    "pubkv_%=:\n\t"
    "v_mfma_f32_16x16x32_bf16 v[32:35], a[16:19], a[48:51], v[32:35]\n\t"
    "ds_read_b128 a[64:67], %5 offset:0\n\t"
    "ds_read_b128 a[68:71], %5 offset:2048\n\t"
    "ds_read_b128 a[72:75], %5 offset:4096\n\t"
    "s_add_u32 m0, s57, 0x10000\n\t"
    "s_mov_b32 s56, 0xd8000\n\t"
    "v_mfma_f32_16x16x32_bf16 v[36:39], a[20:23], a[48:51], v[36:39]\n\t"
    "ds_read_b128 a[76:79], %5 offset:6144\n\t"
    "ds_read_b128 a[80:83], v240 offset:0\n\t"
    "ds_read_b128 a[84:87], v240 offset:2048\n\t"
    "buffer_load_dwordx4 %8, s[44:47], s56 offen offset:0 lds\n\t"
    "s_mov_b32 s56, 0xd8080\n\t"
    "v_mfma_f32_16x16x32_bf16 v[40:43], a[24:27], a[48:51], v[40:43]\n\t"
    "ds_read_b128 a[88:91], v240 offset:4096\n\t"
    "ds_read_b128 a[92:95], v240 offset:6144\n\t"
    "buffer_load_dwordx4 %8, s[44:47], s56 offen offset:1024 lds\n\t"
    "v_mfma_f32_16x16x32_bf16 v[44:47], a[28:31], a[48:51], v[44:47]\n\t"
    "ds_read_b64_tr_b16 a[0:1], %6 offset:32768\n\t"
    "ds_read_b64_tr_b16 a[2:3], %6 offset:35840\n\t"
    "ds_read_b64_tr_b16 a[4:5], %6 offset:33792\n\t"
    "s_mov_b32 s56, 0xd8100\n\t"
    "v_mfma_f32_16x16x32_bf16 v[48:51], a[16:19], a[52:55], v[48:51]\n\t"
    "ds_read_b64_tr_b16 a[6:7], %6 offset:36864\n\t"
    "ds_read_b64_tr_b16 a[8:9], %6 offset:34816\n\t"
    "ds_read_b64_tr_b16 a[10:11], %6 offset:37888\n\t"
    "buffer_load_dwordx4 %8, s[44:47], s56 offen offset:2048 lds\n\t"
    "v_mfma_f32_16x16x32_bf16 v[52:55], a[20:23], a[52:55], v[52:55]\n\t"
    "ds_read_b64_tr_b16 a[12:13], %7 offset:32768\n\t"
    "ds_read_b64_tr_b16 a[14:15], %7 offset:35840\n\t"
    "v_mfma_f32_16x16x32_bf16 v[56:59], a[24:27], a[52:55], v[56:59]\n\t"
    "v_mfma_f32_16x16x32_bf16 v[60:63], a[28:31], a[52:55], v[60:63]\n\t"
    "s_waitcnt lgkmcnt(8)\n\t"
    "s_mov_b32 s58, 0x280\n\t"
    "s_add_u32 m0, s62, 0\n\t"
    "v_mfma_f32_16x16x32_bf16 v[64:67], a[16:19], a[56:59], v[64:67]\n\t"
    "buffer_load_dwordx4 v232, s[40:43], s58 offen offset:0 lds\n\t"
    "v_mfma_f32_16x16x32_bf16 v[68:71], a[20:23], a[56:59], v[68:71]\n\t"
    "buffer_load_dwordx4 v233, s[40:43], s58 offen offset:1024 lds\n\t"
    "v_mfma_f32_16x16x32_bf16 v[72:75], a[24:27], a[56:59], v[72:75]\n\t"
    "buffer_load_dwordx4 v234, s[40:43], s58 offen offset:2048 lds\n\t"
    "v_mfma_f32_16x16x32_bf16 v[76:79], a[28:31], a[56:59], v[76:79]\n\t"
    "buffer_load_dwordx4 v235, s[40:43], s58 offen offset:3072 lds\n\t"
    "s_add_u32 m0, s62, 0x1000\n\t"
    "v_mfma_f32_16x16x32_bf16 v[80:83], a[16:19], a[60:63], v[80:83]\n\t"
    "buffer_load_dwordx4 v236, s[40:43], s58 offen offset:0 lds\n\t"
    "v_mfma_f32_16x16x32_bf16 v[84:87], a[20:23], a[60:63], v[84:87]\n\t"
    "buffer_load_dwordx4 v237, s[40:43], s58 offen offset:1024 lds\n\t"
    "v_mfma_f32_16x16x32_bf16 v[88:91], a[24:27], a[60:63], v[88:91]\n\t"
    "buffer_load_dwordx4 v238, s[40:43], s58 offen offset:2048 lds\n\t"
    "v_mfma_f32_16x16x32_bf16 v[92:95], a[28:31], a[60:63], v[92:95]\n\t"
    "buffer_load_dwordx4 v239, s[40:43], s58 offen offset:3072 lds\n\t"
    "s_waitcnt lgkmcnt(0)\n\t"
    "v_mfma_f32_16x16x32_bf16 v[32:35], a[0:3], a[64:67], v[32:35]\n\t"
    "ds_read_b64_tr_b16 a[16:17], %6 offset:38912\n\t"
    "v_mfma_f32_16x16x32_bf16 v[36:39], a[4:7], a[64:67], v[36:39]\n\t"
    "ds_read_b64_tr_b16 a[18:19], %6 offset:41984\n\t"
    "v_mfma_f32_16x16x32_bf16 v[40:43], a[8:11], a[64:67], v[40:43]\n\t"
    "ds_read_b64_tr_b16 a[20:21], %6 offset:39936\n\t"
    "v_mfma_f32_16x16x32_bf16 v[44:47], a[12:15], a[64:67], v[44:47]\n\t"
    "ds_read_b64_tr_b16 a[22:23], %6 offset:43008\n\t"
    "v_mfma_f32_16x16x32_bf16 v[48:51], a[0:3], a[68:71], v[48:51]\n\t"
    "ds_read_b64_tr_b16 a[24:25], %6 offset:40960\n\t"
    "v_mfma_f32_16x16x32_bf16 v[52:55], a[4:7], a[68:71], v[52:55]\n\t"
    "ds_read_b64_tr_b16 a[26:27], %6 offset:44032\n\t"
    "v_mfma_f32_16x16x32_bf16 v[56:59], a[8:11], a[68:71], v[56:59]\n\t"
    "ds_read_b64_tr_b16 a[28:29], %7 offset:38912\n\t"
    "v_mfma_f32_16x16x32_bf16 v[60:63], a[12:15], a[68:71], v[60:63]\n\t"
    "ds_read_b64_tr_b16 a[30:31], %7 offset:41984\n\t"
    "v_mfma_f32_16x16x32_bf16 v[64:67], a[0:3], a[72:75], v[64:67]\n\t"
    "v_mfma_f32_16x16x32_bf16 v[68:71], a[4:7], a[72:75], v[68:71]\n\t"
    "v_mfma_f32_16x16x32_bf16 v[72:75], a[8:11], a[72:75], v[72:75]\n\t"
    "v_mfma_f32_16x16x32_bf16 v[76:79], a[12:15], a[72:75], v[76:79]\n\t"
    "v_mfma_f32_16x16x32_bf16 v[80:83], a[0:3], a[76:79], v[80:83]\n\t"
    "v_mfma_f32_16x16x32_bf16 v[84:87], a[4:7], a[76:79], v[84:87]\n\t"
    "v_mfma_f32_16x16x32_bf16 v[88:91], a[8:11], a[76:79], v[88:91]\n\t"
    "v_mfma_f32_16x16x32_bf16 v[92:95], a[12:15], a[76:79], v[92:95]\n\t"
    "v_add_u32 v242, 32, v115\n\t"
    "v_cndmask_b32 v117, v122, v242, vcc\n\t"
    "v_cndmask_b32 v118, v122, v117, s[66:67]\n\t"
    "ds_read_u16 v98, v115 offset:0\n\t"
    "ds_read_u16 v202, v115 offset:36\n\t"
    "ds_read_u16 v99, v116 offset:72\n\t"
    "ds_read_u16 v203, v116 offset:108\n\t"
    "ds_read_u16 v102, v117 offset:0\n\t"
    "ds_read_u16 v206, v117 offset:36\n\t"
    "ds_read_u16 v103, v118 offset:72\n\t"
    "ds_read_u16 v207, v118 offset:108\n\t"
    "v_cmp_eq_u32 vcc, 1, v113\n\t"
    "s_nop 1\n\t"
    "v_cndmask_b32 v115, v122, v114, vcc\n\t"
    "v_cndmask_b32 v116, v122, v115, s[66:67]\n\t"
    "v_add_u32 v241, -2, v112\n\t"
    "v_cmp_gt_u32 vcc, 2, v241\n\t"
    "v_add_u32 v242, 28, v115\n\t"
    "s_nop 0\n\t"
    "v_cndmask_b32 v117, v122, v242, vcc\n\t"
    "v_cndmask_b32 v118, v122, v117, s[66:67]\n\t"
    "ds_read_u16 v106, v115 offset:0\n\t"
    "ds_read_u16 v210, v115 offset:36\n\t"
    "ds_read_u16 v107, v116 offset:72\n\t"
    "ds_read_u16 v211, v116 offset:108\n\t"
    "ds_read_u16 v110, v117 offset:0\n\t"
    "ds_read_u16 v214, v117 offset:36\n\t"
    "ds_read_u16 v111, v118 offset:72\n\t"
    "ds_read_u16 v215, v118 offset:108\n\t"
    "s_waitcnt vmcnt(8) lgkmcnt(0)\n\t"
    "s_barrier\n\t"
    "v_mfma_f32_16x16x32_bf16 v[32:35], a[16:19], a[80:83], v[32:35]\n\t"
    "ds_read_b128 a[32:35], %5 offset:32768\n\t"
    "ds_read_b128 a[36:39], %5 offset:34816\n\t"
    "ds_read_b128 a[40:43], %5 offset:36864\n\t"
    "s_add_u32 m0, s57, 0x18000\n\t"
    "s_mov_b32 s56, 0x10e000\n\t"
    "v_mfma_f32_16x16x32_bf16 v[36:39], a[20:23], a[80:83], v[36:39]\n\t"
    "ds_read_b128 a[44:47], %5 offset:38912\n\t"
    "ds_read_b128 a[48:51], v240 offset:32768\n\t"
    "ds_read_b128 a[52:55], v240 offset:34816\n\t"
    "buffer_load_dwordx4 %8, s[44:47], s56 offen offset:0 lds\n\t"
    "s_mov_b32 s56, 0x10e080\n\t"
    "v_mfma_f32_16x16x32_bf16 v[40:43], a[24:27], a[80:83], v[40:43]\n\t"
    "ds_read_b128 a[56:59], v240 offset:36864\n\t"
    "ds_read_b128 a[60:63], v240 offset:38912\n\t"
    "buffer_load_dwordx4 %8, s[44:47], s56 offen offset:1024 lds\n\t"
    "v_mfma_f32_16x16x32_bf16 v[44:47], a[28:31], a[80:83], v[44:47]\n\t"
    "ds_read_b64_tr_b16 a[0:1], %6 offset:0\n\t"
    "ds_read_b64_tr_b16 a[2:3], %6 offset:3072\n\t"
    "ds_read_b64_tr_b16 a[4:5], %6 offset:1024\n\t"
    "s_mov_b32 s56, 0x10e100\n\t"
    "v_mfma_f32_16x16x32_bf16 v[48:51], a[16:19], a[84:87], v[48:51]\n\t"
    "ds_read_b64_tr_b16 a[6:7], %6 offset:4096\n\t"
    "ds_read_b64_tr_b16 a[8:9], %6 offset:2048\n\t"
    "ds_read_b64_tr_b16 a[10:11], %6 offset:5120\n\t"
    "buffer_load_dwordx4 %8, s[44:47], s56 offen offset:2048 lds\n\t"
    "v_mfma_f32_16x16x32_bf16 v[52:55], a[20:23], a[84:87], v[52:55]\n\t"
    "ds_read_b64_tr_b16 a[12:13], %7 offset:0\n\t"
    "ds_read_b64_tr_b16 a[14:15], %7 offset:3072\n\t"
    "v_mfma_f32_16x16x32_bf16 v[56:59], a[24:27], a[84:87], v[56:59]\n\t"
    "v_mfma_f32_16x16x32_bf16 v[60:63], a[28:31], a[84:87], v[60:63]\n\t"
    "s_waitcnt lgkmcnt(8)\n\t"
    "s_mov_b32 s58, 0x300\n\t"
    "s_add_u32 m0, s62, 0x8000\n\t"
    "v_mfma_f32_16x16x32_bf16 v[64:67], a[16:19], a[88:91], v[64:67]\n\t"
    "buffer_load_dwordx4 v232, s[40:43], s58 offen offset:0 lds\n\t"
    "v_mfma_f32_16x16x32_bf16 v[68:71], a[20:23], a[88:91], v[68:71]\n\t"
    "buffer_load_dwordx4 v233, s[40:43], s58 offen offset:1024 lds\n\t"
    "v_mfma_f32_16x16x32_bf16 v[72:75], a[24:27], a[88:91], v[72:75]\n\t"
    "buffer_load_dwordx4 v234, s[40:43], s58 offen offset:2048 lds\n\t"
    "v_mfma_f32_16x16x32_bf16 v[76:79], a[28:31], a[88:91], v[76:79]\n\t"
    "buffer_load_dwordx4 v235, s[40:43], s58 offen offset:3072 lds\n\t"
    "s_add_u32 m0, s62, 0x9000\n\t"
    "v_mfma_f32_16x16x32_bf16 v[80:83], a[16:19], a[92:95], v[80:83]\n\t"
    "buffer_load_dwordx4 v236, s[40:43], s58 offen offset:0 lds\n\t"
    "v_mfma_f32_16x16x32_bf16 v[84:87], a[20:23], a[92:95], v[84:87]\n\t"
    "buffer_load_dwordx4 v237, s[40:43], s58 offen offset:1024 lds\n\t"
    "v_mfma_f32_16x16x32_bf16 v[88:91], a[24:27], a[92:95], v[88:91]\n\t"
    "buffer_load_dwordx4 v238, s[40:43], s58 offen offset:2048 lds\n\t"
    "v_mfma_f32_16x16x32_bf16 v[92:95], a[28:31], a[92:95], v[92:95]\n\t"
    "buffer_load_dwordx4 v239, s[40:43], s58 offen offset:3072 lds\n\t"
    "s_waitcnt lgkmcnt(0)\n\t"
    "v_mfma_f32_16x16x32_bf16 v[32:35], a[0:3], a[32:35], v[32:35]\n\t"
    "ds_read_b64_tr_b16 a[16:17], %6 offset:6144\n\t"
    "v_mfma_f32_16x16x32_bf16 v[36:39], a[4:7], a[32:35], v[36:39]\n\t"
    "ds_read_b64_tr_b16 a[18:19], %6 offset:9216\n\t"
    "v_mfma_f32_16x16x32_bf16 v[40:43], a[8:11], a[32:35], v[40:43]\n\t"
    "ds_read_b64_tr_b16 a[20:21], %6 offset:7168\n\t"
    "v_mfma_f32_16x16x32_bf16 v[44:47], a[12:15], a[32:35], v[44:47]\n\t"
    "ds_read_b64_tr_b16 a[22:23], %6 offset:10240\n\t"
    "v_mfma_f32_16x16x32_bf16 v[48:51], a[0:3], a[36:39], v[48:51]\n\t"
    "ds_read_b64_tr_b16 a[24:25], %6 offset:8192\n\t"
    "v_mfma_f32_16x16x32_bf16 v[52:55], a[4:7], a[36:39], v[52:55]\n\t"
    "ds_read_b64_tr_b16 a[26:27], %6 offset:11264\n\t"
    "v_mfma_f32_16x16x32_bf16 v[56:59], a[8:11], a[36:39], v[56:59]\n\t"
    "ds_read_b64_tr_b16 a[28:29], %7 offset:6144\n\t"
    "v_mfma_f32_16x16x32_bf16 v[60:63], a[12:15], a[36:39], v[60:63]\n\t"
    "ds_read_b64_tr_b16 a[30:31], %7 offset:9216\n\t"
    "v_mfma_f32_16x16x32_bf16 v[64:67], a[0:3], a[40:43], v[64:67]\n\t"
    "v_mfma_f32_16x16x32_bf16 v[68:71], a[4:7], a[40:43], v[68:71]\n\t"
    "v_mfma_f32_16x16x32_bf16 v[72:75], a[8:11], a[40:43], v[72:75]\n\t"
    "v_mfma_f32_16x16x32_bf16 v[76:79], a[12:15], a[40:43], v[76:79]\n\t"
    "v_mfma_f32_16x16x32_bf16 v[80:83], a[0:3], a[44:47], v[80:83]\n\t"
    "v_mfma_f32_16x16x32_bf16 v[84:87], a[4:7], a[44:47], v[84:87]\n\t"
    "v_mfma_f32_16x16x32_bf16 v[88:91], a[8:11], a[44:47], v[88:91]\n\t"
    "v_mfma_f32_16x16x32_bf16 v[92:95], a[12:15], a[44:47], v[92:95]\n\t"
    "v_lshl_or_b32 v96, v200, 16, v96\n\t"
    "v_lshl_or_b32 v97, v201, 16, v97\n\t"
    "v_lshl_or_b32 v98, v202, 16, v98\n\t"
    "v_lshl_or_b32 v99, v203, 16, v99\n\t"
    "v_lshl_or_b32 v100, v204, 16, v100\n\t"
    "v_lshl_or_b32 v101, v205, 16, v101\n\t"
    "v_lshl_or_b32 v102, v206, 16, v102\n\t"
    "v_lshl_or_b32 v103, v207, 16, v103\n\t"
    "v_lshl_or_b32 v104, v208, 16, v104\n\t"
    "v_lshl_or_b32 v105, v209, 16, v105\n\t"
    "v_lshl_or_b32 v106, v210, 16, v106\n\t"
    "v_lshl_or_b32 v107, v211, 16, v107\n\t"
    "v_lshl_or_b32 v108, v212, 16, v108\n\t"
    "v_lshl_or_b32 v109, v213, 16, v109\n\t"
    "v_lshl_or_b32 v110, v214, 16, v110\n\t"
    "v_lshl_or_b32 v111, v215, 16, v111\n\t"
    "s_waitcnt vmcnt(8) lgkmcnt(0)\n\t"
    "s_barrier\n\t"
    "v_mfma_f32_16x16x32_bf16 v[32:35], a[16:19], a[48:51], v[32:35]\n\t"
    "ds_read_b128 a[64:67], %5 offset:0\n\t"
    "ds_read_b128 a[68:71], %5 offset:2048\n\t"
    "ds_read_b128 a[72:75], %5 offset:4096\n\t"
    "s_add_u32 m0, s57, 0x10000\n\t"
    "s_mov_b32 s56, 0x144000\n\t"
    "v_mfma_f32_16x16x32_bf16 v[36:39], a[20:23], a[48:51], v[36:39]\n\t"
    "ds_read_b128 a[76:79], %5 offset:6144\n\t"
    "ds_read_b128 a[80:83], v240 offset:0\n\t"
    "ds_read_b128 a[84:87], v240 offset:2048\n\t"
    "buffer_load_dwordx4 %8, s[44:47], s56 offen offset:0 lds\n\t"
    "s_mov_b32 s56, 0x144080\n\t"
    "v_mfma_f32_16x16x32_bf16 v[40:43], a[24:27], a[48:51], v[40:43]\n\t"
    "ds_read_b128 a[88:91], v240 offset:4096\n\t"
    "ds_read_b128 a[92:95], v240 offset:6144\n\t"
    "buffer_load_dwordx4 %8, s[44:47], s56 offen offset:1024 lds\n\t"
    "v_mfma_f32_16x16x32_bf16 v[44:47], a[28:31], a[48:51], v[44:47]\n\t"
    "ds_read_b64_tr_b16 a[0:1], %6 offset:32768\n\t"
    "ds_read_b64_tr_b16 a[2:3], %6 offset:35840\n\t"
    "ds_read_b64_tr_b16 a[4:5], %6 offset:33792\n\t"
    "s_mov_b32 s56, 0x144100\n\t"
    "v_mfma_f32_16x16x32_bf16 v[48:51], a[16:19], a[52:55], v[48:51]\n\t"
    "ds_read_b64_tr_b16 a[6:7], %6 offset:36864\n\t"
    "ds_read_b64_tr_b16 a[8:9], %6 offset:34816\n\t"
    "ds_read_b64_tr_b16 a[10:11], %6 offset:37888\n\t"
    "buffer_load_dwordx4 %8, s[44:47], s56 offen offset:2048 lds\n\t"
    "v_mfma_f32_16x16x32_bf16 v[52:55], a[20:23], a[52:55], v[52:55]\n\t"
    "ds_read_b64_tr_b16 a[12:13], %7 offset:32768\n\t"
    "ds_read_b64_tr_b16 a[14:15], %7 offset:35840\n\t"
    "v_mfma_f32_16x16x32_bf16 v[56:59], a[24:27], a[52:55], v[56:59]\n\t"
    "v_mfma_f32_16x16x32_bf16 v[60:63], a[28:31], a[52:55], v[60:63]\n\t"
    "s_waitcnt lgkmcnt(8)\n\t"
    "s_mov_b32 s58, 0x380\n\t"
    "s_add_u32 m0, s62, 0\n\t"
    "v_mfma_f32_16x16x32_bf16 v[64:67], a[16:19], a[56:59], v[64:67]\n\t"
    "buffer_load_dwordx4 v232, s[40:43], s58 offen offset:0 lds\n\t"
    "v_mfma_f32_16x16x32_bf16 v[68:71], a[20:23], a[56:59], v[68:71]\n\t"
    "buffer_load_dwordx4 v233, s[40:43], s58 offen offset:1024 lds\n\t"
    "v_mfma_f32_16x16x32_bf16 v[72:75], a[24:27], a[56:59], v[72:75]\n\t"
    "buffer_load_dwordx4 v234, s[40:43], s58 offen offset:2048 lds\n\t"
    "v_mfma_f32_16x16x32_bf16 v[76:79], a[28:31], a[56:59], v[76:79]\n\t"
    "buffer_load_dwordx4 v235, s[40:43], s58 offen offset:3072 lds\n\t"
    "s_add_u32 m0, s62, 0x1000\n\t"
    "v_mfma_f32_16x16x32_bf16 v[80:83], a[16:19], a[60:63], v[80:83]\n\t"
    "buffer_load_dwordx4 v236, s[40:43], s58 offen offset:0 lds\n\t"
    "v_mfma_f32_16x16x32_bf16 v[84:87], a[20:23], a[60:63], v[84:87]\n\t"
    "buffer_load_dwordx4 v237, s[40:43], s58 offen offset:1024 lds\n\t"
    "v_mfma_f32_16x16x32_bf16 v[88:91], a[24:27], a[60:63], v[88:91]\n\t"
    "buffer_load_dwordx4 v238, s[40:43], s58 offen offset:2048 lds\n\t"
    "v_mfma_f32_16x16x32_bf16 v[92:95], a[28:31], a[60:63], v[92:95]\n\t"
    "buffer_load_dwordx4 v239, s[40:43], s58 offen offset:3072 lds\n\t"
    "s_waitcnt lgkmcnt(0)\n\t"
    "v_mfma_f32_16x16x32_bf16 v[32:35], a[0:3], a[64:67], v[32:35]\n\t"
    "ds_read_b64_tr_b16 a[16:17], %6 offset:38912\n\t"
    "v_mfma_f32_16x16x32_bf16 v[36:39], a[4:7], a[64:67], v[36:39]\n\t"
    "ds_read_b64_tr_b16 a[18:19], %6 offset:41984\n\t"
    "v_mfma_f32_16x16x32_bf16 v[40:43], a[8:11], a[64:67], v[40:43]\n\t"
    "ds_read_b64_tr_b16 a[20:21], %6 offset:39936\n\t"
    "v_mfma_f32_16x16x32_bf16 v[44:47], a[12:15], a[64:67], v[44:47]\n\t"
    "ds_read_b64_tr_b16 a[22:23], %6 offset:43008\n\t"
    "v_mfma_f32_16x16x32_bf16 v[48:51], a[0:3], a[68:71], v[48:51]\n\t"
    "ds_read_b64_tr_b16 a[24:25], %6 offset:40960\n\t"
    "v_mfma_f32_16x16x32_bf16 v[52:55], a[4:7], a[68:71], v[52:55]\n\t"
    "ds_read_b64_tr_b16 a[26:27], %6 offset:44032\n\t"
    "v_mfma_f32_16x16x32_bf16 v[56:59], a[8:11], a[68:71], v[56:59]\n\t"
    "ds_read_b64_tr_b16 a[28:29], %7 offset:38912\n\t"
    "v_mfma_f32_16x16x32_bf16 v[60:63], a[12:15], a[68:71], v[60:63]\n\t"
    "ds_read_b64_tr_b16 a[30:31], %7 offset:41984\n\t"
    "v_mfma_f32_16x16x32_bf16 v[64:67], a[0:3], a[72:75], v[64:67]\n\t"
    "v_mfma_f32_16x16x32_bf16 v[68:71], a[4:7], a[72:75], v[68:71]\n\t"
    "v_mfma_f32_16x16x32_bf16 v[72:75], a[8:11], a[72:75], v[72:75]\n\t"
    "v_mfma_f32_16x16x32_bf16 v[76:79], a[12:15], a[72:75], v[76:79]\n\t"
    "v_mfma_f32_16x16x32_bf16 v[80:83], a[0:3], a[76:79], v[80:83]\n\t"
    "v_mfma_f32_16x16x32_bf16 v[84:87], a[4:7], a[76:79], v[84:87]\n\t"
    "v_mfma_f32_16x16x32_bf16 v[88:91], a[8:11], a[76:79], v[88:91]\n\t"
    "v_mfma_f32_16x16x32_bf16 v[92:95], a[12:15], a[76:79], v[92:95]\n\t"
    "s_waitcnt vmcnt(8) lgkmcnt(0)\n\t"
    "s_barrier\n\t"
    "v_mfma_f32_16x16x32_bf16 v[32:35], a[16:19], a[80:83], v[32:35]\n\t"
    "ds_read_b128 a[32:35], %5 offset:32768\n\t"
    "ds_read_b128 a[36:39], %5 offset:34816\n\t"
    "ds_read_b128 a[40:43], %5 offset:36864\n\t"
    "s_add_u32 m0, s57, 0x18000\n\t"
    "s_mov_b32 s56, 0x17a000\n\t"
    "v_mfma_f32_16x16x32_bf16 v[36:39], a[20:23], a[80:83], v[36:39]\n\t"
    "ds_read_b128 a[44:47], %5 offset:38912\n\t"
    "ds_read_b128 a[48:51], v240 offset:32768\n\t"
    "ds_read_b128 a[52:55], v240 offset:34816\n\t"
    "buffer_load_dwordx4 %8, s[44:47], s56 offen offset:0 lds\n\t"
    "s_mov_b32 s56, 0x17a080\n\t"
    "v_mfma_f32_16x16x32_bf16 v[40:43], a[24:27], a[80:83], v[40:43]\n\t"
    "ds_read_b128 a[56:59], v240 offset:36864\n\t"
    "ds_read_b128 a[60:63], v240 offset:38912\n\t"
    "buffer_load_dwordx4 %8, s[44:47], s56 offen offset:1024 lds\n\t"
    "v_mfma_f32_16x16x32_bf16 v[44:47], a[28:31], a[80:83], v[44:47]\n\t"
    "ds_read_b64_tr_b16 a[0:1], %6 offset:0\n\t"
    "ds_read_b64_tr_b16 a[2:3], %6 offset:3072\n\t"
    "ds_read_b64_tr_b16 a[4:5], %6 offset:1024\n\t"
    "s_mov_b32 s56, 0x17a100\n\t"
    "v_mfma_f32_16x16x32_bf16 v[48:51], a[16:19], a[84:87], v[48:51]\n\t"
    "ds_read_b64_tr_b16 a[6:7], %6 offset:4096\n\t"
    "ds_read_b64_tr_b16 a[8:9], %6 offset:2048\n\t"
    "ds_read_b64_tr_b16 a[10:11], %6 offset:5120\n\t"
    "buffer_load_dwordx4 %8, s[44:47], s56 offen offset:2048 lds\n\t"
    "v_mfma_f32_16x16x32_bf16 v[52:55], a[20:23], a[84:87], v[52:55]\n\t"
    "ds_read_b64_tr_b16 a[12:13], %7 offset:0\n\t"
    "ds_read_b64_tr_b16 a[14:15], %7 offset:3072\n\t"
    "v_mfma_f32_16x16x32_bf16 v[56:59], a[24:27], a[84:87], v[56:59]\n\t"
    "v_mfma_f32_16x16x32_bf16 v[60:63], a[28:31], a[84:87], v[60:63]\n\t"
    "s_waitcnt lgkmcnt(8)\n\t"
    "s_mov_b32 s58, 0x400\n\t"
    "s_add_u32 m0, s62, 0x8000\n\t"
    "v_mfma_f32_16x16x32_bf16 v[64:67], a[16:19], a[88:91], v[64:67]\n\t"
    "buffer_load_dwordx4 v232, s[40:43], s58 offen offset:0 lds\n\t"
    "v_mfma_f32_16x16x32_bf16 v[68:71], a[20:23], a[88:91], v[68:71]\n\t"
    "buffer_load_dwordx4 v233, s[40:43], s58 offen offset:1024 lds\n\t"
    "v_mfma_f32_16x16x32_bf16 v[72:75], a[24:27], a[88:91], v[72:75]\n\t"
    "buffer_load_dwordx4 v234, s[40:43], s58 offen offset:2048 lds\n\t"
    "v_mfma_f32_16x16x32_bf16 v[76:79], a[28:31], a[88:91], v[76:79]\n\t"
    "buffer_load_dwordx4 v235, s[40:43], s58 offen offset:3072 lds\n\t"
    "s_add_u32 m0, s62, 0x9000\n\t"
    "v_mfma_f32_16x16x32_bf16 v[80:83], a[16:19], a[92:95], v[80:83]\n\t"
    "buffer_load_dwordx4 v236, s[40:43], s58 offen offset:0 lds\n\t"
    "v_mfma_f32_16x16x32_bf16 v[84:87], a[20:23], a[92:95], v[84:87]\n\t"
    "buffer_load_dwordx4 v237, s[40:43], s58 offen offset:1024 lds\n\t"
    "v_mfma_f32_16x16x32_bf16 v[88:91], a[24:27], a[92:95], v[88:91]\n\t"
    "buffer_load_dwordx4 v238, s[40:43], s58 offen offset:2048 lds\n\t"
    "v_mfma_f32_16x16x32_bf16 v[92:95], a[28:31], a[92:95], v[92:95]\n\t"
    "buffer_load_dwordx4 v239, s[40:43], s58 offen offset:3072 lds\n\t"
    "s_waitcnt lgkmcnt(0)\n\t"
    "v_mfma_f32_16x16x32_bf16 v[32:35], a[0:3], a[32:35], v[32:35]\n\t"
    "ds_read_b64_tr_b16 a[16:17], %6 offset:6144\n\t"
    "v_mfma_f32_16x16x32_bf16 v[36:39], a[4:7], a[32:35], v[36:39]\n\t"
    "ds_read_b64_tr_b16 a[18:19], %6 offset:9216\n\t"
    "v_mfma_f32_16x16x32_bf16 v[40:43], a[8:11], a[32:35], v[40:43]\n\t"
    "ds_read_b64_tr_b16 a[20:21], %6 offset:7168\n\t"
    "v_mfma_f32_16x16x32_bf16 v[44:47], a[12:15], a[32:35], v[44:47]\n\t"
    "ds_read_b64_tr_b16 a[22:23], %6 offset:10240\n\t"
    "v_mfma_f32_16x16x32_bf16 v[48:51], a[0:3], a[36:39], v[48:51]\n\t"
    "ds_read_b64_tr_b16 a[24:25], %6 offset:8192\n\t"
    "v_mfma_f32_16x16x32_bf16 v[52:55], a[4:7], a[36:39], v[52:55]\n\t"
    "ds_read_b64_tr_b16 a[26:27], %6 offset:11264\n\t"
    "v_mfma_f32_16x16x32_bf16 v[56:59], a[8:11], a[36:39], v[56:59]\n\t"
    "ds_read_b64_tr_b16 a[28:29], %7 offset:6144\n\t"
    "v_mfma_f32_16x16x32_bf16 v[60:63], a[12:15], a[36:39], v[60:63]\n\t"
    "ds_read_b64_tr_b16 a[30:31], %7 offset:9216\n\t"
    "v_mfma_f32_16x16x32_bf16 v[64:67], a[0:3], a[40:43], v[64:67]\n\t"
    "v_mfma_f32_16x16x32_bf16 v[68:71], a[4:7], a[40:43], v[68:71]\n\t"
    "v_mfma_f32_16x16x32_bf16 v[72:75], a[8:11], a[40:43], v[72:75]\n\t"
    "v_mfma_f32_16x16x32_bf16 v[76:79], a[12:15], a[40:43], v[76:79]\n\t"
    "v_mfma_f32_16x16x32_bf16 v[80:83], a[0:3], a[44:47], v[80:83]\n\t"
    "v_mfma_f32_16x16x32_bf16 v[84:87], a[4:7], a[44:47], v[84:87]\n\t"
    "v_mfma_f32_16x16x32_bf16 v[88:91], a[8:11], a[44:47], v[88:91]\n\t"
    "v_mfma_f32_16x16x32_bf16 v[92:95], a[12:15], a[44:47], v[92:95]\n\t"
    "s_waitcnt vmcnt(8) lgkmcnt(0)\n\t"
    "s_barrier\n\t"
    "v_mfma_f32_16x16x32_bf16 v[32:35], a[16:19], a[48:51], v[32:35]\n\t"
    "ds_read_b128 a[64:67], %5 offset:0\n\t"
    "ds_read_b128 a[68:71], %5 offset:2048\n\t"
    "ds_read_b128 a[72:75], %5 offset:4096\n\t"
    "s_add_u32 m0, s57, 0x10000\n\t"
    "s_mov_b32 s56, 0x1b0000\n\t"
    "v_mfma_f32_16x16x32_bf16 v[36:39], a[20:23], a[48:51], v[36:39]\n\t"
    "ds_read_b128 a[76:79], %5 offset:6144\n\t"
    "ds_read_b128 a[80:83], v240 offset:0\n\t"
    "ds_read_b128 a[84:87], v240 offset:2048\n\t"
    "buffer_load_dwordx4 %8, s[44:47], s56 offen offset:0 lds\n\t"
    "s_mov_b32 s56, 0x1b0080\n\t"
    "v_mfma_f32_16x16x32_bf16 v[40:43], a[24:27], a[48:51], v[40:43]\n\t"
    "ds_read_b128 a[88:91], v240 offset:4096\n\t"
    "ds_read_b128 a[92:95], v240 offset:6144\n\t"
    "buffer_load_dwordx4 %8, s[44:47], s56 offen offset:1024 lds\n\t"
    "v_mfma_f32_16x16x32_bf16 v[44:47], a[28:31], a[48:51], v[44:47]\n\t"
    "ds_read_b64_tr_b16 a[0:1], %6 offset:32768\n\t"
    "ds_read_b64_tr_b16 a[2:3], %6 offset:35840\n\t"
    "ds_read_b64_tr_b16 a[4:5], %6 offset:33792\n\t"
    "s_mov_b32 s56, 0x1b0100\n\t"
    "v_mfma_f32_16x16x32_bf16 v[48:51], a[16:19], a[52:55], v[48:51]\n\t"
    "ds_read_b64_tr_b16 a[6:7], %6 offset:36864\n\t"
    "ds_read_b64_tr_b16 a[8:9], %6 offset:34816\n\t"
    "ds_read_b64_tr_b16 a[10:11], %6 offset:37888\n\t"
    "buffer_load_dwordx4 %8, s[44:47], s56 offen offset:2048 lds\n\t"
    "v_mfma_f32_16x16x32_bf16 v[52:55], a[20:23], a[52:55], v[52:55]\n\t"
    "ds_read_b64_tr_b16 a[12:13], %7 offset:32768\n\t"
    "ds_read_b64_tr_b16 a[14:15], %7 offset:35840\n\t"
    "v_mfma_f32_16x16x32_bf16 v[56:59], a[24:27], a[52:55], v[56:59]\n\t"
    "v_mfma_f32_16x16x32_bf16 v[60:63], a[28:31], a[52:55], v[60:63]\n\t"
    "v_mfma_f32_16x16x32_bf16 v[64:67], a[16:19], a[56:59], v[64:67]\n\t"
    "v_mfma_f32_16x16x32_bf16 v[68:71], a[20:23], a[56:59], v[68:71]\n\t"
    "v_mfma_f32_16x16x32_bf16 v[72:75], a[24:27], a[56:59], v[72:75]\n\t"
    "v_mfma_f32_16x16x32_bf16 v[76:79], a[28:31], a[56:59], v[76:79]\n\t"
    "v_mfma_f32_16x16x32_bf16 v[80:83], a[16:19], a[60:63], v[80:83]\n\t"
    "v_mfma_f32_16x16x32_bf16 v[84:87], a[20:23], a[60:63], v[84:87]\n\t"
    "v_mfma_f32_16x16x32_bf16 v[88:91], a[24:27], a[60:63], v[88:91]\n\t"
    "v_mfma_f32_16x16x32_bf16 v[92:95], a[28:31], a[60:63], v[92:95]\n\t"
    "s_waitcnt lgkmcnt(4)\n\t"
    "v_mfma_f32_16x16x32_bf16 v[32:35], a[0:3], a[64:67], v[32:35]\n\t"
    "ds_read_b64_tr_b16 a[16:17], %6 offset:38912\n\t"
    "v_mfma_f32_16x16x32_bf16 v[36:39], a[4:7], a[64:67], v[36:39]\n\t"
    "ds_read_b64_tr_b16 a[18:19], %6 offset:41984\n\t"
    "s_waitcnt lgkmcnt(4)\n\t"
    "v_mfma_f32_16x16x32_bf16 v[40:43], a[8:11], a[64:67], v[40:43]\n\t"
    "ds_read_b64_tr_b16 a[20:21], %6 offset:39936\n\t"
    "s_waitcnt lgkmcnt(3)\n\t"
    "v_mfma_f32_16x16x32_bf16 v[44:47], a[12:15], a[64:67], v[44:47]\n\t"
    "ds_read_b64_tr_b16 a[22:23], %6 offset:43008\n\t"
    "v_mfma_f32_16x16x32_bf16 v[48:51], a[0:3], a[68:71], v[48:51]\n\t"
    "ds_read_b64_tr_b16 a[24:25], %6 offset:40960\n\t"
    "v_mfma_f32_16x16x32_bf16 v[52:55], a[4:7], a[68:71], v[52:55]\n\t"
    "ds_read_b64_tr_b16 a[26:27], %6 offset:44032\n\t"
    "v_mfma_f32_16x16x32_bf16 v[56:59], a[8:11], a[68:71], v[56:59]\n\t"
    "ds_read_b64_tr_b16 a[28:29], %7 offset:38912\n\t"
    "v_mfma_f32_16x16x32_bf16 v[60:63], a[12:15], a[68:71], v[60:63]\n\t"
    "ds_read_b64_tr_b16 a[30:31], %7 offset:41984\n\t"
    "v_mfma_f32_16x16x32_bf16 v[64:67], a[0:3], a[72:75], v[64:67]\n\t"
    "v_mfma_f32_16x16x32_bf16 v[68:71], a[4:7], a[72:75], v[68:71]\n\t"
    "v_mfma_f32_16x16x32_bf16 v[72:75], a[8:11], a[72:75], v[72:75]\n\t"
    "v_mfma_f32_16x16x32_bf16 v[76:79], a[12:15], a[72:75], v[76:79]\n\t"
    "v_mfma_f32_16x16x32_bf16 v[80:83], a[0:3], a[76:79], v[80:83]\n\t"
    "v_mfma_f32_16x16x32_bf16 v[84:87], a[4:7], a[76:79], v[84:87]\n\t"
    "v_mfma_f32_16x16x32_bf16 v[88:91], a[8:11], a[76:79], v[88:91]\n\t"
    "v_mfma_f32_16x16x32_bf16 v[92:95], a[12:15], a[76:79], v[92:95]\n\t"
    "s_waitcnt vmcnt(0) lgkmcnt(0)\n\t"
    "s_barrier\n\t"
    "v_mfma_f32_16x16x32_bf16 v[32:35], a[16:19], a[80:83], v[32:35]\n\t"
    "ds_read_b128 a[32:35], %5 offset:32768\n\t"
    "ds_read_b128 a[36:39], %5 offset:34816\n\t"
    "ds_read_b128 a[40:43], %5 offset:36864\n\t"
    "v_mfma_f32_16x16x32_bf16 v[36:39], a[20:23], a[80:83], v[36:39]\n\t"
    "ds_read_b128 a[44:47], %5 offset:38912\n\t"
    "ds_read_b128 a[48:51], v240 offset:32768\n\t"
    "ds_read_b128 a[52:55], v240 offset:34816\n\t"
    "buffer_load_dwordx2 v[246:247], %11, s[48:51], 0 offen offset:0 sc1\n\t"
    "v_mfma_f32_16x16x32_bf16 v[40:43], a[24:27], a[80:83], v[40:43]\n\t"
    "ds_read_b128 a[56:59], v240 offset:36864\n\t"
    "ds_read_b128 a[60:63], v240 offset:38912\n\t"
    "v_mfma_f32_16x16x32_bf16 v[44:47], a[28:31], a[80:83], v[44:47]\n\t"
    "ds_read_b64_tr_b16 a[0:1], %6 offset:0\n\t"
    "ds_read_b64_tr_b16 a[2:3], %6 offset:3072\n\t"
    "ds_read_b64_tr_b16 a[4:5], %6 offset:1024\n\t"
    "v_mfma_f32_16x16x32_bf16 v[48:51], a[16:19], a[84:87], v[48:51]\n\t"
    "ds_read_b64_tr_b16 a[6:7], %6 offset:4096\n\t"
    "ds_read_b64_tr_b16 a[8:9], %6 offset:2048\n\t"
    "ds_read_b64_tr_b16 a[10:11], %6 offset:5120\n\t"
    "v_mfma_f32_16x16x32_bf16 v[52:55], a[20:23], a[84:87], v[52:55]\n\t"
    "ds_read_b64_tr_b16 a[12:13], %7 offset:0\n\t"
    "ds_read_b64_tr_b16 a[14:15], %7 offset:3072\n\t"
    "v_mfma_f32_16x16x32_bf16 v[56:59], a[24:27], a[84:87], v[56:59]\n\t"
    "v_mfma_f32_16x16x32_bf16 v[60:63], a[28:31], a[84:87], v[60:63]\n\t"
    "v_mfma_f32_16x16x32_bf16 v[64:67], a[16:19], a[88:91], v[64:67]\n\t"
    "v_mfma_f32_16x16x32_bf16 v[68:71], a[20:23], a[88:91], v[68:71]\n\t"
    "v_mfma_f32_16x16x32_bf16 v[72:75], a[24:27], a[88:91], v[72:75]\n\t"
    "v_mfma_f32_16x16x32_bf16 v[76:79], a[28:31], a[88:91], v[76:79]\n\t"
    "v_mfma_f32_16x16x32_bf16 v[80:83], a[16:19], a[92:95], v[80:83]\n\t"
    "v_mfma_f32_16x16x32_bf16 v[84:87], a[20:23], a[92:95], v[84:87]\n\t"
    "v_mfma_f32_16x16x32_bf16 v[88:91], a[24:27], a[92:95], v[88:91]\n\t"
    "v_mfma_f32_16x16x32_bf16 v[92:95], a[28:31], a[92:95], v[92:95]\n\t"
    "s_waitcnt lgkmcnt(5)\n\t"
    "v_mfma_f32_16x16x32_bf16 v[32:35], a[0:3], a[32:35], v[32:35]\n\t"
    "ds_read_b64_tr_b16 a[16:17], %6 offset:6144\n\t"
    "s_waitcnt lgkmcnt(4)\n\t"
    "v_mfma_f32_16x16x32_bf16 v[36:39], a[4:7], a[32:35], v[36:39]\n\t"
    "ds_read_b64_tr_b16 a[18:19], %6 offset:9216\n\t"
    "s_waitcnt lgkmcnt(4)\n\t"
    "v_mfma_f32_16x16x32_bf16 v[40:43], a[8:11], a[32:35], v[40:43]\n\t"
    "ds_read_b64_tr_b16 a[20:21], %6 offset:7168\n\t"
    "s_waitcnt lgkmcnt(3)\n\t"
    "v_mfma_f32_16x16x32_bf16 v[44:47], a[12:15], a[32:35], v[44:47]\n\t"
    "ds_read_b64_tr_b16 a[22:23], %6 offset:10240\n\t"
    "v_mfma_f32_16x16x32_bf16 v[48:51], a[0:3], a[36:39], v[48:51]\n\t"
    "ds_read_b64_tr_b16 a[24:25], %6 offset:8192\n\t"
    "v_mfma_f32_16x16x32_bf16 v[52:55], a[4:7], a[36:39], v[52:55]\n\t"
    "ds_read_b64_tr_b16 a[26:27], %6 offset:11264\n\t"
    "v_mfma_f32_16x16x32_bf16 v[56:59], a[8:11], a[36:39], v[56:59]\n\t"
    "ds_read_b64_tr_b16 a[28:29], %7 offset:6144\n\t"
    "v_mfma_f32_16x16x32_bf16 v[60:63], a[12:15], a[36:39], v[60:63]\n\t"
    "ds_read_b64_tr_b16 a[30:31], %7 offset:9216\n\t"
    "v_mfma_f32_16x16x32_bf16 v[64:67], a[0:3], a[40:43], v[64:67]\n\t"
    "v_mfma_f32_16x16x32_bf16 v[68:71], a[4:7], a[40:43], v[68:71]\n\t"
    "v_mfma_f32_16x16x32_bf16 v[72:75], a[8:11], a[40:43], v[72:75]\n\t"
    "v_mfma_f32_16x16x32_bf16 v[76:79], a[12:15], a[40:43], v[76:79]\n\t"
    "v_mfma_f32_16x16x32_bf16 v[80:83], a[0:3], a[44:47], v[80:83]\n\t"
    "v_mfma_f32_16x16x32_bf16 v[84:87], a[4:7], a[44:47], v[84:87]\n\t"
    "v_mfma_f32_16x16x32_bf16 v[88:91], a[8:11], a[44:47], v[88:91]\n\t"
    "v_mfma_f32_16x16x32_bf16 v[92:95], a[12:15], a[44:47], v[92:95]\n\t"
    "s_waitcnt lgkmcnt(4)\n\t"
    "v_mfma_f32_16x16x32_bf16 v[32:35], a[16:19], a[48:51], v[32:35]\n\t"
    "v_mfma_f32_16x16x32_bf16 v[36:39], a[20:23], a[48:51], v[36:39]\n\t"
    "s_waitcnt lgkmcnt(2)\n\t"
    "v_mfma_f32_16x16x32_bf16 v[40:43], a[24:27], a[48:51], v[40:43]\n\t"
    "s_waitcnt lgkmcnt(0)\n\t"
    "v_mfma_f32_16x16x32_bf16 v[44:47], a[28:31], a[48:51], v[44:47]\n\t"
    "v_mfma_f32_16x16x32_bf16 v[48:51], a[16:19], a[52:55], v[48:51]\n\t"
    "v_mfma_f32_16x16x32_bf16 v[52:55], a[20:23], a[52:55], v[52:55]\n\t"
    "v_mfma_f32_16x16x32_bf16 v[56:59], a[24:27], a[52:55], v[56:59]\n\t"
    "v_mfma_f32_16x16x32_bf16 v[60:63], a[28:31], a[52:55], v[60:63]\n\t"
    "v_mfma_f32_16x16x32_bf16 v[64:67], a[16:19], a[56:59], v[64:67]\n\t"
    "v_mfma_f32_16x16x32_bf16 v[68:71], a[20:23], a[56:59], v[68:71]\n\t"
    "v_mfma_f32_16x16x32_bf16 v[72:75], a[24:27], a[56:59], v[72:75]\n\t"
    "v_mfma_f32_16x16x32_bf16 v[76:79], a[28:31], a[56:59], v[76:79]\n\t"
    "v_mfma_f32_16x16x32_bf16 v[80:83], a[16:19], a[60:63], v[80:83]\n\t"
    "v_mfma_f32_16x16x32_bf16 v[84:87], a[20:23], a[60:63], v[84:87]\n\t"
    "v_mfma_f32_16x16x32_bf16 v[88:91], a[24:27], a[60:63], v[88:91]\n\t"
    "v_mfma_f32_16x16x32_bf16 v[92:95], a[28:31], a[60:63], v[92:95]\n\t"
    "; ---- w\n\t"
    "s_mov_b32 s58, 0x40000\n\t"
    "kvw_chk_%=:\n\t"
    "s_waitcnt vmcnt(0)\n\t"
    "v_cmp_ne_u64 vcc, %12, v[246:247]\n\t"
    "s_nop 0\n\t"
    "s_cbranch_vccz kvw_ok_%=\n\t"
    "s_sleep 2\n\t"
    "buffer_load_dwordx2 v[246:247], %11, s[48:51], 0 offen offset:0 sc1\n\t"
    "s_waitcnt vmcnt(0)\n\t"
    "s_sub_u32 s58, s58, 1\n\t"
    "s_cmp_lg_u32 s58, 0\n\t"
    "s_cbranch_scc1 kvw_chk_%=\n\t"
    "kvw_ok_%=:\n\t"
    "s_lshr_b32 s58, %0, 1\n\t"
    "s_and_b32 s59, %0, 1\n\t"
    "s_lshl_b32 s58, s58, 15\n\t"
    "s_lshl_b32 s59, s59, 13\n\t"
    "s_add_u32 s58, s58, s59\n\t"
    "s_waitcnt lgkmcnt(0)\n\t"
    "s_barrier\n\t"
    "s_mov_b32 m0, s58\n\t"
    "s_nop 0\n\t"
    "buffer_load_dwordx4 v244, s[52:55], 0 offen offset:0 sc1 lds\n\t"
    "buffer_load_dwordx4 v244, s[52:55], 0 offen offset:1024 sc1 lds\n\t"
    "buffer_load_dwordx4 v244, s[52:55], 0 offen offset:2048 sc1 lds\n\t"
    "buffer_load_dwordx4 v244, s[52:55], 0 offen offset:3072 sc1 lds\n\t"
    "s_add_u32 m0, s58, 0x1000\n\t"
    "s_nop 0\n\t"
    "buffer_load_dwordx4 v245, s[52:55], 0 offen offset:0 sc1 lds\n\t"
    "buffer_load_dwordx4 v245, s[52:55], 0 offen offset:1024 sc1 lds\n\t"
    "buffer_load_dwordx4 v245, s[52:55], 0 offen offset:2048 sc1 lds\n\t"
    "buffer_load_dwordx4 v245, s[52:55], 0 offen offset:3072 sc1 lds\n\t"
    "v_lshlrev_b32 v241, 3, v244\n\t"
    "s_mov_b32 s58, 0x10000\n\t"
    "s_mov_b32 s59, 0x4000\n\t"
    "buffer_load_dword a95, v241, s[52:55], s58 offen sc1\n\t"
    "buffer_load_dword a95, v241, s[52:55], s59 offen sc1\n\t"
    "; ---- post\n\t"
    "s_add_u32 s65, %0, 1\n\t"
    "s_lshr_b32 s58, s65, 1\n\t"
    "s_add_u32 s65, s65, s58\n\t"
    "s_lshl_b32 s65, s65, 14\n\t"
    "v_mul_u32_u24 v114, 0x50, v112\n\t"
    "v_lshl_add_u32 v114, v113, 4, v114\n\t"
    "v_add_u32 v114, s65, v114\n\t"
    "v_lshlrev_b32 v115, 4, v112\n\t"
    "v_lshl_add_u32 v115, v113, 20, v115\n\t"
    "v_add_u32 v115, s65, v115\n\t"
    "v_mul_u32_u24 v116, 0x48, v112\n\t"
    "v_add_u32 v116, s65, v116\n\t"
    "v_add_u32 v241, 1, v113\n\t"
    "v_mul_u32_u24 v241, 11, v241\n\t"
    "v_lshrrev_b32 v241, 5, v241\n\t"
    "v_cmp_eq_u32 vcc, 2, v113\n\t"
    "v_add_u32 v242, 1, v113\n\t"
    "v_min_u32 v242, 3, v242\n\t"
    "v_cndmask_b32 v242, v242, 0, vcc\n\t"
    "v_lshl_add_u32 v117, v242, 3, v116\n\t"
    "v_lshl_add_u32 v116, v113, 3, v116\n\t"
    "s_mov_b64 s[66:67], vcc\n\t"
    "v_lshrrev_b32 v120, 4, v244\n\t"
    "v_mul_u32_u24 v120, 0x50, v120\n\t"
    "v_add_u32 v120, s65, v120\n\t"
    "v_add_u32 v121, s65, v244\n\t"
    "v_mov_b32 v123, 0x3f80\n\t"
    "v_mov_b32 v124, 0\n\t"
    "v_cvt_pk_bf16_f32 v128, v32, v33\n\t"
    "v_cvt_pk_bf16_f32 v129, v34, v35\n\t"
    "v_cvt_pk_bf16_f32 v130, v44, v45\n\t"
    "v_cvt_pk_bf16_f32 v131, v46, v47\n\t"
    "v_cvt_pk_bf16_f32 v132, v36, v37\n\t"
    "v_cvt_pk_bf16_f32 v133, v38, v39\n\t"
    "v_mov_b32 v134, v130\n\t"
    "v_mov_b32 v135, v131\n\t"
    "v_cvt_pk_bf16_f32 v144, v48, v49\n\t"
    "v_cvt_pk_bf16_f32 v145, v50, v51\n\t"
    "v_cvt_pk_bf16_f32 v146, v60, v61\n\t"
    "v_cvt_pk_bf16_f32 v147, v62, v63\n\t"
    "v_cvt_pk_bf16_f32 v148, v52, v53\n\t"
    "v_cvt_pk_bf16_f32 v149, v54, v55\n\t"
    "v_mov_b32 v150, v146\n\t"
    "v_mov_b32 v151, v147\n\t"
    "v_cvt_pk_bf16_f32 v160, v64, v65\n\t"
    "v_cvt_pk_bf16_f32 v161, v66, v67\n\t"
    "v_cvt_pk_bf16_f32 v162, v76, v77\n\t"
    "v_cvt_pk_bf16_f32 v163, v78, v79\n\t"
    "v_cvt_pk_bf16_f32 v164, v68, v69\n\t"
    "v_cvt_pk_bf16_f32 v165, v70, v71\n\t"
    "v_mov_b32 v166, v162\n\t"
    "v_mov_b32 v167, v163\n\t"
    "v_cvt_pk_bf16_f32 v176, v80, v81\n\t"
    "v_cvt_pk_bf16_f32 v177, v82, v83\n\t"
    "v_cvt_pk_bf16_f32 v178, v92, v93\n\t"
    "v_cvt_pk_bf16_f32 v179, v94, v95\n\t"
    "v_cvt_pk_bf16_f32 v180, v84, v85\n\t"
    "v_cvt_pk_bf16_f32 v181, v86, v87\n\t"
    "v_mov_b32 v182, v178\n\t"
    "v_mov_b32 v183, v179\n\t"
    "v_mfma_f32_16x16x32_bf16 v[32:35], v[96:99], v[128:131], 0\n\t"
    "v_cvt_pk_bf16_f32 v136, v40, v41\n\t"
    "v_cvt_pk_bf16_f32 v137, v42, v43\n\t"
    "v_mfma_f32_16x16x32_bf16 v[36:39], v[104:107], v[132:135], 0\n\t"
    "v_cndmask_b32 v138, v123, v130, s[66:67]\n\t"
    "v_cndmask_b32 v139, v124, v131, s[66:67]\n\t"
    "v_mfma_f32_16x16x32_bf16 v[44:47], v[100:103], v[128:131], 0\n\t"
    "v_cvt_pk_bf16_f32 v152, v56, v57\n\t"
    "v_cvt_pk_bf16_f32 v153, v58, v59\n\t"
    "v_mfma_f32_16x16x32_bf16 v[48:51], v[96:99], v[144:147], 0\n\t"
    "v_cndmask_b32 v154, v123, v146, s[66:67]\n\t"
    "v_cndmask_b32 v155, v124, v147, s[66:67]\n\t"
    "v_mfma_f32_16x16x32_bf16 v[52:55], v[104:107], v[148:151], 0\n\t"
    "v_cvt_pk_bf16_f32 v168, v72, v73\n\t"
    "v_cvt_pk_bf16_f32 v169, v74, v75\n\t"
    "v_mfma_f32_16x16x32_bf16 v[60:63], v[100:103], v[144:147], 0\n\t"
    "v_cndmask_b32 v170, v123, v162, s[66:67]\n\t"
    "v_cndmask_b32 v171, v124, v163, s[66:67]\n\t"
    "v_mfma_f32_16x16x32_bf16 v[64:67], v[96:99], v[160:163], 0\n\t"
    "v_cvt_pk_bf16_f32 v184, v88, v89\n\t"
    "v_cvt_pk_bf16_f32 v185, v90, v91\n\t"
    "v_mfma_f32_16x16x32_bf16 v[68:71], v[104:107], v[164:167], 0\n\t"
    "v_cndmask_b32 v186, v123, v178, s[66:67]\n\t"
    "v_cndmask_b32 v187, v124, v179, s[66:67]\n\t"
    "v_mfma_f32_16x16x32_bf16 v[76:79], v[100:103], v[160:163], 0\n\t"
    "v_mfma_f32_16x16x32_bf16 v[80:83], v[96:99], v[176:179], 0\n\t"
    "v_mfma_f32_16x16x32_bf16 v[84:87], v[104:107], v[180:183], 0\n\t"
    "v_mfma_f32_16x16x32_bf16 v[92:95], v[100:103], v[176:179], 0\n\t"
    "v_mfma_f32_16x16x32_bf16 v[44:47], v[108:111], v[132:135], v[44:47]\n\t"
    "v_mfma_f32_16x16x32_bf16 v[60:63], v[108:111], v[148:151], v[60:63]\n\t"
    "v_mfma_f32_16x16x32_bf16 v[76:79], v[108:111], v[164:167], v[76:79]\n\t"
    "v_mfma_f32_16x16x32_bf16 v[92:95], v[108:111], v[180:183], v[92:95]\n\t"
    "ds_write_b128 v114, v[32:35] offset:0\n\t"
    "ds_write_b128 v114, v[36:39] offset:6144\n\t"
    "ds_write_b128 v114, v[48:51] offset:1280\n\t"
    "ds_write_b128 v114, v[52:55] offset:7424\n\t"
    "ds_write_b128 v114, v[64:67] offset:2560\n\t"
    "ds_write_b128 v114, v[68:71] offset:8704\n\t"
    "ds_write_b128 v114, v[80:83] offset:3840\n\t"
    "ds_write_b128 v114, v[84:87] offset:9984\n\t"
    "ds_write_b128 v115, v[44:47] offset:5120\n\t"
    "ds_write_b128 v115, v[60:63] offset:5376\n\t"
    "ds_write_b128 v115, v[76:79] offset:5632\n\t"
    "ds_write_b128 v115, v[92:95] offset:5888\n\t"
    "ds_read_b128 v[32:35], v120 offset:0\n\t"
    "ds_read_b128 v[36:39], v120 offset:16\n\t"
    "ds_read_b128 v[40:43], v120 offset:32\n\t"
    "ds_read_b128 v[44:47], v120 offset:48\n\t"
    "ds_read_b128 v[52:55], v120 offset:6144\n\t"
    "ds_read_b128 v[56:59], v120 offset:6160\n\t"
    "ds_read_b128 v[60:63], v120 offset:6176\n\t"
    "ds_read_b128 v[64:67], v120 offset:6192\n\t"
    "ds_read_b128 v[72:75], v121 offset:5120\n\t"
    "ds_write_b64 v116, v[136:137] offset:11264\n\t"
    "ds_write_b64 v117, v[138:139] offset:11296\n\t"
    "ds_write_b64 v116, v[152:153] offset:12416\n\t"
    "ds_write_b64 v117, v[154:155] offset:12448\n\t"
    "ds_write_b64 v116, v[168:169] offset:13568\n\t"
    "ds_write_b64 v117, v[170:171] offset:13600\n\t"
    "ds_write_b64 v116, v[184:185] offset:14720\n\t"
    "ds_write_b64 v117, v[186:187] offset:14752\n\t"
    "s_waitcnt lgkmcnt(8)\n\t"
    "v_mov_b64 v[48:49], v[72:73]\n\t"
    "v_mov_b64 v[68:69], v[74:75]\n\t"
    "s_mov_b32 s68, 0x3fb8aa3b\n\t"
    "s_mov_b32 s70, 0xbfb8aa3b\n\t"
    "v_max_f32 v76, v32, v33\n\t"
    "v_min_f32 v78, v32, v33\n\t"
    "v_max_f32 v77, v52, v53\n\t"
    "v_min_f32 v79, v52, v53\n\t"
    "v_max3_f32 v76, v76, v34, v35\n\t"
    "v_min3_f32 v78, v78, v34, v35\n\t"
    "v_max3_f32 v77, v77, v54, v55\n\t"
    "v_min3_f32 v79, v79, v54, v55\n\t"
    "v_max3_f32 v76, v76, v36, v37\n\t"
    "v_min3_f32 v78, v78, v36, v37\n\t"
    "v_max3_f32 v77, v77, v56, v57\n\t"
    "v_min3_f32 v79, v79, v56, v57\n\t"
    "v_max3_f32 v76, v76, v38, v39\n\t"
    "v_min3_f32 v78, v78, v38, v39\n\t"
    "v_max3_f32 v77, v77, v58, v59\n\t"
    "v_min3_f32 v79, v79, v58, v59\n\t"
    "v_max3_f32 v76, v76, v40, v41\n\t"
    "v_min3_f32 v78, v78, v40, v41\n\t"
    "v_max3_f32 v77, v77, v60, v61\n\t"
    "v_min3_f32 v79, v79, v60, v61\n\t"
    "v_max3_f32 v76, v76, v42, v43\n\t"
    "v_min3_f32 v78, v78, v42, v43\n\t"
    "v_max3_f32 v77, v77, v62, v63\n\t"
    "v_min3_f32 v79, v79, v62, v63\n\t"
    "v_max3_f32 v76, v76, v44, v45\n\t"
    "v_min3_f32 v78, v78, v44, v45\n\t"
    "v_max3_f32 v77, v77, v64, v65\n\t"
    "v_min3_f32 v79, v79, v64, v65\n\t"
    "v_max3_f32 v76, v76, v46, v47\n\t"
    "v_min3_f32 v78, v78, v46, v47\n\t"
    "v_max3_f32 v77, v77, v66, v67\n\t"
    "v_min3_f32 v79, v79, v66, v67\n\t"
    "v_max3_f32 v76, v76, v48, v49\n\t"
    "v_min3_f32 v78, v78, v48, v49\n\t"
    "v_max3_f32 v77, v77, v68, v69\n\t"
    "v_min3_f32 v79, v79, v68, v69\n\t"
    "v_mul_f32 v80, 0xbfb8aa3b, v76\n\t"
    "v_mul_f32 v84, 0x3fb8aa3b, v78\n\t"
    "v_mul_f32 v82, 0xbfb8aa3b, v77\n\t"
    "v_mul_f32 v86, 0x3fb8aa3b, v79\n\t"
    "v_pk_fma_f32 v[128:129], v[32:33], s[68:69], v[80:81] op_sel_hi:[1,0,0]\n\t"
    "v_pk_fma_f32 v[146:147], v[32:33], s[70:71], v[84:85] op_sel_hi:[1,0,0]\n\t"
    "v_pk_fma_f32 v[130:131], v[34:35], s[68:69], v[80:81] op_sel_hi:[1,0,0]\n\t"
    "v_pk_fma_f32 v[148:149], v[34:35], s[70:71], v[84:85] op_sel_hi:[1,0,0]\n\t"
    "v_pk_fma_f32 v[132:133], v[36:37], s[68:69], v[80:81] op_sel_hi:[1,0,0]\n\t"
    "v_pk_fma_f32 v[150:151], v[36:37], s[70:71], v[84:85] op_sel_hi:[1,0,0]\n\t"
    "v_pk_fma_f32 v[134:135], v[38:39], s[68:69], v[80:81] op_sel_hi:[1,0,0]\n\t"
    "v_pk_fma_f32 v[152:153], v[38:39], s[70:71], v[84:85] op_sel_hi:[1,0,0]\n\t"
    "v_pk_fma_f32 v[136:137], v[40:41], s[68:69], v[80:81] op_sel_hi:[1,0,0]\n\t"
    "v_pk_fma_f32 v[154:155], v[40:41], s[70:71], v[84:85] op_sel_hi:[1,0,0]\n\t"
    "v_pk_fma_f32 v[138:139], v[42:43], s[68:69], v[80:81] op_sel_hi:[1,0,0]\n\t"
    "v_pk_fma_f32 v[156:157], v[42:43], s[70:71], v[84:85] op_sel_hi:[1,0,0]\n\t"
    "v_pk_fma_f32 v[140:141], v[44:45], s[68:69], v[80:81] op_sel_hi:[1,0,0]\n\t"
    "v_pk_fma_f32 v[158:159], v[44:45], s[70:71], v[84:85] op_sel_hi:[1,0,0]\n\t"
    "v_pk_fma_f32 v[142:143], v[46:47], s[68:69], v[80:81] op_sel_hi:[1,0,0]\n\t"
    "v_pk_fma_f32 v[160:161], v[46:47], s[70:71], v[84:85] op_sel_hi:[1,0,0]\n\t"
    "v_pk_fma_f32 v[144:145], v[48:49], s[68:69], v[80:81] op_sel_hi:[1,0,0]\n\t"
    "v_pk_fma_f32 v[162:163], v[48:49], s[70:71], v[84:85] op_sel_hi:[1,0,0]\n\t"
    "v_pk_fma_f32 v[164:165], v[52:53], s[68:69], v[82:83] op_sel_hi:[1,0,0]\n\t"
    "v_pk_fma_f32 v[182:183], v[52:53], s[70:71], v[86:87] op_sel_hi:[1,0,0]\n\t"
    "v_pk_fma_f32 v[166:167], v[54:55], s[68:69], v[82:83] op_sel_hi:[1,0,0]\n\t"
    "v_pk_fma_f32 v[184:185], v[54:55], s[70:71], v[86:87] op_sel_hi:[1,0,0]\n\t"
    "v_pk_fma_f32 v[168:169], v[56:57], s[68:69], v[82:83] op_sel_hi:[1,0,0]\n\t"
    "v_pk_fma_f32 v[186:187], v[56:57], s[70:71], v[86:87] op_sel_hi:[1,0,0]\n\t"
    "v_pk_fma_f32 v[170:171], v[58:59], s[68:69], v[82:83] op_sel_hi:[1,0,0]\n\t"
    "v_pk_fma_f32 v[188:189], v[58:59], s[70:71], v[86:87] op_sel_hi:[1,0,0]\n\t"
    "v_pk_fma_f32 v[172:173], v[60:61], s[68:69], v[82:83] op_sel_hi:[1,0,0]\n\t"
    "v_pk_fma_f32 v[190:191], v[60:61], s[70:71], v[86:87] op_sel_hi:[1,0,0]\n\t"
    "v_pk_fma_f32 v[174:175], v[62:63], s[68:69], v[82:83] op_sel_hi:[1,0,0]\n\t"
    "v_pk_fma_f32 v[192:193], v[62:63], s[70:71], v[86:87] op_sel_hi:[1,0,0]\n\t"
    "v_pk_fma_f32 v[176:177], v[64:65], s[68:69], v[82:83] op_sel_hi:[1,0,0]\n\t"
    "v_pk_fma_f32 v[194:195], v[64:65], s[70:71], v[86:87] op_sel_hi:[1,0,0]\n\t"
    "v_pk_fma_f32 v[178:179], v[66:67], s[68:69], v[82:83] op_sel_hi:[1,0,0]\n\t"
    "v_pk_fma_f32 v[196:197], v[66:67], s[70:71], v[86:87] op_sel_hi:[1,0,0]\n\t"
    "v_pk_fma_f32 v[180:181], v[68:69], s[68:69], v[82:83] op_sel_hi:[1,0,0]\n\t"
    "v_pk_fma_f32 v[198:199], v[68:69], s[70:71], v[86:87] op_sel_hi:[1,0,0]\n\t"
    "v_exp_f32 v164, v164\n\t"
    "v_exp_f32 v165, v165\n\t"
    "v_exp_f32 v166, v166\n\t"
    "v_exp_f32 v167, v167\n\t"
    "v_exp_f32 v168, v168\n\t"
    "v_exp_f32 v169, v169\n\t"
    "v_exp_f32 v170, v170\n\t"
    "v_exp_f32 v171, v171\n\t"
    "v_exp_f32 v172, v172\n\t"
    "v_exp_f32 v173, v173\n\t"
    "v_exp_f32 v174, v174\n\t"
    "v_exp_f32 v175, v175\n\t"
    "v_exp_f32 v176, v176\n\t"
    "v_exp_f32 v177, v177\n\t"
    "v_exp_f32 v178, v178\n\t"
    "v_exp_f32 v179, v179\n\t"
    "v_exp_f32 v180, v180\n\t"
    "v_exp_f32 v181, v181\n\t"
    "v_exp_f32 v182, v182\n\t"
    "v_exp_f32 v183, v183\n\t"
    "v_exp_f32 v184, v184\n\t"
    "v_exp_f32 v185, v185\n\t"
    "v_exp_f32 v186, v186\n\t"
    "v_exp_f32 v187, v187\n\t"
    "v_exp_f32 v188, v188\n\t"
    "v_exp_f32 v189, v189\n\t"
    "v_exp_f32 v190, v190\n\t"
    "v_exp_f32 v191, v191\n\t"
    "v_exp_f32 v192, v192\n\t"
    "v_exp_f32 v193, v193\n\t"
    "v_exp_f32 v194, v194\n\t"
    "v_exp_f32 v195, v195\n\t"
    "v_exp_f32 v196, v196\n\t"
    "v_exp_f32 v197, v197\n\t"
    "v_exp_f32 v198, v198\n\t"
    "v_exp_f32 v199, v199\n\t"
    "v_pk_add_f32 v[92:93], v[164:165], v[166:167]\n\t"
    "v_pk_add_f32 v[94:95], v[182:183], v[184:185]\n\t"
    "v_pk_add_f32 v[92:93], v[92:93], v[168:169]\n\t"
    "v_pk_add_f32 v[94:95], v[94:95], v[186:187]\n\t"
    "v_pk_add_f32 v[92:93], v[92:93], v[170:171]\n\t"
    "v_pk_add_f32 v[94:95], v[94:95], v[188:189]\n\t"
    "v_pk_add_f32 v[92:93], v[92:93], v[172:173]\n\t"
    "v_pk_add_f32 v[94:95], v[94:95], v[190:191]\n\t"
    "v_pk_add_f32 v[92:93], v[92:93], v[174:175]\n\t"
    "v_pk_add_f32 v[94:95], v[94:95], v[192:193]\n\t"
    "v_pk_add_f32 v[92:93], v[92:93], v[176:177]\n\t"
    "v_pk_add_f32 v[94:95], v[94:95], v[194:195]\n\t"
    "v_pk_add_f32 v[92:93], v[92:93], v[178:179]\n\t"
    "v_pk_add_f32 v[94:95], v[94:95], v[196:197]\n\t"
    "v_pk_add_f32 v[92:93], v[92:93], v[180:181]\n\t"
    "v_pk_add_f32 v[94:95], v[94:95], v[198:199]\n\t"
    "v_add_f32 v92, v92, v93\n\t"
    "v_add_f32 v94, v94, v95\n\t"
    "v_rcp_f32 v92, v92\n\t"
    "v_rcp_f32 v94, v94\n\t"
    "v_pk_mul_f32 v[164:165], v[164:165], v[92:93] op_sel_hi:[1,0]\n\t"
    "v_pk_mul_f32 v[166:167], v[166:167], v[92:93] op_sel_hi:[1,0]\n\t"
    "v_pk_mul_f32 v[168:169], v[168:169], v[92:93] op_sel_hi:[1,0]\n\t"
    "v_pk_mul_f32 v[170:171], v[170:171], v[92:93] op_sel_hi:[1,0]\n\t"
    "v_pk_mul_f32 v[172:173], v[172:173], v[92:93] op_sel_hi:[1,0]\n\t"
    "v_pk_mul_f32 v[174:175], v[174:175], v[92:93] op_sel_hi:[1,0]\n\t"
    "v_pk_mul_f32 v[176:177], v[176:177], v[92:93] op_sel_hi:[1,0]\n\t"
    "v_pk_mul_f32 v[178:179], v[178:179], v[92:93] op_sel_hi:[1,0]\n\t"
    "v_pk_mul_f32 v[180:181], v[180:181], v[92:93] op_sel_hi:[1,0]\n\t"
    "v_pk_mul_f32 v[182:183], v[182:183], v[94:95] op_sel_hi:[1,0]\n\t"
    "v_pk_mul_f32 v[184:185], v[184:185], v[94:95] op_sel_hi:[1,0]\n\t"
    "v_pk_mul_f32 v[186:187], v[186:187], v[94:95] op_sel_hi:[1,0]\n\t"
    "v_pk_mul_f32 v[188:189], v[188:189], v[94:95] op_sel_hi:[1,0]\n\t"
    "v_pk_mul_f32 v[190:191], v[190:191], v[94:95] op_sel_hi:[1,0]\n\t"
    "v_pk_mul_f32 v[192:193], v[192:193], v[94:95] op_sel_hi:[1,0]\n\t"
    "v_pk_mul_f32 v[194:195], v[194:195], v[94:95] op_sel_hi:[1,0]\n\t"
    "v_pk_mul_f32 v[196:197], v[196:197], v[94:95] op_sel_hi:[1,0]\n\t"
    "v_pk_mul_f32 v[198:199], v[198:199], v[94:95] op_sel_hi:[1,0]\n\t"
    "v_cvt_pk_bf16_f32 v32, v164, v165\n\t"
    "v_cvt_pk_bf16_f32 v33, v166, v167\n\t"
    "v_cvt_pk_bf16_f32 v34, v168, v169\n\t"
    "v_cvt_pk_bf16_f32 v35, v170, v171\n\t"
    "v_cvt_pk_bf16_f32 v36, v172, v173\n\t"
    "v_cvt_pk_bf16_f32 v37, v174, v175\n\t"
    "v_cvt_pk_bf16_f32 v38, v176, v177\n\t"
    "v_cvt_pk_bf16_f32 v39, v178, v179\n\t"
    "v_cvt_pk_bf16_f32 v40, v180, v181\n\t"
    "v_cvt_pk_bf16_f32 v41, v182, v183\n\t"
    "v_cvt_pk_bf16_f32 v42, v184, v185\n\t"
    "v_cvt_pk_bf16_f32 v43, v186, v187\n\t"
    "v_cvt_pk_bf16_f32 v44, v188, v189\n\t"
    "v_cvt_pk_bf16_f32 v45, v190, v191\n\t"
    "v_cvt_pk_bf16_f32 v46, v192, v193\n\t"
    "v_cvt_pk_bf16_f32 v47, v194, v195\n\t"
    "v_cvt_pk_bf16_f32 v48, v196, v197\n\t"
    "v_cvt_pk_bf16_f32 v49, v198, v199\n\t"
    "v_mov_b32 v50, 0\n\t"
    "v_mov_b32 v51, 0\n\t"
    "ds_write_b128 v120, v[32:35] offset:0\n\t"
    "ds_write_b128 v120, v[36:39] offset:16\n\t"
    "ds_write_b128 v120, v[40:43] offset:32\n\t"
    "ds_write_b128 v120, v[44:47] offset:48\n\t"
    "ds_write_b128 v120, v[48:51] offset:64\n\t"
    "v_lshlrev_b32 v241, 3, v113\n\t"
    "v_bfe_u32 v242, v244, 6, 2\n\t"
    "v_add_u32 v241, v241, v242\n\t"
    "v_bfe_u32 v242, v244, 4, 2\n\t"
    "v_mul_u32_u24 v118, 0x50, v241\n\t"
    "v_add_u32 v118, s65, v118\n\t"
    "v_mul_u32_u24 v119, 0x48, v241\n\t"
    "v_add_u32 v119, s65, v119\n\t"
    "v_lshl_add_u32 v119, v242, 3, v119\n\t"
    "v_min_u32 v241, 1, v242\n\t"
    "v_lshl_add_u32 v114, v241, 3, v118\n\t"
    "v_lshl_add_u32 v118, v242, 3, v118\n\t"
    "ds_read_b64_tr_b16 a[0:1], v118 offset:0\n\t"
    "ds_read_b64_tr_b16 a[4:5], v118 offset:32\n\t"
    "ds_read_b64_tr_b16 a[8:9], v114 offset:64\n\t"
    "ds_read_b64_tr_b16 a[12:13], v119 offset:11264\n\t"
    "ds_read_b64_tr_b16 a[16:17], v119 offset:11296\n\t"
    "ds_read_b64_tr_b16 a[2:3], v118 offset:320\n\t"
    "ds_read_b64_tr_b16 a[6:7], v118 offset:352\n\t"
    "ds_read_b64_tr_b16 a[10:11], v114 offset:384\n\t"
    "ds_read_b64_tr_b16 a[14:15], v119 offset:11552\n\t"
    "ds_read_b64_tr_b16 a[18:19], v119 offset:11584\n\t"
    "ds_read_b64_tr_b16 a[20:21], v118 offset:2560\n\t"
    "ds_read_b64_tr_b16 a[24:25], v118 offset:2592\n\t"
    "ds_read_b64_tr_b16 a[28:29], v114 offset:2624\n\t"
    "ds_read_b64_tr_b16 a[32:33], v119 offset:13568\n\t"
    "ds_read_b64_tr_b16 a[36:37], v119 offset:13600\n\t"
    "ds_read_b64_tr_b16 a[22:23], v118 offset:2880\n\t"
    "ds_read_b64_tr_b16 a[26:27], v118 offset:2912\n\t"
    "ds_read_b64_tr_b16 a[30:31], v114 offset:2944\n\t"
    "ds_read_b64_tr_b16 a[34:35], v119 offset:13856\n\t"
    "ds_read_b64_tr_b16 a[38:39], v119 offset:13888\n\t"
    "v_exp_f32 v128, v128\n\t"
    "v_exp_f32 v129, v129\n\t"
    "v_exp_f32 v130, v130\n\t"
    "v_exp_f32 v131, v131\n\t"
    "v_exp_f32 v132, v132\n\t"
    "v_exp_f32 v133, v133\n\t"
    "v_exp_f32 v134, v134\n\t"
    "v_exp_f32 v135, v135\n\t"
    "v_exp_f32 v136, v136\n\t"
    "v_exp_f32 v137, v137\n\t"
    "v_exp_f32 v138, v138\n\t"
    "v_exp_f32 v139, v139\n\t"
    "s_waitcnt lgkmcnt(10)\n\t"
    "v_mfma_f32_16x16x32_bf16 v[96:99], a[0:3], a[12:15], 0\n\t"
    "v_exp_f32 v140, v140\n\t"
    "v_exp_f32 v141, v141\n\t"
    "v_mfma_f32_16x16x32_bf16 v[100:103], a[0:3], a[16:19], 0\n\t"
    "v_exp_f32 v142, v142\n\t"
    "v_exp_f32 v143, v143\n\t"
    "v_mfma_f32_16x16x32_bf16 v[104:107], a[4:7], a[12:15], 0\n\t"
    "v_exp_f32 v144, v144\n\t"
    "v_exp_f32 v145, v145\n\t"
    "v_mfma_f32_16x16x32_bf16 v[108:111], a[4:7], a[16:19], 0\n\t"
    "v_exp_f32 v146, v146\n\t"
    "v_exp_f32 v147, v147\n\t"
    "v_mfma_f32_16x16x32_bf16 v[112:115], a[8:11], a[12:15], 0\n\t"
    "v_exp_f32 v148, v148\n\t"
    "v_exp_f32 v149, v149\n\t"
    "v_mfma_f32_16x16x32_bf16 v[116:119], a[8:11], a[16:19], 0\n\t"
    "v_exp_f32 v150, v150\n\t"
    "v_exp_f32 v151, v151\n\t"
    "s_waitcnt lgkmcnt(0)\n\t"
    "v_mfma_f32_16x16x32_bf16 v[96:99], a[20:23], a[32:35], v[96:99]\n\t"
    "v_exp_f32 v152, v152\n\t"
    "v_exp_f32 v153, v153\n\t"
    "v_mfma_f32_16x16x32_bf16 v[100:103], a[20:23], a[36:39], v[100:103]\n\t"
    "v_exp_f32 v154, v154\n\t"
    "v_exp_f32 v155, v155\n\t"
    "v_mfma_f32_16x16x32_bf16 v[104:107], a[24:27], a[32:35], v[104:107]\n\t"
    "v_exp_f32 v156, v156\n\t"
    "v_exp_f32 v157, v157\n\t"
    "v_mfma_f32_16x16x32_bf16 v[108:111], a[24:27], a[36:39], v[108:111]\n\t"
    "v_exp_f32 v158, v158\n\t"
    "v_exp_f32 v159, v159\n\t"
    "v_mfma_f32_16x16x32_bf16 v[112:115], a[28:31], a[32:35], v[112:115]\n\t"
    "v_exp_f32 v160, v160\n\t"
    "v_exp_f32 v161, v161\n\t"
    "v_mfma_f32_16x16x32_bf16 v[116:119], a[28:31], a[36:39], v[116:119]\n\t"
    "v_exp_f32 v162, v162\n\t"
    "v_exp_f32 v163, v163\n\t"
    "v_pk_add_f32 v[88:89], v[128:129], v[130:131]\n\t"
    "v_pk_add_f32 v[90:91], v[146:147], v[148:149]\n\t"
    "v_pk_add_f32 v[88:89], v[88:89], v[132:133]\n\t"
    "v_pk_add_f32 v[90:91], v[90:91], v[150:151]\n\t"
    "ds_write_b128 v121, v[96:99] offset:6144\n\t"
    "ds_write_b128 v121, v[100:103] offset:7168\n\t"
    "ds_write_b128 v121, v[104:107] offset:8192\n\t"
    "ds_write_b128 v121, v[108:111] offset:9216\n\t"
    "ds_write_b128 v121, v[112:115] offset:10240\n\t"
    "ds_write_b128 v121, v[116:119] offset:11264\n\t"
    "v_pk_add_f32 v[88:89], v[88:89], v[134:135]\n\t"
    "v_pk_add_f32 v[90:91], v[90:91], v[152:153]\n\t"
    "v_pk_add_f32 v[88:89], v[88:89], v[136:137]\n\t"
    "v_pk_add_f32 v[90:91], v[90:91], v[154:155]\n\t"
    "v_pk_add_f32 v[88:89], v[88:89], v[138:139]\n\t"
    "v_pk_add_f32 v[90:91], v[90:91], v[156:157]\n\t"
    "v_pk_add_f32 v[88:89], v[88:89], v[140:141]\n\t"
    "v_pk_add_f32 v[90:91], v[90:91], v[158:159]\n\t"
    "v_pk_add_f32 v[88:89], v[88:89], v[142:143]\n\t"
    "v_pk_add_f32 v[90:91], v[90:91], v[160:161]\n\t"
    "v_pk_add_f32 v[88:89], v[88:89], v[144:145]\n\t"
    "v_pk_add_f32 v[90:91], v[90:91], v[162:163]\n\t"
    "v_add_f32 v88, v88, v89\n\t"
    "v_add_f32 v90, v90, v91\n\t"
    "v_rcp_f32 v88, v88\n\t"
    "v_rcp_f32 v90, v90\n\t"
    "v_pk_mul_f32 v[128:129], v[128:129], v[88:89] op_sel_hi:[1,0]\n\t"
    "v_pk_mul_f32 v[130:131], v[130:131], v[88:89] op_sel_hi:[1,0]\n\t"
    "v_pk_mul_f32 v[132:133], v[132:133], v[88:89] op_sel_hi:[1,0]\n\t"
    "v_pk_mul_f32 v[134:135], v[134:135], v[88:89] op_sel_hi:[1,0]\n\t"
    "v_pk_mul_f32 v[136:137], v[136:137], v[88:89] op_sel_hi:[1,0]\n\t"
    "v_pk_mul_f32 v[138:139], v[138:139], v[88:89] op_sel_hi:[1,0]\n\t"
    "v_pk_mul_f32 v[140:141], v[140:141], v[88:89] op_sel_hi:[1,0]\n\t"
    "v_pk_mul_f32 v[142:143], v[142:143], v[88:89] op_sel_hi:[1,0]\n\t"
    "v_pk_mul_f32 v[144:145], v[144:145], v[88:89] op_sel_hi:[1,0]\n\t"
    "v_pk_mul_f32 v[146:147], v[146:147], v[90:91] op_sel_hi:[1,0]\n\t"
    "v_pk_mul_f32 v[148:149], v[148:149], v[90:91] op_sel_hi:[1,0]\n\t"
    "v_pk_mul_f32 v[150:151], v[150:151], v[90:91] op_sel_hi:[1,0]\n\t"
    "v_pk_mul_f32 v[152:153], v[152:153], v[90:91] op_sel_hi:[1,0]\n\t"
    "v_pk_mul_f32 v[154:155], v[154:155], v[90:91] op_sel_hi:[1,0]\n\t"
    "v_pk_mul_f32 v[156:157], v[156:157], v[90:91] op_sel_hi:[1,0]\n\t"
    "v_pk_mul_f32 v[158:159], v[158:159], v[90:91] op_sel_hi:[1,0]\n\t"
    "v_pk_mul_f32 v[160:161], v[160:161], v[90:91] op_sel_hi:[1,0]\n\t"
    "v_pk_mul_f32 v[162:163], v[162:163], v[90:91] op_sel_hi:[1,0]\n\t"
    "s_lshl_b32 s58, %10, 2\n\t"
    "s_add_u32 s58, s58, %0\n\t"
    "s_mul_i32 s58, s58, 0x1400\n\t"
    "s_sub_u32 s58, 0x800000, s58\n\t"
    "s_sub_u32 s40, s48, s58\n\t"
    "s_subb_u32 s41, s49, 0\n\t"
    "s_mov_b32 s42, 0x1400\n\t"
    "s_mul_i32 s58, %10, 0x1800\n\t"
    "s_sub_u32 s58, 0x200000, s58\n\t"
    "s_sub_u32 s44, s48, s58\n\t"
    "s_subb_u32 s45, s49, 0\n\t"
    "s_mov_b32 s46, 0x1800\n\t"
    "s_waitcnt vmcnt(0) lgkmcnt(0)\n\t"
    "s_barrier\n\t"
    "s_mul_i32 s58, %0, 0x600\n\t"
    "v_add_u32 v241, s58, v244\n\t"
    "v_add_u32 v242, 0x10000, v241\n\t"
    "ds_read_b128 v[56:59], v241 offset:22528\n\t"
    "ds_read_b128 v[72:75], v241 offset:23552\n\t"
    "ds_read_b128 v[60:63], v241 offset:55296\n\t"
    "ds_read_b128 v[76:79], v241 offset:56320\n\t"
    "ds_read_b128 v[64:67], v242 offset:6144\n\t"
    "ds_read_b128 v[80:83], v242 offset:7168\n\t"
    "ds_read_b128 v[68:71], v242 offset:38912\n\t"
    "ds_read_b128 v[84:87], v242 offset:39936\n\t"
    "v_cvt_pk_bf16_f32 v32, v128, v129\n\t"
    "v_cvt_pk_bf16_f32 v33, v130, v131\n\t"
    "v_cvt_pk_bf16_f32 v34, v144, v145\n\t"
    "v_cvt_pk_bf16_f32 v35, v146, v147\n\t"
    "buffer_store_dwordx4 v[32:35], v244, s[40:43], 0 offen offset:0 sc1\n\t"
    "v_cvt_pk_bf16_f32 v36, v132, v133\n\t"
    "v_cvt_pk_bf16_f32 v37, v134, v135\n\t"
    "v_cvt_pk_bf16_f32 v38, v148, v149\n\t"
    "v_cvt_pk_bf16_f32 v39, v150, v151\n\t"
    "buffer_store_dwordx4 v[36:39], v244, s[40:43], 0 offen offset:1024 sc1\n\t"
    "v_cvt_pk_bf16_f32 v40, v136, v137\n\t"
    "v_cvt_pk_bf16_f32 v41, v138, v139\n\t"
    "v_cvt_pk_bf16_f32 v42, v152, v153\n\t"
    "v_cvt_pk_bf16_f32 v43, v154, v155\n\t"
    "buffer_store_dwordx4 v[40:43], v244, s[40:43], 0 offen offset:2048 sc1\n\t"
    "v_cvt_pk_bf16_f32 v44, v140, v141\n\t"
    "v_cvt_pk_bf16_f32 v45, v142, v143\n\t"
    "v_cvt_pk_bf16_f32 v46, v156, v157\n\t"
    "v_cvt_pk_bf16_f32 v47, v158, v159\n\t"
    "buffer_store_dwordx4 v[44:47], v244, s[40:43], 0 offen offset:3072 sc1\n\t"
    "v_cvt_pk_bf16_f32 v48, v160, v161\n\t"
    "v_cvt_pk_bf16_f32 v49, v162, v163\n\t"
    "v_mov_b32 v50, 0\n\t"
    "v_mov_b32 v51, 0\n\t"
    "buffer_store_dwordx4 v[48:51], v245, s[40:43], 0 offen offset:0 sc1\n\t"
    "s_waitcnt lgkmcnt(0)\n\t"
    "v_pk_add_f32 v[56:57], v[56:57], v[60:61]\n\t"
    "v_pk_add_f32 v[58:59], v[58:59], v[62:63]\n\t"
    "v_pk_add_f32 v[64:65], v[64:65], v[68:69]\n\t"
    "v_pk_add_f32 v[66:67], v[66:67], v[70:71]\n\t"
    "v_pk_add_f32 v[56:57], v[56:57], v[64:65]\n\t"
    "v_pk_add_f32 v[58:59], v[58:59], v[66:67]\n\t"
    "v_pk_add_f32 v[72:73], v[72:73], v[76:77]\n\t"
    "v_pk_add_f32 v[74:75], v[74:75], v[78:79]\n\t"
    "v_pk_add_f32 v[80:81], v[80:81], v[84:85]\n\t"
    "v_pk_add_f32 v[82:83], v[82:83], v[86:87]\n\t"
    "v_pk_add_f32 v[72:73], v[72:73], v[80:81]\n\t"
    "v_pk_add_f32 v[74:75], v[74:75], v[82:83]\n\t"
    "v_add_u32 v242, 0x400, v241\n\t"
    "v_lshl_add_u32 v242, v244, 21, v242\n\t"
    "v_cmp_gt_u32 vcc, 0x200, v244\n\t"
    "v_add_u32 v114, 0x400, v241\n\t"
    "s_nop 0\n\t"
    "v_cndmask_b32 v242, v242, v114, vcc\n\t"
    "buffer_store_dwordx4 v[56:59], v241, s[44:47], 0 offen offset:0 sc1\n\t"
    "buffer_store_dwordx4 v[72:75], v242, s[44:47], 0 offen offset:0 sc1\n\t"
    "s_nop 1\n\t"
      :: "s"(w), "s"(pRb), "s"(2048u * 1152u - offRb), "s"(pTb), "s"(576u * 3456u - offTb), "v"(w * 8192 + c15 * 128 + ((g4 ^ ((c15 >> 1) & 7)) * 16)),
         "v"(0x10000 + (l >> 5) * 512 + r8 * 64 + ((o + 8 * p4) ^ (32 * (r8 >> 2)))), "v"(0x10000 + (l >> 5) * 512 + (p4 < 2 ? p4 : 2) * 1024 + r8 * 64 + ((o + 32) ^ (32 * (r8 >> 2)))),
         "v"(bt_voff(l)), "s"(uni64(ws + WS_FLAG)), "s"(bid), "v"(FL_KV + ((bid & 31) + 32 * (l & 7)) * 128), "s"(tag), "s"(pKV) DBGOP(3)
      : "v14", "v15", "v16", "v17", "v18", "v19", "v20", "v21", "v22", "v23", "v24", "v25", "v26", "v27", "v28", "v29", "v30", "v31", "v32", "v33", "v34", "v35", "v36", "v37", "v38", "v39", "v40", "v41", "v42", "v43", "v44", "v45", "v46", "v47", "v48", "v49", "v50", "v51", "v52", "v53", "v54", "v55", "v56", "v57", "v58", "v59", "v60", "v61", "v62", "v63", "v64", "v65", "v66", "v67", "v68", "v69", "v70", "v71", "v72", "v73", "v74", "v75", "v76", "v77", "v78", "v79", "v80", "v81", "v82", "v83", "v84", "v85", "v86", "v87", "v88", "v89", "v90", "v91", "v92", "v93", "v94", "v95", "v96", "v97", "v98", "v99", "v100", "v101", "v102", "v103", "v104", "v105", "v106", "v107", "v108", "v109", "v110", "v111", "v112", "v113", "v114", "v115", "v116", "v117", "v118", "v119", "v120", "v121", "v122", "v123", "v124", "v125", "v126", "v127", "v128", "v129", "v130", "v131", "v132", "v133", "v134", "v135", "v136", "v137", "v138", "v139", "v140", "v141", "v142", "v143", "v144", "v145", "v146", "v147", "v148", "v149", "v150", "v151", "v152", "v153", "v154", "v155", "v156", "v157", "v158", "v159", "v160", "v161", "v162", "v163", "v164", "v165", "v166", "v167", "v168", "v169", "v170", "v171", "v172", "v173", "v174", "v175", "v176", "v177", "v178", "v179", "v180", "v181", "v182", "v183", "v184", "v185", "v186", "v187", "v188", "v189", "v190", "v191", "v192", "v193", "v194", "v195", "v196", "v197", "v198", "v199", "v200", "v201", "v202", "v203", "v204", "v205", "v206", "v207", "v208", "v209", "v210", "v211", "v212", "v213", "v214", "v215", "v216", "v217", "v218", "v219", "v220", "v221", "v222", "v223", "v224", "v225", "v226", "v227", "v228", "v229", "v230", "v231", "v232", "v233", "v234", "v235", "v236", "v237", "v238", "v239", "v240", "v241", "v242", "v243", "v244", "v245", "v246", "v247", "v248", "v249", "v250", "v251", "a0", "a1", "a2", "a3", "a4", "a5", "a6", "a7", "a8", "a9", "a10", "a11", "a12", "a13", "a14", "a15", "a16", "a17", "a18", "a19", "a20", "a21", "a22", "a23", "a24", "a25", "a26", "a27", "a28", "a29", "a30", "a31", "a32", "a33", "a34", "a35", "a36", "a37", "a38", "a39", "a40", "a41", "a42", "a43", "a44", "a45", "a46", "a47", "a48", "a49", "a50", "a51", "a52", "a53", "a54", "a55", "a56", "a57", "a58", "a59", "a60", "a61", "a62", "a63", "a64", "a65", "a66", "a67", "a68", "a69", "a70", "a71", "a72", "a73", "a74", "a75", "a76", "a77", "a78", "a79", "a80", "a81", "a82", "a83", "a84", "a85", "a86", "a87", "a88", "a89", "a90", "a91", "a92", "a93", "a94", "a95", "s40", "s41", "s42", "s43", "s44", "s45", "s46", "s47", "s48", "s49", "s50", "s51", "s52", "s53", "s54", "s55", "s56", "s57", "s58", "s59", "s60", "s61", "s62", "s63", "s64", "s65", "s66", "s67", "s68", "s69", "s70", "s71", "s72", "s73", "s74", "s75", "s76", "vcc", "scc", "memory");
  }
  {
    const int l = fresh_lane();
    const unsigned c15 = l & 15, g4 = l >> 4, hb = bid >> 3, sh = (hb & 1) * 2, ca = 18 * hb - sh + 4 * g4, cr = 18 * hb + 16 - sh;
#define OCOL(c) (((c) >> 6) * 16384 + (((c) >> 5) & 1) * 1024 + (((c) >> 2) & 3) * 256 + (((c) >> 4) & 1) * 8 + ((c) & 3) * 2 + c15 * 16)
    const unsigned c8 = (g4 == 0 && sh) ? cr : ca, c4 = sh ? ca + 2 : cr, o8 = OCOL(c8), o4 = OCOL(c4) | (g4 << 28);
#undef OCOL
    const char* pf = bid < 192 ? wo + bid * 24576u : bwo + (bid - 192) * 24576u;
    asm volatile(
    "; ---- att0\n\t"
    "s_waitcnt lgkmcnt(0)\n\t"
    "s_barrier\n\t"
    "s_and_b32 s68, %0, 1\n\t"
    "s_lshl_b32 s68, s68, 15\n\t"
    "s_add_u32 s68, s68, 0x14000\n\t"
    "v_add_u32 v26, s68, %6\n\t"
    "s_lshr_b32 s68, %0, 1\n\t"
    "s_lshl_b32 s68, s68, 15\n\t"
    "v_add_u32 v24, s68, %6\n\t"
    "s_mov_b64 s[48:49], %1\n\t"
    "s_mov_b32 s50, 0x100000\n\t"
    "s_mov_b32 s51, 0x20000\n\t"
    "v_add_u32 v27, 0x1000, %6\n\t"
    "s_mov_b32 s70, 0x10000\n\t"
    "s_lshl_b32 s71, %4, 16\n\t"
    "s_and_b32 s84, %0, 1\n\t"
    "s_lshl_b32 s84, s84, 13\n\t"
    "s_add_u32 s64, s68, s84\n\t"
    "s_lshr_b32 s68, s68, 3\n\t"
    "s_add_u32 s66, s64, 0x10000\n\t"
    "s_add_u32 s66, s66, s68\n\t"
    "s_xor_b32 s64, s64, 0x4000\n\t"
    "ds_read_b128 a[128:131], v26 offset:0\n\t"
    "ds_read_b128 a[132:135], v26 offset:1024\n\t"
    "ds_read_b128 a[136:139], v26 offset:2048\n\t"
    "ds_read_b128 a[140:143], v26 offset:3072\n\t"
    "ds_read_b128 a[144:147], v26 offset:4096\n\t"
    "ds_read_b128 a[148:151], v26 offset:5120\n\t"
    "ds_read_b128 a[152:155], v26 offset:6144\n\t"
    "ds_read_b128 a[156:159], v26 offset:7168\n\t"
    "ds_read_b128 a[160:163], v26 offset:8192\n\t"
    "ds_read_b128 a[164:167], v26 offset:9216\n\t"
    "ds_read_b128 a[168:171], v26 offset:10240\n\t"
    "ds_read_b128 a[172:175], v26 offset:11264\n\t"
    "ds_read_b128 a[176:179], v26 offset:12288\n\t"
    "ds_read_b128 a[180:183], v26 offset:13312\n\t"
    "ds_read_b128 a[184:187], v26 offset:14336\n\t"
    "ds_read_b128 a[188:191], v26 offset:15360\n\t"
    "s_mov_b32 m0, s64\n\t"
    "s_nop 0\n\t"
    "buffer_load_dwordx4 %6, s[48:51], s70 offen offset:0 sc1 lds\n\t"
    "buffer_load_dwordx4 %6, s[48:51], s70 offen offset:1024 sc1 lds\n\t"
    "buffer_load_dwordx4 %6, s[48:51], s70 offen offset:2048 sc1 lds\n\t"
    "buffer_load_dwordx4 %6, s[48:51], s70 offen offset:3072 sc1 lds\n\t"
    "s_add_u32 m0, s64, 0x1000\n\t"
    "s_nop 0\n\t"
    "buffer_load_dwordx4 v27, s[48:51], s70 offen offset:0 sc1 lds\n\t"
    "buffer_load_dwordx4 v27, s[48:51], s70 offen offset:1024 sc1 lds\n\t"
    "buffer_load_dwordx4 v27, s[48:51], s70 offen offset:2048 sc1 lds\n\t"
    "buffer_load_dwordx4 v27, s[48:51], s70 offen offset:3072 sc1 lds\n\t"
    "s_add_u32 s70, s70, 0x10000\n\t"
    "s_xor_b32 s64, s64, 0x4000\n\t"
    "s_cmp_eq_u32 s70, s71\n\t"
    "s_cselect_b32 s70, 0, s70\n\t"
    "s_add_u32 s52, s48, 0x4000\n\t"
    "s_addc_u32 s53, s49, 0\n\t"
    "s_mov_b32 s54, 0x100000\n\t"
    "s_mov_b32 s55, 0x20000\n\t"
    "s_mov_b64 s[56:57], %2\n\t"
    "s_mov_b32 s58, 0x2000\n\t"
    "s_mov_b32 s59, 0x20000\n\t"
    "s_mov_b64 s[40:41], %8\n\t"
    "s_mov_b32 s42, 0x40000\n\t"
    "s_mov_b32 s43, 0x20000\n\t"
    "s_mov_b64 s[44:45], %11\n\t"
    "s_mov_b32 s46, %12\n\t"
    "s_mov_b32 s47, 0x20000\n\t"
    "s_mov_b32 s65, 0\n\t"
    "s_lshr_b32 s68, %0, 1\n\t"
    "s_and_b32 s84, %0, 1\n\t"
    "s_cmp_eq_u32 s68, s84\n\t"
    "s_cselect_b32 s82, 1, 0\n\t"
    "s_cmp_eq_u32 %0, 2\n\t"
    "s_cselect_b32 s83, 1, 0\n\t"
    "v_bfe_u32 v20, %6, 4, 4\n\t"
    "v_lshrrev_b32 v21, 8, %6\n\t"
    "v_lshlrev_b32 v21, 2, v21\n\t"
    "v_sub_u32 v20, v20, v21\n\t"
    "v_cmp_le_i32 s[86:87], 0, v20\n\t"
    "v_cmp_le_i32 s[88:89], 1, v20\n\t"
    "v_cmp_le_i32 s[90:91], 2, v20\n\t"
    "v_cmp_le_i32 s[92:93], 3, v20\n\t"
    "v_add_u32 v25, 0x10000, v24\n\t"
    "s_mov_b32 s81, 0\n\t"
    "s_mov_b32 s69, %4\n\t"
    "s_lshl_b32 s68, s84, 14\n\t"
    "s_add_u32 s68, s68, 0x20000\n\t"
    "v_add_u32 v26, s68, %6\n\t"
    "s_mov_b32 s85, 0x1000\n\t"
    "v_mov_b32 v16, 0x3f803f80\n\t"
    "v_mov_b32 v17, 0x3f803f80\n\t"
    "v_mov_b32 v18, 0x3f803f80\n\t"
    "v_mov_b32 v19, 0x3f803f80\n\t"
    "v_lshrrev_b32 v12, 6, %6\n\t"
    "v_lshlrev_b32 v12, 7, v12\n\t"
    "v_and_b32 v13, 48, %6\n\t"
    "v_lshl_add_u32 v12, v13, 1, v12\n\t"
    "s_lshl_b32 s60, %0, 11\n\t"
    "s_lshr_b32 s68, %15, 3\n\t"
    "s_and_b32 s61, s68, 1\n\t"
    "s_mul_i32 s68, s68, 0xc000\n\t"
    "s_sub_u32 s68, 0x200000, s68\n\t"
    "s_sub_u32 s72, s40, s68\n\t"
    "s_subb_u32 s73, s41, 0\n\t"
    "s_mov_b32 s74, 0xc000\n\t"
    "s_mov_b32 s75, 0x20000\n\t"
    "s_lshl_b32 s68, %15, 2\n\t"
    "s_add_u32 s68, s68, %0\n\t"
    "s_mul_i32 s68, s68, 0x1400\n\t"
    "s_sub_u32 s68, 0x800000, s68\n\t"
    "s_sub_u32 s76, s40, s68\n\t"
    "s_subb_u32 s77, s41, 0\n\t"
    "s_mov_b32 s78, 0x1400\n\t"
    "s_mov_b32 s79, 0x20000\n\t"
    "s_mul_i32 s68, %0, 0x600\n\t"
    "v_add_u32 v6, s68, %6\n\t"
    "v_add_u32 v7, 0x400, v6\n\t"
    "v_mov_b32 v13, 0x7ff00000\n\t"
    "v_cmp_gt_u32 vcc, 0x200, %6\n\t"
    "s_nop 1\n\t"
    "v_cndmask_b32 v7, v13, v7, vcc\n\t"
    "v_and_b32 v8, 0xf0, %6\n\t"
    "v_add_u32 v9, 0x1000, v8\n\t"
    "v_cmp_gt_u32 vcc, 0x100, %6\n\t"
    "s_nop 1\n\t"
    "v_cndmask_b32 v9, v13, v9, vcc\n\t"
    "v_and_b32 v13, 0x300, %6\n\t"
    "v_lshl_add_u32 v8, v13, 2, v8\n\t"
    "v_mov_b32 v10, 0\n\t"
    "v_mov_b32 v11, 0\n\t"
    "v_accvgpr_write_b32 a64, 0\n\t"
    "v_accvgpr_write_b32 a65, 0\n\t"
    "v_accvgpr_write_b32 a66, 0\n\t"
    "v_accvgpr_write_b32 a67, 0\n\t"
    "v_accvgpr_write_b32 a68, 0\n\t"
    "v_accvgpr_write_b32 a69, 0\n\t"
    "v_accvgpr_write_b32 a70, 0\n\t"
    "v_accvgpr_write_b32 a71, 0\n\t"
    "v_accvgpr_write_b32 a72, 0\n\t"
    "v_accvgpr_write_b32 a73, 0\n\t"
    "v_accvgpr_write_b32 a74, 0\n\t"
    "v_accvgpr_write_b32 a75, 0\n\t"
    "v_accvgpr_write_b32 a76, 0\n\t"
    "v_accvgpr_write_b32 a77, 0\n\t"
    "v_accvgpr_write_b32 a78, 0\n\t"
    "v_accvgpr_write_b32 a79, 0\n\t"
    "v_accvgpr_write_b32 a80, 0\n\t"
    "v_accvgpr_write_b32 a81, 0\n\t"
    "v_accvgpr_write_b32 a82, 0\n\t"
    "v_accvgpr_write_b32 a83, 0\n\t"
    "v_accvgpr_write_b32 a84, 0\n\t"
    "v_accvgpr_write_b32 a85, 0\n\t"
    "v_accvgpr_write_b32 a86, 0\n\t"
    "v_accvgpr_write_b32 a87, 0\n\t"
    "v_accvgpr_write_b32 a88, 0\n\t"
    "v_accvgpr_write_b32 a89, 0\n\t"
    "v_accvgpr_write_b32 a90, 0\n\t"
    "v_accvgpr_write_b32 a91, 0\n\t"
    "v_accvgpr_write_b32 a92, 0\n\t"
    "v_accvgpr_write_b32 a93, 0\n\t"
    "v_accvgpr_write_b32 a94, 0\n\t"
    "v_accvgpr_write_b32 a95, 0\n\t"
    "v_accvgpr_write_b32 a96, 0\n\t"
    "v_accvgpr_write_b32 a97, 0\n\t"
    "v_accvgpr_write_b32 a98, 0\n\t"
    "v_accvgpr_write_b32 a99, 0\n\t"
    "v_accvgpr_write_b32 a100, 0\n\t"
    "v_accvgpr_write_b32 a101, 0\n\t"
    "v_accvgpr_write_b32 a102, 0\n\t"
    "v_accvgpr_write_b32 a103, 0\n\t"
    "v_accvgpr_write_b32 a104, 0\n\t"
    "v_accvgpr_write_b32 a105, 0\n\t"
    "v_accvgpr_write_b32 a106, 0\n\t"
    "v_accvgpr_write_b32 a107, 0\n\t"
    "v_accvgpr_write_b32 a108, 0\n\t"
    "v_accvgpr_write_b32 a109, 0\n\t"
    "v_accvgpr_write_b32 a110, 0\n\t"
    "v_accvgpr_write_b32 a111, 0\n\t"
    "v_accvgpr_write_b32 a112, 0\n\t"
    "v_accvgpr_write_b32 a113, 0\n\t"
    "v_accvgpr_write_b32 a114, 0\n\t"
    "v_accvgpr_write_b32 a115, 0\n\t"
    "v_accvgpr_write_b32 a116, 0\n\t"
    "v_accvgpr_write_b32 a117, 0\n\t"
    "v_accvgpr_write_b32 a118, 0\n\t"
    "v_accvgpr_write_b32 a119, 0\n\t"
    "v_accvgpr_write_b32 a120, 0\n\t"
    "v_accvgpr_write_b32 a121, 0\n\t"
    "v_accvgpr_write_b32 a122, 0\n\t"
    "v_accvgpr_write_b32 a123, 0\n\t"
    "v_accvgpr_write_b32 a124, 0\n\t"
    "v_accvgpr_write_b32 a125, 0\n\t"
    "v_accvgpr_write_b32 a126, 0\n\t"
    "v_accvgpr_write_b32 a127, 0\n\t"
    "; ---- blk_start\n\t"
    "blk_start_%=:\n\t"
    "ds_read_b128 a[192:195], v24 offset:0\n\t"
    "ds_read_b128 a[196:199], v24 offset:1024\n\t"
    "ds_read_b128 a[200:203], v24 offset:2048\n\t"
    "ds_read_b128 a[204:207], v24 offset:3072\n\t"
    "ds_read_b128 a[208:211], v24 offset:4096\n\t"
    "ds_read_b128 a[212:215], v24 offset:5120\n\t"
    "v_mov_b32 v32, 0\n\t"
    "v_mov_b32 v33, 0\n\t"
    "v_mov_b32 v34, 0\n\t"
    "v_mov_b32 v35, 0\n\t"
    "s_sub_u32 s67, s69, s83\n\t"
    "s_cmp_lt_u32 s67, 2\n\t"
    "s_cbranch_scc1 short_%=\n\t"
    "s_bitcmp1_b32 s67, 0\n\t"
    "s_cbranch_scc0 s0_0_%=\n\t"
    "; ---- s0_1\n\t"
    "s0_1_%=:\n\t"
    "s_waitcnt lgkmcnt(5)\n\t"
    "v_mfma_f32_16x16x32_bf16 v[128:131], a[192:195], a[128:131], 0\n\t"
    "ds_read_b128 a[216:219], v24 offset:6144\n\t"
    "v_accvgpr_write_b32 a0, 0\n\t"
    "v_mfma_f32_16x16x32_bf16 v[132:135], a[192:195], a[144:147], 0\n\t"
    "v_accvgpr_write_b32 a1, 0\n\t"
    "v_accvgpr_write_b32 a2, 0\n\t"
    "v_mfma_f32_16x16x32_bf16 v[136:139], a[192:195], a[160:163], 0\n\t"
    "v_accvgpr_write_b32 a3, 0\n\t"
    "s_mov_b32 m0, s66\n\t"
    "v_accvgpr_write_b32 a4, 0\n\t"
    "v_mfma_f32_16x16x32_bf16 v[140:143], a[192:195], a[176:179], 0\n\t"
    "buffer_load_dwordx4 %6, s[52:55], s65 offen offset:0 sc1 lds\n\t"
    "s_waitcnt lgkmcnt(4)\n\t"
    "v_mfma_f32_16x16x32_bf16 v[128:131], a[196:199], a[132:135], v[128:131]\n\t"
    "v_accvgpr_write_b32 a5, 0\n\t"
    "v_accvgpr_write_b32 a6, 0\n\t"
    "v_mfma_f32_16x16x32_bf16 v[132:135], a[196:199], a[148:151], v[132:135]\n\t"
    "ds_read_b128 a[220:223], v24 offset:7168\n\t"
    "v_accvgpr_write_b32 a7, 0\n\t"
    "v_mfma_f32_16x16x32_bf16 v[136:139], a[196:199], a[164:167], v[136:139]\n\t"
    "buffer_load_dwordx4 %6, s[52:55], s65 offen offset:1024 sc1 lds\n\t"
    "v_mfma_f32_16x16x32_bf16 v[140:143], a[196:199], a[180:183], v[140:143]\n\t"
    "v_accvgpr_write_b32 a8, 0\n\t"
    "v_accvgpr_write_b32 a9, 0\n\t"
    "v_mfma_f32_16x16x32_bf16 v[128:131], a[200:203], a[136:139], v[128:131]\n\t"
    "v_accvgpr_write_b32 a10, 0\n\t"
    "v_accvgpr_write_b32 a11, 0\n\t"
    "v_accvgpr_write_b32 a12, 0\n\t"
    "v_mfma_f32_16x16x32_bf16 v[132:135], a[200:203], a[152:155], v[132:135]\n\t"
    "ds_read_b128 a[224:227], v24 offset:8192\n\t"
    "v_accvgpr_write_b32 a13, 0\n\t"
    "v_mfma_f32_16x16x32_bf16 v[136:139], a[200:203], a[168:171], v[136:139]\n\t"
    "buffer_load_dwordx4 %6, s[52:55], s65 offen offset:2048 sc1 lds\n\t"
    "v_mfma_f32_16x16x32_bf16 v[140:143], a[200:203], a[184:187], v[140:143]\n\t"
    "v_accvgpr_write_b32 a14, 0\n\t"
    "v_accvgpr_write_b32 a15, 0\n\t"
    "s_waitcnt lgkmcnt(4)\n\t"
    "v_mfma_f32_16x16x32_bf16 v[128:131], a[204:207], a[140:143], v[128:131]\n\t"
    "v_accvgpr_write_b32 a16, 0\n\t"
    "v_accvgpr_write_b32 a17, 0\n\t"
    "v_accvgpr_write_b32 a18, 0\n\t"
    "v_mfma_f32_16x16x32_bf16 v[132:135], a[204:207], a[156:159], v[132:135]\n\t"
    "ds_read_b128 a[228:231], v24 offset:9216\n\t"
    "v_accvgpr_write_b32 a19, 0\n\t"
    "v_mfma_f32_16x16x32_bf16 v[136:139], a[204:207], a[172:175], v[136:139]\n\t"
    "buffer_load_dwordx4 %6, s[52:55], s65 offen offset:3072 sc1 lds\n\t"
    "v_mfma_f32_16x16x32_bf16 v[140:143], a[204:207], a[188:191], v[140:143]\n\t"
    "v_accvgpr_write_b32 a20, 0\n\t"
    "v_accvgpr_write_b32 a21, 0\n\t"
    "v_mfma_f32_16x16x32_bf16 v[144:147], a[208:211], a[128:131], 0\n\t"
    "v_accvgpr_write_b32 a22, 0\n\t"
    "v_accvgpr_write_b32 a23, 0\n\t"
    "v_accvgpr_write_b32 a24, 0\n\t"
    "v_mfma_f32_16x16x32_bf16 v[148:151], a[208:211], a[144:147], 0\n\t"
    "ds_read_b128 a[232:235], v24 offset:10240\n\t"
    "v_accvgpr_write_b32 a25, 0\n\t"
    "v_mfma_f32_16x16x32_bf16 v[152:155], a[208:211], a[160:163], 0\n\t"
    "v_accvgpr_write_b32 a26, 0\n\t"
    "v_accvgpr_write_b32 a27, 0\n\t"
    "v_mfma_f32_16x16x32_bf16 v[156:159], a[208:211], a[176:179], 0\n\t"
    "v_accvgpr_write_b32 a28, 0\n\t"
    "s_xor_b32 m0, s66, 0x1000\n\t"
    "v_accvgpr_write_b32 a29, 0\n\t"
    "s_waitcnt lgkmcnt(4)\n\t"
    "v_mfma_f32_16x16x32_bf16 v[144:147], a[212:215], a[132:135], v[144:147]\n\t"
    "buffer_load_dwordx4 v27, s[52:55], s65 offen offset:0 sc1 lds\n\t"
    "v_mfma_f32_16x16x32_bf16 v[148:151], a[212:215], a[148:151], v[148:151]\n\t"
    "v_max3_f32 v28, v128, v129, v130\n\t"
    "v_max3_f32 v29, v132, v133, v134\n\t"
    "v_mfma_f32_16x16x32_bf16 v[152:155], a[212:215], a[164:167], v[152:155]\n\t"
    "buffer_load_dwordx4 v27, s[52:55], s65 offen offset:1024 sc1 lds\n\t"
    "v_mfma_f32_16x16x32_bf16 v[156:159], a[212:215], a[180:183], v[156:159]\n\t"
    "ds_read_b128 a[236:239], v24 offset:11264\n\t"
    "v_max3_f32 v30, v136, v137, v138\n\t"
    "v_max3_f32 v31, v140, v141, v142\n\t"
    "v_max_f32 v28, v28, v131\n\t"
    "v_mfma_f32_16x16x32_bf16 v[144:147], a[216:219], a[136:139], v[144:147]\n\t"
    "v_max_f32 v29, v29, v135\n\t"
    "v_max_f32 v30, v30, v139\n\t"
    "v_mfma_f32_16x16x32_bf16 v[148:151], a[216:219], a[152:155], v[148:151]\n\t"
    "v_max_f32 v31, v31, v143\n\t"
    "v_mov_b32 v20, v28\n\t"
    "v_mfma_f32_16x16x32_bf16 v[152:155], a[216:219], a[168:171], v[152:155]\n\t"
    "buffer_load_dwordx4 v27, s[52:55], s65 offen offset:2048 sc1 lds\n\t"
    "v_mfma_f32_16x16x32_bf16 v[156:159], a[216:219], a[184:187], v[156:159]\n\t"
    "ds_read_b128 a[240:243], v24 offset:12288\n\t"
    "v_mov_b32 v21, v29\n\t"
    "v_mov_b32 v22, v30\n\t"
    "v_mov_b32 v23, v31\n\t"
    "s_waitcnt lgkmcnt(4)\n\t"
    "v_mfma_f32_16x16x32_bf16 v[144:147], a[220:223], a[140:143], v[144:147]\n\t"
    "v_permlane16_swap_b32 v28, v20\n\t"
    "v_permlane16_swap_b32 v29, v21\n\t"
    "v_permlane16_swap_b32 v30, v22\n\t"
    "v_permlane16_swap_b32 v31, v23\n\t"
    "v_mfma_f32_16x16x32_bf16 v[148:151], a[220:223], a[156:159], v[148:151]\n\t"
    "v_max_f32 v28, v28, v20\n\t"
    "v_max_f32 v29, v29, v21\n\t"
    "v_mfma_f32_16x16x32_bf16 v[152:155], a[220:223], a[172:175], v[152:155]\n\t"
    "buffer_load_dwordx4 v27, s[52:55], s65 offen offset:3072 sc1 lds\n\t"
    "v_mfma_f32_16x16x32_bf16 v[156:159], a[220:223], a[188:191], v[156:159]\n\t"
    "ds_read_b128 a[244:247], v24 offset:13312\n\t"
    "v_max_f32 v30, v30, v22\n\t"
    "v_max_f32 v31, v31, v23\n\t"
    "v_mov_b32 v20, v28\n\t"
    "v_mfma_f32_16x16x32_bf16 v[160:163], a[224:227], a[128:131], 0\n\t"
    "v_mov_b32 v21, v29\n\t"
    "v_mov_b32 v22, v30\n\t"
    "v_mov_b32 v23, v31\n\t"
    "v_mfma_f32_16x16x32_bf16 v[164:167], a[224:227], a[144:147], 0\n\t"
    "v_permlane32_swap_b32 v28, v20\n\t"
    "v_permlane32_swap_b32 v29, v21\n\t"
    "v_mfma_f32_16x16x32_bf16 v[168:171], a[224:227], a[160:163], 0\n\t"
    "v_permlane32_swap_b32 v30, v22\n\t"
    "v_permlane32_swap_b32 v31, v23\n\t"
    "v_max_f32 v28, v28, v20\n\t"
    "v_mfma_f32_16x16x32_bf16 v[172:175], a[224:227], a[176:179], 0\n\t"
    "ds_read_b128 a[248:251], v24 offset:14336\n\t"
    "v_max_f32 v29, v29, v21\n\t"
    "v_max_f32 v30, v30, v22\n\t"
    "s_waitcnt lgkmcnt(4)\n\t"
    "v_mfma_f32_16x16x32_bf16 v[160:163], a[228:231], a[132:135], v[160:163]\n\t"
    "v_max_f32 v31, v31, v23\n\t"
    "v_ceil_f32 v28, v28\n\t"
    "v_mfma_f32_16x16x32_bf16 v[164:167], a[228:231], a[148:151], v[164:167]\n\t"
    "v_ceil_f32 v29, v29\n\t"
    "v_ceil_f32 v30, v30\n\t"
    "v_mfma_f32_16x16x32_bf16 v[168:171], a[228:231], a[164:167], v[168:171]\n\t"
    "v_ceil_f32 v31, v31\n\t"
    "v_sub_f32 v48, 0xc2c80000, v28\n\t"
    "v_mfma_f32_16x16x32_bf16 v[172:175], a[228:231], a[180:183], v[172:175]\n\t"
    "ds_read_b128 a[252:255], v24 offset:15360\n\t"
    "v_sub_f32 v49, 0xc2c80000, v28\n\t"
    "v_sub_f32 v50, 0xc2c80000, v28\n\t"
    "v_mfma_f32_16x16x32_bf16 v[160:163], a[232:235], a[136:139], v[160:163]\n\t"
    "v_sub_f32 v51, 0xc2c80000, v28\n\t"
    "v_sub_f32 v52, 0xc2c80000, v29\n\t"
    "v_sub_f32 v53, 0xc2c80000, v29\n\t"
    "v_mfma_f32_16x16x32_bf16 v[164:167], a[232:235], a[152:155], v[164:167]\n\t"
    "v_sub_f32 v54, 0xc2c80000, v29\n\t"
    "v_sub_f32 v55, 0xc2c80000, v29\n\t"
    "v_mfma_f32_16x16x32_bf16 v[168:171], a[232:235], a[168:171], v[168:171]\n\t"
    "v_sub_f32 v56, 0xc2c80000, v30\n\t"
    "v_sub_f32 v57, 0xc2c80000, v30\n\t"
    "v_mfma_f32_16x16x32_bf16 v[172:175], a[232:235], a[184:187], v[172:175]\n\t"
    "v_sub_f32 v58, 0xc2c80000, v30\n\t"
    "v_sub_f32 v59, 0xc2c80000, v30\n\t"
    "s_waitcnt lgkmcnt(3)\n\t"
    "v_mfma_f32_16x16x32_bf16 v[160:163], a[236:239], a[140:143], v[160:163]\n\t"
    "v_sub_f32 v60, 0xc2c80000, v31\n\t"
    "v_sub_f32 v61, 0xc2c80000, v31\n\t"
    "v_mfma_f32_16x16x32_bf16 v[164:167], a[236:239], a[156:159], v[164:167]\n\t"
    "v_sub_f32 v62, 0xc2c80000, v31\n\t"
    "v_sub_f32 v63, 0xc2c80000, v31\n\t"
    "v_mfma_f32_16x16x32_bf16 v[168:171], a[236:239], a[172:175], v[168:171]\n\t"
    "v_accvgpr_write_b32 a30, 0\n\t"
    "v_accvgpr_write_b32 a31, 0\n\t"
    "v_mfma_f32_16x16x32_bf16 v[172:175], a[236:239], a[188:191], v[172:175]\n\t"
    "v_accvgpr_write_b32 a32, 0\n\t"
    "v_accvgpr_write_b32 a33, 0\n\t"
    "v_mfma_f32_16x16x32_bf16 v[176:179], a[240:243], a[128:131], v[48:51]\n\t"
    "v_accvgpr_write_b32 a34, 0\n\t"
    "v_accvgpr_write_b32 a35, 0\n\t"
    "v_mfma_f32_16x16x32_bf16 v[180:183], a[240:243], a[144:147], v[52:55]\n\t"
    "v_accvgpr_write_b32 a36, 0\n\t"
    "v_accvgpr_write_b32 a37, 0\n\t"
    "v_mfma_f32_16x16x32_bf16 v[184:187], a[240:243], a[160:163], v[56:59]\n\t"
    "v_accvgpr_write_b32 a38, 0\n\t"
    "v_accvgpr_write_b32 a39, 0\n\t"
    "v_mfma_f32_16x16x32_bf16 v[188:191], a[240:243], a[176:179], v[60:63]\n\t"
    "v_accvgpr_write_b32 a40, 0\n\t"
    "v_accvgpr_write_b32 a41, 0\n\t"
    "s_waitcnt lgkmcnt(1)\n\t"
    "v_mfma_f32_16x16x32_bf16 v[176:179], a[244:247], a[132:135], v[176:179]\n\t"
    "v_accvgpr_write_b32 a42, 0\n\t"
    "v_accvgpr_write_b32 a43, 0\n\t"
    "v_mfma_f32_16x16x32_bf16 v[180:183], a[244:247], a[148:151], v[180:183]\n\t"
    "v_accvgpr_write_b32 a44, 0\n\t"
    "v_accvgpr_write_b32 a45, 0\n\t"
    "v_mfma_f32_16x16x32_bf16 v[184:187], a[244:247], a[164:167], v[184:187]\n\t"
    "v_accvgpr_write_b32 a46, 0\n\t"
    "v_accvgpr_write_b32 a47, 0\n\t"
    "v_mfma_f32_16x16x32_bf16 v[188:191], a[244:247], a[180:183], v[188:191]\n\t"
    "v_accvgpr_write_b32 a48, 0\n\t"
    "v_accvgpr_write_b32 a49, 0\n\t"
    "v_mfma_f32_16x16x32_bf16 v[176:179], a[248:251], a[136:139], v[176:179]\n\t"
    "v_accvgpr_write_b32 a50, 0\n\t"
    "v_accvgpr_write_b32 a51, 0\n\t"
    "v_mfma_f32_16x16x32_bf16 v[180:183], a[248:251], a[152:155], v[180:183]\n\t"
    "v_accvgpr_write_b32 a52, 0\n\t"
    "v_accvgpr_write_b32 a53, 0\n\t"
    "v_mfma_f32_16x16x32_bf16 v[184:187], a[248:251], a[168:171], v[184:187]\n\t"
    "v_accvgpr_write_b32 a54, 0\n\t"
    "v_accvgpr_write_b32 a55, 0\n\t"
    "v_mfma_f32_16x16x32_bf16 v[188:191], a[248:251], a[184:187], v[188:191]\n\t"
    "v_accvgpr_write_b32 a56, 0\n\t"
    "v_accvgpr_write_b32 a57, 0\n\t"
    "s_waitcnt lgkmcnt(0)\n\t"
    "v_mfma_f32_16x16x32_bf16 v[176:179], a[252:255], a[140:143], v[176:179]\n\t"
    "v_accvgpr_write_b32 a58, 0\n\t"
    "v_accvgpr_write_b32 a59, 0\n\t"
    "v_mfma_f32_16x16x32_bf16 v[180:183], a[252:255], a[156:159], v[180:183]\n\t"
    "v_accvgpr_write_b32 a60, 0\n\t"
    "v_xor_b32 v24, 0x4000, v24\n\t"
    "v_mfma_f32_16x16x32_bf16 v[184:187], a[252:255], a[172:175], v[184:187]\n\t"
    "s_add_u32 s65, s65, 0x10000\n\t"
    "s_xor_b32 s66, s66, 0x4000\n\t"
    "v_mfma_f32_16x16x32_bf16 v[188:191], a[252:255], a[188:191], v[188:191]\n\t"
    "v_accvgpr_write_b32 a61, 0\n\t"
    "v_accvgpr_write_b32 a62, 0\n\t"
    "v_accvgpr_write_b32 a63, 0\n\t"
    "s_sub_u32 s67, s67, 1\n\t"
    "s_cmp_lg_u32 s81, 0\n\t"
    "s_cbranch_scc1 s0b_1_%=\n\t"
    "; ---- s0a_1\n\t"
    "s_waitcnt vmcnt(0)\n\t"
    "; ---- s0b_1\n\t"
    "s0b_1_%=:\n\t"
    "s_waitcnt vmcnt(18) lgkmcnt(0)\n\t"
    "s_barrier\n\t"
    "ds_read_b128 a[192:195], v24 offset:0\n\t"
    "ds_read_b128 a[196:199], v24 offset:1024\n\t"
    "ds_read_b128 a[200:203], v24 offset:2048\n\t"
    "ds_read_b128 a[204:207], v24 offset:3072\n\t"
    "ds_read_b128 a[208:211], v24 offset:4096\n\t"
    "ds_read_b128 a[212:215], v24 offset:5120\n\t"
    "s_or_b32 s68, %0, s81\n\t"
    "s_cmp_lg_u32 s68, 0\n\t"
    "s_cbranch_scc1 pubb1_%=\n\t"
    "v_mov_b64 v[36:37], %10\n\t"
    "v_mov_b32 v38, 0\n\t"
    "s_mov_b64 s[62:63], exec\n\t"
    "s_mov_b64 exec, 1\n\t"
    "buffer_store_dwordx2 v[36:37], v38, s[40:43], %9 offen offset:0 sc1\n\t"
    "s_mov_b64 exec, s[62:63]\n\t"
    "pubb1_%=:\n\t"
    "; ---- a1_0\n\t"
    "s_waitcnt lgkmcnt(5)\n\t"
    "v_mfma_f32_16x16x32_bf16 v[64:67], a[192:195], a[128:131], v[48:51]\n\t"
    "ds_read_b128 a[216:219], v24 offset:6144\n\t"
    "v_add_f32 v128, v48, v128\n\t"
    "v_add_f32 v129, v48, v129\n\t"
    "v_mfma_f32_16x16x32_bf16 v[68:71], a[192:195], a[144:147], v[52:55]\n\t"
    "s_mov_b32 m0, s64\n\t"
    "v_exp_f32 v40, v128\n\t"
    "v_add_f32 v130, v48, v130\n\t"
    "v_mfma_f32_16x16x32_bf16 v[72:75], a[192:195], a[160:163], v[56:59]\n\t"
    "buffer_load_dwordx4 %6, s[48:51], s70 offen offset:0 sc1 lds\n\t"
    "v_add_f32 v131, v48, v131\n\t"
    "v_mfma_f32_16x16x32_bf16 v[76:79], a[192:195], a[176:179], v[60:63]\n\t"
    "v_exp_f32 v41, v129\n\t"
    "v_exp_f32 v42, v130\n\t"
    "s_waitcnt lgkmcnt(4)\n\t"
    "v_mfma_f32_16x16x32_bf16 v[64:67], a[196:199], a[132:135], v[64:67]\n\t"
    "buffer_load_dwordx4 %6, s[48:51], s70 offen offset:1024 sc1 lds\n\t"
    "v_cvt_pk_bf16_f32 v208, v40, v41\n\t"
    "v_mfma_f32_16x16x32_bf16 v[68:71], a[196:199], a[148:151], v[68:71]\n\t"
    "v_exp_f32 v43, v131\n\t"
    "v_add_f32 v132, v52, v132\n\t"
    "v_add_f32 v133, v52, v133\n\t"
    "v_mfma_f32_16x16x32_bf16 v[72:75], a[196:199], a[164:167], v[72:75]\n\t"
    "ds_read_b128 a[220:223], v24 offset:7168\n\t"
    "v_cvt_pk_bf16_f32 v209, v42, v43\n\t"
    "v_exp_f32 v44, v132\n\t"
    "v_mfma_f32_16x16x32_bf16 v[76:79], a[196:199], a[180:183], v[76:79]\n\t"
    "v_exp_f32 v45, v133\n\t"
    "v_add_f32 v134, v52, v134\n\t"
    "v_mfma_f32_16x16x32_bf16 v[64:67], a[200:203], a[136:139], v[64:67]\n\t"
    "buffer_load_dwordx4 %6, s[48:51], s70 offen offset:2048 sc1 lds\n\t"
    "v_cvt_pk_bf16_f32 v212, v44, v45\n\t"
    "v_add_f32 v135, v52, v135\n\t"
    "v_mfma_f32_16x16x32_bf16 v[68:71], a[200:203], a[152:155], v[68:71]\n\t"
    "v_exp_f32 v46, v134\n\t"
    "v_add_f32 v136, v56, v136\n\t"
    "v_exp_f32 v47, v135\n\t"
    "v_mfma_f32_16x16x32_bf16 v[72:75], a[200:203], a[168:171], v[72:75]\n\t"
    "v_cvt_pk_bf16_f32 v213, v46, v47\n\t"
    "v_add_f32 v137, v56, v137\n\t"
    "v_exp_f32 v40, v136\n\t"
    "v_mfma_f32_16x16x32_bf16 v[76:79], a[200:203], a[184:187], v[76:79]\n\t"
    "ds_read_b128 a[224:227], v24 offset:8192\n\t"
    "v_add_f32 v138, v56, v138\n\t"
    "v_exp_f32 v41, v137\n\t"
    "s_waitcnt lgkmcnt(4)\n\t"
    "v_mfma_f32_16x16x32_bf16 v[64:67], a[204:207], a[140:143], v[64:67]\n\t"
    "buffer_load_dwordx4 %6, s[48:51], s70 offen offset:3072 sc1 lds\n\t"
    "v_cvt_pk_bf16_f32 v216, v40, v41\n\t"
    "v_exp_f32 v42, v138\n\t"
    "v_mfma_f32_16x16x32_bf16 v[68:71], a[204:207], a[156:159], v[68:71]\n\t"
    "v_add_f32 v139, v56, v139\n\t"
    "v_add_f32 v140, v60, v140\n\t"
    "v_exp_f32 v43, v139\n\t"
    "v_mfma_f32_16x16x32_bf16 v[72:75], a[204:207], a[172:175], v[72:75]\n\t"
    "v_cvt_pk_bf16_f32 v217, v42, v43\n\t"
    "v_add_f32 v141, v60, v141\n\t"
    "v_exp_f32 v44, v140\n\t"
    "v_mfma_f32_16x16x32_bf16 v[76:79], a[204:207], a[188:191], v[76:79]\n\t"
    "v_exp_f32 v45, v141\n\t"
    "v_add_f32 v142, v60, v142\n\t"
    "v_mfma_f32_16x16x32_bf16 v[80:83], a[208:211], a[128:131], v[48:51]\n\t"
    "ds_read_b128 a[228:231], v24 offset:9216\n\t"
    "v_cvt_pk_bf16_f32 v220, v44, v45\n\t"
    "v_exp_f32 v46, v142\n\t"
    "v_add_f32 v143, v60, v143\n\t"
    "v_mfma_f32_16x16x32_bf16 v[84:87], a[208:211], a[144:147], v[52:55]\n\t"
    "s_add_u32 m0, s64, 0x1000\n\t"
    "v_add_f32 v144, v48, v144\n\t"
    "v_exp_f32 v47, v143\n\t"
    "v_mfma_f32_16x16x32_bf16 v[88:91], a[208:211], a[160:163], v[56:59]\n\t"
    "buffer_load_dwordx4 v27, s[48:51], s70 offen offset:0 sc1 lds\n\t"
    "v_cvt_pk_bf16_f32 v221, v46, v47\n\t"
    "v_exp_f32 v40, v144\n\t"
    "v_mfma_f32_16x16x32_bf16 v[92:95], a[208:211], a[176:179], v[60:63]\n\t"
    "buffer_load_dwordx4 v27, s[48:51], s70 offen offset:1024 sc1 lds\n\t"
    "v_add_f32 v145, v48, v145\n\t"
    "v_exp_f32 v41, v145\n\t"
    "s_waitcnt lgkmcnt(3)\n\t"
    "v_mfma_f32_16x16x32_bf16 v[80:83], a[212:215], a[132:135], v[80:83]\n\t"
    "ds_read_b128 a[232:235], v24 offset:10240\n\t"
    "v_add_f32 v146, v48, v146\n\t"
    "v_cvt_pk_bf16_f32 v210, v40, v41\n\t"
    "v_exp_f32 v42, v146\n\t"
    "v_mfma_f32_16x16x32_bf16 v[84:87], a[212:215], a[148:151], v[84:87]\n\t"
    "v_add_f32 v147, v48, v147\n\t"
    "v_add_f32 v148, v52, v148\n\t"
    "v_exp_f32 v43, v147\n\t"
    "v_mfma_f32_16x16x32_bf16 v[88:91], a[212:215], a[164:167], v[88:91]\n\t"
    "v_cvt_pk_bf16_f32 v211, v42, v43\n\t"
    "v_add_f32 v149, v52, v149\n\t"
    "v_exp_f32 v44, v148\n\t"
    "v_add_f32 v150, v52, v150\n\t"
    "v_mfma_f32_16x16x32_bf16 v[92:95], a[212:215], a[180:183], v[92:95]\n\t"
    "buffer_load_dwordx4 v27, s[48:51], s70 offen offset:2048 sc1 lds\n\t"
    "v_exp_f32 v45, v149\n\t"
    "v_mfma_f32_16x16x32_bf16 v[80:83], a[216:219], a[136:139], v[80:83]\n\t"
    "v_exp_f32 v46, v150\n\t"
    "v_add_f32 v151, v52, v151\n\t"
    "v_cvt_pk_bf16_f32 v214, v44, v45\n\t"
    "v_mfma_f32_16x16x32_bf16 v[84:87], a[216:219], a[152:155], v[84:87]\n\t"
    "ds_read_b128 a[236:239], v24 offset:11264\n\t"
    "v_exp_f32 v47, v151\n\t"
    "v_add_f32 v152, v56, v152\n\t"
    "v_mfma_f32_16x16x32_bf16 v[88:91], a[216:219], a[168:171], v[88:91]\n\t"
    "buffer_load_dwordx4 v27, s[48:51], s70 offen offset:3072 sc1 lds\n\t"
    "v_cvt_pk_bf16_f32 v215, v46, v47\n\t"
    "v_exp_f32 v40, v152\n\t"
    "v_mfma_f32_16x16x32_bf16 v[92:95], a[216:219], a[184:187], v[92:95]\n\t"
    "v_add_f32 v153, v56, v153\n\t"
    "v_add_f32 v154, v56, v154\n\t"
    "v_exp_f32 v41, v153\n\t"
    "s_waitcnt lgkmcnt(3)\n\t"
    "v_mfma_f32_16x16x32_bf16 v[80:83], a[220:223], a[140:143], v[80:83]\n\t"
    "v_cvt_pk_bf16_f32 v218, v40, v41\n\t"
    "v_add_f32 v155, v56, v155\n\t"
    "v_exp_f32 v42, v154\n\t"
    "v_mfma_f32_16x16x32_bf16 v[84:87], a[220:223], a[156:159], v[84:87]\n\t"
    "ds_read_b128 a[240:243], v24 offset:12288\n\t"
    "v_exp_f32 v43, v155\n\t"
    "v_add_f32 v156, v60, v156\n\t"
    "v_mfma_f32_16x16x32_bf16 v[88:91], a[220:223], a[172:175], v[88:91]\n\t"
    "v_cvt_pk_bf16_f32 v219, v42, v43\n\t"
    "v_add_f32 v157, v60, v157\n\t"
    "v_exp_f32 v44, v156\n\t"
    "v_mfma_f32_16x16x32_bf16 v[92:95], a[220:223], a[188:191], v[92:95]\n\t"
    "v_exp_f32 v45, v157\n\t"
    "v_add_f32 v158, v60, v158\n\t"
    "v_exp_f32 v46, v158\n\t"
    "v_mfma_f32_16x16x32_bf16 v[96:99], a[224:227], a[128:131], v[48:51]\n\t"
    "v_cvt_pk_bf16_f32 v222, v44, v45\n\t"
    "v_add_f32 v159, v60, v159\n\t"
    "s_mov_b32 m0, s66\n\t"
    "v_exp_f32 v47, v159\n\t"
    "v_mfma_f32_16x16x32_bf16 v[100:103], a[224:227], a[144:147], v[52:55]\n\t"
    "buffer_load_dwordx4 %6, s[52:55], s65 offen offset:0 sc1 lds\n\t"
    "ds_read_b128 a[244:247], v24 offset:13312\n\t"
    "v_add_f32 v160, v48, v160\n\t"
    "v_cvt_pk_bf16_f32 v223, v46, v47\n\t"
    "v_mfma_f32_16x16x32_bf16 v[104:107], a[224:227], a[160:163], v[56:59]\n\t"
    "buffer_load_dwordx4 %6, s[52:55], s65 offen offset:1024 sc1 lds\n\t"
    "v_exp_f32 v40, v160\n\t"
    "v_add_f32 v161, v48, v161\n\t"
    "v_mfma_f32_16x16x32_bf16 v[108:111], a[224:227], a[176:179], v[60:63]\n\t"
    "v_exp_f32 v41, v161\n\t"
    "v_add_f32 v162, v48, v162\n\t"
    "v_exp_f32 v42, v162\n\t"
    "s_waitcnt lgkmcnt(4)\n\t"
    "v_mfma_f32_16x16x32_bf16 v[96:99], a[228:231], a[132:135], v[96:99]\n\t"
    "v_cvt_pk_bf16_f32 v224, v40, v41\n\t"
    "v_add_f32 v163, v48, v163\n\t"
    "v_exp_f32 v43, v163\n\t"
    "v_add_f32 v164, v52, v164\n\t"
    "v_mfma_f32_16x16x32_bf16 v[100:103], a[228:231], a[148:151], v[100:103]\n\t"
    "buffer_load_dwordx4 %6, s[52:55], s65 offen offset:2048 sc1 lds\n\t"
    "v_cvt_pk_bf16_f32 v225, v42, v43\n\t"
    "v_exp_f32 v44, v164\n\t"
    "v_mfma_f32_16x16x32_bf16 v[240:243], v[16:19], v[208:211], 0\n\t"
    "ds_read_b128 a[248:251], v24 offset:14336\n\t"
    "v_add_f32 v165, v52, v165\n\t"
    "v_exp_f32 v45, v165\n\t"
    "v_add_f32 v166, v52, v166\n\t"
    "v_mfma_f32_16x16x32_bf16 v[104:107], a[228:231], a[164:167], v[104:107]\n\t"
    "v_cvt_pk_bf16_f32 v228, v44, v45\n\t"
    "v_add_f32 v167, v52, v167\n\t"
    "v_exp_f32 v46, v166\n\t"
    "v_mfma_f32_16x16x32_bf16 v[108:111], a[228:231], a[180:183], v[108:111]\n\t"
    "v_exp_f32 v47, v167\n\t"
    "v_add_f32 v168, v56, v168\n\t"
    "v_mfma_f32_16x16x32_bf16 v[244:247], v[16:19], v[212:215], 0\n\t"
    "buffer_load_dwordx4 %6, s[52:55], s65 offen offset:3072 sc1 lds\n\t"
    "v_cvt_pk_bf16_f32 v229, v46, v47\n\t"
    "v_exp_f32 v40, v168\n\t"
    "s_waitcnt lgkmcnt(4)\n\t"
    "v_mfma_f32_16x16x32_bf16 v[96:99], a[232:235], a[136:139], v[96:99]\n\t"
    "ds_read_b128 a[252:255], v24 offset:15360\n\t"
    "v_add_f32 v169, v56, v169\n\t"
    "v_exp_f32 v41, v169\n\t"
    "v_add_f32 v170, v56, v170\n\t"
    "v_mfma_f32_16x16x32_bf16 v[100:103], a[232:235], a[152:155], v[100:103]\n\t"
    "v_add_f32 v171, v56, v171\n\t"
    "v_cvt_pk_bf16_f32 v232, v40, v41\n\t"
    "v_exp_f32 v42, v170\n\t"
    "v_mfma_f32_16x16x32_bf16 v[248:251], v[16:19], v[216:219], 0\n\t"
    "v_exp_f32 v43, v171\n\t"
    "v_add_f32 v172, v60, v172\n\t"
    "v_exp_f32 v44, v172\n\t"
    "v_mfma_f32_16x16x32_bf16 v[104:107], a[232:235], a[168:171], v[104:107]\n\t"
    "v_cvt_pk_bf16_f32 v233, v42, v43\n\t"
    "v_add_f32 v173, v60, v173\n\t"
    "v_exp_f32 v45, v173\n\t"
    "v_add_f32 v174, v60, v174\n\t"
    "v_mfma_f32_16x16x32_bf16 v[108:111], a[232:235], a[184:187], v[108:111]\n\t"
    "v_cvt_pk_bf16_f32 v236, v44, v45\n\t"
    "v_add_f32 v175, v60, v175\n\t"
    "v_exp_f32 v46, v174\n\t"
    "v_mfma_f32_16x16x32_bf16 v[252:255], v[16:19], v[220:223], 0\n\t"
    "s_xor_b32 m0, s66, 0x1000\n\t"
    "v_exp_f32 v47, v175\n\t"
    "s_waitcnt lgkmcnt(3)\n\t"
    "v_mfma_f32_16x16x32_bf16 v[96:99], a[236:239], a[140:143], v[96:99]\n\t"
    "buffer_load_dwordx4 v27, s[52:55], s65 offen offset:0 sc1 lds\n\t"
    "v_cvt_pk_bf16_f32 v237, v46, v47\n\t"
    "v_exp_f32 v40, v176\n\t"
    "v_mfma_f32_16x16x32_bf16 v[100:103], a[236:239], a[156:159], v[100:103]\n\t"
    "buffer_load_dwordx4 v27, s[52:55], s65 offen offset:1024 sc1 lds\n\t"
    "v_exp_f32 v41, v177\n\t"
    "v_mfma_f32_16x16x32_bf16 v[104:107], a[236:239], a[172:175], v[104:107]\n\t"
    "v_cvt_pk_bf16_f32 v226, v40, v41\n\t"
    "v_exp_f32 v42, v178\n\t"
    "v_mfma_f32_16x16x32_bf16 v[108:111], a[236:239], a[188:191], v[108:111]\n\t"
    "v_exp_f32 v43, v179\n\t"
    "v_mfma_f32_16x16x32_bf16 v[112:115], a[240:243], a[128:131], v[48:51]\n\t"
    "buffer_load_dwordx4 v27, s[52:55], s65 offen offset:2048 sc1 lds\n\t"
    "v_cvt_pk_bf16_f32 v227, v42, v43\n\t"
    "v_exp_f32 v44, v180\n\t"
    "v_mfma_f32_16x16x32_bf16 v[116:119], a[240:243], a[144:147], v[52:55]\n\t"
    "v_exp_f32 v45, v181\n\t"
    "v_mfma_f32_16x16x32_bf16 v[120:123], a[240:243], a[160:163], v[56:59]\n\t"
    "v_cvt_pk_bf16_f32 v230, v44, v45\n\t"
    "v_exp_f32 v46, v182\n\t"
    "v_mfma_f32_16x16x32_bf16 v[124:127], a[240:243], a[176:179], v[60:63]\n\t"
    "v_exp_f32 v47, v183\n\t"
    "s_waitcnt lgkmcnt(1)\n\t"
    "v_mfma_f32_16x16x32_bf16 v[112:115], a[244:247], a[132:135], v[112:115]\n\t"
    "buffer_load_dwordx4 v27, s[52:55], s65 offen offset:3072 sc1 lds\n\t"
    "v_cvt_pk_bf16_f32 v231, v46, v47\n\t"
    "v_exp_f32 v40, v184\n\t"
    "v_mfma_f32_16x16x32_bf16 v[116:119], a[244:247], a[148:151], v[116:119]\n\t"
    "v_exp_f32 v41, v185\n\t"
    "v_mfma_f32_16x16x32_bf16 v[120:123], a[244:247], a[164:167], v[120:123]\n\t"
    "v_cvt_pk_bf16_f32 v234, v40, v41\n\t"
    "v_xor_b32 v24, 0x4000, v24\n\t"
    "v_exp_f32 v42, v186\n\t"
    "v_mfma_f32_16x16x32_bf16 v[124:127], a[244:247], a[180:183], v[124:127]\n\t"
    "v_exp_f32 v43, v187\n\t"
    "v_mfma_f32_16x16x32_bf16 v[112:115], a[248:251], a[136:139], v[112:115]\n\t"
    "v_cvt_pk_bf16_f32 v235, v42, v43\n\t"
    "v_exp_f32 v44, v188\n\t"
    "v_mfma_f32_16x16x32_bf16 v[116:119], a[248:251], a[152:155], v[116:119]\n\t"
    "v_exp_f32 v45, v189\n\t"
    "v_mfma_f32_16x16x32_bf16 v[120:123], a[248:251], a[168:171], v[120:123]\n\t"
    "v_cvt_pk_bf16_f32 v238, v44, v45\n\t"
    "v_exp_f32 v46, v190\n\t"
    "v_mfma_f32_16x16x32_bf16 v[124:127], a[248:251], a[184:187], v[124:127]\n\t"
    "v_exp_f32 v47, v191\n\t"
    "s_waitcnt lgkmcnt(0)\n\t"
    "v_mfma_f32_16x16x32_bf16 v[112:115], a[252:255], a[140:143], v[112:115]\n\t"
    "v_cvt_pk_bf16_f32 v239, v46, v47\n\t"
    "s_add_u32 s70, s70, 0x10000\n\t"
    "s_xor_b32 s64, s64, 0x4000\n\t"
    "v_mfma_f32_16x16x32_bf16 v[240:243], v[16:19], v[224:227], v[240:243]\n\t"
    "s_cmp_eq_u32 s70, s71\n\t"
    "s_cselect_b32 s70, 0, s70\n\t"
    "v_mfma_f32_16x16x32_bf16 v[116:119], a[252:255], a[156:159], v[116:119]\n\t"
    "s_add_u32 s65, s65, 0x10000\n\t"
    "s_xor_b32 s66, s66, 0x4000\n\t"
    "v_mfma_f32_16x16x32_bf16 v[244:247], v[16:19], v[228:231], v[244:247]\n\t"
    "v_mfma_f32_16x16x32_bf16 v[120:123], a[252:255], a[172:175], v[120:123]\n\t"
    "v_mfma_f32_16x16x32_bf16 v[248:251], v[16:19], v[232:235], v[248:251]\n\t"
    "v_mfma_f32_16x16x32_bf16 v[124:127], a[252:255], a[188:191], v[124:127]\n\t"
    "v_mfma_f32_16x16x32_bf16 v[252:255], v[16:19], v[236:239], v[252:255]\n\t"
    "s_sub_u32 s67, s67, 1\n\t"
    "s_waitcnt vmcnt(8)\n\t"
    "s_barrier\n\t"
    "ds_read_b128 a[192:195], v25 offset:0\n\t"
    "ds_read_b128 a[196:199], v25 offset:1024\n\t"
    "ds_read_b128 a[200:203], v25 offset:2048\n\t"
    "ds_read_b128 a[204:207], v25 offset:3072\n\t"
    "ds_read_b128 a[208:211], v25 offset:4096\n\t"
    "ds_read_b128 a[212:215], v25 offset:5120\n\t"
    "s_branch bndx_0_%=\n\t"
    "; ---- s0_0\n\t"
    "s0_0_%=:\n\t"
    "s_waitcnt lgkmcnt(5)\n\t"
    "v_mfma_f32_16x16x32_bf16 v[64:67], a[192:195], a[128:131], 0\n\t"
    "ds_read_b128 a[216:219], v24 offset:6144\n\t"
    "v_accvgpr_write_b32 a0, 0\n\t"
    "v_mfma_f32_16x16x32_bf16 v[68:71], a[192:195], a[144:147], 0\n\t"
    "v_accvgpr_write_b32 a1, 0\n\t"
    "v_accvgpr_write_b32 a2, 0\n\t"
    "v_mfma_f32_16x16x32_bf16 v[72:75], a[192:195], a[160:163], 0\n\t"
    "v_accvgpr_write_b32 a3, 0\n\t"
    "s_mov_b32 m0, s66\n\t"
    "v_accvgpr_write_b32 a4, 0\n\t"
    "v_mfma_f32_16x16x32_bf16 v[76:79], a[192:195], a[176:179], 0\n\t"
    "buffer_load_dwordx4 %6, s[52:55], s65 offen offset:0 sc1 lds\n\t"
    "s_waitcnt lgkmcnt(4)\n\t"
    "v_mfma_f32_16x16x32_bf16 v[64:67], a[196:199], a[132:135], v[64:67]\n\t"
    "v_accvgpr_write_b32 a5, 0\n\t"
    "v_accvgpr_write_b32 a6, 0\n\t"
    "v_mfma_f32_16x16x32_bf16 v[68:71], a[196:199], a[148:151], v[68:71]\n\t"
    "ds_read_b128 a[220:223], v24 offset:7168\n\t"
    "v_accvgpr_write_b32 a7, 0\n\t"
    "v_mfma_f32_16x16x32_bf16 v[72:75], a[196:199], a[164:167], v[72:75]\n\t"
    "buffer_load_dwordx4 %6, s[52:55], s65 offen offset:1024 sc1 lds\n\t"
    "v_mfma_f32_16x16x32_bf16 v[76:79], a[196:199], a[180:183], v[76:79]\n\t"
    "v_accvgpr_write_b32 a8, 0\n\t"
    "v_accvgpr_write_b32 a9, 0\n\t"
    "v_mfma_f32_16x16x32_bf16 v[64:67], a[200:203], a[136:139], v[64:67]\n\t"
    "v_accvgpr_write_b32 a10, 0\n\t"
    "v_accvgpr_write_b32 a11, 0\n\t"
    "v_accvgpr_write_b32 a12, 0\n\t"
    "v_mfma_f32_16x16x32_bf16 v[68:71], a[200:203], a[152:155], v[68:71]\n\t"
    "ds_read_b128 a[224:227], v24 offset:8192\n\t"
    "v_accvgpr_write_b32 a13, 0\n\t"
    "v_mfma_f32_16x16x32_bf16 v[72:75], a[200:203], a[168:171], v[72:75]\n\t"
    "buffer_load_dwordx4 %6, s[52:55], s65 offen offset:2048 sc1 lds\n\t"
    "v_mfma_f32_16x16x32_bf16 v[76:79], a[200:203], a[184:187], v[76:79]\n\t"
    "v_accvgpr_write_b32 a14, 0\n\t"
    "v_accvgpr_write_b32 a15, 0\n\t"
    "s_waitcnt lgkmcnt(4)\n\t"
    "v_mfma_f32_16x16x32_bf16 v[64:67], a[204:207], a[140:143], v[64:67]\n\t"
    "v_accvgpr_write_b32 a16, 0\n\t"
    "v_accvgpr_write_b32 a17, 0\n\t"
    "v_accvgpr_write_b32 a18, 0\n\t"
    "v_mfma_f32_16x16x32_bf16 v[68:71], a[204:207], a[156:159], v[68:71]\n\t"
    "ds_read_b128 a[228:231], v24 offset:9216\n\t"
    "v_accvgpr_write_b32 a19, 0\n\t"
    "v_mfma_f32_16x16x32_bf16 v[72:75], a[204:207], a[172:175], v[72:75]\n\t"
    "buffer_load_dwordx4 %6, s[52:55], s65 offen offset:3072 sc1 lds\n\t"
    "v_mfma_f32_16x16x32_bf16 v[76:79], a[204:207], a[188:191], v[76:79]\n\t"
    "v_accvgpr_write_b32 a20, 0\n\t"
    "v_accvgpr_write_b32 a21, 0\n\t"
    "v_mfma_f32_16x16x32_bf16 v[80:83], a[208:211], a[128:131], 0\n\t"
    "v_accvgpr_write_b32 a22, 0\n\t"
    "v_accvgpr_write_b32 a23, 0\n\t"
    "v_accvgpr_write_b32 a24, 0\n\t"
    "v_mfma_f32_16x16x32_bf16 v[84:87], a[208:211], a[144:147], 0\n\t"
    "ds_read_b128 a[232:235], v24 offset:10240\n\t"
    "v_accvgpr_write_b32 a25, 0\n\t"
    "v_mfma_f32_16x16x32_bf16 v[88:91], a[208:211], a[160:163], 0\n\t"
    "v_accvgpr_write_b32 a26, 0\n\t"
    "v_accvgpr_write_b32 a27, 0\n\t"
    "v_mfma_f32_16x16x32_bf16 v[92:95], a[208:211], a[176:179], 0\n\t"
    "v_accvgpr_write_b32 a28, 0\n\t"
    "s_xor_b32 m0, s66, 0x1000\n\t"
    "v_accvgpr_write_b32 a29, 0\n\t"
    "s_waitcnt lgkmcnt(4)\n\t"
    "v_mfma_f32_16x16x32_bf16 v[80:83], a[212:215], a[132:135], v[80:83]\n\t"
    "buffer_load_dwordx4 v27, s[52:55], s65 offen offset:0 sc1 lds\n\t"
    "v_mfma_f32_16x16x32_bf16 v[84:87], a[212:215], a[148:151], v[84:87]\n\t"
    "v_max3_f32 v28, v64, v65, v66\n\t"
    "v_max3_f32 v29, v68, v69, v70\n\t"
    "v_mfma_f32_16x16x32_bf16 v[88:91], a[212:215], a[164:167], v[88:91]\n\t"
    "buffer_load_dwordx4 v27, s[52:55], s65 offen offset:1024 sc1 lds\n\t"
    "v_mfma_f32_16x16x32_bf16 v[92:95], a[212:215], a[180:183], v[92:95]\n\t"
    "ds_read_b128 a[236:239], v24 offset:11264\n\t"
    "v_max3_f32 v30, v72, v73, v74\n\t"
    "v_max3_f32 v31, v76, v77, v78\n\t"
    "v_max_f32 v28, v28, v67\n\t"
    "v_mfma_f32_16x16x32_bf16 v[80:83], a[216:219], a[136:139], v[80:83]\n\t"
    "v_max_f32 v29, v29, v71\n\t"
    "v_max_f32 v30, v30, v75\n\t"
    "v_mfma_f32_16x16x32_bf16 v[84:87], a[216:219], a[152:155], v[84:87]\n\t"
    "v_max_f32 v31, v31, v79\n\t"
    "v_mov_b32 v20, v28\n\t"
    "v_mfma_f32_16x16x32_bf16 v[88:91], a[216:219], a[168:171], v[88:91]\n\t"
    "buffer_load_dwordx4 v27, s[52:55], s65 offen offset:2048 sc1 lds\n\t"
    "v_mfma_f32_16x16x32_bf16 v[92:95], a[216:219], a[184:187], v[92:95]\n\t"
    "ds_read_b128 a[240:243], v24 offset:12288\n\t"
    "v_mov_b32 v21, v29\n\t"
    "v_mov_b32 v22, v30\n\t"
    "v_mov_b32 v23, v31\n\t"
    "s_waitcnt lgkmcnt(4)\n\t"
    "v_mfma_f32_16x16x32_bf16 v[80:83], a[220:223], a[140:143], v[80:83]\n\t"
    "v_permlane16_swap_b32 v28, v20\n\t"
    "v_permlane16_swap_b32 v29, v21\n\t"
    "v_permlane16_swap_b32 v30, v22\n\t"
    "v_permlane16_swap_b32 v31, v23\n\t"
    "v_mfma_f32_16x16x32_bf16 v[84:87], a[220:223], a[156:159], v[84:87]\n\t"
    "v_max_f32 v28, v28, v20\n\t"
    "v_max_f32 v29, v29, v21\n\t"
    "v_mfma_f32_16x16x32_bf16 v[88:91], a[220:223], a[172:175], v[88:91]\n\t"
    "buffer_load_dwordx4 v27, s[52:55], s65 offen offset:3072 sc1 lds\n\t"
    "v_mfma_f32_16x16x32_bf16 v[92:95], a[220:223], a[188:191], v[92:95]\n\t"
    "ds_read_b128 a[244:247], v24 offset:13312\n\t"
    "v_max_f32 v30, v30, v22\n\t"
    "v_max_f32 v31, v31, v23\n\t"
    "v_mov_b32 v20, v28\n\t"
    "v_mfma_f32_16x16x32_bf16 v[96:99], a[224:227], a[128:131], 0\n\t"
    "v_mov_b32 v21, v29\n\t"
    "v_mov_b32 v22, v30\n\t"
    "v_mov_b32 v23, v31\n\t"
    "v_mfma_f32_16x16x32_bf16 v[100:103], a[224:227], a[144:147], 0\n\t"
    "v_permlane32_swap_b32 v28, v20\n\t"
    "v_permlane32_swap_b32 v29, v21\n\t"
    "v_mfma_f32_16x16x32_bf16 v[104:107], a[224:227], a[160:163], 0\n\t"
    "v_permlane32_swap_b32 v30, v22\n\t"
    "v_permlane32_swap_b32 v31, v23\n\t"
    "v_max_f32 v28, v28, v20\n\t"
    "v_mfma_f32_16x16x32_bf16 v[108:111], a[224:227], a[176:179], 0\n\t"
    "ds_read_b128 a[248:251], v24 offset:14336\n\t"
    "v_max_f32 v29, v29, v21\n\t"
    "v_max_f32 v30, v30, v22\n\t"
    "s_waitcnt lgkmcnt(4)\n\t"
    "v_mfma_f32_16x16x32_bf16 v[96:99], a[228:231], a[132:135], v[96:99]\n\t"
    "v_max_f32 v31, v31, v23\n\t"
    "v_ceil_f32 v28, v28\n\t"
    "v_mfma_f32_16x16x32_bf16 v[100:103], a[228:231], a[148:151], v[100:103]\n\t"
    "v_ceil_f32 v29, v29\n\t"
    "v_ceil_f32 v30, v30\n\t"
    "v_mfma_f32_16x16x32_bf16 v[104:107], a[228:231], a[164:167], v[104:107]\n\t"
    "v_ceil_f32 v31, v31\n\t"
    "v_sub_f32 v48, 0xc2c80000, v28\n\t"
    "v_mfma_f32_16x16x32_bf16 v[108:111], a[228:231], a[180:183], v[108:111]\n\t"
    "ds_read_b128 a[252:255], v24 offset:15360\n\t"
    "v_sub_f32 v49, 0xc2c80000, v28\n\t"
    "v_sub_f32 v50, 0xc2c80000, v28\n\t"
    "v_mfma_f32_16x16x32_bf16 v[96:99], a[232:235], a[136:139], v[96:99]\n\t"
    "v_sub_f32 v51, 0xc2c80000, v28\n\t"
    "v_sub_f32 v52, 0xc2c80000, v29\n\t"
    "v_sub_f32 v53, 0xc2c80000, v29\n\t"
    "v_mfma_f32_16x16x32_bf16 v[100:103], a[232:235], a[152:155], v[100:103]\n\t"
    "v_sub_f32 v54, 0xc2c80000, v29\n\t"
    "v_sub_f32 v55, 0xc2c80000, v29\n\t"
    "v_mfma_f32_16x16x32_bf16 v[104:107], a[232:235], a[168:171], v[104:107]\n\t"
    "v_sub_f32 v56, 0xc2c80000, v30\n\t"
    "v_sub_f32 v57, 0xc2c80000, v30\n\t"
    "v_mfma_f32_16x16x32_bf16 v[108:111], a[232:235], a[184:187], v[108:111]\n\t"
    "v_sub_f32 v58, 0xc2c80000, v30\n\t"
    "v_sub_f32 v59, 0xc2c80000, v30\n\t"
    "s_waitcnt lgkmcnt(3)\n\t"
    "v_mfma_f32_16x16x32_bf16 v[96:99], a[236:239], a[140:143], v[96:99]\n\t"
    "v_sub_f32 v60, 0xc2c80000, v31\n\t"
    "v_sub_f32 v61, 0xc2c80000, v31\n\t"
    "v_mfma_f32_16x16x32_bf16 v[100:103], a[236:239], a[156:159], v[100:103]\n\t"
    "v_sub_f32 v62, 0xc2c80000, v31\n\t"
    "v_sub_f32 v63, 0xc2c80000, v31\n\t"
    "v_mfma_f32_16x16x32_bf16 v[104:107], a[236:239], a[172:175], v[104:107]\n\t"
    "v_accvgpr_write_b32 a30, 0\n\t"
    "v_accvgpr_write_b32 a31, 0\n\t"
    "v_mfma_f32_16x16x32_bf16 v[108:111], a[236:239], a[188:191], v[108:111]\n\t"
    "v_accvgpr_write_b32 a32, 0\n\t"
    "v_accvgpr_write_b32 a33, 0\n\t"
    "v_mfma_f32_16x16x32_bf16 v[112:115], a[240:243], a[128:131], v[48:51]\n\t"
    "v_accvgpr_write_b32 a34, 0\n\t"
    "v_accvgpr_write_b32 a35, 0\n\t"
    "v_mfma_f32_16x16x32_bf16 v[116:119], a[240:243], a[144:147], v[52:55]\n\t"
    "v_accvgpr_write_b32 a36, 0\n\t"
    "v_accvgpr_write_b32 a37, 0\n\t"
    "v_mfma_f32_16x16x32_bf16 v[120:123], a[240:243], a[160:163], v[56:59]\n\t"
    "v_accvgpr_write_b32 a38, 0\n\t"
    "v_accvgpr_write_b32 a39, 0\n\t"
    "v_mfma_f32_16x16x32_bf16 v[124:127], a[240:243], a[176:179], v[60:63]\n\t"
    "v_accvgpr_write_b32 a40, 0\n\t"
    "v_accvgpr_write_b32 a41, 0\n\t"
    "s_waitcnt lgkmcnt(1)\n\t"
    "v_mfma_f32_16x16x32_bf16 v[112:115], a[244:247], a[132:135], v[112:115]\n\t"
    "v_accvgpr_write_b32 a42, 0\n\t"
    "v_accvgpr_write_b32 a43, 0\n\t"
    "v_mfma_f32_16x16x32_bf16 v[116:119], a[244:247], a[148:151], v[116:119]\n\t"
    "v_accvgpr_write_b32 a44, 0\n\t"
    "v_accvgpr_write_b32 a45, 0\n\t"
    "v_mfma_f32_16x16x32_bf16 v[120:123], a[244:247], a[164:167], v[120:123]\n\t"
    "v_accvgpr_write_b32 a46, 0\n\t"
    "v_accvgpr_write_b32 a47, 0\n\t"
    "v_mfma_f32_16x16x32_bf16 v[124:127], a[244:247], a[180:183], v[124:127]\n\t"
    "v_accvgpr_write_b32 a48, 0\n\t"
    "v_accvgpr_write_b32 a49, 0\n\t"
    "v_mfma_f32_16x16x32_bf16 v[112:115], a[248:251], a[136:139], v[112:115]\n\t"
    "v_accvgpr_write_b32 a50, 0\n\t"
    "v_accvgpr_write_b32 a51, 0\n\t"
    "v_mfma_f32_16x16x32_bf16 v[116:119], a[248:251], a[152:155], v[116:119]\n\t"
    "v_accvgpr_write_b32 a52, 0\n\t"
    "v_accvgpr_write_b32 a53, 0\n\t"
    "v_mfma_f32_16x16x32_bf16 v[120:123], a[248:251], a[168:171], v[120:123]\n\t"
    "v_accvgpr_write_b32 a54, 0\n\t"
    "v_accvgpr_write_b32 a55, 0\n\t"
    "v_mfma_f32_16x16x32_bf16 v[124:127], a[248:251], a[184:187], v[124:127]\n\t"
    "v_accvgpr_write_b32 a56, 0\n\t"
    "v_accvgpr_write_b32 a57, 0\n\t"
    "s_waitcnt lgkmcnt(0)\n\t"
    "v_mfma_f32_16x16x32_bf16 v[112:115], a[252:255], a[140:143], v[112:115]\n\t"
    "v_accvgpr_write_b32 a58, 0\n\t"
    "v_accvgpr_write_b32 a59, 0\n\t"
    "v_mfma_f32_16x16x32_bf16 v[116:119], a[252:255], a[156:159], v[116:119]\n\t"
    "v_accvgpr_write_b32 a60, 0\n\t"
    "v_xor_b32 v24, 0x4000, v24\n\t"
    "v_mfma_f32_16x16x32_bf16 v[120:123], a[252:255], a[172:175], v[120:123]\n\t"
    "s_add_u32 s65, s65, 0x10000\n\t"
    "s_xor_b32 s66, s66, 0x4000\n\t"
    "v_mfma_f32_16x16x32_bf16 v[124:127], a[252:255], a[188:191], v[124:127]\n\t"
    "v_accvgpr_write_b32 a61, 0\n\t"
    "v_accvgpr_write_b32 a62, 0\n\t"
    "v_accvgpr_write_b32 a63, 0\n\t"
    "s_sub_u32 s67, s67, 1\n\t"
    "s_cmp_lg_u32 s81, 0\n\t"
    "s_cbranch_scc1 s0b_0_%=\n\t"
    "; ---- s0a_0\n\t"
    "s_waitcnt vmcnt(0)\n\t"
    "; ---- s0b_0\n\t"
    "s0b_0_%=:\n\t"
    "s_waitcnt vmcnt(18) lgkmcnt(0)\n\t"
    "s_barrier\n\t"
    "ds_read_b128 a[192:195], v24 offset:0\n\t"
    "ds_read_b128 a[196:199], v24 offset:1024\n\t"
    "ds_read_b128 a[200:203], v24 offset:2048\n\t"
    "ds_read_b128 a[204:207], v24 offset:3072\n\t"
    "ds_read_b128 a[208:211], v24 offset:4096\n\t"
    "ds_read_b128 a[212:215], v24 offset:5120\n\t"
    "s_or_b32 s68, %0, s81\n\t"
    "s_cmp_lg_u32 s68, 0\n\t"
    "s_cbranch_scc1 pubb0_%=\n\t"
    "v_mov_b64 v[36:37], %10\n\t"
    "v_mov_b32 v38, 0\n\t"
    "s_mov_b64 s[62:63], exec\n\t"
    "s_mov_b64 exec, 1\n\t"
    "buffer_store_dwordx2 v[36:37], v38, s[40:43], %9 offen offset:0 sc1\n\t"
    "s_mov_b64 exec, s[62:63]\n\t"
    "pubb0_%=:\n\t"
    "; ---- a1_1\n\t"
    "s_waitcnt lgkmcnt(5)\n\t"
    "v_mfma_f32_16x16x32_bf16 v[128:131], a[192:195], a[128:131], v[48:51]\n\t"
    "ds_read_b128 a[216:219], v24 offset:6144\n\t"
    "v_add_f32 v64, v48, v64\n\t"
    "v_add_f32 v65, v48, v65\n\t"
    "v_mfma_f32_16x16x32_bf16 v[132:135], a[192:195], a[144:147], v[52:55]\n\t"
    "s_mov_b32 m0, s64\n\t"
    "v_exp_f32 v40, v64\n\t"
    "v_add_f32 v66, v48, v66\n\t"
    "v_mfma_f32_16x16x32_bf16 v[136:139], a[192:195], a[160:163], v[56:59]\n\t"
    "buffer_load_dwordx4 %6, s[48:51], s70 offen offset:0 sc1 lds\n\t"
    "v_add_f32 v67, v48, v67\n\t"
    "v_mfma_f32_16x16x32_bf16 v[140:143], a[192:195], a[176:179], v[60:63]\n\t"
    "v_exp_f32 v41, v65\n\t"
    "v_exp_f32 v42, v66\n\t"
    "s_waitcnt lgkmcnt(4)\n\t"
    "v_mfma_f32_16x16x32_bf16 v[128:131], a[196:199], a[132:135], v[128:131]\n\t"
    "buffer_load_dwordx4 %6, s[48:51], s70 offen offset:1024 sc1 lds\n\t"
    "v_cvt_pk_bf16_f32 v192, v40, v41\n\t"
    "v_mfma_f32_16x16x32_bf16 v[132:135], a[196:199], a[148:151], v[132:135]\n\t"
    "v_exp_f32 v43, v67\n\t"
    "v_add_f32 v68, v52, v68\n\t"
    "v_add_f32 v69, v52, v69\n\t"
    "v_mfma_f32_16x16x32_bf16 v[136:139], a[196:199], a[164:167], v[136:139]\n\t"
    "ds_read_b128 a[220:223], v24 offset:7168\n\t"
    "v_cvt_pk_bf16_f32 v193, v42, v43\n\t"
    "v_exp_f32 v44, v68\n\t"
    "v_mfma_f32_16x16x32_bf16 v[140:143], a[196:199], a[180:183], v[140:143]\n\t"
    "v_exp_f32 v45, v69\n\t"
    "v_add_f32 v70, v52, v70\n\t"
    "v_mfma_f32_16x16x32_bf16 v[128:131], a[200:203], a[136:139], v[128:131]\n\t"
    "buffer_load_dwordx4 %6, s[48:51], s70 offen offset:2048 sc1 lds\n\t"
    "v_cvt_pk_bf16_f32 v196, v44, v45\n\t"
    "v_add_f32 v71, v52, v71\n\t"
    "v_mfma_f32_16x16x32_bf16 v[132:135], a[200:203], a[152:155], v[132:135]\n\t"
    "v_exp_f32 v46, v70\n\t"
    "v_add_f32 v72, v56, v72\n\t"
    "v_exp_f32 v47, v71\n\t"
    "v_mfma_f32_16x16x32_bf16 v[136:139], a[200:203], a[168:171], v[136:139]\n\t"
    "v_cvt_pk_bf16_f32 v197, v46, v47\n\t"
    "v_add_f32 v73, v56, v73\n\t"
    "v_exp_f32 v40, v72\n\t"
    "v_mfma_f32_16x16x32_bf16 v[140:143], a[200:203], a[184:187], v[140:143]\n\t"
    "ds_read_b128 a[224:227], v24 offset:8192\n\t"
    "v_add_f32 v74, v56, v74\n\t"
    "v_exp_f32 v41, v73\n\t"
    "s_waitcnt lgkmcnt(4)\n\t"
    "v_mfma_f32_16x16x32_bf16 v[128:131], a[204:207], a[140:143], v[128:131]\n\t"
    "buffer_load_dwordx4 %6, s[48:51], s70 offen offset:3072 sc1 lds\n\t"
    "v_cvt_pk_bf16_f32 v200, v40, v41\n\t"
    "v_exp_f32 v42, v74\n\t"
    "v_mfma_f32_16x16x32_bf16 v[132:135], a[204:207], a[156:159], v[132:135]\n\t"
    "v_add_f32 v75, v56, v75\n\t"
    "v_add_f32 v76, v60, v76\n\t"
    "v_exp_f32 v43, v75\n\t"
    "v_mfma_f32_16x16x32_bf16 v[136:139], a[204:207], a[172:175], v[136:139]\n\t"
    "v_cvt_pk_bf16_f32 v201, v42, v43\n\t"
    "v_add_f32 v77, v60, v77\n\t"
    "v_exp_f32 v44, v76\n\t"
    "v_mfma_f32_16x16x32_bf16 v[140:143], a[204:207], a[188:191], v[140:143]\n\t"
    "v_exp_f32 v45, v77\n\t"
    "v_add_f32 v78, v60, v78\n\t"
    "v_mfma_f32_16x16x32_bf16 v[144:147], a[208:211], a[128:131], v[48:51]\n\t"
    "ds_read_b128 a[228:231], v24 offset:9216\n\t"
    "v_cvt_pk_bf16_f32 v204, v44, v45\n\t"
    "v_exp_f32 v46, v78\n\t"
    "v_add_f32 v79, v60, v79\n\t"
    "v_mfma_f32_16x16x32_bf16 v[148:151], a[208:211], a[144:147], v[52:55]\n\t"
    "s_add_u32 m0, s64, 0x1000\n\t"
    "v_add_f32 v80, v48, v80\n\t"
    "v_exp_f32 v47, v79\n\t"
    "v_mfma_f32_16x16x32_bf16 v[152:155], a[208:211], a[160:163], v[56:59]\n\t"
    "buffer_load_dwordx4 v27, s[48:51], s70 offen offset:0 sc1 lds\n\t"
    "v_cvt_pk_bf16_f32 v205, v46, v47\n\t"
    "v_exp_f32 v40, v80\n\t"
    "v_mfma_f32_16x16x32_bf16 v[156:159], a[208:211], a[176:179], v[60:63]\n\t"
    "buffer_load_dwordx4 v27, s[48:51], s70 offen offset:1024 sc1 lds\n\t"
    "v_add_f32 v81, v48, v81\n\t"
    "v_exp_f32 v41, v81\n\t"
    "s_waitcnt lgkmcnt(3)\n\t"
    "v_mfma_f32_16x16x32_bf16 v[144:147], a[212:215], a[132:135], v[144:147]\n\t"
    "ds_read_b128 a[232:235], v24 offset:10240\n\t"
    "v_add_f32 v82, v48, v82\n\t"
    "v_cvt_pk_bf16_f32 v194, v40, v41\n\t"
    "v_exp_f32 v42, v82\n\t"
    "v_mfma_f32_16x16x32_bf16 v[148:151], a[212:215], a[148:151], v[148:151]\n\t"
    "v_add_f32 v83, v48, v83\n\t"
    "v_add_f32 v84, v52, v84\n\t"
    "v_exp_f32 v43, v83\n\t"
    "v_mfma_f32_16x16x32_bf16 v[152:155], a[212:215], a[164:167], v[152:155]\n\t"
    "v_cvt_pk_bf16_f32 v195, v42, v43\n\t"
    "v_add_f32 v85, v52, v85\n\t"
    "v_exp_f32 v44, v84\n\t"
    "v_add_f32 v86, v52, v86\n\t"
    "v_mfma_f32_16x16x32_bf16 v[156:159], a[212:215], a[180:183], v[156:159]\n\t"
    "buffer_load_dwordx4 v27, s[48:51], s70 offen offset:2048 sc1 lds\n\t"
    "v_exp_f32 v45, v85\n\t"
    "v_mfma_f32_16x16x32_bf16 v[144:147], a[216:219], a[136:139], v[144:147]\n\t"
    "v_exp_f32 v46, v86\n\t"
    "v_add_f32 v87, v52, v87\n\t"
    "v_cvt_pk_bf16_f32 v198, v44, v45\n\t"
    "v_mfma_f32_16x16x32_bf16 v[148:151], a[216:219], a[152:155], v[148:151]\n\t"
    "ds_read_b128 a[236:239], v24 offset:11264\n\t"
    "v_exp_f32 v47, v87\n\t"
    "v_add_f32 v88, v56, v88\n\t"
    "v_mfma_f32_16x16x32_bf16 v[152:155], a[216:219], a[168:171], v[152:155]\n\t"
    "buffer_load_dwordx4 v27, s[48:51], s70 offen offset:3072 sc1 lds\n\t"
    "v_cvt_pk_bf16_f32 v199, v46, v47\n\t"
    "v_exp_f32 v40, v88\n\t"
    "v_mfma_f32_16x16x32_bf16 v[156:159], a[216:219], a[184:187], v[156:159]\n\t"
    "v_add_f32 v89, v56, v89\n\t"
    "v_add_f32 v90, v56, v90\n\t"
    "v_exp_f32 v41, v89\n\t"
    "s_waitcnt lgkmcnt(3)\n\t"
    "v_mfma_f32_16x16x32_bf16 v[144:147], a[220:223], a[140:143], v[144:147]\n\t"
    "v_cvt_pk_bf16_f32 v202, v40, v41\n\t"
    "v_add_f32 v91, v56, v91\n\t"
    "v_exp_f32 v42, v90\n\t"
    "v_mfma_f32_16x16x32_bf16 v[148:151], a[220:223], a[156:159], v[148:151]\n\t"
    "ds_read_b128 a[240:243], v24 offset:12288\n\t"
    "v_exp_f32 v43, v91\n\t"
    "v_add_f32 v92, v60, v92\n\t"
    "v_mfma_f32_16x16x32_bf16 v[152:155], a[220:223], a[172:175], v[152:155]\n\t"
    "v_cvt_pk_bf16_f32 v203, v42, v43\n\t"
    "v_add_f32 v93, v60, v93\n\t"
    "v_exp_f32 v44, v92\n\t"
    "v_mfma_f32_16x16x32_bf16 v[156:159], a[220:223], a[188:191], v[156:159]\n\t"
    "v_exp_f32 v45, v93\n\t"
    "v_add_f32 v94, v60, v94\n\t"
    "v_exp_f32 v46, v94\n\t"
    "v_mfma_f32_16x16x32_bf16 v[160:163], a[224:227], a[128:131], v[48:51]\n\t"
    "v_cvt_pk_bf16_f32 v206, v44, v45\n\t"
    "v_add_f32 v95, v60, v95\n\t"
    "s_mov_b32 m0, s66\n\t"
    "v_exp_f32 v47, v95\n\t"
    "v_mfma_f32_16x16x32_bf16 v[164:167], a[224:227], a[144:147], v[52:55]\n\t"
    "buffer_load_dwordx4 %6, s[52:55], s65 offen offset:0 sc1 lds\n\t"
    "ds_read_b128 a[244:247], v24 offset:13312\n\t"
    "v_add_f32 v96, v48, v96\n\t"
    "v_cvt_pk_bf16_f32 v207, v46, v47\n\t"
    "v_mfma_f32_16x16x32_bf16 v[168:171], a[224:227], a[160:163], v[56:59]\n\t"
    "buffer_load_dwordx4 %6, s[52:55], s65 offen offset:1024 sc1 lds\n\t"
    "v_exp_f32 v40, v96\n\t"
    "v_add_f32 v97, v48, v97\n\t"
    "v_mfma_f32_16x16x32_bf16 v[172:175], a[224:227], a[176:179], v[60:63]\n\t"
    "v_exp_f32 v41, v97\n\t"
    "v_add_f32 v98, v48, v98\n\t"
    "v_exp_f32 v42, v98\n\t"
    "s_waitcnt lgkmcnt(4)\n\t"
    "v_mfma_f32_16x16x32_bf16 v[160:163], a[228:231], a[132:135], v[160:163]\n\t"
    "v_cvt_pk_bf16_f32 v224, v40, v41\n\t"
    "v_add_f32 v99, v48, v99\n\t"
    "v_exp_f32 v43, v99\n\t"
    "v_add_f32 v100, v52, v100\n\t"
    "v_mfma_f32_16x16x32_bf16 v[164:167], a[228:231], a[148:151], v[164:167]\n\t"
    "buffer_load_dwordx4 %6, s[52:55], s65 offen offset:2048 sc1 lds\n\t"
    "v_cvt_pk_bf16_f32 v225, v42, v43\n\t"
    "v_exp_f32 v44, v100\n\t"
    "v_mfma_f32_16x16x32_bf16 v[240:243], v[16:19], v[192:195], 0\n\t"
    "ds_read_b128 a[248:251], v24 offset:14336\n\t"
    "v_add_f32 v101, v52, v101\n\t"
    "v_exp_f32 v45, v101\n\t"
    "v_add_f32 v102, v52, v102\n\t"
    "v_mfma_f32_16x16x32_bf16 v[168:171], a[228:231], a[164:167], v[168:171]\n\t"
    "v_cvt_pk_bf16_f32 v228, v44, v45\n\t"
    "v_add_f32 v103, v52, v103\n\t"
    "v_exp_f32 v46, v102\n\t"
    "v_mfma_f32_16x16x32_bf16 v[172:175], a[228:231], a[180:183], v[172:175]\n\t"
    "v_exp_f32 v47, v103\n\t"
    "v_add_f32 v104, v56, v104\n\t"
    "v_mfma_f32_16x16x32_bf16 v[244:247], v[16:19], v[196:199], 0\n\t"
    "buffer_load_dwordx4 %6, s[52:55], s65 offen offset:3072 sc1 lds\n\t"
    "v_cvt_pk_bf16_f32 v229, v46, v47\n\t"
    "v_exp_f32 v40, v104\n\t"
    "s_waitcnt lgkmcnt(4)\n\t"
    "v_mfma_f32_16x16x32_bf16 v[160:163], a[232:235], a[136:139], v[160:163]\n\t"
    "ds_read_b128 a[252:255], v24 offset:15360\n\t"
    "v_add_f32 v105, v56, v105\n\t"
    "v_exp_f32 v41, v105\n\t"
    "v_add_f32 v106, v56, v106\n\t"
    "v_mfma_f32_16x16x32_bf16 v[164:167], a[232:235], a[152:155], v[164:167]\n\t"
    "v_add_f32 v107, v56, v107\n\t"
    "v_cvt_pk_bf16_f32 v232, v40, v41\n\t"
    "v_exp_f32 v42, v106\n\t"
    "v_mfma_f32_16x16x32_bf16 v[248:251], v[16:19], v[200:203], 0\n\t"
    "v_exp_f32 v43, v107\n\t"
    "v_add_f32 v108, v60, v108\n\t"
    "v_exp_f32 v44, v108\n\t"
    "v_mfma_f32_16x16x32_bf16 v[168:171], a[232:235], a[168:171], v[168:171]\n\t"
    "v_cvt_pk_bf16_f32 v233, v42, v43\n\t"
    "v_add_f32 v109, v60, v109\n\t"
    "v_exp_f32 v45, v109\n\t"
    "v_add_f32 v110, v60, v110\n\t"
    "v_mfma_f32_16x16x32_bf16 v[172:175], a[232:235], a[184:187], v[172:175]\n\t"
    "v_cvt_pk_bf16_f32 v236, v44, v45\n\t"
    "v_add_f32 v111, v60, v111\n\t"
    "v_exp_f32 v46, v110\n\t"
    "v_mfma_f32_16x16x32_bf16 v[252:255], v[16:19], v[204:207], 0\n\t"
    "s_xor_b32 m0, s66, 0x1000\n\t"
    "v_exp_f32 v47, v111\n\t"
    "s_waitcnt lgkmcnt(3)\n\t"
    "v_mfma_f32_16x16x32_bf16 v[160:163], a[236:239], a[140:143], v[160:163]\n\t"
    "buffer_load_dwordx4 v27, s[52:55], s65 offen offset:0 sc1 lds\n\t"
    "v_cvt_pk_bf16_f32 v237, v46, v47\n\t"
    "v_exp_f32 v40, v112\n\t"
    "v_mfma_f32_16x16x32_bf16 v[164:167], a[236:239], a[156:159], v[164:167]\n\t"
    "buffer_load_dwordx4 v27, s[52:55], s65 offen offset:1024 sc1 lds\n\t"
    "v_exp_f32 v41, v113\n\t"
    "v_mfma_f32_16x16x32_bf16 v[168:171], a[236:239], a[172:175], v[168:171]\n\t"
    "v_cvt_pk_bf16_f32 v226, v40, v41\n\t"
    "v_exp_f32 v42, v114\n\t"
    "v_mfma_f32_16x16x32_bf16 v[172:175], a[236:239], a[188:191], v[172:175]\n\t"
    "v_exp_f32 v43, v115\n\t"
    "v_mfma_f32_16x16x32_bf16 v[176:179], a[240:243], a[128:131], v[48:51]\n\t"
    "buffer_load_dwordx4 v27, s[52:55], s65 offen offset:2048 sc1 lds\n\t"
    "v_cvt_pk_bf16_f32 v227, v42, v43\n\t"
    "v_exp_f32 v44, v116\n\t"
    "v_mfma_f32_16x16x32_bf16 v[180:183], a[240:243], a[144:147], v[52:55]\n\t"
    "v_exp_f32 v45, v117\n\t"
    "v_mfma_f32_16x16x32_bf16 v[184:187], a[240:243], a[160:163], v[56:59]\n\t"
    "v_cvt_pk_bf16_f32 v230, v44, v45\n\t"
    "v_exp_f32 v46, v118\n\t"
    "v_mfma_f32_16x16x32_bf16 v[188:191], a[240:243], a[176:179], v[60:63]\n\t"
    "v_exp_f32 v47, v119\n\t"
    "s_waitcnt lgkmcnt(1)\n\t"
    "v_mfma_f32_16x16x32_bf16 v[176:179], a[244:247], a[132:135], v[176:179]\n\t"
    "buffer_load_dwordx4 v27, s[52:55], s65 offen offset:3072 sc1 lds\n\t"
    "v_cvt_pk_bf16_f32 v231, v46, v47\n\t"
    "v_exp_f32 v40, v120\n\t"
    "v_mfma_f32_16x16x32_bf16 v[180:183], a[244:247], a[148:151], v[180:183]\n\t"
    "v_exp_f32 v41, v121\n\t"
    "v_mfma_f32_16x16x32_bf16 v[184:187], a[244:247], a[164:167], v[184:187]\n\t"
    "v_cvt_pk_bf16_f32 v234, v40, v41\n\t"
    "v_xor_b32 v24, 0x4000, v24\n\t"
    "v_exp_f32 v42, v122\n\t"
    "v_mfma_f32_16x16x32_bf16 v[188:191], a[244:247], a[180:183], v[188:191]\n\t"
    "v_exp_f32 v43, v123\n\t"
    "v_mfma_f32_16x16x32_bf16 v[176:179], a[248:251], a[136:139], v[176:179]\n\t"
    "v_cvt_pk_bf16_f32 v235, v42, v43\n\t"
    "v_exp_f32 v44, v124\n\t"
    "v_mfma_f32_16x16x32_bf16 v[180:183], a[248:251], a[152:155], v[180:183]\n\t"
    "v_exp_f32 v45, v125\n\t"
    "v_mfma_f32_16x16x32_bf16 v[184:187], a[248:251], a[168:171], v[184:187]\n\t"
    "v_cvt_pk_bf16_f32 v238, v44, v45\n\t"
    "v_exp_f32 v46, v126\n\t"
    "v_mfma_f32_16x16x32_bf16 v[188:191], a[248:251], a[184:187], v[188:191]\n\t"
    "v_exp_f32 v47, v127\n\t"
    "s_waitcnt lgkmcnt(0)\n\t"
    "v_mfma_f32_16x16x32_bf16 v[176:179], a[252:255], a[140:143], v[176:179]\n\t"
    "v_cvt_pk_bf16_f32 v239, v46, v47\n\t"
    "s_add_u32 s70, s70, 0x10000\n\t"
    "s_xor_b32 s64, s64, 0x4000\n\t"
    "v_mfma_f32_16x16x32_bf16 v[240:243], v[16:19], v[224:227], v[240:243]\n\t"
    "s_cmp_eq_u32 s70, s71\n\t"
    "s_cselect_b32 s70, 0, s70\n\t"
    "v_mfma_f32_16x16x32_bf16 v[180:183], a[252:255], a[156:159], v[180:183]\n\t"
    "s_add_u32 s65, s65, 0x10000\n\t"
    "s_xor_b32 s66, s66, 0x4000\n\t"
    "v_mfma_f32_16x16x32_bf16 v[244:247], v[16:19], v[228:231], v[244:247]\n\t"
    "v_mfma_f32_16x16x32_bf16 v[184:187], a[252:255], a[172:175], v[184:187]\n\t"
    "v_mfma_f32_16x16x32_bf16 v[248:251], v[16:19], v[232:235], v[248:251]\n\t"
    "v_mfma_f32_16x16x32_bf16 v[188:191], a[252:255], a[188:191], v[188:191]\n\t"
    "v_mfma_f32_16x16x32_bf16 v[252:255], v[16:19], v[236:239], v[252:255]\n\t"
    "s_sub_u32 s67, s67, 1\n\t"
    "s_add_u32 s68, s67, s83\n\t"
    "s_cmp_eq_u32 s68, 0\n\t"
    "s_cbranch_scc1 a1_last_%=\n\t"
    "; ---- a1_mid\n\t"
    "s_waitcnt vmcnt(8)\n\t"
    "s_barrier\n\t"
    "ds_read_b128 a[192:195], v25 offset:0\n\t"
    "ds_read_b128 a[196:199], v25 offset:1024\n\t"
    "ds_read_b128 a[200:203], v25 offset:2048\n\t"
    "ds_read_b128 a[204:207], v25 offset:3072\n\t"
    "ds_read_b128 a[208:211], v25 offset:4096\n\t"
    "ds_read_b128 a[212:215], v25 offset:5120\n\t"
    "s_branch bndx_1_%=\n\t"
    "; ---- a1_last\n\t"
    "a1_last_%=:\n\t"
    "s_waitcnt vmcnt(0)\n\t"
    "s_barrier\n\t"
    "ds_read_b128 a[192:195], v25 offset:0\n\t"
    "ds_read_b128 a[196:199], v25 offset:1024\n\t"
    "ds_read_b128 a[200:203], v25 offset:2048\n\t"
    "ds_read_b128 a[204:207], v25 offset:3072\n\t"
    "ds_read_b128 a[208:211], v25 offset:4096\n\t"
    "ds_read_b128 a[212:215], v25 offset:5120\n\t"
    "s_branch lastx_%=\n\t"
    "; ---- bnd_0\n\t"
    "bnd_0_%=:\n\t"
    "s_sub_u32 s67, s67, 1\n\t"
    "s_waitcnt vmcnt(9)\n\t"
    "s_barrier\n\t"
    "; ---- bndx_0\n\t"
    "bndx_0_%=:\n\t"
    "v_max3_f32 v20, v240, v244, v248\n\t"
    "v_max_f32 v20, v20, v252\n\t"
    "v_cmp_nge_f32 vcc, 0x5d800000, v20\n\t"
    "s_nop 0\n\t"
    "s_cbranch_vccnz rare_b0_%=\n\t"
    "cont_b0_%=:\n\t"
    "; ---- b_0\n\t"
    "s_waitcnt lgkmcnt(5)\n\t"
    "v_mfma_f32_16x16x32_bf16 a[0:3], a[192:195], v[208:211], a[0:3]\n\t"
    "v_add_f32 v32, v32, v240\n\t"
    "v_add_f32 v33, v33, v244\n\t"
    "v_mfma_f32_16x16x32_bf16 a[4:7], a[192:195], v[212:215], a[4:7]\n\t"
    "v_add_f32 v34, v34, v248\n\t"
    "v_add_f32 v35, v35, v252\n\t"
    "v_mfma_f32_16x16x32_bf16 a[8:11], a[192:195], v[216:219], a[8:11]\n\t"
    "ds_read_b128 a[216:219], v25 offset:6144\n\t"
    "s_mov_b32 m0, s64\n\t"
    "v_mfma_f32_16x16x32_bf16 a[12:15], a[192:195], v[220:223], a[12:15]\n\t"
    "buffer_load_dwordx4 %6, s[48:51], s70 offen offset:0 sc1 lds\n\t"
    "s_waitcnt lgkmcnt(4)\n\t"
    "v_mfma_f32_16x16x32_bf16 a[16:19], a[196:199], v[208:211], a[16:19]\n\t"
    "v_exp_f32 v40, v64\n\t"
    "v_exp_f32 v41, v65\n\t"
    "v_mfma_f32_16x16x32_bf16 a[20:23], a[196:199], v[212:215], a[20:23]\n\t"
    "ds_read_b128 a[220:223], v25 offset:7168\n\t"
    "v_cvt_pk_bf16_f32 v192, v40, v41\n\t"
    "v_mfma_f32_16x16x32_bf16 a[24:27], a[196:199], v[216:219], a[24:27]\n\t"
    "buffer_load_dwordx4 %6, s[48:51], s70 offen offset:1024 sc1 lds\n\t"
    "v_mfma_f32_16x16x32_bf16 a[28:31], a[196:199], v[220:223], a[28:31]\n\t"
    "v_exp_f32 v42, v66\n\t"
    "v_mfma_f32_16x16x32_bf16 a[32:35], a[200:203], v[208:211], a[32:35]\n\t"
    "v_exp_f32 v43, v67\n\t"
    "v_mfma_f32_16x16x32_bf16 a[36:39], a[200:203], v[212:215], a[36:39]\n\t"
    "ds_read_b128 a[224:227], v25 offset:8192\n\t"
    "v_cvt_pk_bf16_f32 v193, v42, v43\n\t"
    "v_mfma_f32_16x16x32_bf16 a[40:43], a[200:203], v[216:219], a[40:43]\n\t"
    "v_exp_f32 v44, v68\n\t"
    "v_mfma_f32_16x16x32_bf16 a[44:47], a[200:203], v[220:223], a[44:47]\n\t"
    "buffer_load_dwordx4 %6, s[48:51], s70 offen offset:2048 sc1 lds\n\t"
    "s_waitcnt lgkmcnt(3)\n\t"
    "v_mfma_f32_16x16x32_bf16 a[48:51], a[204:207], v[208:211], a[48:51]\n\t"
    "v_exp_f32 v45, v69\n\t"
    "v_mfma_f32_16x16x32_bf16 a[52:55], a[204:207], v[212:215], a[52:55]\n\t"
    "ds_read_b128 a[228:231], v25 offset:9216\n\t"
    "v_cvt_pk_bf16_f32 v196, v44, v45\n\t"
    "v_mfma_f32_16x16x32_bf16 a[56:59], a[204:207], v[216:219], a[56:59]\n\t"
    "v_exp_f32 v46, v70\n\t"
    "v_mfma_f32_16x16x32_bf16 a[60:63], a[204:207], v[220:223], a[60:63]\n\t"
    "v_exp_f32 v47, v71\n\t"
    "v_mfma_f32_16x16x32_bf16 a[64:67], a[208:211], v[208:211], a[64:67]\n\t"
    "buffer_load_dwordx4 %6, s[48:51], s70 offen offset:3072 sc1 lds\n\t"
    "v_mfma_f32_16x16x32_bf16 a[68:71], a[208:211], v[212:215], a[68:71]\n\t"
    "ds_read_b128 a[232:235], v25 offset:10240\n\t"
    "v_cvt_pk_bf16_f32 v197, v46, v47\n\t"
    "v_mfma_f32_16x16x32_bf16 a[72:75], a[208:211], v[216:219], a[72:75]\n\t"
    "v_exp_f32 v40, v72\n\t"
    "v_mfma_f32_16x16x32_bf16 a[76:79], a[208:211], v[220:223], a[76:79]\n\t"
    "v_exp_f32 v41, v73\n\t"
    "v_mfma_f32_16x16x32_bf16 a[80:83], a[212:215], v[208:211], a[80:83]\n\t"
    "ds_read_b128 a[236:239], v25 offset:11264\n\t"
    "v_cvt_pk_bf16_f32 v200, v40, v41\n\t"
    "v_mfma_f32_16x16x32_bf16 a[84:87], a[212:215], v[212:215], a[84:87]\n\t"
    "v_exp_f32 v42, v74\n\t"
    "v_mfma_f32_16x16x32_bf16 a[88:91], a[212:215], v[216:219], a[88:91]\n\t"
    "ds_read_b128 a[240:243], v25 offset:12288\n\t"
    "s_add_u32 m0, s64, 0x1000\n\t"
    "v_mfma_f32_16x16x32_bf16 a[92:95], a[212:215], v[220:223], a[92:95]\n\t"
    "buffer_load_dwordx4 v27, s[48:51], s70 offen offset:0 sc1 lds\n\t"
    "s_waitcnt lgkmcnt(4)\n\t"
    "v_mfma_f32_16x16x32_bf16 a[96:99], a[216:219], v[208:211], a[96:99]\n\t"
    "v_exp_f32 v43, v75\n\t"
    "v_mfma_f32_16x16x32_bf16 a[100:103], a[216:219], v[212:215], a[100:103]\n\t"
    "ds_read_b128 a[244:247], v25 offset:13312\n\t"
    "v_cvt_pk_bf16_f32 v201, v42, v43\n\t"
    "v_mfma_f32_16x16x32_bf16 a[104:107], a[216:219], v[216:219], a[104:107]\n\t"
    "buffer_load_dwordx4 v27, s[48:51], s70 offen offset:1024 sc1 lds\n\t"
    "v_mfma_f32_16x16x32_bf16 a[108:111], a[216:219], v[220:223], a[108:111]\n\t"
    "v_exp_f32 v44, v76\n\t"
    "v_mfma_f32_16x16x32_bf16 a[112:115], a[220:223], v[208:211], a[112:115]\n\t"
    "v_exp_f32 v45, v77\n\t"
    "v_mfma_f32_16x16x32_bf16 a[116:119], a[220:223], v[212:215], a[116:119]\n\t"
    "ds_read_b128 a[248:251], v25 offset:14336\n\t"
    "v_cvt_pk_bf16_f32 v204, v44, v45\n\t"
    "v_mfma_f32_16x16x32_bf16 a[120:123], a[220:223], v[216:219], a[120:123]\n\t"
    "v_exp_f32 v46, v78\n\t"
    "v_mfma_f32_16x16x32_bf16 a[124:127], a[220:223], v[220:223], a[124:127]\n\t"
    "buffer_load_dwordx4 v27, s[48:51], s70 offen offset:2048 sc1 lds\n\t"
    "v_mfma_f32_16x16x32_bf16 a[0:3], a[224:227], v[224:227], a[0:3]\n\t"
    "v_exp_f32 v47, v79\n\t"
    "v_mfma_f32_16x16x32_bf16 a[4:7], a[224:227], v[228:231], a[4:7]\n\t"
    "ds_read_b128 a[252:255], v25 offset:15360\n\t"
    "v_cvt_pk_bf16_f32 v205, v46, v47\n\t"
    "v_mfma_f32_16x16x32_bf16 a[8:11], a[224:227], v[232:235], a[8:11]\n\t"
    "v_exp_f32 v40, v80\n\t"
    "v_mfma_f32_16x16x32_bf16 a[12:15], a[224:227], v[236:239], a[12:15]\n\t"
    "v_exp_f32 v41, v81\n\t"
    "s_waitcnt lgkmcnt(4)\n\t"
    "v_mfma_f32_16x16x32_bf16 a[16:19], a[228:231], v[224:227], a[16:19]\n\t"
    "buffer_load_dwordx4 v27, s[48:51], s70 offen offset:3072 sc1 lds\n\t"
    "v_mfma_f32_16x16x32_bf16 a[20:23], a[228:231], v[228:231], a[20:23]\n\t"
    "ds_read_b128 a[192:195], v24 offset:0\n\t"
    "v_cvt_pk_bf16_f32 v194, v40, v41\n\t"
    "v_exp_f32 v42, v82\n\t"
    "v_mfma_f32_16x16x32_bf16 a[24:27], a[228:231], v[232:235], a[24:27]\n\t"
    "v_exp_f32 v43, v83\n\t"
    "v_mfma_f32_16x16x32_bf16 a[28:31], a[228:231], v[236:239], a[28:31]\n\t"
    "ds_read_b128 a[196:199], v24 offset:1024\n\t"
    "v_cvt_pk_bf16_f32 v195, v42, v43\n\t"
    "v_mfma_f32_16x16x32_bf16 a[32:35], a[232:235], v[224:227], a[32:35]\n\t"
    "v_exp_f32 v44, v84\n\t"
    "v_mfma_f32_16x16x32_bf16 a[36:39], a[232:235], v[228:231], a[36:39]\n\t"
    "buffer_load_dwordx2 v[10:11], %16, s[40:43], 0 offen offset:0 sc1\n\t"
    "v_mfma_f32_16x16x32_bf16 a[40:43], a[232:235], v[232:235], a[40:43]\n\t"
    "v_exp_f32 v45, v85\n\t"
    "v_mfma_f32_16x16x32_bf16 a[44:47], a[232:235], v[236:239], a[44:47]\n\t"
    "ds_read_b128 a[200:203], v24 offset:2048\n\t"
    "v_cvt_pk_bf16_f32 v198, v44, v45\n\t"
    "v_mfma_f32_16x16x32_bf16 a[48:51], a[236:239], v[224:227], a[48:51]\n\t"
    "v_exp_f32 v46, v86\n\t"
    "v_mfma_f32_16x16x32_bf16 a[52:55], a[236:239], v[228:231], a[52:55]\n\t"
    "v_exp_f32 v47, v87\n\t"
    "v_mfma_f32_16x16x32_bf16 a[56:59], a[236:239], v[232:235], a[56:59]\n\t"
    "ds_read_b128 a[204:207], v24 offset:3072\n\t"
    "v_cvt_pk_bf16_f32 v199, v46, v47\n\t"
    "v_mfma_f32_16x16x32_bf16 a[60:63], a[236:239], v[236:239], a[60:63]\n\t"
    "v_exp_f32 v40, v88\n\t"
    "s_waitcnt lgkmcnt(5)\n\t"
    "v_mfma_f32_16x16x32_bf16 a[64:67], a[240:243], v[224:227], a[64:67]\n\t"
    "v_exp_f32 v41, v89\n\t"
    "v_mfma_f32_16x16x32_bf16 a[68:71], a[240:243], v[228:231], a[68:71]\n\t"
    "v_cvt_pk_bf16_f32 v202, v40, v41\n\t"
    "v_mfma_f32_16x16x32_bf16 a[72:75], a[240:243], v[232:235], a[72:75]\n\t"
    "v_exp_f32 v42, v90\n\t"
    "v_mfma_f32_16x16x32_bf16 a[76:79], a[240:243], v[236:239], a[76:79]\n\t"
    "v_exp_f32 v43, v91\n\t"
    "v_mfma_f32_16x16x32_bf16 a[80:83], a[244:247], v[224:227], a[80:83]\n\t"
    "ds_read_b128 a[208:211], v24 offset:4096\n\t"
    "v_cvt_pk_bf16_f32 v203, v42, v43\n\t"
    "v_mfma_f32_16x16x32_bf16 a[84:87], a[244:247], v[228:231], a[84:87]\n\t"
    "v_exp_f32 v44, v92\n\t"
    "v_mfma_f32_16x16x32_bf16 a[88:91], a[244:247], v[232:235], a[88:91]\n\t"
    "v_exp_f32 v45, v93\n\t"
    "v_mfma_f32_16x16x32_bf16 a[92:95], a[244:247], v[236:239], a[92:95]\n\t"
    "s_add_u32 s70, s70, 0x10000\n\t"
    "s_xor_b32 s64, s64, 0x4000\n\t"
    "v_mfma_f32_16x16x32_bf16 a[96:99], a[248:251], v[224:227], a[96:99]\n\t"
    "ds_read_b128 a[212:215], v24 offset:5120\n\t"
    "v_cvt_pk_bf16_f32 v206, v44, v45\n\t"
    "v_mfma_f32_16x16x32_bf16 a[100:103], a[248:251], v[228:231], a[100:103]\n\t"
    "v_exp_f32 v46, v94\n\t"
    "v_mfma_f32_16x16x32_bf16 a[104:107], a[248:251], v[232:235], a[104:107]\n\t"
    "s_cmp_eq_u32 s70, s71\n\t"
    "s_cselect_b32 s70, 0, s70\n\t"
    "v_mfma_f32_16x16x32_bf16 a[108:111], a[248:251], v[236:239], a[108:111]\n\t"
    "v_exp_f32 v47, v95\n\t"
    "s_waitcnt lgkmcnt(6)\n\t"
    "v_mfma_f32_16x16x32_bf16 a[112:115], a[252:255], v[224:227], a[112:115]\n\t"
    "v_xor_b32 v25, 0x4000, v25\n\t"
    "v_cvt_pk_bf16_f32 v207, v46, v47\n\t"
    "v_mfma_f32_16x16x32_bf16 a[116:119], a[252:255], v[228:231], a[116:119]\n\t"
    "v_mfma_f32_16x16x32_bf16 a[120:123], a[252:255], v[232:235], a[120:123]\n\t"
    "v_mfma_f32_16x16x32_bf16 a[124:127], a[252:255], v[236:239], a[124:127]\n\t"
    "; ---- a_1\n\t"
    "s_waitcnt vmcnt(9)\n\t"
    "s_barrier\n\t"
    "s_waitcnt lgkmcnt(4)\n\t"
    "v_mfma_f32_16x16x32_bf16 v[128:131], a[192:195], a[128:131], v[48:51]\n\t"
    "ds_read_b128 a[216:219], v24 offset:6144\n\t"
    "s_mov_b32 m0, s66\n\t"
    "v_mfma_f32_16x16x32_bf16 v[132:135], a[192:195], a[144:147], v[52:55]\n\t"
    "buffer_load_dwordx4 %6, s[52:55], s65 offen offset:0 sc1 lds\n\t"
    "v_mfma_f32_16x16x32_bf16 v[136:139], a[192:195], a[160:163], v[56:59]\n\t"
    "v_exp_f32 v40, v96\n\t"
    "v_mfma_f32_16x16x32_bf16 v[140:143], a[192:195], a[176:179], v[60:63]\n\t"
    "v_exp_f32 v41, v97\n\t"
    "v_mfma_f32_16x16x32_bf16 v[128:131], a[196:199], a[132:135], v[128:131]\n\t"
    "ds_read_b128 a[220:223], v24 offset:7168\n\t"
    "v_cvt_pk_bf16_f32 v224, v40, v41\n\t"
    "v_mfma_f32_16x16x32_bf16 v[132:135], a[196:199], a[148:151], v[132:135]\n\t"
    "v_exp_f32 v42, v98\n\t"
    "v_mfma_f32_16x16x32_bf16 v[240:243], v[16:19], v[192:195], 0\n\t"
    "buffer_load_dwordx4 %6, s[52:55], s65 offen offset:1024 sc1 lds\n\t"
    "v_mfma_f32_16x16x32_bf16 v[136:139], a[196:199], a[164:167], v[136:139]\n\t"
    "v_exp_f32 v43, v99\n\t"
    "v_mfma_f32_16x16x32_bf16 v[140:143], a[196:199], a[180:183], v[140:143]\n\t"
    "ds_read_b128 a[224:227], v24 offset:8192\n\t"
    "v_cvt_pk_bf16_f32 v225, v42, v43\n\t"
    "s_waitcnt lgkmcnt(4)\n\t"
    "v_mfma_f32_16x16x32_bf16 v[128:131], a[200:203], a[136:139], v[128:131]\n\t"
    "v_exp_f32 v44, v100\n\t"
    "v_mfma_f32_16x16x32_bf16 v[132:135], a[200:203], a[152:155], v[132:135]\n\t"
    "v_exp_f32 v45, v101\n\t"
    "v_mfma_f32_16x16x32_bf16 v[136:139], a[200:203], a[168:171], v[136:139]\n\t"
    "buffer_load_dwordx4 %6, s[52:55], s65 offen offset:2048 sc1 lds\n\t"
    "v_mfma_f32_16x16x32_bf16 v[140:143], a[200:203], a[184:187], v[140:143]\n\t"
    "ds_read_b128 a[228:231], v24 offset:9216\n\t"
    "v_cvt_pk_bf16_f32 v228, v44, v45\n\t"
    "v_mfma_f32_16x16x32_bf16 v[128:131], a[204:207], a[140:143], v[128:131]\n\t"
    "v_exp_f32 v46, v102\n\t"
    "v_mfma_f32_16x16x32_bf16 v[132:135], a[204:207], a[156:159], v[132:135]\n\t"
    "v_exp_f32 v47, v103\n\t"
    "v_mfma_f32_16x16x32_bf16 v[244:247], v[16:19], v[196:199], 0\n\t"
    "ds_read_b128 a[232:235], v24 offset:10240\n\t"
    "v_cvt_pk_bf16_f32 v229, v46, v47\n\t"
    "v_mfma_f32_16x16x32_bf16 v[136:139], a[204:207], a[172:175], v[136:139]\n\t"
    "buffer_load_dwordx4 %6, s[52:55], s65 offen offset:3072 sc1 lds\n\t"
    "v_mfma_f32_16x16x32_bf16 v[140:143], a[204:207], a[188:191], v[140:143]\n\t"
    "v_exp_f32 v40, v104\n\t"
    "v_mfma_f32_16x16x32_bf16 v[144:147], a[208:211], a[128:131], v[48:51]\n\t"
    "v_exp_f32 v41, v105\n\t"
    "v_mfma_f32_16x16x32_bf16 v[148:151], a[208:211], a[144:147], v[52:55]\n\t"
    "v_cvt_pk_bf16_f32 v232, v40, v41\n\t"
    "s_xor_b32 m0, s66, 0x1000\n\t"
    "v_mfma_f32_16x16x32_bf16 v[152:155], a[208:211], a[160:163], v[56:59]\n\t"
    "v_exp_f32 v42, v106\n\t"
    "v_mfma_f32_16x16x32_bf16 v[156:159], a[208:211], a[176:179], v[60:63]\n\t"
    "buffer_load_dwordx4 v27, s[52:55], s65 offen offset:0 sc1 lds\n\t"
    "s_waitcnt lgkmcnt(4)\n\t"
    "v_mfma_f32_16x16x32_bf16 v[144:147], a[212:215], a[132:135], v[144:147]\n\t"
    "v_exp_f32 v43, v107\n\t"
    "v_mfma_f32_16x16x32_bf16 v[148:151], a[212:215], a[148:151], v[148:151]\n\t"
    "ds_read_b128 a[236:239], v24 offset:11264\n\t"
    "v_cvt_pk_bf16_f32 v233, v42, v43\n\t"
    "v_mfma_f32_16x16x32_bf16 v[248:251], v[16:19], v[200:203], 0\n\t"
    "v_exp_f32 v44, v108\n\t"
    "v_mfma_f32_16x16x32_bf16 v[152:155], a[212:215], a[164:167], v[152:155]\n\t"
    "v_exp_f32 v45, v109\n\t"
    "v_mfma_f32_16x16x32_bf16 v[156:159], a[212:215], a[180:183], v[156:159]\n\t"
    "buffer_load_dwordx4 v27, s[52:55], s65 offen offset:1024 sc1 lds\n\t"
    "v_mfma_f32_16x16x32_bf16 v[144:147], a[216:219], a[136:139], v[144:147]\n\t"
    "ds_read_b128 a[240:243], v24 offset:12288\n\t"
    "v_cvt_pk_bf16_f32 v236, v44, v45\n\t"
    "v_exp_f32 v46, v110\n\t"
    "v_mfma_f32_16x16x32_bf16 v[148:151], a[216:219], a[152:155], v[148:151]\n\t"
    "v_exp_f32 v47, v111\n\t"
    "v_mfma_f32_16x16x32_bf16 v[152:155], a[216:219], a[168:171], v[152:155]\n\t"
    "ds_read_b128 a[244:247], v24 offset:13312\n\t"
    "v_cvt_pk_bf16_f32 v237, v46, v47\n\t"
    "v_mfma_f32_16x16x32_bf16 v[156:159], a[216:219], a[184:187], v[156:159]\n\t"
    "v_exp_f32 v40, v112\n\t"
    "s_waitcnt lgkmcnt(4)\n\t"
    "v_mfma_f32_16x16x32_bf16 v[144:147], a[220:223], a[140:143], v[144:147]\n\t"
    "buffer_load_dwordx4 v27, s[52:55], s65 offen offset:2048 sc1 lds\n\t"
    "v_mfma_f32_16x16x32_bf16 v[148:151], a[220:223], a[156:159], v[148:151]\n\t"
    "v_exp_f32 v41, v113\n\t"
    "v_mfma_f32_16x16x32_bf16 v[252:255], v[16:19], v[204:207], 0\n\t"
    "ds_read_b128 a[248:251], v24 offset:14336\n\t"
    "v_cvt_pk_bf16_f32 v226, v40, v41\n\t"
    "v_mfma_f32_16x16x32_bf16 v[152:155], a[220:223], a[172:175], v[152:155]\n\t"
    "v_exp_f32 v42, v114\n\t"
    "v_mfma_f32_16x16x32_bf16 v[156:159], a[220:223], a[188:191], v[156:159]\n\t"
    "v_exp_f32 v43, v115\n\t"
    "v_mfma_f32_16x16x32_bf16 v[160:163], a[224:227], a[128:131], v[48:51]\n\t"
    "buffer_load_dwordx4 v27, s[52:55], s65 offen offset:3072 sc1 lds\n\t"
    "v_mfma_f32_16x16x32_bf16 v[164:167], a[224:227], a[144:147], v[52:55]\n\t"
    "ds_read_b128 a[252:255], v24 offset:15360\n\t"
    "v_cvt_pk_bf16_f32 v227, v42, v43\n\t"
    "v_mfma_f32_16x16x32_bf16 v[168:171], a[224:227], a[160:163], v[56:59]\n\t"
    "v_exp_f32 v44, v116\n\t"
    "v_mfma_f32_16x16x32_bf16 v[172:175], a[224:227], a[176:179], v[60:63]\n\t"
    "v_exp_f32 v45, v117\n\t"
    "v_mfma_f32_16x16x32_bf16 v[160:163], a[228:231], a[132:135], v[160:163]\n\t"
    "ds_read_b128 a[192:195], v25 offset:0\n\t"
    "v_cvt_pk_bf16_f32 v230, v44, v45\n\t"
    "v_mfma_f32_16x16x32_bf16 v[164:167], a[228:231], a[148:151], v[164:167]\n\t"
    "v_exp_f32 v46, v118\n\t"
    "v_mfma_f32_16x16x32_bf16 v[168:171], a[228:231], a[164:167], v[168:171]\n\t"
    "v_exp_f32 v47, v119\n\t"
    "v_mfma_f32_16x16x32_bf16 v[172:175], a[228:231], a[180:183], v[172:175]\n\t"
    "ds_read_b128 a[196:199], v25 offset:1024\n\t"
    "v_cvt_pk_bf16_f32 v231, v46, v47\n\t"
    "s_waitcnt lgkmcnt(5)\n\t"
    "v_mfma_f32_16x16x32_bf16 v[160:163], a[232:235], a[136:139], v[160:163]\n\t"
    "v_exp_f32 v40, v120\n\t"
    "v_mfma_f32_16x16x32_bf16 v[164:167], a[232:235], a[152:155], v[164:167]\n\t"
    "v_exp_f32 v41, v121\n\t"
    "v_mfma_f32_16x16x32_bf16 v[168:171], a[232:235], a[168:171], v[168:171]\n\t"
    "ds_read_b128 a[200:203], v25 offset:2048\n\t"
    "v_cvt_pk_bf16_f32 v234, v40, v41\n\t"
    "v_mfma_f32_16x16x32_bf16 v[172:175], a[232:235], a[184:187], v[172:175]\n\t"
    "v_exp_f32 v42, v122\n\t"
    "v_mfma_f32_16x16x32_bf16 v[160:163], a[236:239], a[140:143], v[160:163]\n\t"
    "v_exp_f32 v43, v123\n\t"
    "v_mfma_f32_16x16x32_bf16 v[164:167], a[236:239], a[156:159], v[164:167]\n\t"
    "v_cvt_pk_bf16_f32 v235, v42, v43\n\t"
    "v_exp_f32 v44, v124\n\t"
    "v_mfma_f32_16x16x32_bf16 v[168:171], a[236:239], a[172:175], v[168:171]\n\t"
    "v_exp_f32 v45, v125\n\t"
    "v_mfma_f32_16x16x32_bf16 v[172:175], a[236:239], a[188:191], v[172:175]\n\t"
    "ds_read_b128 a[204:207], v25 offset:3072\n\t"
    "v_cvt_pk_bf16_f32 v238, v44, v45\n\t"
    "v_mfma_f32_16x16x32_bf16 v[176:179], a[240:243], a[128:131], v[48:51]\n\t"
    "v_exp_f32 v46, v126\n\t"
    "v_mfma_f32_16x16x32_bf16 v[180:183], a[240:243], a[144:147], v[52:55]\n\t"
    "v_exp_f32 v47, v127\n\t"
    "v_mfma_f32_16x16x32_bf16 v[184:187], a[240:243], a[160:163], v[56:59]\n\t"
    "v_cvt_pk_bf16_f32 v239, v46, v47\n\t"
    "v_mfma_f32_16x16x32_bf16 v[188:191], a[240:243], a[176:179], v[60:63]\n\t"
    "s_add_u32 s65, s65, 0x10000\n\t"
    "s_xor_b32 s66, s66, 0x4000\n\t"
    "s_waitcnt lgkmcnt(4)\n\t"
    "v_mfma_f32_16x16x32_bf16 v[176:179], a[244:247], a[132:135], v[176:179]\n\t"
    "ds_read_b128 a[208:211], v25 offset:4096\n\t"
    "v_mfma_f32_16x16x32_bf16 v[180:183], a[244:247], a[148:151], v[180:183]\n\t"
    "v_mfma_f32_16x16x32_bf16 v[184:187], a[244:247], a[164:167], v[184:187]\n\t"
    "v_xor_b32 v24, 0x4000, v24\n\t"
    "v_mfma_f32_16x16x32_bf16 v[188:191], a[244:247], a[180:183], v[188:191]\n\t"
    "v_mfma_f32_16x16x32_bf16 v[240:243], v[16:19], v[224:227], v[240:243]\n\t"
    "ds_read_b128 a[212:215], v25 offset:5120\n\t"
    "v_mfma_f32_16x16x32_bf16 v[176:179], a[248:251], a[136:139], v[176:179]\n\t"
    "v_mfma_f32_16x16x32_bf16 v[180:183], a[248:251], a[152:155], v[180:183]\n\t"
    "v_mfma_f32_16x16x32_bf16 v[244:247], v[16:19], v[228:231], v[244:247]\n\t"
    "v_mfma_f32_16x16x32_bf16 v[184:187], a[248:251], a[168:171], v[184:187]\n\t"
    "v_mfma_f32_16x16x32_bf16 v[188:191], a[248:251], a[184:187], v[188:191]\n\t"
    "v_mfma_f32_16x16x32_bf16 v[248:251], v[16:19], v[232:235], v[248:251]\n\t"
    "v_mfma_f32_16x16x32_bf16 v[176:179], a[252:255], a[140:143], v[176:179]\n\t"
    "v_mfma_f32_16x16x32_bf16 v[180:183], a[252:255], a[156:159], v[180:183]\n\t"
    "v_mfma_f32_16x16x32_bf16 v[252:255], v[16:19], v[236:239], v[252:255]\n\t"
    "v_mfma_f32_16x16x32_bf16 v[184:187], a[252:255], a[172:175], v[184:187]\n\t"
    "v_mfma_f32_16x16x32_bf16 v[188:191], a[252:255], a[188:191], v[188:191]\n\t"
    "; ---- bnd_1\n\t"
    "bnd_1_%=:\n\t"
    "s_sub_u32 s67, s67, 1\n\t"
    "s_add_u32 s68, s67, s83\n\t"
    "s_cmp_eq_u32 s68, 0\n\t"
    "s_cbranch_scc1 last_%=\n\t"
    "s_waitcnt vmcnt(9)\n\t"
    "s_barrier\n\t"
    "; ---- bndx_1\n\t"
    "bndx_1_%=:\n\t"
    "v_max3_f32 v20, v240, v244, v248\n\t"
    "v_max_f32 v20, v20, v252\n\t"
    "v_cmp_nge_f32 vcc, 0x5d800000, v20\n\t"
    "s_nop 0\n\t"
    "s_cbranch_vccnz rare_b1_%=\n\t"
    "cont_b1_%=:\n\t"
    "; ---- b_1\n\t"
    "s_waitcnt lgkmcnt(4)\n\t"
    "v_mfma_f32_16x16x32_bf16 a[0:3], a[192:195], v[192:195], a[0:3]\n\t"
    "v_add_f32 v32, v32, v240\n\t"
    "v_add_f32 v33, v33, v244\n\t"
    "v_mfma_f32_16x16x32_bf16 a[4:7], a[192:195], v[196:199], a[4:7]\n\t"
    "v_add_f32 v34, v34, v248\n\t"
    "v_add_f32 v35, v35, v252\n\t"
    "v_mfma_f32_16x16x32_bf16 a[8:11], a[192:195], v[200:203], a[8:11]\n\t"
    "ds_read_b128 a[216:219], v25 offset:6144\n\t"
    "s_mov_b32 m0, s64\n\t"
    "v_mfma_f32_16x16x32_bf16 a[12:15], a[192:195], v[204:207], a[12:15]\n\t"
    "buffer_load_dwordx4 %6, s[48:51], s70 offen offset:0 sc1 lds\n\t"
    "v_mfma_f32_16x16x32_bf16 a[16:19], a[196:199], v[192:195], a[16:19]\n\t"
    "v_exp_f32 v40, v128\n\t"
    "v_exp_f32 v41, v129\n\t"
    "v_mfma_f32_16x16x32_bf16 a[20:23], a[196:199], v[196:199], a[20:23]\n\t"
    "ds_read_b128 a[220:223], v25 offset:7168\n\t"
    "v_cvt_pk_bf16_f32 v208, v40, v41\n\t"
    "v_mfma_f32_16x16x32_bf16 a[24:27], a[196:199], v[200:203], a[24:27]\n\t"
    "buffer_load_dwordx4 %6, s[48:51], s70 offen offset:1024 sc1 lds\n\t"
    "v_mfma_f32_16x16x32_bf16 a[28:31], a[196:199], v[204:207], a[28:31]\n\t"
    "v_exp_f32 v42, v130\n\t"
    "s_waitcnt lgkmcnt(3)\n\t"
    "v_mfma_f32_16x16x32_bf16 a[32:35], a[200:203], v[192:195], a[32:35]\n\t"
    "v_exp_f32 v43, v131\n\t"
    "v_mfma_f32_16x16x32_bf16 a[36:39], a[200:203], v[196:199], a[36:39]\n\t"
    "ds_read_b128 a[224:227], v25 offset:8192\n\t"
    "v_cvt_pk_bf16_f32 v209, v42, v43\n\t"
    "v_mfma_f32_16x16x32_bf16 a[40:43], a[200:203], v[200:203], a[40:43]\n\t"
    "v_exp_f32 v44, v132\n\t"
    "v_mfma_f32_16x16x32_bf16 a[44:47], a[200:203], v[204:207], a[44:47]\n\t"
    "buffer_load_dwordx4 %6, s[48:51], s70 offen offset:2048 sc1 lds\n\t"
    "v_mfma_f32_16x16x32_bf16 a[48:51], a[204:207], v[192:195], a[48:51]\n\t"
    "v_exp_f32 v45, v133\n\t"
    "v_mfma_f32_16x16x32_bf16 a[52:55], a[204:207], v[196:199], a[52:55]\n\t"
    "ds_read_b128 a[228:231], v25 offset:9216\n\t"
    "v_cvt_pk_bf16_f32 v212, v44, v45\n\t"
    "v_mfma_f32_16x16x32_bf16 a[56:59], a[204:207], v[200:203], a[56:59]\n\t"
    "v_exp_f32 v46, v134\n\t"
    "v_mfma_f32_16x16x32_bf16 a[60:63], a[204:207], v[204:207], a[60:63]\n\t"
    "v_exp_f32 v47, v135\n\t"
    "v_mfma_f32_16x16x32_bf16 a[64:67], a[208:211], v[192:195], a[64:67]\n\t"
    "buffer_load_dwordx4 %6, s[48:51], s70 offen offset:3072 sc1 lds\n\t"
    "v_mfma_f32_16x16x32_bf16 a[68:71], a[208:211], v[196:199], a[68:71]\n\t"
    "ds_read_b128 a[232:235], v25 offset:10240\n\t"
    "v_cvt_pk_bf16_f32 v213, v46, v47\n\t"
    "v_mfma_f32_16x16x32_bf16 a[72:75], a[208:211], v[200:203], a[72:75]\n\t"
    "v_exp_f32 v40, v136\n\t"
    "v_mfma_f32_16x16x32_bf16 a[76:79], a[208:211], v[204:207], a[76:79]\n\t"
    "v_exp_f32 v41, v137\n\t"
    "s_waitcnt lgkmcnt(3)\n\t"
    "v_mfma_f32_16x16x32_bf16 a[80:83], a[212:215], v[192:195], a[80:83]\n\t"
    "ds_read_b128 a[236:239], v25 offset:11264\n\t"
    "v_cvt_pk_bf16_f32 v216, v40, v41\n\t"
    "v_mfma_f32_16x16x32_bf16 a[84:87], a[212:215], v[196:199], a[84:87]\n\t"
    "v_exp_f32 v42, v138\n\t"
    "v_mfma_f32_16x16x32_bf16 a[88:91], a[212:215], v[200:203], a[88:91]\n\t"
    "ds_read_b128 a[240:243], v25 offset:12288\n\t"
    "s_add_u32 m0, s64, 0x1000\n\t"
    "v_mfma_f32_16x16x32_bf16 a[92:95], a[212:215], v[204:207], a[92:95]\n\t"
    "buffer_load_dwordx4 v27, s[48:51], s70 offen offset:0 sc1 lds\n\t"
    "v_mfma_f32_16x16x32_bf16 a[96:99], a[216:219], v[192:195], a[96:99]\n\t"
    "v_exp_f32 v43, v139\n\t"
    "v_mfma_f32_16x16x32_bf16 a[100:103], a[216:219], v[196:199], a[100:103]\n\t"
    "ds_read_b128 a[244:247], v25 offset:13312\n\t"
    "v_cvt_pk_bf16_f32 v217, v42, v43\n\t"
    "v_mfma_f32_16x16x32_bf16 a[104:107], a[216:219], v[200:203], a[104:107]\n\t"
    "buffer_load_dwordx4 v27, s[48:51], s70 offen offset:1024 sc1 lds\n\t"
    "v_mfma_f32_16x16x32_bf16 a[108:111], a[216:219], v[204:207], a[108:111]\n\t"
    "v_exp_f32 v44, v140\n\t"
    "v_mfma_f32_16x16x32_bf16 a[112:115], a[220:223], v[192:195], a[112:115]\n\t"
    "v_exp_f32 v45, v141\n\t"
    "v_mfma_f32_16x16x32_bf16 a[116:119], a[220:223], v[196:199], a[116:119]\n\t"
    "ds_read_b128 a[248:251], v25 offset:14336\n\t"
    "v_cvt_pk_bf16_f32 v220, v44, v45\n\t"
    "v_mfma_f32_16x16x32_bf16 a[120:123], a[220:223], v[200:203], a[120:123]\n\t"
    "v_exp_f32 v46, v142\n\t"
    "v_mfma_f32_16x16x32_bf16 a[124:127], a[220:223], v[204:207], a[124:127]\n\t"
    "buffer_load_dwordx4 v27, s[48:51], s70 offen offset:2048 sc1 lds\n\t"
    "s_waitcnt lgkmcnt(4)\n\t"
    "v_mfma_f32_16x16x32_bf16 a[0:3], a[224:227], v[224:227], a[0:3]\n\t"
    "v_exp_f32 v47, v143\n\t"
    "v_mfma_f32_16x16x32_bf16 a[4:7], a[224:227], v[228:231], a[4:7]\n\t"
    "ds_read_b128 a[252:255], v25 offset:15360\n\t"
    "v_cvt_pk_bf16_f32 v221, v46, v47\n\t"
    "v_mfma_f32_16x16x32_bf16 a[8:11], a[224:227], v[232:235], a[8:11]\n\t"
    "v_exp_f32 v40, v144\n\t"
    "v_mfma_f32_16x16x32_bf16 a[12:15], a[224:227], v[236:239], a[12:15]\n\t"
    "v_exp_f32 v41, v145\n\t"
    "v_mfma_f32_16x16x32_bf16 a[16:19], a[228:231], v[224:227], a[16:19]\n\t"
    "buffer_load_dwordx4 v27, s[48:51], s70 offen offset:3072 sc1 lds\n\t"
    "v_mfma_f32_16x16x32_bf16 a[20:23], a[228:231], v[228:231], a[20:23]\n\t"
    "ds_read_b128 a[192:195], v24 offset:0\n\t"
    "v_cvt_pk_bf16_f32 v210, v40, v41\n\t"
    "v_exp_f32 v42, v146\n\t"
    "v_mfma_f32_16x16x32_bf16 a[24:27], a[228:231], v[232:235], a[24:27]\n\t"
    "v_exp_f32 v43, v147\n\t"
    "v_mfma_f32_16x16x32_bf16 a[28:31], a[228:231], v[236:239], a[28:31]\n\t"
    "ds_read_b128 a[196:199], v24 offset:1024\n\t"
    "v_cvt_pk_bf16_f32 v211, v42, v43\n\t"
    "v_mfma_f32_16x16x32_bf16 a[32:35], a[232:235], v[224:227], a[32:35]\n\t"
    "v_exp_f32 v44, v148\n\t"
    "v_mfma_f32_16x16x32_bf16 a[36:39], a[232:235], v[228:231], a[36:39]\n\t"
    "buffer_load_dword v13, v12, s[44:47], s60 offen\n\t"
    "v_mfma_f32_16x16x32_bf16 a[40:43], a[232:235], v[232:235], a[40:43]\n\t"
    "v_exp_f32 v45, v149\n\t"
    "v_mfma_f32_16x16x32_bf16 a[44:47], a[232:235], v[236:239], a[44:47]\n\t"
    "ds_read_b128 a[200:203], v24 offset:2048\n\t"
    "v_cvt_pk_bf16_f32 v214, v44, v45\n\t"
    "s_waitcnt lgkmcnt(5)\n\t"
    "v_mfma_f32_16x16x32_bf16 a[48:51], a[236:239], v[224:227], a[48:51]\n\t"
    "v_exp_f32 v46, v150\n\t"
    "v_mfma_f32_16x16x32_bf16 a[52:55], a[236:239], v[228:231], a[52:55]\n\t"
    "v_exp_f32 v47, v151\n\t"
    "v_mfma_f32_16x16x32_bf16 a[56:59], a[236:239], v[232:235], a[56:59]\n\t"
    "ds_read_b128 a[204:207], v24 offset:3072\n\t"
    "v_cvt_pk_bf16_f32 v215, v46, v47\n\t"
    "v_mfma_f32_16x16x32_bf16 a[60:63], a[236:239], v[236:239], a[60:63]\n\t"
    "v_exp_f32 v40, v152\n\t"
    "v_mfma_f32_16x16x32_bf16 a[64:67], a[240:243], v[224:227], a[64:67]\n\t"
    "v_exp_f32 v41, v153\n\t"
    "v_mfma_f32_16x16x32_bf16 a[68:71], a[240:243], v[228:231], a[68:71]\n\t"
    "v_cvt_pk_bf16_f32 v218, v40, v41\n\t"
    "v_mfma_f32_16x16x32_bf16 a[72:75], a[240:243], v[232:235], a[72:75]\n\t"
    "v_exp_f32 v42, v154\n\t"
    "v_mfma_f32_16x16x32_bf16 a[76:79], a[240:243], v[236:239], a[76:79]\n\t"
    "v_exp_f32 v43, v155\n\t"
    "v_mfma_f32_16x16x32_bf16 a[80:83], a[244:247], v[224:227], a[80:83]\n\t"
    "ds_read_b128 a[208:211], v24 offset:4096\n\t"
    "v_cvt_pk_bf16_f32 v219, v42, v43\n\t"
    "v_mfma_f32_16x16x32_bf16 a[84:87], a[244:247], v[228:231], a[84:87]\n\t"
    "v_exp_f32 v44, v156\n\t"
    "v_mfma_f32_16x16x32_bf16 a[88:91], a[244:247], v[232:235], a[88:91]\n\t"
    "v_exp_f32 v45, v157\n\t"
    "v_mfma_f32_16x16x32_bf16 a[92:95], a[244:247], v[236:239], a[92:95]\n\t"
    "s_add_u32 s70, s70, 0x10000\n\t"
    "s_xor_b32 s64, s64, 0x4000\n\t"
    "s_waitcnt lgkmcnt(5)\n\t"
    "v_mfma_f32_16x16x32_bf16 a[96:99], a[248:251], v[224:227], a[96:99]\n\t"
    "ds_read_b128 a[212:215], v24 offset:5120\n\t"
    "v_cvt_pk_bf16_f32 v222, v44, v45\n\t"
    "v_mfma_f32_16x16x32_bf16 a[100:103], a[248:251], v[228:231], a[100:103]\n\t"
    "v_exp_f32 v46, v158\n\t"
    "v_mfma_f32_16x16x32_bf16 a[104:107], a[248:251], v[232:235], a[104:107]\n\t"
    "s_cmp_eq_u32 s70, s71\n\t"
    "s_cselect_b32 s70, 0, s70\n\t"
    "v_mfma_f32_16x16x32_bf16 a[108:111], a[248:251], v[236:239], a[108:111]\n\t"
    "v_exp_f32 v47, v159\n\t"
    "v_mfma_f32_16x16x32_bf16 a[112:115], a[252:255], v[224:227], a[112:115]\n\t"
    "s_add_u32 s60, s60, 0x2000\n\t"
    "v_xor_b32 v25, 0x4000, v25\n\t"
    "v_mfma_f32_16x16x32_bf16 a[116:119], a[252:255], v[228:231], a[116:119]\n\t"
    "v_cvt_pk_bf16_f32 v223, v46, v47\n\t"
    "v_mfma_f32_16x16x32_bf16 a[120:123], a[252:255], v[232:235], a[120:123]\n\t"
    "v_mfma_f32_16x16x32_bf16 a[124:127], a[252:255], v[236:239], a[124:127]\n\t"
    "s_cmp_eq_u32 s67, 0\n\t"
    "s_cbranch_scc1 alite_%=\n\t"
    "; ---- a_0\n\t"
    "s_waitcnt vmcnt(9)\n\t"
    "s_barrier\n\t"
    "s_waitcnt lgkmcnt(4)\n\t"
    "v_mfma_f32_16x16x32_bf16 v[64:67], a[192:195], a[128:131], v[48:51]\n\t"
    "ds_read_b128 a[216:219], v24 offset:6144\n\t"
    "s_mov_b32 m0, s66\n\t"
    "v_mfma_f32_16x16x32_bf16 v[68:71], a[192:195], a[144:147], v[52:55]\n\t"
    "buffer_load_dwordx4 %6, s[52:55], s65 offen offset:0 sc1 lds\n\t"
    "v_mfma_f32_16x16x32_bf16 v[72:75], a[192:195], a[160:163], v[56:59]\n\t"
    "v_exp_f32 v40, v160\n\t"
    "v_mfma_f32_16x16x32_bf16 v[76:79], a[192:195], a[176:179], v[60:63]\n\t"
    "v_exp_f32 v41, v161\n\t"
    "v_mfma_f32_16x16x32_bf16 v[64:67], a[196:199], a[132:135], v[64:67]\n\t"
    "ds_read_b128 a[220:223], v24 offset:7168\n\t"
    "v_cvt_pk_bf16_f32 v224, v40, v41\n\t"
    "v_mfma_f32_16x16x32_bf16 v[68:71], a[196:199], a[148:151], v[68:71]\n\t"
    "v_exp_f32 v42, v162\n\t"
    "v_mfma_f32_16x16x32_bf16 v[240:243], v[16:19], v[208:211], 0\n\t"
    "buffer_load_dwordx4 %6, s[52:55], s65 offen offset:1024 sc1 lds\n\t"
    "v_mfma_f32_16x16x32_bf16 v[72:75], a[196:199], a[164:167], v[72:75]\n\t"
    "v_exp_f32 v43, v163\n\t"
    "v_mfma_f32_16x16x32_bf16 v[76:79], a[196:199], a[180:183], v[76:79]\n\t"
    "ds_read_b128 a[224:227], v24 offset:8192\n\t"
    "v_cvt_pk_bf16_f32 v225, v42, v43\n\t"
    "s_waitcnt lgkmcnt(4)\n\t"
    "v_mfma_f32_16x16x32_bf16 v[64:67], a[200:203], a[136:139], v[64:67]\n\t"
    "v_exp_f32 v44, v164\n\t"
    "v_mfma_f32_16x16x32_bf16 v[68:71], a[200:203], a[152:155], v[68:71]\n\t"
    "v_exp_f32 v45, v165\n\t"
    "v_mfma_f32_16x16x32_bf16 v[72:75], a[200:203], a[168:171], v[72:75]\n\t"
    "buffer_load_dwordx4 %6, s[52:55], s65 offen offset:2048 sc1 lds\n\t"
    "v_mfma_f32_16x16x32_bf16 v[76:79], a[200:203], a[184:187], v[76:79]\n\t"
    "ds_read_b128 a[228:231], v24 offset:9216\n\t"
    "v_cvt_pk_bf16_f32 v228, v44, v45\n\t"
    "v_mfma_f32_16x16x32_bf16 v[64:67], a[204:207], a[140:143], v[64:67]\n\t"
    "v_exp_f32 v46, v166\n\t"
    "v_mfma_f32_16x16x32_bf16 v[68:71], a[204:207], a[156:159], v[68:71]\n\t"
    "v_exp_f32 v47, v167\n\t"
    "v_mfma_f32_16x16x32_bf16 v[244:247], v[16:19], v[212:215], 0\n\t"
    "ds_read_b128 a[232:235], v24 offset:10240\n\t"
    "v_cvt_pk_bf16_f32 v229, v46, v47\n\t"
    "v_mfma_f32_16x16x32_bf16 v[72:75], a[204:207], a[172:175], v[72:75]\n\t"
    "buffer_load_dwordx4 %6, s[52:55], s65 offen offset:3072 sc1 lds\n\t"
    "v_mfma_f32_16x16x32_bf16 v[76:79], a[204:207], a[188:191], v[76:79]\n\t"
    "v_exp_f32 v40, v168\n\t"
    "v_mfma_f32_16x16x32_bf16 v[80:83], a[208:211], a[128:131], v[48:51]\n\t"
    "v_exp_f32 v41, v169\n\t"
    "v_mfma_f32_16x16x32_bf16 v[84:87], a[208:211], a[144:147], v[52:55]\n\t"
    "v_cvt_pk_bf16_f32 v232, v40, v41\n\t"
    "s_xor_b32 m0, s66, 0x1000\n\t"
    "v_mfma_f32_16x16x32_bf16 v[88:91], a[208:211], a[160:163], v[56:59]\n\t"
    "v_exp_f32 v42, v170\n\t"
    "v_mfma_f32_16x16x32_bf16 v[92:95], a[208:211], a[176:179], v[60:63]\n\t"
    "buffer_load_dwordx4 v27, s[52:55], s65 offen offset:0 sc1 lds\n\t"
    "s_waitcnt lgkmcnt(4)\n\t"
    "v_mfma_f32_16x16x32_bf16 v[80:83], a[212:215], a[132:135], v[80:83]\n\t"
    "v_exp_f32 v43, v171\n\t"
    "v_mfma_f32_16x16x32_bf16 v[84:87], a[212:215], a[148:151], v[84:87]\n\t"
    "ds_read_b128 a[236:239], v24 offset:11264\n\t"
    "v_cvt_pk_bf16_f32 v233, v42, v43\n\t"
    "v_mfma_f32_16x16x32_bf16 v[248:251], v[16:19], v[216:219], 0\n\t"
    "v_exp_f32 v44, v172\n\t"
    "v_mfma_f32_16x16x32_bf16 v[88:91], a[212:215], a[164:167], v[88:91]\n\t"
    "v_exp_f32 v45, v173\n\t"
    "v_mfma_f32_16x16x32_bf16 v[92:95], a[212:215], a[180:183], v[92:95]\n\t"
    "buffer_load_dwordx4 v27, s[52:55], s65 offen offset:1024 sc1 lds\n\t"
    "v_mfma_f32_16x16x32_bf16 v[80:83], a[216:219], a[136:139], v[80:83]\n\t"
    "ds_read_b128 a[240:243], v24 offset:12288\n\t"
    "v_cvt_pk_bf16_f32 v236, v44, v45\n\t"
    "v_exp_f32 v46, v174\n\t"
    "v_mfma_f32_16x16x32_bf16 v[84:87], a[216:219], a[152:155], v[84:87]\n\t"
    "v_exp_f32 v47, v175\n\t"
    "v_mfma_f32_16x16x32_bf16 v[88:91], a[216:219], a[168:171], v[88:91]\n\t"
    "ds_read_b128 a[244:247], v24 offset:13312\n\t"
    "v_cvt_pk_bf16_f32 v237, v46, v47\n\t"
    "v_mfma_f32_16x16x32_bf16 v[92:95], a[216:219], a[184:187], v[92:95]\n\t"
    "v_exp_f32 v40, v176\n\t"
    "s_waitcnt lgkmcnt(4)\n\t"
    "v_mfma_f32_16x16x32_bf16 v[80:83], a[220:223], a[140:143], v[80:83]\n\t"
    "buffer_load_dwordx4 v27, s[52:55], s65 offen offset:2048 sc1 lds\n\t"
    "v_mfma_f32_16x16x32_bf16 v[84:87], a[220:223], a[156:159], v[84:87]\n\t"
    "v_exp_f32 v41, v177\n\t"
    "v_mfma_f32_16x16x32_bf16 v[252:255], v[16:19], v[220:223], 0\n\t"
    "ds_read_b128 a[248:251], v24 offset:14336\n\t"
    "v_cvt_pk_bf16_f32 v226, v40, v41\n\t"
    "v_mfma_f32_16x16x32_bf16 v[88:91], a[220:223], a[172:175], v[88:91]\n\t"
    "v_exp_f32 v42, v178\n\t"
    "v_mfma_f32_16x16x32_bf16 v[92:95], a[220:223], a[188:191], v[92:95]\n\t"
    "v_exp_f32 v43, v179\n\t"
    "v_mfma_f32_16x16x32_bf16 v[96:99], a[224:227], a[128:131], v[48:51]\n\t"
    "buffer_load_dwordx4 v27, s[52:55], s65 offen offset:3072 sc1 lds\n\t"
    "v_mfma_f32_16x16x32_bf16 v[100:103], a[224:227], a[144:147], v[52:55]\n\t"
    "ds_read_b128 a[252:255], v24 offset:15360\n\t"
    "v_cvt_pk_bf16_f32 v227, v42, v43\n\t"
    "v_mfma_f32_16x16x32_bf16 v[104:107], a[224:227], a[160:163], v[56:59]\n\t"
    "v_exp_f32 v44, v180\n\t"
    "v_mfma_f32_16x16x32_bf16 v[108:111], a[224:227], a[176:179], v[60:63]\n\t"
    "v_exp_f32 v45, v181\n\t"
    "v_mfma_f32_16x16x32_bf16 v[96:99], a[228:231], a[132:135], v[96:99]\n\t"
    "ds_read_b128 a[192:195], v25 offset:0\n\t"
    "v_cvt_pk_bf16_f32 v230, v44, v45\n\t"
    "v_mfma_f32_16x16x32_bf16 v[100:103], a[228:231], a[148:151], v[100:103]\n\t"
    "v_exp_f32 v46, v182\n\t"
    "v_mfma_f32_16x16x32_bf16 v[104:107], a[228:231], a[164:167], v[104:107]\n\t"
    "v_exp_f32 v47, v183\n\t"
    "v_mfma_f32_16x16x32_bf16 v[108:111], a[228:231], a[180:183], v[108:111]\n\t"
    "ds_read_b128 a[196:199], v25 offset:1024\n\t"
    "v_cvt_pk_bf16_f32 v231, v46, v47\n\t"
    "s_waitcnt lgkmcnt(5)\n\t"
    "v_mfma_f32_16x16x32_bf16 v[96:99], a[232:235], a[136:139], v[96:99]\n\t"
    "v_exp_f32 v40, v184\n\t"
    "v_mfma_f32_16x16x32_bf16 v[100:103], a[232:235], a[152:155], v[100:103]\n\t"
    "v_exp_f32 v41, v185\n\t"
    "v_mfma_f32_16x16x32_bf16 v[104:107], a[232:235], a[168:171], v[104:107]\n\t"
    "ds_read_b128 a[200:203], v25 offset:2048\n\t"
    "v_cvt_pk_bf16_f32 v234, v40, v41\n\t"
    "v_mfma_f32_16x16x32_bf16 v[108:111], a[232:235], a[184:187], v[108:111]\n\t"
    "v_exp_f32 v42, v186\n\t"
    "v_mfma_f32_16x16x32_bf16 v[96:99], a[236:239], a[140:143], v[96:99]\n\t"
    "v_exp_f32 v43, v187\n\t"
    "v_mfma_f32_16x16x32_bf16 v[100:103], a[236:239], a[156:159], v[100:103]\n\t"
    "v_cvt_pk_bf16_f32 v235, v42, v43\n\t"
    "v_exp_f32 v44, v188\n\t"
    "v_mfma_f32_16x16x32_bf16 v[104:107], a[236:239], a[172:175], v[104:107]\n\t"
    "v_exp_f32 v45, v189\n\t"
    "v_mfma_f32_16x16x32_bf16 v[108:111], a[236:239], a[188:191], v[108:111]\n\t"
    "ds_read_b128 a[204:207], v25 offset:3072\n\t"
    "v_cvt_pk_bf16_f32 v238, v44, v45\n\t"
    "v_mfma_f32_16x16x32_bf16 v[112:115], a[240:243], a[128:131], v[48:51]\n\t"
    "v_exp_f32 v46, v190\n\t"
    "v_mfma_f32_16x16x32_bf16 v[116:119], a[240:243], a[144:147], v[52:55]\n\t"
    "v_exp_f32 v47, v191\n\t"
    "v_mfma_f32_16x16x32_bf16 v[120:123], a[240:243], a[160:163], v[56:59]\n\t"
    "v_cvt_pk_bf16_f32 v239, v46, v47\n\t"
    "v_mfma_f32_16x16x32_bf16 v[124:127], a[240:243], a[176:179], v[60:63]\n\t"
    "s_add_u32 s65, s65, 0x10000\n\t"
    "s_xor_b32 s66, s66, 0x4000\n\t"
    "s_waitcnt lgkmcnt(4)\n\t"
    "v_mfma_f32_16x16x32_bf16 v[112:115], a[244:247], a[132:135], v[112:115]\n\t"
    "ds_read_b128 a[208:211], v25 offset:4096\n\t"
    "v_mfma_f32_16x16x32_bf16 v[116:119], a[244:247], a[148:151], v[116:119]\n\t"
    "v_mfma_f32_16x16x32_bf16 v[120:123], a[244:247], a[164:167], v[120:123]\n\t"
    "v_xor_b32 v24, 0x4000, v24\n\t"
    "v_mfma_f32_16x16x32_bf16 v[124:127], a[244:247], a[180:183], v[124:127]\n\t"
    "v_mfma_f32_16x16x32_bf16 v[240:243], v[16:19], v[224:227], v[240:243]\n\t"
    "ds_read_b128 a[212:215], v25 offset:5120\n\t"
    "v_mfma_f32_16x16x32_bf16 v[112:115], a[248:251], a[136:139], v[112:115]\n\t"
    "v_mfma_f32_16x16x32_bf16 v[116:119], a[248:251], a[152:155], v[116:119]\n\t"
    "v_mfma_f32_16x16x32_bf16 v[244:247], v[16:19], v[228:231], v[244:247]\n\t"
    "v_mfma_f32_16x16x32_bf16 v[120:123], a[248:251], a[168:171], v[120:123]\n\t"
    "v_mfma_f32_16x16x32_bf16 v[124:127], a[248:251], a[184:187], v[124:127]\n\t"
    "v_mfma_f32_16x16x32_bf16 v[248:251], v[16:19], v[232:235], v[248:251]\n\t"
    "v_mfma_f32_16x16x32_bf16 v[112:115], a[252:255], a[140:143], v[112:115]\n\t"
    "v_mfma_f32_16x16x32_bf16 v[116:119], a[252:255], a[156:159], v[116:119]\n\t"
    "v_mfma_f32_16x16x32_bf16 v[252:255], v[16:19], v[236:239], v[252:255]\n\t"
    "v_mfma_f32_16x16x32_bf16 v[120:123], a[252:255], a[172:175], v[120:123]\n\t"
    "v_mfma_f32_16x16x32_bf16 v[124:127], a[252:255], a[188:191], v[124:127]\n\t"
    "s_branch bnd_0_%=\n\t"
    "; ---- last\n\t"
    "last_%=:\n\t"
    "s_waitcnt vmcnt(0)\n\t"
    "s_barrier\n\t"
    "s_mov_b32 s68, 0x40000\n\t"
    "bw_chk_%=:\n\t"
    "v_cmp_ne_u64 vcc, %10, v[10:11]\n\t"
    "s_nop 0\n\t"
    "s_cbranch_vccz bw_ok_%=\n\t"
    "s_sleep 2\n\t"
    "buffer_load_dwordx2 v[10:11], %16, s[40:43], 0 offen offset:0 sc1\n\t"
    "s_waitcnt vmcnt(0)\n\t"
    "s_sub_u32 s68, s68, 1\n\t"
    "s_cmp_lg_u32 s68, 0\n\t"
    "s_cbranch_scc1 bw_chk_%=\n\t"
    "bw_ok_%=:\n\t"
    "; ---- lastx\n\t"
    "lastx_%=:\n\t"
    "s_cmp_eq_u32 s82, 0\n\t"
    "s_cbranch_scc1 lastm_%=\n\t"
    "; ---- lastmask\n\t"
    "v_mov_b32 v20, 0xff800000\n\t"
    "v_cndmask_b32 v128, v20, v128, s[86:87]\n\t"
    "v_cndmask_b32 v129, v20, v129, s[88:89]\n\t"
    "v_cndmask_b32 v130, v20, v130, s[90:91]\n\t"
    "v_cndmask_b32 v131, v20, v131, s[92:93]\n\t"
    "v_mov_b32 v144, 0xff800000\n\t"
    "v_mov_b32 v145, 0xff800000\n\t"
    "v_mov_b32 v146, 0xff800000\n\t"
    "v_mov_b32 v147, 0xff800000\n\t"
    "v_mov_b32 v160, 0xff800000\n\t"
    "v_mov_b32 v161, 0xff800000\n\t"
    "v_mov_b32 v162, 0xff800000\n\t"
    "v_mov_b32 v163, 0xff800000\n\t"
    "v_mov_b32 v176, 0xff800000\n\t"
    "v_mov_b32 v177, 0xff800000\n\t"
    "v_mov_b32 v178, 0xff800000\n\t"
    "v_mov_b32 v179, 0xff800000\n\t"
    "v_cndmask_b32 v148, v20, v148, s[86:87]\n\t"
    "v_cndmask_b32 v149, v20, v149, s[88:89]\n\t"
    "v_cndmask_b32 v150, v20, v150, s[90:91]\n\t"
    "v_cndmask_b32 v151, v20, v151, s[92:93]\n\t"
    "v_mov_b32 v164, 0xff800000\n\t"
    "v_mov_b32 v165, 0xff800000\n\t"
    "v_mov_b32 v166, 0xff800000\n\t"
    "v_mov_b32 v167, 0xff800000\n\t"
    "v_mov_b32 v180, 0xff800000\n\t"
    "v_mov_b32 v181, 0xff800000\n\t"
    "v_mov_b32 v182, 0xff800000\n\t"
    "v_mov_b32 v183, 0xff800000\n\t"
    "v_cndmask_b32 v168, v20, v168, s[86:87]\n\t"
    "v_cndmask_b32 v169, v20, v169, s[88:89]\n\t"
    "v_cndmask_b32 v170, v20, v170, s[90:91]\n\t"
    "v_cndmask_b32 v171, v20, v171, s[92:93]\n\t"
    "v_mov_b32 v184, 0xff800000\n\t"
    "v_mov_b32 v185, 0xff800000\n\t"
    "v_mov_b32 v186, 0xff800000\n\t"
    "v_mov_b32 v187, 0xff800000\n\t"
    "v_cndmask_b32 v188, v20, v188, s[86:87]\n\t"
    "v_cndmask_b32 v189, v20, v189, s[88:89]\n\t"
    "v_cndmask_b32 v190, v20, v190, s[90:91]\n\t"
    "v_cndmask_b32 v191, v20, v191, s[92:93]\n\t"
    "; ---- lastm\n\t"
    "lastm_%=:\n\t"
    "v_max3_f32 v20, v240, v244, v248\n\t"
    "v_max_f32 v20, v20, v252\n\t"
    "v_cmp_nge_f32 vcc, 0x5d800000, v20\n\t"
    "s_nop 0\n\t"
    "s_cbranch_vccnz rare_bl_%=\n\t"
    "cont_bl_%=:\n\t"
    "; ---- bl\n\t"
    "s_waitcnt lgkmcnt(4)\n\t"
    "v_mfma_f32_16x16x32_bf16 a[0:3], a[192:195], v[192:195], a[0:3]\n\t"
    "v_add_f32 v32, v32, v240\n\t"
    "v_add_f32 v33, v33, v244\n\t"
    "v_add_f32 v34, v34, v248\n\t"
    "v_mfma_f32_16x16x32_bf16 a[4:7], a[192:195], v[196:199], a[4:7]\n\t"
    "ds_read_b128 a[216:219], v25 offset:6144\n\t"
    "v_add_f32 v35, v35, v252\n\t"
    "s_mov_b32 s84, 0\n\t"
    "v_mfma_f32_16x16x32_bf16 a[8:11], a[192:195], v[200:203], a[8:11]\n\t"
    "buffer_load_dwordx4 v[64:67], v6, s[72:75], s84 offen offset:0 sc1\n\t"
    "v_mfma_f32_16x16x32_bf16 a[12:15], a[192:195], v[204:207], a[12:15]\n\t"
    "ds_read_b128 a[128:131], v26 offset:0\n\t"
    "v_exp_f32 v40, v128\n\t"
    "v_exp_f32 v41, v129\n\t"
    "v_mfma_f32_16x16x32_bf16 a[16:19], a[196:199], v[192:195], a[16:19]\n\t"
    "v_cvt_pk_bf16_f32 v208, v40, v41\n\t"
    "v_exp_f32 v42, v130\n\t"
    "v_mfma_f32_16x16x32_bf16 a[20:23], a[196:199], v[196:199], a[20:23]\n\t"
    "ds_read_b128 a[220:223], v25 offset:7168\n\t"
    "v_exp_f32 v43, v131\n\t"
    "v_mfma_f32_16x16x32_bf16 a[24:27], a[196:199], v[200:203], a[24:27]\n\t"
    "buffer_load_dwordx4 v[96:99], v7, s[72:75], s84 offen offset:0 sc1\n\t"
    "v_mfma_f32_16x16x32_bf16 a[28:31], a[196:199], v[204:207], a[28:31]\n\t"
    "v_cvt_pk_bf16_f32 v209, v42, v43\n\t"
    "v_exp_f32 v44, v132\n\t"
    "v_exp_f32 v45, v133\n\t"
    "s_waitcnt lgkmcnt(5)\n\t"
    "v_mfma_f32_16x16x32_bf16 a[32:35], a[200:203], v[192:195], a[32:35]\n\t"
    "ds_read_b128 a[132:135], v26 offset:1024\n\t"
    "ds_read_b128 a[224:227], v25 offset:8192\n\t"
    "v_cvt_pk_bf16_f32 v212, v44, v45\n\t"
    "v_exp_f32 v46, v134\n\t"
    "v_mfma_f32_16x16x32_bf16 a[36:39], a[200:203], v[196:199], a[36:39]\n\t"
    "v_exp_f32 v47, v135\n\t"
    "v_mfma_f32_16x16x32_bf16 a[40:43], a[200:203], v[200:203], a[40:43]\n\t"
    "v_cvt_pk_bf16_f32 v213, v46, v47\n\t"
    "v_exp_f32 v40, v136\n\t"
    "v_mfma_f32_16x16x32_bf16 a[44:47], a[200:203], v[204:207], a[44:47]\n\t"
    "ds_read_b128 a[136:139], v26 offset:2048\n\t"
    "v_exp_f32 v41, v137\n\t"
    "v_mfma_f32_16x16x32_bf16 a[48:51], a[204:207], v[192:195], a[48:51]\n\t"
    "v_cvt_pk_bf16_f32 v216, v40, v41\n\t"
    "v_exp_f32 v42, v138\n\t"
    "s_mov_b32 s84, 0x1800\n\t"
    "v_mfma_f32_16x16x32_bf16 a[52:55], a[204:207], v[196:199], a[52:55]\n\t"
    "buffer_load_dwordx4 v[68:71], v6, s[72:75], s84 offen offset:0 sc1\n\t"
    "v_mfma_f32_16x16x32_bf16 a[56:59], a[204:207], v[200:203], a[56:59]\n\t"
    "buffer_load_dwordx4 v[100:103], v7, s[72:75], s84 offen offset:0 sc1\n\t"
    "v_mfma_f32_16x16x32_bf16 a[60:63], a[204:207], v[204:207], a[60:63]\n\t"
    "ds_read_b128 a[140:143], v26 offset:3072\n\t"
    "v_exp_f32 v43, v139\n\t"
    "v_exp_f32 v44, v140\n\t"
    "s_waitcnt lgkmcnt(7)\n\t"
    "v_mfma_f32_16x16x32_bf16 a[64:67], a[208:211], v[192:195], a[64:67]\n\t"
    "ds_read_b128 a[228:231], v25 offset:9216\n\t"
    "ds_read_b128 a[144:147], v26 offset:4096\n\t"
    "v_cvt_pk_bf16_f32 v217, v42, v43\n\t"
    "v_exp_f32 v45, v141\n\t"
    "v_mfma_f32_16x16x32_bf16 a[68:71], a[208:211], v[196:199], a[68:71]\n\t"
    "ds_read_b128 a[232:235], v25 offset:10240\n\t"
    "v_cvt_pk_bf16_f32 v220, v44, v45\n\t"
    "v_exp_f32 v46, v142\n\t"
    "v_mfma_f32_16x16x32_bf16 a[72:75], a[208:211], v[200:203], a[72:75]\n\t"
    "v_exp_f32 v47, v143\n\t"
    "v_exp_f32 v40, v144\n\t"
    "v_mfma_f32_16x16x32_bf16 a[76:79], a[208:211], v[204:207], a[76:79]\n\t"
    "ds_read_b128 a[148:151], v26 offset:5120\n\t"
    "v_cvt_pk_bf16_f32 v221, v46, v47\n\t"
    "v_exp_f32 v41, v145\n\t"
    "v_mfma_f32_16x16x32_bf16 a[80:83], a[212:215], v[192:195], a[80:83]\n\t"
    "v_cvt_pk_bf16_f32 v210, v40, v41\n\t"
    "s_mov_b32 s84, 0x3000\n\t"
    "v_exp_f32 v42, v146\n\t"
    "v_mfma_f32_16x16x32_bf16 a[84:87], a[212:215], v[196:199], a[84:87]\n\t"
    "buffer_load_dwordx4 v[72:75], v6, s[72:75], s84 offen offset:0 sc1\n\t"
    "v_mfma_f32_16x16x32_bf16 a[88:91], a[212:215], v[200:203], a[88:91]\n\t"
    "buffer_load_dwordx4 v[104:107], v7, s[72:75], s84 offen offset:0 sc1\n\t"
    "v_mfma_f32_16x16x32_bf16 a[92:95], a[212:215], v[204:207], a[92:95]\n\t"
    "ds_read_b128 a[236:239], v25 offset:11264\n\t"
    "v_exp_f32 v43, v147\n\t"
    "v_exp_f32 v44, v148\n\t"
    "s_waitcnt lgkmcnt(9)\n\t"
    "v_mfma_f32_16x16x32_bf16 a[96:99], a[216:219], v[192:195], a[96:99]\n\t"
    "ds_read_b128 a[152:155], v26 offset:6144\n\t"
    "v_cvt_pk_bf16_f32 v211, v42, v43\n\t"
    "v_exp_f32 v45, v149\n\t"
    "v_mfma_f32_16x16x32_bf16 a[100:103], a[216:219], v[196:199], a[100:103]\n\t"
    "v_exp_f32 v46, v150\n\t"
    "v_cvt_pk_bf16_f32 v214, v44, v45\n\t"
    "v_exp_f32 v47, v151\n\t"
    "v_mfma_f32_16x16x32_bf16 a[104:107], a[216:219], v[200:203], a[104:107]\n\t"
    "ds_read_b128 a[156:159], v26 offset:7168\n\t"
    "v_cvt_pk_bf16_f32 v215, v46, v47\n\t"
    "v_exp_f32 v40, v152\n\t"
    "v_mfma_f32_16x16x32_bf16 a[108:111], a[216:219], v[204:207], a[108:111]\n\t"
    "ds_read_b128 a[240:243], v25 offset:12288\n\t"
    "v_exp_f32 v41, v153\n\t"
    "v_exp_f32 v42, v154\n\t"
    "v_mfma_f32_16x16x32_bf16 a[112:115], a[220:223], v[192:195], a[112:115]\n\t"
    "v_cvt_pk_bf16_f32 v218, v40, v41\n\t"
    "s_mov_b32 s84, 0x4800\n\t"
    "v_exp_f32 v43, v155\n\t"
    "v_mfma_f32_16x16x32_bf16 a[116:119], a[220:223], v[196:199], a[116:119]\n\t"
    "buffer_load_dwordx4 v[76:79], v6, s[72:75], s84 offen offset:0 sc1\n\t"
    "v_mfma_f32_16x16x32_bf16 a[120:123], a[220:223], v[200:203], a[120:123]\n\t"
    "buffer_load_dwordx4 v[108:111], v7, s[72:75], s84 offen offset:0 sc1\n\t"
    "v_mfma_f32_16x16x32_bf16 a[124:127], a[220:223], v[204:207], a[124:127]\n\t"
    "ds_read_b128 a[160:163], v26 offset:8192\n\t"
    "v_cvt_pk_bf16_f32 v219, v42, v43\n\t"
    "v_exp_f32 v44, v156\n\t"
    "s_waitcnt lgkmcnt(8)\n\t"
    "v_mfma_f32_16x16x32_bf16 a[0:3], a[224:227], v[224:227], a[0:3]\n\t"
    "ds_read_b128 a[244:247], v25 offset:13312\n\t"
    "v_exp_f32 v45, v157\n\t"
    "v_exp_f32 v46, v158\n\t"
    "v_mfma_f32_16x16x32_bf16 a[4:7], a[224:227], v[228:231], a[4:7]\n\t"
    "ds_read_b128 a[164:167], v26 offset:9216\n\t"
    "v_cvt_pk_bf16_f32 v222, v44, v45\n\t"
    "v_exp_f32 v47, v159\n\t"
    "v_mfma_f32_16x16x32_bf16 a[8:11], a[224:227], v[232:235], a[8:11]\n\t"
    "ds_read_b128 a[248:251], v25 offset:14336\n\t"
    "v_cvt_pk_bf16_f32 v223, v46, v47\n\t"
    "v_exp_f32 v40, v160\n\t"
    "v_mfma_f32_16x16x32_bf16 a[12:15], a[224:227], v[236:239], a[12:15]\n\t"
    "ds_read_b128 a[168:171], v26 offset:10240\n\t"
    "v_exp_f32 v41, v161\n\t"
    "v_exp_f32 v42, v162\n\t"
    "v_mfma_f32_16x16x32_bf16 a[16:19], a[228:231], v[224:227], a[16:19]\n\t"
    "v_cvt_pk_bf16_f32 v192, v40, v41\n\t"
    "s_mov_b32 s84, 0x6000\n\t"
    "v_exp_f32 v43, v163\n\t"
    "v_mfma_f32_16x16x32_bf16 a[20:23], a[228:231], v[228:231], a[20:23]\n\t"
    "buffer_load_dwordx4 v[80:83], v6, s[72:75], s84 offen offset:0 sc1\n\t"
    "v_mfma_f32_16x16x32_bf16 v[240:243], v[16:19], v[208:211], 0\n\t"
    "buffer_load_dwordx4 v[112:115], v7, s[72:75], s84 offen offset:0 sc1\n\t"
    "v_mfma_f32_16x16x32_bf16 a[24:27], a[228:231], v[232:235], a[24:27]\n\t"
    "ds_read_b128 a[172:175], v26 offset:11264\n\t"
    "v_cvt_pk_bf16_f32 v193, v42, v43\n\t"
    "v_exp_f32 v44, v164\n\t"
    "v_mfma_f32_16x16x32_bf16 a[28:31], a[228:231], v[236:239], a[28:31]\n\t"
    "ds_read_b128 a[252:255], v25 offset:15360\n\t"
    "v_exp_f32 v45, v165\n\t"
    "v_exp_f32 v46, v166\n\t"
    "v_mfma_f32_16x16x32_bf16 v[244:247], v[16:19], v[212:215], 0\n\t"
    "ds_read_b128 a[176:179], v26 offset:12288\n\t"
    "v_cvt_pk_bf16_f32 v196, v44, v45\n\t"
    "v_exp_f32 v47, v167\n\t"
    "s_waitcnt lgkmcnt(11)\n\t"
    "v_mfma_f32_16x16x32_bf16 a[32:35], a[232:235], v[224:227], a[32:35]\n\t"
    "ds_read_b128 a[180:183], v26 offset:13312\n\t"
    "v_cvt_pk_bf16_f32 v197, v46, v47\n\t"
    "v_exp_f32 v40, v168\n\t"
    "v_mfma_f32_16x16x32_bf16 a[36:39], a[232:235], v[228:231], a[36:39]\n\t"
    "v_exp_f32 v41, v169\n\t"
    "v_exp_f32 v42, v170\n\t"
    "v_mfma_f32_16x16x32_bf16 v[248:251], v[16:19], v[216:219], 0\n\t"
    "v_cvt_pk_bf16_f32 v200, v40, v41\n\t"
    "s_mov_b32 s84, 0x7800\n\t"
    "v_exp_f32 v43, v171\n\t"
    "v_mfma_f32_16x16x32_bf16 a[40:43], a[232:235], v[232:235], a[40:43]\n\t"
    "buffer_load_dwordx4 v[84:87], v6, s[72:75], s84 offen offset:0 sc1\n\t"
    "v_mfma_f32_16x16x32_bf16 a[44:47], a[232:235], v[236:239], a[44:47]\n\t"
    "buffer_load_dwordx4 v[116:119], v7, s[72:75], s84 offen offset:0 sc1\n\t"
    "v_mfma_f32_16x16x32_bf16 v[252:255], v[16:19], v[220:223], 0\n\t"
    "v_cvt_pk_bf16_f32 v201, v42, v43\n\t"
    "v_xor_b32 v25, 0x4000, v25\n\t"
    "v_exp_f32 v44, v172\n\t"
    "v_mfma_f32_16x16x32_bf16 a[48:51], a[236:239], v[224:227], a[48:51]\n\t"
    "ds_read_b128 a[184:187], v26 offset:14336\n\t"
    "v_exp_f32 v45, v173\n\t"
    "v_exp_f32 v46, v174\n\t"
    "v_mfma_f32_16x16x32_bf16 a[52:55], a[236:239], v[228:231], a[52:55]\n\t"
    "ds_read_b128 a[188:191], v26 offset:15360\n\t"
    "v_cvt_pk_bf16_f32 v204, v44, v45\n\t"
    "v_exp_f32 v47, v175\n\t"
    "v_mfma_f32_16x16x32_bf16 a[56:59], a[236:239], v[232:235], a[56:59]\n\t"
    "v_cvt_pk_bf16_f32 v205, v46, v47\n\t"
    "s_mov_b32 s84, 0x9000\n\t"
    "v_exp_f32 v40, v176\n\t"
    "v_mfma_f32_16x16x32_bf16 a[60:63], a[236:239], v[236:239], a[60:63]\n\t"
    "buffer_load_dwordx4 v[88:91], v6, s[72:75], s84 offen offset:0 sc1\n\t"
    "s_waitcnt lgkmcnt(9)\n\t"
    "v_mfma_f32_16x16x32_bf16 a[64:67], a[240:243], v[224:227], a[64:67]\n\t"
    "v_exp_f32 v41, v177\n\t"
    "v_exp_f32 v42, v178\n\t"
    "v_mfma_f32_16x16x32_bf16 a[68:71], a[240:243], v[228:231], a[68:71]\n\t"
    "ds_read_b128 a[192:195], v25 offset:4096\n\t"
    "v_cvt_pk_bf16_f32 v194, v40, v41\n\t"
    "v_exp_f32 v43, v179\n\t"
    "v_mfma_f32_16x16x32_bf16 a[72:75], a[240:243], v[232:235], a[72:75]\n\t"
    "buffer_load_dwordx4 v[120:123], v7, s[72:75], s84 offen offset:0 sc1\n\t"
    "v_mfma_f32_16x16x32_bf16 a[76:79], a[240:243], v[236:239], a[76:79]\n\t"
    "ds_read_b128 a[196:199], v25 offset:12288\n\t"
    "v_cvt_pk_bf16_f32 v195, v42, v43\n\t"
    "v_exp_f32 v44, v180\n\t"
    "v_mfma_f32_16x16x32_bf16 a[80:83], a[244:247], v[224:227], a[80:83]\n\t"
    "v_exp_f32 v45, v181\n\t"
    "v_exp_f32 v46, v182\n\t"
    "v_mfma_f32_16x16x32_bf16 a[84:87], a[244:247], v[228:231], a[84:87]\n\t"
    "v_cvt_pk_bf16_f32 v198, v44, v45\n\t"
    "s_mov_b32 s84, 0xa800\n\t"
    "v_exp_f32 v47, v183\n\t"
    "v_mfma_f32_16x16x32_bf16 a[88:91], a[244:247], v[232:235], a[88:91]\n\t"
    "buffer_load_dwordx4 v[92:95], v6, s[72:75], s84 offen offset:0 sc1\n\t"
    "v_mfma_f32_16x16x32_bf16 a[92:95], a[244:247], v[236:239], a[92:95]\n\t"
    "ds_read_b128 a[200:203], v25 offset:5120\n\t"
    "v_cvt_pk_bf16_f32 v199, v46, v47\n\t"
    "v_exp_f32 v40, v184\n\t"
    "s_waitcnt lgkmcnt(7)\n\t"
    "v_mfma_f32_16x16x32_bf16 a[96:99], a[248:251], v[224:227], a[96:99]\n\t"
    "v_exp_f32 v41, v185\n\t"
    "v_exp_f32 v42, v186\n\t"
    "v_mfma_f32_16x16x32_bf16 a[100:103], a[248:251], v[228:231], a[100:103]\n\t"
    "ds_read_b128 a[204:207], v25 offset:13312\n\t"
    "v_cvt_pk_bf16_f32 v202, v40, v41\n\t"
    "v_exp_f32 v43, v187\n\t"
    "v_mfma_f32_16x16x32_bf16 a[104:107], a[248:251], v[232:235], a[104:107]\n\t"
    "buffer_load_dwordx4 v[124:127], v7, s[72:75], s84 offen offset:0 sc1\n\t"
    "v_mfma_f32_16x16x32_bf16 a[108:111], a[248:251], v[236:239], a[108:111]\n\t"
    "ds_read_b128 a[208:211], v25 offset:6144\n\t"
    "v_cvt_pk_bf16_f32 v203, v42, v43\n\t"
    "v_exp_f32 v44, v188\n\t"
    "v_mfma_f32_16x16x32_bf16 a[112:115], a[252:255], v[224:227], a[112:115]\n\t"
    "v_exp_f32 v45, v189\n\t"
    "v_exp_f32 v46, v190\n\t"
    "v_mfma_f32_16x16x32_bf16 v[240:243], v[16:19], v[192:195], v[240:243]\n\t"
    "ds_read_b128 a[212:215], v25 offset:14336\n\t"
    "v_cvt_pk_bf16_f32 v206, v44, v45\n\t"
    "v_exp_f32 v47, v191\n\t"
    "v_mfma_f32_16x16x32_bf16 a[116:119], a[252:255], v[228:231], a[116:119]\n\t"
    "v_cvt_pk_bf16_f32 v207, v46, v47\n\t"
    "v_mfma_f32_16x16x32_bf16 v[244:247], v[16:19], v[196:199], v[244:247]\n\t"
    "v_mfma_f32_16x16x32_bf16 a[120:123], a[252:255], v[232:235], a[120:123]\n\t"
    "v_mfma_f32_16x16x32_bf16 v[248:251], v[16:19], v[200:203], v[248:251]\n\t"
    "v_mfma_f32_16x16x32_bf16 a[124:127], a[252:255], v[236:239], a[124:127]\n\t"
    "v_mfma_f32_16x16x32_bf16 v[252:255], v[16:19], v[204:207], v[252:255]\n\t"
    "s_branch pvl_%=\n\t"
    "; ---- short\n\t"
    "short_%=:\n\t"
    "s_mov_b32 m0, s66\n\t"
    "s_nop 0\n\t"
    "buffer_load_dwordx4 %6, s[52:55], s65 offen offset:0 sc1 lds\n\t"
    "buffer_load_dwordx4 %6, s[52:55], s65 offen offset:1024 sc1 lds\n\t"
    "buffer_load_dwordx4 %6, s[52:55], s65 offen offset:2048 sc1 lds\n\t"
    "buffer_load_dwordx4 %6, s[52:55], s65 offen offset:3072 sc1 lds\n\t"
    "s_xor_b32 m0, s66, 0x1000\n\t"
    "s_nop 0\n\t"
    "buffer_load_dwordx4 v27, s[52:55], s65 offen offset:0 sc1 lds\n\t"
    "buffer_load_dwordx4 v27, s[52:55], s65 offen offset:1024 sc1 lds\n\t"
    "buffer_load_dwordx4 v27, s[52:55], s65 offen offset:2048 sc1 lds\n\t"
    "buffer_load_dwordx4 v27, s[52:55], s65 offen offset:3072 sc1 lds\n\t"
    "s_add_u32 s65, s65, 0x10000\n\t"
    "s_xor_b32 s66, s66, 0x4000\n\t"
    "v_accvgpr_write_b32 a0, 0\n\t"
    "v_accvgpr_write_b32 a1, 0\n\t"
    "v_accvgpr_write_b32 a2, 0\n\t"
    "v_accvgpr_write_b32 a3, 0\n\t"
    "v_accvgpr_write_b32 a4, 0\n\t"
    "v_accvgpr_write_b32 a5, 0\n\t"
    "v_accvgpr_write_b32 a6, 0\n\t"
    "v_accvgpr_write_b32 a7, 0\n\t"
    "v_accvgpr_write_b32 a8, 0\n\t"
    "v_accvgpr_write_b32 a9, 0\n\t"
    "v_accvgpr_write_b32 a10, 0\n\t"
    "v_accvgpr_write_b32 a11, 0\n\t"
    "v_accvgpr_write_b32 a12, 0\n\t"
    "v_accvgpr_write_b32 a13, 0\n\t"
    "v_accvgpr_write_b32 a14, 0\n\t"
    "v_accvgpr_write_b32 a15, 0\n\t"
    "v_accvgpr_write_b32 a16, 0\n\t"
    "v_accvgpr_write_b32 a17, 0\n\t"
    "v_accvgpr_write_b32 a18, 0\n\t"
    "v_accvgpr_write_b32 a19, 0\n\t"
    "v_accvgpr_write_b32 a20, 0\n\t"
    "v_accvgpr_write_b32 a21, 0\n\t"
    "v_accvgpr_write_b32 a22, 0\n\t"
    "v_accvgpr_write_b32 a23, 0\n\t"
    "v_accvgpr_write_b32 a24, 0\n\t"
    "v_accvgpr_write_b32 a25, 0\n\t"
    "v_accvgpr_write_b32 a26, 0\n\t"
    "v_accvgpr_write_b32 a27, 0\n\t"
    "v_accvgpr_write_b32 a28, 0\n\t"
    "v_accvgpr_write_b32 a29, 0\n\t"
    "v_accvgpr_write_b32 a30, 0\n\t"
    "v_accvgpr_write_b32 a31, 0\n\t"
    "v_accvgpr_write_b32 a32, 0\n\t"
    "v_accvgpr_write_b32 a33, 0\n\t"
    "v_accvgpr_write_b32 a34, 0\n\t"
    "v_accvgpr_write_b32 a35, 0\n\t"
    "v_accvgpr_write_b32 a36, 0\n\t"
    "v_accvgpr_write_b32 a37, 0\n\t"
    "v_accvgpr_write_b32 a38, 0\n\t"
    "v_accvgpr_write_b32 a39, 0\n\t"
    "v_accvgpr_write_b32 a40, 0\n\t"
    "v_accvgpr_write_b32 a41, 0\n\t"
    "v_accvgpr_write_b32 a42, 0\n\t"
    "v_accvgpr_write_b32 a43, 0\n\t"
    "v_accvgpr_write_b32 a44, 0\n\t"
    "v_accvgpr_write_b32 a45, 0\n\t"
    "v_accvgpr_write_b32 a46, 0\n\t"
    "v_accvgpr_write_b32 a47, 0\n\t"
    "v_accvgpr_write_b32 a48, 0\n\t"
    "v_accvgpr_write_b32 a49, 0\n\t"
    "v_accvgpr_write_b32 a50, 0\n\t"
    "v_accvgpr_write_b32 a51, 0\n\t"
    "v_accvgpr_write_b32 a52, 0\n\t"
    "v_accvgpr_write_b32 a53, 0\n\t"
    "v_accvgpr_write_b32 a54, 0\n\t"
    "v_accvgpr_write_b32 a55, 0\n\t"
    "v_accvgpr_write_b32 a56, 0\n\t"
    "v_accvgpr_write_b32 a57, 0\n\t"
    "v_accvgpr_write_b32 a58, 0\n\t"
    "v_accvgpr_write_b32 a59, 0\n\t"
    "v_accvgpr_write_b32 a60, 0\n\t"
    "v_accvgpr_write_b32 a61, 0\n\t"
    "v_accvgpr_write_b32 a62, 0\n\t"
    "v_accvgpr_write_b32 a63, 0\n\t"
    "s_cmp_eq_u32 s67, 0\n\t"
    "s_cbranch_scc1 empty_%=\n\t"
    "; ---- one\n\t"
    "s_waitcnt lgkmcnt(2)\n\t"
    "v_mfma_f32_16x16x32_bf16 v[128:131], a[192:195], a[128:131], 0\n\t"
    "ds_read_b128 a[216:219], v24 offset:6144\n\t"
    "ds_read_b128 a[220:223], v24 offset:7168\n\t"
    "v_mfma_f32_16x16x32_bf16 v[132:135], a[192:195], a[144:147], 0\n\t"
    "v_mfma_f32_16x16x32_bf16 v[136:139], a[192:195], a[160:163], 0\n\t"
    "ds_read_b128 a[224:227], v24 offset:8192\n\t"
    "v_mfma_f32_16x16x32_bf16 v[140:143], a[192:195], a[176:179], 0\n\t"
    "v_mfma_f32_16x16x32_bf16 v[128:131], a[196:199], a[132:135], v[128:131]\n\t"
    "v_mfma_f32_16x16x32_bf16 v[132:135], a[196:199], a[148:151], v[132:135]\n\t"
    "v_mfma_f32_16x16x32_bf16 v[136:139], a[196:199], a[164:167], v[136:139]\n\t"
    "ds_read_b128 a[228:231], v24 offset:9216\n\t"
    "v_mfma_f32_16x16x32_bf16 v[140:143], a[196:199], a[180:183], v[140:143]\n\t"
    "v_mfma_f32_16x16x32_bf16 v[128:131], a[200:203], a[136:139], v[128:131]\n\t"
    "v_mfma_f32_16x16x32_bf16 v[132:135], a[200:203], a[152:155], v[132:135]\n\t"
    "v_mfma_f32_16x16x32_bf16 v[136:139], a[200:203], a[168:171], v[136:139]\n\t"
    "ds_read_b128 a[232:235], v24 offset:10240\n\t"
    "v_mfma_f32_16x16x32_bf16 v[140:143], a[200:203], a[184:187], v[140:143]\n\t"
    "v_mfma_f32_16x16x32_bf16 v[128:131], a[204:207], a[140:143], v[128:131]\n\t"
    "v_mfma_f32_16x16x32_bf16 v[132:135], a[204:207], a[156:159], v[132:135]\n\t"
    "v_mfma_f32_16x16x32_bf16 v[136:139], a[204:207], a[172:175], v[136:139]\n\t"
    "ds_read_b128 a[236:239], v24 offset:11264\n\t"
    "v_mfma_f32_16x16x32_bf16 v[140:143], a[204:207], a[188:191], v[140:143]\n\t"
    "s_waitcnt lgkmcnt(6)\n\t"
    "v_mfma_f32_16x16x32_bf16 v[144:147], a[208:211], a[128:131], 0\n\t"
    "v_mfma_f32_16x16x32_bf16 v[148:151], a[208:211], a[144:147], 0\n\t"
    "v_mfma_f32_16x16x32_bf16 v[152:155], a[208:211], a[160:163], 0\n\t"
    "ds_read_b128 a[240:243], v24 offset:12288\n\t"
    "v_mfma_f32_16x16x32_bf16 v[156:159], a[208:211], a[176:179], 0\n\t"
    "v_mfma_f32_16x16x32_bf16 v[144:147], a[212:215], a[132:135], v[144:147]\n\t"
    "v_mfma_f32_16x16x32_bf16 v[148:151], a[212:215], a[148:151], v[148:151]\n\t"
    "v_mfma_f32_16x16x32_bf16 v[152:155], a[212:215], a[164:167], v[152:155]\n\t"
    "ds_read_b128 a[244:247], v24 offset:13312\n\t"
    "v_mfma_f32_16x16x32_bf16 v[156:159], a[212:215], a[180:183], v[156:159]\n\t"
    "s_waitcnt lgkmcnt(4)\n\t"
    "v_mfma_f32_16x16x32_bf16 v[144:147], a[216:219], a[136:139], v[144:147]\n\t"
    "v_mfma_f32_16x16x32_bf16 v[148:151], a[216:219], a[152:155], v[148:151]\n\t"
    "v_mfma_f32_16x16x32_bf16 v[152:155], a[216:219], a[168:171], v[152:155]\n\t"
    "ds_read_b128 a[248:251], v24 offset:14336\n\t"
    "v_mfma_f32_16x16x32_bf16 v[156:159], a[216:219], a[184:187], v[156:159]\n\t"
    "v_mfma_f32_16x16x32_bf16 v[144:147], a[220:223], a[140:143], v[144:147]\n\t"
    "v_mfma_f32_16x16x32_bf16 v[148:151], a[220:223], a[156:159], v[148:151]\n\t"
    "v_mfma_f32_16x16x32_bf16 v[152:155], a[220:223], a[172:175], v[152:155]\n\t"
    "ds_read_b128 a[252:255], v24 offset:15360\n\t"
    "v_mfma_f32_16x16x32_bf16 v[156:159], a[220:223], a[188:191], v[156:159]\n\t"
    "v_mfma_f32_16x16x32_bf16 v[160:163], a[224:227], a[128:131], 0\n\t"
    "v_mfma_f32_16x16x32_bf16 v[164:167], a[224:227], a[144:147], 0\n\t"
    "v_mfma_f32_16x16x32_bf16 v[168:171], a[224:227], a[160:163], 0\n\t"
    "v_mfma_f32_16x16x32_bf16 v[172:175], a[224:227], a[176:179], 0\n\t"
    "v_mfma_f32_16x16x32_bf16 v[160:163], a[228:231], a[132:135], v[160:163]\n\t"
    "v_mfma_f32_16x16x32_bf16 v[164:167], a[228:231], a[148:151], v[164:167]\n\t"
    "v_mfma_f32_16x16x32_bf16 v[168:171], a[228:231], a[164:167], v[168:171]\n\t"
    "v_mfma_f32_16x16x32_bf16 v[172:175], a[228:231], a[180:183], v[172:175]\n\t"
    "s_waitcnt lgkmcnt(3)\n\t"
    "v_mfma_f32_16x16x32_bf16 v[160:163], a[232:235], a[136:139], v[160:163]\n\t"
    "v_mfma_f32_16x16x32_bf16 v[164:167], a[232:235], a[152:155], v[164:167]\n\t"
    "v_mfma_f32_16x16x32_bf16 v[168:171], a[232:235], a[168:171], v[168:171]\n\t"
    "v_mfma_f32_16x16x32_bf16 v[172:175], a[232:235], a[184:187], v[172:175]\n\t"
    "v_mfma_f32_16x16x32_bf16 v[160:163], a[236:239], a[140:143], v[160:163]\n\t"
    "v_mfma_f32_16x16x32_bf16 v[164:167], a[236:239], a[156:159], v[164:167]\n\t"
    "v_mfma_f32_16x16x32_bf16 v[168:171], a[236:239], a[172:175], v[168:171]\n\t"
    "v_mfma_f32_16x16x32_bf16 v[172:175], a[236:239], a[188:191], v[172:175]\n\t"
    "v_mfma_f32_16x16x32_bf16 v[176:179], a[240:243], a[128:131], 0\n\t"
    "v_mfma_f32_16x16x32_bf16 v[180:183], a[240:243], a[144:147], 0\n\t"
    "v_mfma_f32_16x16x32_bf16 v[184:187], a[240:243], a[160:163], 0\n\t"
    "v_mfma_f32_16x16x32_bf16 v[188:191], a[240:243], a[176:179], 0\n\t"
    "s_waitcnt lgkmcnt(0)\n\t"
    "v_mfma_f32_16x16x32_bf16 v[176:179], a[244:247], a[132:135], v[176:179]\n\t"
    "v_mfma_f32_16x16x32_bf16 v[180:183], a[244:247], a[148:151], v[180:183]\n\t"
    "v_mfma_f32_16x16x32_bf16 v[184:187], a[244:247], a[164:167], v[184:187]\n\t"
    "v_mfma_f32_16x16x32_bf16 v[188:191], a[244:247], a[180:183], v[188:191]\n\t"
    "v_mfma_f32_16x16x32_bf16 v[176:179], a[248:251], a[136:139], v[176:179]\n\t"
    "v_mfma_f32_16x16x32_bf16 v[180:183], a[248:251], a[152:155], v[180:183]\n\t"
    "v_mfma_f32_16x16x32_bf16 v[184:187], a[248:251], a[168:171], v[184:187]\n\t"
    "v_mfma_f32_16x16x32_bf16 v[188:191], a[248:251], a[184:187], v[188:191]\n\t"
    "v_mfma_f32_16x16x32_bf16 v[176:179], a[252:255], a[140:143], v[176:179]\n\t"
    "v_mfma_f32_16x16x32_bf16 v[180:183], a[252:255], a[156:159], v[180:183]\n\t"
    "v_mfma_f32_16x16x32_bf16 v[184:187], a[252:255], a[172:175], v[184:187]\n\t"
    "v_mfma_f32_16x16x32_bf16 v[188:191], a[252:255], a[188:191], v[188:191]\n\t"
    "v_xor_b32 v24, 0x4000, v24\n\t"
    "s_waitcnt vmcnt(0) lgkmcnt(0)\n\t"
    "s_barrier\n\t"
    "s_cmp_eq_u32 s82, 0\n\t"
    "s_cbranch_scc1 one2_%=\n\t"
    "; ---- onemask\n\t"
    "v_mov_b32 v20, 0xff800000\n\t"
    "v_cndmask_b32 v128, v20, v128, s[86:87]\n\t"
    "v_cndmask_b32 v129, v20, v129, s[88:89]\n\t"
    "v_cndmask_b32 v130, v20, v130, s[90:91]\n\t"
    "v_cndmask_b32 v131, v20, v131, s[92:93]\n\t"
    "v_mov_b32 v144, 0xff800000\n\t"
    "v_mov_b32 v145, 0xff800000\n\t"
    "v_mov_b32 v146, 0xff800000\n\t"
    "v_mov_b32 v147, 0xff800000\n\t"
    "v_mov_b32 v160, 0xff800000\n\t"
    "v_mov_b32 v161, 0xff800000\n\t"
    "v_mov_b32 v162, 0xff800000\n\t"
    "v_mov_b32 v163, 0xff800000\n\t"
    "v_mov_b32 v176, 0xff800000\n\t"
    "v_mov_b32 v177, 0xff800000\n\t"
    "v_mov_b32 v178, 0xff800000\n\t"
    "v_mov_b32 v179, 0xff800000\n\t"
    "v_cndmask_b32 v148, v20, v148, s[86:87]\n\t"
    "v_cndmask_b32 v149, v20, v149, s[88:89]\n\t"
    "v_cndmask_b32 v150, v20, v150, s[90:91]\n\t"
    "v_cndmask_b32 v151, v20, v151, s[92:93]\n\t"
    "v_mov_b32 v164, 0xff800000\n\t"
    "v_mov_b32 v165, 0xff800000\n\t"
    "v_mov_b32 v166, 0xff800000\n\t"
    "v_mov_b32 v167, 0xff800000\n\t"
    "v_mov_b32 v180, 0xff800000\n\t"
    "v_mov_b32 v181, 0xff800000\n\t"
    "v_mov_b32 v182, 0xff800000\n\t"
    "v_mov_b32 v183, 0xff800000\n\t"
    "v_cndmask_b32 v168, v20, v168, s[86:87]\n\t"
    "v_cndmask_b32 v169, v20, v169, s[88:89]\n\t"
    "v_cndmask_b32 v170, v20, v170, s[90:91]\n\t"
    "v_cndmask_b32 v171, v20, v171, s[92:93]\n\t"
    "v_mov_b32 v184, 0xff800000\n\t"
    "v_mov_b32 v185, 0xff800000\n\t"
    "v_mov_b32 v186, 0xff800000\n\t"
    "v_mov_b32 v187, 0xff800000\n\t"
    "v_cndmask_b32 v188, v20, v188, s[86:87]\n\t"
    "v_cndmask_b32 v189, v20, v189, s[88:89]\n\t"
    "v_cndmask_b32 v190, v20, v190, s[90:91]\n\t"
    "v_cndmask_b32 v191, v20, v191, s[92:93]\n\t"
    "; ---- one2\n\t"
    "one2_%=:\n\t"
    "s_cmp_eq_u32 s83, 0\n\t"
    "s_cbranch_scc0 one_skip_%=\n\t"
    "; ---- one_v\n\t"
    "ds_read_b128 a[192:195], v25 offset:4096\n\t"
    "ds_read_b128 a[196:199], v25 offset:12288\n\t"
    "ds_read_b128 a[200:203], v25 offset:5120\n\t"
    "ds_read_b128 a[204:207], v25 offset:13312\n\t"
    "ds_read_b128 a[208:211], v25 offset:6144\n\t"
    "ds_read_b128 a[212:215], v25 offset:14336\n\t"
    "s_branch one3_%=\n\t"
    "; ---- one_skip\n\t"
    "one_skip_%=:\n\t"
    "s_mov_b32 m0, s64\n\t"
    "s_nop 0\n\t"
    "buffer_load_dwordx4 %6, s[48:51], s70 offen offset:0 sc1 lds\n\t"
    "buffer_load_dwordx4 %6, s[48:51], s70 offen offset:1024 sc1 lds\n\t"
    "buffer_load_dwordx4 %6, s[48:51], s70 offen offset:2048 sc1 lds\n\t"
    "buffer_load_dwordx4 %6, s[48:51], s70 offen offset:3072 sc1 lds\n\t"
    "s_add_u32 m0, s64, 0x1000\n\t"
    "s_nop 0\n\t"
    "buffer_load_dwordx4 v27, s[48:51], s70 offen offset:0 sc1 lds\n\t"
    "buffer_load_dwordx4 v27, s[48:51], s70 offen offset:1024 sc1 lds\n\t"
    "buffer_load_dwordx4 v27, s[48:51], s70 offen offset:2048 sc1 lds\n\t"
    "buffer_load_dwordx4 v27, s[48:51], s70 offen offset:3072 sc1 lds\n\t"
    "s_add_u32 s70, s70, 0x10000\n\t"
    "s_xor_b32 s64, s64, 0x4000\n\t"
    "s_cmp_eq_u32 s70, s71\n\t"
    "s_cselect_b32 s70, 0, s70\n\t"
    "s_mov_b32 m0, s66\n\t"
    "s_nop 0\n\t"
    "buffer_load_dwordx4 %6, s[52:55], s65 offen offset:0 sc1 lds\n\t"
    "buffer_load_dwordx4 %6, s[52:55], s65 offen offset:1024 sc1 lds\n\t"
    "buffer_load_dwordx4 %6, s[52:55], s65 offen offset:2048 sc1 lds\n\t"
    "buffer_load_dwordx4 %6, s[52:55], s65 offen offset:3072 sc1 lds\n\t"
    "s_xor_b32 m0, s66, 0x1000\n\t"
    "s_nop 0\n\t"
    "buffer_load_dwordx4 v27, s[52:55], s65 offen offset:0 sc1 lds\n\t"
    "buffer_load_dwordx4 v27, s[52:55], s65 offen offset:1024 sc1 lds\n\t"
    "buffer_load_dwordx4 v27, s[52:55], s65 offen offset:2048 sc1 lds\n\t"
    "buffer_load_dwordx4 v27, s[52:55], s65 offen offset:3072 sc1 lds\n\t"
    "s_add_u32 s65, s65, 0x10000\n\t"
    "s_xor_b32 s66, s66, 0x4000\n\t"
    "; ---- one3\n\t"
    "one3_%=:\n\t"
    "v_max3_f32 v28, v128, v129, v130\n\t"
    "v_max3_f32 v29, v132, v133, v134\n\t"
    "v_max3_f32 v30, v136, v137, v138\n\t"
    "v_max3_f32 v31, v140, v141, v142\n\t"
    "v_max3_f32 v28, v28, v131, v144\n\t"
    "v_max3_f32 v29, v29, v135, v148\n\t"
    "v_max3_f32 v30, v30, v139, v152\n\t"
    "v_max3_f32 v31, v31, v143, v156\n\t"
    "v_max3_f32 v28, v28, v145, v146\n\t"
    "v_max3_f32 v29, v29, v149, v150\n\t"
    "v_max3_f32 v30, v30, v153, v154\n\t"
    "v_max3_f32 v31, v31, v157, v158\n\t"
    "v_max3_f32 v28, v28, v147, v160\n\t"
    "v_max3_f32 v29, v29, v151, v164\n\t"
    "v_max3_f32 v30, v30, v155, v168\n\t"
    "v_max3_f32 v31, v31, v159, v172\n\t"
    "v_max3_f32 v28, v28, v161, v162\n\t"
    "v_max3_f32 v29, v29, v165, v166\n\t"
    "v_max3_f32 v30, v30, v169, v170\n\t"
    "v_max3_f32 v31, v31, v173, v174\n\t"
    "v_max3_f32 v28, v28, v163, v176\n\t"
    "v_max3_f32 v29, v29, v167, v180\n\t"
    "v_max3_f32 v30, v30, v171, v184\n\t"
    "v_max3_f32 v31, v31, v175, v188\n\t"
    "v_max3_f32 v28, v28, v177, v178\n\t"
    "v_max3_f32 v29, v29, v181, v182\n\t"
    "v_max3_f32 v30, v30, v185, v186\n\t"
    "v_max3_f32 v31, v31, v189, v190\n\t"
    "v_max_f32 v28, v28, v179\n\t"
    "v_max_f32 v29, v29, v183\n\t"
    "v_max_f32 v30, v30, v187\n\t"
    "v_max_f32 v31, v31, v191\n\t"
    "v_mov_b32 v20, v28\n\t"
    "v_mov_b32 v21, v29\n\t"
    "v_mov_b32 v22, v30\n\t"
    "v_mov_b32 v23, v31\n\t"
    "v_permlane16_swap_b32 v28, v20\n\t"
    "v_permlane16_swap_b32 v29, v21\n\t"
    "v_permlane16_swap_b32 v30, v22\n\t"
    "v_permlane16_swap_b32 v31, v23\n\t"
    "v_max_f32 v28, v28, v20\n\t"
    "v_max_f32 v29, v29, v21\n\t"
    "v_max_f32 v30, v30, v22\n\t"
    "v_max_f32 v31, v31, v23\n\t"
    "v_mov_b32 v20, v28\n\t"
    "v_mov_b32 v21, v29\n\t"
    "v_mov_b32 v22, v30\n\t"
    "v_mov_b32 v23, v31\n\t"
    "v_permlane32_swap_b32 v28, v20\n\t"
    "v_permlane32_swap_b32 v29, v21\n\t"
    "v_permlane32_swap_b32 v30, v22\n\t"
    "v_permlane32_swap_b32 v31, v23\n\t"
    "v_max_f32 v28, v28, v20\n\t"
    "v_max_f32 v29, v29, v21\n\t"
    "v_max_f32 v30, v30, v22\n\t"
    "v_max_f32 v31, v31, v23\n\t"
    "v_ceil_f32 v28, v28\n\t"
    "v_ceil_f32 v29, v29\n\t"
    "v_ceil_f32 v30, v30\n\t"
    "v_ceil_f32 v31, v31\n\t"
    "v_sub_f32 v48, 0xc2c80000, v28\n\t"
    "v_sub_f32 v49, 0xc2c80000, v28\n\t"
    "v_sub_f32 v50, 0xc2c80000, v28\n\t"
    "v_sub_f32 v51, 0xc2c80000, v28\n\t"
    "v_sub_f32 v52, 0xc2c80000, v29\n\t"
    "v_sub_f32 v53, 0xc2c80000, v29\n\t"
    "v_sub_f32 v54, 0xc2c80000, v29\n\t"
    "v_sub_f32 v55, 0xc2c80000, v29\n\t"
    "v_sub_f32 v56, 0xc2c80000, v30\n\t"
    "v_sub_f32 v57, 0xc2c80000, v30\n\t"
    "v_sub_f32 v58, 0xc2c80000, v30\n\t"
    "v_sub_f32 v59, 0xc2c80000, v30\n\t"
    "v_sub_f32 v60, 0xc2c80000, v31\n\t"
    "v_sub_f32 v61, 0xc2c80000, v31\n\t"
    "v_sub_f32 v62, 0xc2c80000, v31\n\t"
    "v_sub_f32 v63, 0xc2c80000, v31\n\t"
    "v_add_f32 v128, v48, v128\n\t"
    "v_add_f32 v129, v48, v129\n\t"
    "v_add_f32 v130, v48, v130\n\t"
    "v_add_f32 v131, v48, v131\n\t"
    "v_add_f32 v132, v52, v132\n\t"
    "v_add_f32 v133, v52, v133\n\t"
    "v_add_f32 v134, v52, v134\n\t"
    "v_add_f32 v135, v52, v135\n\t"
    "v_exp_f32 v40, v128\n\t"
    "v_exp_f32 v41, v129\n\t"
    "v_exp_f32 v42, v130\n\t"
    "v_exp_f32 v43, v131\n\t"
    "v_exp_f32 v44, v132\n\t"
    "v_exp_f32 v45, v133\n\t"
    "v_exp_f32 v46, v134\n\t"
    "v_exp_f32 v47, v135\n\t"
    "v_cvt_pk_bf16_f32 v208, v40, v41\n\t"
    "v_cvt_pk_bf16_f32 v209, v42, v43\n\t"
    "v_cvt_pk_bf16_f32 v212, v44, v45\n\t"
    "v_cvt_pk_bf16_f32 v213, v46, v47\n\t"
    "v_add_f32 v136, v56, v136\n\t"
    "v_add_f32 v137, v56, v137\n\t"
    "v_add_f32 v138, v56, v138\n\t"
    "v_add_f32 v139, v56, v139\n\t"
    "v_add_f32 v140, v60, v140\n\t"
    "v_add_f32 v141, v60, v141\n\t"
    "v_add_f32 v142, v60, v142\n\t"
    "v_add_f32 v143, v60, v143\n\t"
    "v_exp_f32 v40, v136\n\t"
    "v_exp_f32 v41, v137\n\t"
    "v_exp_f32 v42, v138\n\t"
    "v_exp_f32 v43, v139\n\t"
    "v_exp_f32 v44, v140\n\t"
    "v_exp_f32 v45, v141\n\t"
    "v_exp_f32 v46, v142\n\t"
    "v_exp_f32 v47, v143\n\t"
    "v_cvt_pk_bf16_f32 v216, v40, v41\n\t"
    "v_cvt_pk_bf16_f32 v217, v42, v43\n\t"
    "v_cvt_pk_bf16_f32 v220, v44, v45\n\t"
    "v_cvt_pk_bf16_f32 v221, v46, v47\n\t"
    "v_add_f32 v144, v48, v144\n\t"
    "v_add_f32 v145, v48, v145\n\t"
    "v_add_f32 v146, v48, v146\n\t"
    "v_add_f32 v147, v48, v147\n\t"
    "v_add_f32 v148, v52, v148\n\t"
    "v_add_f32 v149, v52, v149\n\t"
    "v_add_f32 v150, v52, v150\n\t"
    "v_add_f32 v151, v52, v151\n\t"
    "v_exp_f32 v40, v144\n\t"
    "v_exp_f32 v41, v145\n\t"
    "v_exp_f32 v42, v146\n\t"
    "v_exp_f32 v43, v147\n\t"
    "v_exp_f32 v44, v148\n\t"
    "v_exp_f32 v45, v149\n\t"
    "v_exp_f32 v46, v150\n\t"
    "v_exp_f32 v47, v151\n\t"
    "v_cvt_pk_bf16_f32 v210, v40, v41\n\t"
    "v_cvt_pk_bf16_f32 v211, v42, v43\n\t"
    "v_cvt_pk_bf16_f32 v214, v44, v45\n\t"
    "v_cvt_pk_bf16_f32 v215, v46, v47\n\t"
    "v_add_f32 v152, v56, v152\n\t"
    "v_add_f32 v153, v56, v153\n\t"
    "v_add_f32 v154, v56, v154\n\t"
    "v_add_f32 v155, v56, v155\n\t"
    "v_add_f32 v156, v60, v156\n\t"
    "v_add_f32 v157, v60, v157\n\t"
    "v_add_f32 v158, v60, v158\n\t"
    "v_add_f32 v159, v60, v159\n\t"
    "v_exp_f32 v40, v152\n\t"
    "v_exp_f32 v41, v153\n\t"
    "v_exp_f32 v42, v154\n\t"
    "v_exp_f32 v43, v155\n\t"
    "v_exp_f32 v44, v156\n\t"
    "v_exp_f32 v45, v157\n\t"
    "v_exp_f32 v46, v158\n\t"
    "v_exp_f32 v47, v159\n\t"
    "v_cvt_pk_bf16_f32 v218, v40, v41\n\t"
    "v_cvt_pk_bf16_f32 v219, v42, v43\n\t"
    "v_cvt_pk_bf16_f32 v222, v44, v45\n\t"
    "v_cvt_pk_bf16_f32 v223, v46, v47\n\t"
    "v_add_f32 v160, v48, v160\n\t"
    "v_add_f32 v161, v48, v161\n\t"
    "v_add_f32 v162, v48, v162\n\t"
    "v_add_f32 v163, v48, v163\n\t"
    "v_add_f32 v164, v52, v164\n\t"
    "v_add_f32 v165, v52, v165\n\t"
    "v_add_f32 v166, v52, v166\n\t"
    "v_add_f32 v167, v52, v167\n\t"
    "v_exp_f32 v40, v160\n\t"
    "v_exp_f32 v41, v161\n\t"
    "v_exp_f32 v42, v162\n\t"
    "v_exp_f32 v43, v163\n\t"
    "v_exp_f32 v44, v164\n\t"
    "v_exp_f32 v45, v165\n\t"
    "v_exp_f32 v46, v166\n\t"
    "v_exp_f32 v47, v167\n\t"
    "v_cvt_pk_bf16_f32 v192, v40, v41\n\t"
    "v_cvt_pk_bf16_f32 v193, v42, v43\n\t"
    "v_cvt_pk_bf16_f32 v196, v44, v45\n\t"
    "v_cvt_pk_bf16_f32 v197, v46, v47\n\t"
    "v_add_f32 v168, v56, v168\n\t"
    "v_add_f32 v169, v56, v169\n\t"
    "v_add_f32 v170, v56, v170\n\t"
    "v_add_f32 v171, v56, v171\n\t"
    "v_add_f32 v172, v60, v172\n\t"
    "v_add_f32 v173, v60, v173\n\t"
    "v_add_f32 v174, v60, v174\n\t"
    "v_add_f32 v175, v60, v175\n\t"
    "v_exp_f32 v40, v168\n\t"
    "v_exp_f32 v41, v169\n\t"
    "v_exp_f32 v42, v170\n\t"
    "v_exp_f32 v43, v171\n\t"
    "v_exp_f32 v44, v172\n\t"
    "v_exp_f32 v45, v173\n\t"
    "v_exp_f32 v46, v174\n\t"
    "v_exp_f32 v47, v175\n\t"
    "v_cvt_pk_bf16_f32 v200, v40, v41\n\t"
    "v_cvt_pk_bf16_f32 v201, v42, v43\n\t"
    "v_cvt_pk_bf16_f32 v204, v44, v45\n\t"
    "v_cvt_pk_bf16_f32 v205, v46, v47\n\t"
    "v_add_f32 v176, v48, v176\n\t"
    "v_add_f32 v177, v48, v177\n\t"
    "v_add_f32 v178, v48, v178\n\t"
    "v_add_f32 v179, v48, v179\n\t"
    "v_add_f32 v180, v52, v180\n\t"
    "v_add_f32 v181, v52, v181\n\t"
    "v_add_f32 v182, v52, v182\n\t"
    "v_add_f32 v183, v52, v183\n\t"
    "v_exp_f32 v40, v176\n\t"
    "v_exp_f32 v41, v177\n\t"
    "v_exp_f32 v42, v178\n\t"
    "v_exp_f32 v43, v179\n\t"
    "v_exp_f32 v44, v180\n\t"
    "v_exp_f32 v45, v181\n\t"
    "v_exp_f32 v46, v182\n\t"
    "v_exp_f32 v47, v183\n\t"
    "v_cvt_pk_bf16_f32 v194, v40, v41\n\t"
    "v_cvt_pk_bf16_f32 v195, v42, v43\n\t"
    "v_cvt_pk_bf16_f32 v198, v44, v45\n\t"
    "v_cvt_pk_bf16_f32 v199, v46, v47\n\t"
    "v_add_f32 v184, v56, v184\n\t"
    "v_add_f32 v185, v56, v185\n\t"
    "v_add_f32 v186, v56, v186\n\t"
    "v_add_f32 v187, v56, v187\n\t"
    "v_add_f32 v188, v60, v188\n\t"
    "v_add_f32 v189, v60, v189\n\t"
    "v_add_f32 v190, v60, v190\n\t"
    "v_add_f32 v191, v60, v191\n\t"
    "v_exp_f32 v40, v184\n\t"
    "v_exp_f32 v41, v185\n\t"
    "v_exp_f32 v42, v186\n\t"
    "v_exp_f32 v43, v187\n\t"
    "v_exp_f32 v44, v188\n\t"
    "v_exp_f32 v45, v189\n\t"
    "v_exp_f32 v46, v190\n\t"
    "v_exp_f32 v47, v191\n\t"
    "v_cvt_pk_bf16_f32 v202, v40, v41\n\t"
    "v_cvt_pk_bf16_f32 v203, v42, v43\n\t"
    "v_cvt_pk_bf16_f32 v206, v44, v45\n\t"
    "v_cvt_pk_bf16_f32 v207, v46, v47\n\t"
    "v_mfma_f32_16x16x32_bf16 v[240:243], v[16:19], v[208:211], 0\n\t"
    "v_mfma_f32_16x16x32_bf16 v[244:247], v[16:19], v[212:215], 0\n\t"
    "v_mfma_f32_16x16x32_bf16 v[248:251], v[16:19], v[216:219], 0\n\t"
    "v_mfma_f32_16x16x32_bf16 v[252:255], v[16:19], v[220:223], 0\n\t"
    "v_mfma_f32_16x16x32_bf16 v[240:243], v[16:19], v[192:195], v[240:243]\n\t"
    "v_mfma_f32_16x16x32_bf16 v[244:247], v[16:19], v[196:199], v[244:247]\n\t"
    "v_mfma_f32_16x16x32_bf16 v[248:251], v[16:19], v[200:203], v[248:251]\n\t"
    "v_mfma_f32_16x16x32_bf16 v[252:255], v[16:19], v[204:207], v[252:255]\n\t"
    "s_cmp_eq_u32 s83, 0\n\t"
    "s_cbranch_scc0 skipb_%=\n\t"
    "; ---- pvl\n\t"
    "pvl_%=:\n\t"
    "s_nop 4\n\t"
    "v_max3_f32 v20, v240, v244, v248\n\t"
    "s_nop 0\n\t"
    "v_max_f32 v20, v20, v252\n\t"
    "v_cmp_nge_f32 vcc, 0x5d800000, v20\n\t"
    "s_nop 0\n\t"
    "s_cbranch_vccnz rare_pv_%=\n\t"
    "cont_pv_%=:\n\t"
    "; ---- pvl2\n\t"
    "v_add_f32 v32, v32, v240\n\t"
    "v_add_f32 v33, v33, v244\n\t"
    "v_add_f32 v34, v34, v248\n\t"
    "v_add_f32 v35, v35, v252\n\t"
    "s_and_b32 s68, %0, 1\n\t"
    "s_lshl_b32 s84, s68, 13\n\t"
    "v_xor_b32 v38, 0x4000, v24\n\t"
    "v_add_u32 v38, s84, v38\n\t"
    "v_xor_b32 v36, 0x8000, v38\n\t"
    "s_lshl_b32 s84, s68, 15\n\t"
    "s_add_u32 s84, s84, 0x10000\n\t"
    "v_xor_b32 v37, 0x4000, v25\n\t"
    "v_and_b32 v37, 0x43ff, v37\n\t"
    "v_add_u32 v37, s84, v37\n\t"
    "s_cmp_lt_u32 %0, 2\n\t"
    "s_cselect_b64 s[94:95], -1, 0\n\t"
    "v_cndmask_b32 v39, v37, v26, s[94:95]\n\t"
    "v_cndmask_b32 v37, v26, v37, s[94:95]\n\t"
    "ds_write_b128 v36, v[28:31] offset:0\n\t"
    "ds_write_b128 v36, v[32:35] offset:1024\n\t"
    "s_waitcnt lgkmcnt(0)\n\t"
    "s_barrier\n\t"
    "ds_read_b128 v[160:163], v38 offset:0\n\t"
    "ds_read_b128 v[164:167], v38 offset:1024\n\t"
    "; ---- pvl3\n\t"
    "v_mfma_f32_16x16x32_bf16 a[64:67], a[192:195], v[208:211], a[64:67]\n\t"
    "ds_read_b128 a[216:219], v25 offset:7168\n\t"
    "ds_read_b128 a[220:223], v25 offset:15360\n\t"
    "v_mfma_f32_16x16x32_bf16 a[68:71], a[192:195], v[212:215], a[68:71]\n\t"
    "buffer_load_dwordx4 v[128:131], v8, s[76:79], 0 offen offset:0 sc1\n\t"
    "v_mfma_f32_16x16x32_bf16 a[72:75], a[192:195], v[216:219], a[72:75]\n\t"
    "buffer_load_dwordx4 v[144:147], v9, s[76:79], 0 offen offset:0 sc1\n\t"
    "v_mfma_f32_16x16x32_bf16 a[76:79], a[192:195], v[220:223], a[76:79]\n\t"
    "buffer_load_dwordx4 v[132:135], v8, s[76:79], 0 offen offset:256 sc1\n\t"
    "v_mfma_f32_16x16x32_bf16 a[64:67], a[196:199], v[192:195], a[64:67]\n\t"
    "buffer_load_dwordx4 v[148:151], v9, s[76:79], 0 offen offset:256 sc1\n\t"
    "v_mfma_f32_16x16x32_bf16 a[68:71], a[196:199], v[196:199], a[68:71]\n\t"
    "buffer_load_dwordx4 v[136:139], v8, s[76:79], 0 offen offset:512 sc1\n\t"
    "v_mfma_f32_16x16x32_bf16 a[72:75], a[196:199], v[200:203], a[72:75]\n\t"
    "buffer_load_dwordx4 v[152:155], v9, s[76:79], 0 offen offset:512 sc1\n\t"
    "v_mfma_f32_16x16x32_bf16 a[76:79], a[196:199], v[204:207], a[76:79]\n\t"
    "buffer_load_dwordx4 v[140:143], v8, s[76:79], 0 offen offset:768 sc1\n\t"
    "v_mfma_f32_16x16x32_bf16 a[80:83], a[200:203], v[208:211], a[80:83]\n\t"
    "buffer_load_dwordx4 v[156:159], v9, s[76:79], 0 offen offset:768 sc1\n\t"
    "v_mfma_f32_16x16x32_bf16 a[84:87], a[200:203], v[212:215], a[84:87]\n\t"
    "v_log_f32 v40, v32\n\t"
    "s_waitcnt lgkmcnt(2)\n\t"
    "v_log_f32 v44, v164\n\t"
    "v_mfma_f32_16x16x32_bf16 a[88:91], a[200:203], v[216:219], a[88:91]\n\t"
    "v_log_f32 v41, v33\n\t"
    "v_mfma_f32_16x16x32_bf16 a[92:95], a[200:203], v[220:223], a[92:95]\n\t"
    "ds_read_b128 a[224:227], v25 offset:0\n\t"
    "ds_write_b128 v37, a[64:67] offset:0\n\t"
    "v_log_f32 v45, v165\n\t"
    "v_mfma_f32_16x16x32_bf16 a[80:83], a[204:207], v[192:195], a[80:83]\n\t"
    "v_log_f32 v42, v34\n\t"
    "v_log_f32 v46, v166\n\t"
    "v_mfma_f32_16x16x32_bf16 a[84:87], a[204:207], v[196:199], a[84:87]\n\t"
    "v_log_f32 v43, v35\n\t"
    "v_mfma_f32_16x16x32_bf16 a[88:91], a[204:207], v[200:203], a[88:91]\n\t"
    "ds_write_b128 v37, a[68:71] offset:1024\n\t"
    "v_log_f32 v47, v167\n\t"
    "v_mfma_f32_16x16x32_bf16 a[92:95], a[204:207], v[204:207], a[92:95]\n\t"
    "ds_read_b128 a[228:231], v25 offset:8192\n\t"
    "ds_write_b128 v37, a[72:75] offset:2048\n\t"
    "v_rcp_f32 v32, v32\n\t"
    "v_mfma_f32_16x16x32_bf16 a[96:99], a[208:211], v[208:211], a[96:99]\n\t"
    "v_rcp_f32 v164, v164\n\t"
    "v_rcp_f32 v33, v33\n\t"
    "v_mfma_f32_16x16x32_bf16 a[100:103], a[208:211], v[212:215], a[100:103]\n\t"
    "v_rcp_f32 v165, v165\n\t"
    "v_mfma_f32_16x16x32_bf16 a[104:107], a[208:211], v[216:219], a[104:107]\n\t"
    "ds_write_b128 v37, a[76:79] offset:3072\n\t"
    "v_rcp_f32 v34, v34\n\t"
    "v_mfma_f32_16x16x32_bf16 a[108:111], a[208:211], v[220:223], a[108:111]\n\t"
    "ds_read_b128 a[232:235], v25 offset:1024\n\t"
    "ds_write_b128 v37, a[80:83] offset:4096\n\t"
    "v_rcp_f32 v166, v166\n\t"
    "v_mfma_f32_16x16x32_bf16 a[96:99], a[212:215], v[192:195], a[96:99]\n\t"
    "v_rcp_f32 v35, v35\n\t"
    "v_rcp_f32 v167, v167\n\t"
    "v_mfma_f32_16x16x32_bf16 a[100:103], a[212:215], v[196:199], a[100:103]\n\t"
    "v_add_f32 v40, v40, v28\n\t"
    "v_add_f32 v44, v44, v160\n\t"
    "v_mfma_f32_16x16x32_bf16 a[104:107], a[212:215], v[200:203], a[104:107]\n\t"
    "ds_read_b128 a[236:239], v25 offset:9216\n\t"
    "v_add_f32 v41, v41, v29\n\t"
    "v_mfma_f32_16x16x32_bf16 a[108:111], a[212:215], v[204:207], a[108:111]\n\t"
    "ds_write_b128 v37, a[84:87] offset:5120\n\t"
    "v_add_f32 v45, v45, v161\n\t"
    "s_waitcnt lgkmcnt(10)\n\t"
    "v_mfma_f32_16x16x32_bf16 a[112:115], a[216:219], v[208:211], a[112:115]\n\t"
    "v_add_f32 v42, v42, v30\n\t"
    "v_add_f32 v46, v46, v162\n\t"
    "v_mfma_f32_16x16x32_bf16 a[116:119], a[216:219], v[212:215], a[116:119]\n\t"
    "ds_write_b128 v37, a[88:91] offset:6144\n\t"
    "v_add_f32 v43, v43, v31\n\t"
    "v_mfma_f32_16x16x32_bf16 a[120:123], a[216:219], v[216:219], a[120:123]\n\t"
    "ds_read_b128 a[240:243], v25 offset:2048\n\t"
    "v_add_f32 v47, v47, v163\n\t"
    "v_mfma_f32_16x16x32_bf16 a[124:127], a[216:219], v[220:223], a[124:127]\n\t"
    "ds_write_b128 v37, a[92:95] offset:7168\n\t"
    "v_max_f32 v20, v40, v44\n\t"
    "v_mfma_f32_16x16x32_bf16 a[112:115], a[220:223], v[192:195], a[112:115]\n\t"
    "v_max_f32 v21, v41, v45\n\t"
    "v_max_f32 v22, v42, v46\n\t"
    "v_mfma_f32_16x16x32_bf16 a[116:119], a[220:223], v[196:199], a[116:119]\n\t"
    "ds_write_b128 v37, a[96:99] offset:8192\n\t"
    "v_max_f32 v23, v43, v47\n\t"
    "v_mfma_f32_16x16x32_bf16 a[120:123], a[220:223], v[200:203], a[120:123]\n\t"
    "ds_read_b128 a[244:247], v25 offset:10240\n\t"
    "v_sub_f32 v168, v40, v20\n\t"
    "v_mfma_f32_16x16x32_bf16 a[124:127], a[220:223], v[204:207], a[124:127]\n\t"
    "v_sub_f32 v172, v44, v20\n\t"
    "v_sub_f32 v169, v41, v21\n\t"
    "s_waitcnt lgkmcnt(11)\n\t"
    "v_mfma_f32_16x16x32_bf16 a[0:3], a[224:227], v[208:211], a[0:3]\n\t"
    "ds_write_b128 v37, a[100:103] offset:9216\n\t"
    "v_sub_f32 v173, v45, v21\n\t"
    "v_mfma_f32_16x16x32_bf16 a[4:7], a[224:227], v[212:215], a[4:7]\n\t"
    "ds_write_b128 v37, a[104:107] offset:10240\n\t"
    "v_sub_f32 v170, v42, v22\n\t"
    "v_mfma_f32_16x16x32_bf16 a[8:11], a[224:227], v[216:219], a[8:11]\n\t"
    "ds_read_b128 a[248:251], v25 offset:3072\n\t"
    "v_sub_f32 v174, v46, v22\n\t"
    "v_mfma_f32_16x16x32_bf16 a[12:15], a[224:227], v[220:223], a[12:15]\n\t"
    "v_sub_f32 v171, v43, v23\n\t"
    "v_sub_f32 v175, v47, v23\n\t"
    "v_mfma_f32_16x16x32_bf16 a[0:3], a[228:231], v[192:195], a[0:3]\n\t"
    "ds_write_b128 v37, a[108:111] offset:11264\n\t"
    "ds_read_b128 a[252:255], v25 offset:11264\n\t"
    "v_mfma_f32_16x16x32_bf16 a[4:7], a[228:231], v[196:199], a[4:7]\n\t"
    "v_exp_f32 v168, v168\n\t"
    "v_mfma_f32_16x16x32_bf16 a[8:11], a[228:231], v[200:203], a[8:11]\n\t"
    "v_exp_f32 v172, v172\n\t"
    "v_mfma_f32_16x16x32_bf16 a[12:15], a[228:231], v[204:207], a[12:15]\n\t"
    "v_exp_f32 v169, v169\n\t"
    "v_exp_f32 v173, v173\n\t"
    "s_waitcnt lgkmcnt(11)\n\t"
    "v_mfma_f32_16x16x32_bf16 a[16:19], a[232:235], v[208:211], a[16:19]\n\t"
    "ds_write_b128 v37, a[112:115] offset:12288\n\t"
    "ds_write_b128 v37, a[116:119] offset:13312\n\t"
    "v_exp_f32 v170, v170\n\t"
    "v_mfma_f32_16x16x32_bf16 a[20:23], a[232:235], v[212:215], a[20:23]\n\t"
    "v_exp_f32 v174, v174\n\t"
    "v_mfma_f32_16x16x32_bf16 a[24:27], a[232:235], v[216:219], a[24:27]\n\t"
    "v_exp_f32 v171, v171\n\t"
    "v_mfma_f32_16x16x32_bf16 a[28:31], a[232:235], v[220:223], a[28:31]\n\t"
    "ds_write_b128 v37, a[120:123] offset:14336\n\t"
    "v_exp_f32 v175, v175\n\t"
    "v_add_f32 v20, v168, v172\n\t"
    "v_mfma_f32_16x16x32_bf16 a[16:19], a[236:239], v[192:195], a[16:19]\n\t"
    "v_add_f32 v21, v169, v173\n\t"
    "v_add_f32 v22, v170, v174\n\t"
    "v_mfma_f32_16x16x32_bf16 a[20:23], a[236:239], v[196:199], a[20:23]\n\t"
    "ds_write_b128 v37, a[124:127] offset:15360\n\t"
    "v_add_f32 v23, v171, v175\n\t"
    "v_mfma_f32_16x16x32_bf16 a[24:27], a[236:239], v[200:203], a[24:27]\n\t"
    "v_rcp_f32 v20, v20\n\t"
    "v_mfma_f32_16x16x32_bf16 a[28:31], a[236:239], v[204:207], a[28:31]\n\t"
    "v_rcp_f32 v21, v21\n\t"
    "s_waitcnt lgkmcnt(9)\n\t"
    "v_mfma_f32_16x16x32_bf16 a[32:35], a[240:243], v[208:211], a[32:35]\n\t"
    "v_rcp_f32 v22, v22\n\t"
    "v_mfma_f32_16x16x32_bf16 a[36:39], a[240:243], v[212:215], a[36:39]\n\t"
    "v_rcp_f32 v23, v23\n\t"
    "v_mfma_f32_16x16x32_bf16 a[40:43], a[240:243], v[216:219], a[40:43]\n\t"
    "v_mul_f32 v168, v168, v32\n\t"
    "v_mul_f32 v172, v172, v164\n\t"
    "v_mfma_f32_16x16x32_bf16 a[44:47], a[240:243], v[220:223], a[44:47]\n\t"
    "v_mul_f32 v169, v169, v33\n\t"
    "v_mul_f32 v173, v173, v165\n\t"
    "v_mfma_f32_16x16x32_bf16 a[32:35], a[244:247], v[192:195], a[32:35]\n\t"
    "v_mul_f32 v170, v170, v34\n\t"
    "v_mul_f32 v174, v174, v166\n\t"
    "v_mfma_f32_16x16x32_bf16 a[36:39], a[244:247], v[196:199], a[36:39]\n\t"
    "v_mul_f32 v171, v171, v35\n\t"
    "v_mul_f32 v175, v175, v167\n\t"
    "v_mfma_f32_16x16x32_bf16 a[40:43], a[244:247], v[200:203], a[40:43]\n\t"
    "v_mul_f32 v176, v168, v20\n\t"
    "v_mul_f32 v184, v172, v20\n\t"
    "v_mfma_f32_16x16x32_bf16 a[44:47], a[244:247], v[204:207], a[44:47]\n\t"
    "v_mul_f32 v178, v169, v21\n\t"
    "v_mul_f32 v186, v173, v21\n\t"
    "s_waitcnt lgkmcnt(4)\n\t"
    "v_mfma_f32_16x16x32_bf16 a[48:51], a[248:251], v[208:211], a[48:51]\n\t"
    "v_mul_f32 v180, v170, v22\n\t"
    "v_mul_f32 v188, v174, v22\n\t"
    "v_mfma_f32_16x16x32_bf16 a[52:55], a[248:251], v[212:215], a[52:55]\n\t"
    "v_mul_f32 v182, v171, v23\n\t"
    "v_mul_f32 v190, v175, v23\n\t"
    "v_mfma_f32_16x16x32_bf16 a[56:59], a[248:251], v[216:219], a[56:59]\n\t"
    "v_mfma_f32_16x16x32_bf16 a[60:63], a[248:251], v[220:223], a[60:63]\n\t"
    "v_mfma_f32_16x16x32_bf16 a[48:51], a[252:255], v[192:195], a[48:51]\n\t"
    "v_mfma_f32_16x16x32_bf16 a[52:55], a[252:255], v[196:199], a[52:55]\n\t"
    "v_mfma_f32_16x16x32_bf16 a[56:59], a[252:255], v[200:203], a[56:59]\n\t"
    "v_mfma_f32_16x16x32_bf16 a[60:63], a[252:255], v[204:207], a[60:63]\n\t"
    "s_branch merge_%=\n\t"
    "; ---- alite\n\t"
    "alite_%=:\n\t"
    "s_waitcnt vmcnt(9)\n\t"
    "s_barrier\n\t"
    "s_mov_b32 m0, s66\n\t"
    "s_nop 0\n\t"
    "buffer_load_dwordx4 %6, s[52:55], s65 offen offset:0 sc1 lds\n\t"
    "buffer_load_dwordx4 %6, s[52:55], s65 offen offset:1024 sc1 lds\n\t"
    "buffer_load_dwordx4 %6, s[52:55], s65 offen offset:2048 sc1 lds\n\t"
    "buffer_load_dwordx4 %6, s[52:55], s65 offen offset:3072 sc1 lds\n\t"
    "s_xor_b32 m0, s66, 0x1000\n\t"
    "s_nop 0\n\t"
    "buffer_load_dwordx4 v27, s[52:55], s65 offen offset:0 sc1 lds\n\t"
    "buffer_load_dwordx4 v27, s[52:55], s65 offen offset:1024 sc1 lds\n\t"
    "buffer_load_dwordx4 v27, s[52:55], s65 offen offset:2048 sc1 lds\n\t"
    "buffer_load_dwordx4 v27, s[52:55], s65 offen offset:3072 sc1 lds\n\t"
    "s_add_u32 s65, s65, 0x10000\n\t"
    "s_xor_b32 s66, s66, 0x4000\n\t"
    "ds_read_b128 a[128:131], v26 offset:0\n\t"
    "ds_read_b128 a[132:135], v26 offset:1024\n\t"
    "ds_read_b128 a[136:139], v26 offset:2048\n\t"
    "ds_read_b128 a[140:143], v26 offset:3072\n\t"
    "ds_read_b128 a[144:147], v26 offset:4096\n\t"
    "ds_read_b128 a[148:151], v26 offset:5120\n\t"
    "ds_read_b128 a[152:155], v26 offset:6144\n\t"
    "ds_read_b128 a[156:159], v26 offset:7168\n\t"
    "ds_read_b128 a[160:163], v26 offset:8192\n\t"
    "ds_read_b128 a[164:167], v26 offset:9216\n\t"
    "ds_read_b128 a[168:171], v26 offset:10240\n\t"
    "ds_read_b128 a[172:175], v26 offset:11264\n\t"
    "ds_read_b128 a[176:179], v26 offset:12288\n\t"
    "ds_read_b128 a[180:183], v26 offset:13312\n\t"
    "ds_read_b128 a[184:187], v26 offset:14336\n\t"
    "ds_read_b128 a[188:191], v26 offset:15360\n\t"
    "v_exp_f32 v40, v160\n\t"
    "v_exp_f32 v41, v161\n\t"
    "v_exp_f32 v42, v162\n\t"
    "v_cvt_pk_bf16_f32 v192, v40, v41\n\t"
    "v_exp_f32 v43, v163\n\t"
    "s_nop 0\n\t"
    "v_cvt_pk_bf16_f32 v193, v42, v43\n\t"
    "v_exp_f32 v44, v164\n\t"
    "v_exp_f32 v45, v165\n\t"
    "s_nop 0\n\t"
    "v_cvt_pk_bf16_f32 v196, v44, v45\n\t"
    "v_exp_f32 v46, v166\n\t"
    "v_exp_f32 v47, v167\n\t"
    "v_exp_f32 v40, v168\n\t"
    "v_cvt_pk_bf16_f32 v197, v46, v47\n\t"
    "v_exp_f32 v41, v169\n\t"
    "s_nop 0\n\t"
    "v_cvt_pk_bf16_f32 v200, v40, v41\n\t"
    "v_exp_f32 v42, v170\n\t"
    "v_exp_f32 v43, v171\n\t"
    "s_nop 0\n\t"
    "v_cvt_pk_bf16_f32 v201, v42, v43\n\t"
    "v_exp_f32 v44, v172\n\t"
    "v_exp_f32 v45, v173\n\t"
    "s_nop 0\n\t"
    "v_cvt_pk_bf16_f32 v204, v44, v45\n\t"
    "v_exp_f32 v46, v174\n\t"
    "v_exp_f32 v47, v175\n\t"
    "s_nop 0\n\t"
    "v_cvt_pk_bf16_f32 v205, v46, v47\n\t"
    "v_exp_f32 v40, v176\n\t"
    "v_exp_f32 v41, v177\n\t"
    "s_nop 0\n\t"
    "v_cvt_pk_bf16_f32 v194, v40, v41\n\t"
    "v_exp_f32 v42, v178\n\t"
    "v_exp_f32 v43, v179\n\t"
    "s_nop 0\n\t"
    "v_cvt_pk_bf16_f32 v195, v42, v43\n\t"
    "v_exp_f32 v44, v180\n\t"
    "v_exp_f32 v45, v181\n\t"
    "s_nop 0\n\t"
    "v_cvt_pk_bf16_f32 v198, v44, v45\n\t"
    "v_exp_f32 v46, v182\n\t"
    "v_exp_f32 v47, v183\n\t"
    "s_nop 0\n\t"
    "v_cvt_pk_bf16_f32 v199, v46, v47\n\t"
    "v_exp_f32 v40, v184\n\t"
    "v_exp_f32 v41, v185\n\t"
    "s_nop 0\n\t"
    "v_cvt_pk_bf16_f32 v202, v40, v41\n\t"
    "v_exp_f32 v42, v186\n\t"
    "v_exp_f32 v43, v187\n\t"
    "s_nop 0\n\t"
    "v_cvt_pk_bf16_f32 v203, v42, v43\n\t"
    "v_exp_f32 v44, v188\n\t"
    "v_exp_f32 v45, v189\n\t"
    "s_nop 0\n\t"
    "v_cvt_pk_bf16_f32 v206, v44, v45\n\t"
    "v_exp_f32 v46, v190\n\t"
    "v_exp_f32 v47, v191\n\t"
    "s_nop 0\n\t"
    "v_cvt_pk_bf16_f32 v207, v46, v47\n\t"
    "v_mfma_f32_16x16x32_bf16 v[240:243], v[16:19], v[208:211], 0\n\t"
    "v_mfma_f32_16x16x32_bf16 v[244:247], v[16:19], v[212:215], 0\n\t"
    "v_mfma_f32_16x16x32_bf16 v[248:251], v[16:19], v[216:219], 0\n\t"
    "v_mfma_f32_16x16x32_bf16 v[252:255], v[16:19], v[220:223], 0\n\t"
    "v_mfma_f32_16x16x32_bf16 v[240:243], v[16:19], v[192:195], v[240:243]\n\t"
    "v_mfma_f32_16x16x32_bf16 v[244:247], v[16:19], v[196:199], v[244:247]\n\t"
    "v_mfma_f32_16x16x32_bf16 v[248:251], v[16:19], v[200:203], v[248:251]\n\t"
    "v_mfma_f32_16x16x32_bf16 v[252:255], v[16:19], v[204:207], v[252:255]\n\t"
    "; ---- skipb\n\t"
    "skipb_%=:\n\t"
    "s_waitcnt vmcnt(0)\n\t"
    "s_barrier\n\t"
    "s_waitcnt lgkmcnt(15)\n\t"
    "ds_read_b128 a[192:195], v25 offset:4096\n\t"
    "ds_read_b128 a[196:199], v25 offset:12288\n\t"
    "ds_read_b128 a[200:203], v25 offset:5120\n\t"
    "ds_read_b128 a[204:207], v25 offset:13312\n\t"
    "ds_read_b128 a[208:211], v25 offset:6144\n\t"
    "ds_read_b128 a[212:215], v25 offset:14336\n\t"
    "s_mov_b32 s68, 0x40000\n\t"
    "bs_chk_%=:\n\t"
    "v_cmp_ne_u64 vcc, %10, v[10:11]\n\t"
    "s_nop 0\n\t"
    "s_cbranch_vccz bs_ok_%=\n\t"
    "s_sleep 2\n\t"
    "buffer_load_dwordx2 v[10:11], %16, s[40:43], 0 offen offset:0 sc1\n\t"
    "s_waitcnt vmcnt(0)\n\t"
    "s_sub_u32 s68, s68, 1\n\t"
    "s_cmp_lg_u32 s68, 0\n\t"
    "s_cbranch_scc1 bs_chk_%=\n\t"
    "bs_ok_%=:\n\t"
    "v_max3_f32 v20, v240, v244, v248\n\t"
    "v_max_f32 v20, v20, v252\n\t"
    "v_cmp_nge_f32 vcc, 0x5d800000, v20\n\t"
    "s_nop 0\n\t"
    "s_cbranch_vccnz rare_ps_%=\n\t"
    "cont_ps_%=:\n\t"
    "; ---- pvs\n\t"
    "s_waitcnt lgkmcnt(5)\n\t"
    "v_mfma_f32_16x16x32_bf16 a[64:67], a[192:195], v[208:211], a[64:67]\n\t"
    "ds_read_b128 a[216:219], v25 offset:7168\n\t"
    "v_add_f32 v32, v32, v240\n\t"
    "v_mfma_f32_16x16x32_bf16 a[68:71], a[192:195], v[212:215], a[68:71]\n\t"
    "v_add_f32 v33, v33, v244\n\t"
    "v_add_f32 v34, v34, v248\n\t"
    "v_mfma_f32_16x16x32_bf16 a[72:75], a[192:195], v[216:219], a[72:75]\n\t"
    "ds_read_b128 a[220:223], v25 offset:15360\n\t"
    "v_add_f32 v35, v35, v252\n\t"
    "v_mfma_f32_16x16x32_bf16 a[76:79], a[192:195], v[220:223], a[76:79]\n\t"
    "ds_read_b128 a[224:227], v25 offset:0\n\t"
    "s_mov_b32 s84, 0\n\t"
    "s_waitcnt lgkmcnt(5)\n\t"
    "v_mfma_f32_16x16x32_bf16 a[64:67], a[196:199], v[192:195], a[64:67]\n\t"
    "v_mfma_f32_16x16x32_bf16 a[68:71], a[196:199], v[196:199], a[68:71]\n\t"
    "v_mfma_f32_16x16x32_bf16 a[72:75], a[196:199], v[200:203], a[72:75]\n\t"
    "buffer_load_dwordx4 v[64:67], v6, s[72:75], s84 offen offset:0 sc1\n\t"
    "v_mfma_f32_16x16x32_bf16 a[76:79], a[196:199], v[204:207], a[76:79]\n\t"
    "ds_read_b128 a[228:231], v25 offset:8192\n\t"
    "v_mfma_f32_16x16x32_bf16 a[80:83], a[200:203], v[208:211], a[80:83]\n\t"
    "buffer_load_dwordx4 v[96:99], v7, s[72:75], s84 offen offset:0 sc1\n\t"
    "v_mfma_f32_16x16x32_bf16 a[84:87], a[200:203], v[212:215], a[84:87]\n\t"
    "s_mov_b32 s84, 0x1800\n\t"
    "v_mfma_f32_16x16x32_bf16 a[88:91], a[200:203], v[216:219], a[88:91]\n\t"
    "buffer_load_dwordx4 v[68:71], v6, s[72:75], s84 offen offset:0 sc1\n\t"
    "v_mfma_f32_16x16x32_bf16 a[92:95], a[200:203], v[220:223], a[92:95]\n\t"
    "ds_read_b128 a[232:235], v25 offset:1024\n\t"
    "v_mfma_f32_16x16x32_bf16 a[80:83], a[204:207], v[192:195], a[80:83]\n\t"
    "buffer_load_dwordx4 v[100:103], v7, s[72:75], s84 offen offset:0 sc1\n\t"
    "v_mfma_f32_16x16x32_bf16 a[84:87], a[204:207], v[196:199], a[84:87]\n\t"
    "s_mov_b32 s84, 0x3000\n\t"
    "v_mfma_f32_16x16x32_bf16 a[88:91], a[204:207], v[200:203], a[88:91]\n\t"
    "ds_read_b128 a[236:239], v25 offset:9216\n\t"
    "v_mfma_f32_16x16x32_bf16 a[92:95], a[204:207], v[204:207], a[92:95]\n\t"
    "buffer_load_dwordx4 v[72:75], v6, s[72:75], s84 offen offset:0 sc1\n\t"
    "s_waitcnt lgkmcnt(5)\n\t"
    "v_mfma_f32_16x16x32_bf16 a[96:99], a[208:211], v[208:211], a[96:99]\n\t"
    "v_mfma_f32_16x16x32_bf16 a[100:103], a[208:211], v[212:215], a[100:103]\n\t"
    "buffer_load_dwordx4 v[104:107], v7, s[72:75], s84 offen offset:0 sc1\n\t"
    "v_mfma_f32_16x16x32_bf16 a[104:107], a[208:211], v[216:219], a[104:107]\n\t"
    "ds_read_b128 a[240:243], v25 offset:2048\n\t"
    "s_mov_b32 s84, 0x4800\n\t"
    "v_mfma_f32_16x16x32_bf16 a[108:111], a[208:211], v[220:223], a[108:111]\n\t"
    "buffer_load_dwordx4 v[76:79], v6, s[72:75], s84 offen offset:0 sc1\n\t"
    "v_mfma_f32_16x16x32_bf16 a[96:99], a[212:215], v[192:195], a[96:99]\n\t"
    "v_mfma_f32_16x16x32_bf16 a[100:103], a[212:215], v[196:199], a[100:103]\n\t"
    "buffer_load_dwordx4 v[108:111], v7, s[72:75], s84 offen offset:0 sc1\n\t"
    "v_mfma_f32_16x16x32_bf16 a[104:107], a[212:215], v[200:203], a[104:107]\n\t"
    "ds_read_b128 a[244:247], v25 offset:10240\n\t"
    "s_mov_b32 s84, 0x6000\n\t"
    "v_mfma_f32_16x16x32_bf16 a[108:111], a[212:215], v[204:207], a[108:111]\n\t"
    "v_mfma_f32_16x16x32_bf16 a[112:115], a[216:219], v[208:211], a[112:115]\n\t"
    "buffer_load_dwordx4 v[80:83], v6, s[72:75], s84 offen offset:0 sc1\n\t"
    "v_mfma_f32_16x16x32_bf16 a[116:119], a[216:219], v[212:215], a[116:119]\n\t"
    "v_mfma_f32_16x16x32_bf16 a[120:123], a[216:219], v[216:219], a[120:123]\n\t"
    "buffer_load_dwordx4 v[112:115], v7, s[72:75], s84 offen offset:0 sc1\n\t"
    "v_mfma_f32_16x16x32_bf16 a[124:127], a[216:219], v[220:223], a[124:127]\n\t"
    "ds_read_b128 a[248:251], v25 offset:3072\n\t"
    "s_mov_b32 s84, 0x7800\n\t"
    "s_waitcnt lgkmcnt(5)\n\t"
    "v_mfma_f32_16x16x32_bf16 a[112:115], a[220:223], v[192:195], a[112:115]\n\t"
    "buffer_load_dwordx4 v[84:87], v6, s[72:75], s84 offen offset:0 sc1\n\t"
    "v_mfma_f32_16x16x32_bf16 a[116:119], a[220:223], v[196:199], a[116:119]\n\t"
    "v_mfma_f32_16x16x32_bf16 a[120:123], a[220:223], v[200:203], a[120:123]\n\t"
    "buffer_load_dwordx4 v[116:119], v7, s[72:75], s84 offen offset:0 sc1\n\t"
    "v_mfma_f32_16x16x32_bf16 a[124:127], a[220:223], v[204:207], a[124:127]\n\t"
    "ds_read_b128 a[252:255], v25 offset:11264\n\t"
    "s_mov_b32 s84, 0x9000\n\t"
    "v_mfma_f32_16x16x32_bf16 a[0:3], a[224:227], v[208:211], a[0:3]\n\t"
    "v_mfma_f32_16x16x32_bf16 a[4:7], a[224:227], v[212:215], a[4:7]\n\t"
    "buffer_load_dwordx4 v[88:91], v6, s[72:75], s84 offen offset:0 sc1\n\t"
    "v_mfma_f32_16x16x32_bf16 a[8:11], a[224:227], v[216:219], a[8:11]\n\t"
    "v_mfma_f32_16x16x32_bf16 a[12:15], a[224:227], v[220:223], a[12:15]\n\t"
    "buffer_load_dwordx4 v[120:123], v7, s[72:75], s84 offen offset:0 sc1\n\t"
    "v_mfma_f32_16x16x32_bf16 a[0:3], a[228:231], v[192:195], a[0:3]\n\t"
    "s_mov_b32 s84, 0xa800\n\t"
    "v_mfma_f32_16x16x32_bf16 a[4:7], a[228:231], v[196:199], a[4:7]\n\t"
    "buffer_load_dwordx4 v[92:95], v6, s[72:75], s84 offen offset:0 sc1\n\t"
    "v_mfma_f32_16x16x32_bf16 a[8:11], a[228:231], v[200:203], a[8:11]\n\t"
    "v_mfma_f32_16x16x32_bf16 a[12:15], a[228:231], v[204:207], a[12:15]\n\t"
    "buffer_load_dwordx4 v[124:127], v7, s[72:75], s84 offen offset:0 sc1\n\t"
    "s_waitcnt lgkmcnt(2)\n\t"
    "v_mfma_f32_16x16x32_bf16 a[16:19], a[232:235], v[208:211], a[16:19]\n\t"
    "v_mfma_f32_16x16x32_bf16 a[20:23], a[232:235], v[212:215], a[20:23]\n\t"
    "v_mfma_f32_16x16x32_bf16 a[24:27], a[232:235], v[216:219], a[24:27]\n\t"
    "buffer_load_dwordx4 v[128:131], v8, s[76:79], 0 offen offset:0 sc1\n\t"
    "v_mfma_f32_16x16x32_bf16 a[28:31], a[232:235], v[220:223], a[28:31]\n\t"
    "v_mfma_f32_16x16x32_bf16 a[16:19], a[236:239], v[192:195], a[16:19]\n\t"
    "buffer_load_dwordx4 v[144:147], v9, s[76:79], 0 offen offset:0 sc1\n\t"
    "v_mfma_f32_16x16x32_bf16 a[20:23], a[236:239], v[196:199], a[20:23]\n\t"
    "v_mfma_f32_16x16x32_bf16 a[24:27], a[236:239], v[200:203], a[24:27]\n\t"
    "buffer_load_dwordx4 v[132:135], v8, s[76:79], 0 offen offset:256 sc1\n\t"
    "v_mfma_f32_16x16x32_bf16 a[28:31], a[236:239], v[204:207], a[28:31]\n\t"
    "v_mfma_f32_16x16x32_bf16 a[32:35], a[240:243], v[208:211], a[32:35]\n\t"
    "buffer_load_dwordx4 v[148:151], v9, s[76:79], 0 offen offset:256 sc1\n\t"
    "v_mfma_f32_16x16x32_bf16 a[36:39], a[240:243], v[212:215], a[36:39]\n\t"
    "v_mfma_f32_16x16x32_bf16 a[40:43], a[240:243], v[216:219], a[40:43]\n\t"
    "v_mfma_f32_16x16x32_bf16 a[44:47], a[240:243], v[220:223], a[44:47]\n\t"
    "buffer_load_dwordx4 v[136:139], v8, s[76:79], 0 offen offset:512 sc1\n\t"
    "v_mfma_f32_16x16x32_bf16 a[32:35], a[244:247], v[192:195], a[32:35]\n\t"
    "v_mfma_f32_16x16x32_bf16 a[36:39], a[244:247], v[196:199], a[36:39]\n\t"
    "buffer_load_dwordx4 v[152:155], v9, s[76:79], 0 offen offset:512 sc1\n\t"
    "v_mfma_f32_16x16x32_bf16 a[40:43], a[244:247], v[200:203], a[40:43]\n\t"
    "v_mfma_f32_16x16x32_bf16 a[44:47], a[244:247], v[204:207], a[44:47]\n\t"
    "buffer_load_dwordx4 v[140:143], v8, s[76:79], 0 offen offset:768 sc1\n\t"
    "s_waitcnt lgkmcnt(0)\n\t"
    "v_mfma_f32_16x16x32_bf16 a[48:51], a[248:251], v[208:211], a[48:51]\n\t"
    "v_mfma_f32_16x16x32_bf16 a[52:55], a[248:251], v[212:215], a[52:55]\n\t"
    "buffer_load_dwordx4 v[156:159], v9, s[76:79], 0 offen offset:768 sc1\n\t"
    "v_mfma_f32_16x16x32_bf16 a[56:59], a[248:251], v[216:219], a[56:59]\n\t"
    "v_mfma_f32_16x16x32_bf16 a[60:63], a[248:251], v[220:223], a[60:63]\n\t"
    "v_mfma_f32_16x16x32_bf16 a[48:51], a[252:255], v[192:195], a[48:51]\n\t"
    "v_mfma_f32_16x16x32_bf16 a[52:55], a[252:255], v[196:199], a[52:55]\n\t"
    "v_mfma_f32_16x16x32_bf16 a[56:59], a[252:255], v[200:203], a[56:59]\n\t"
    "v_mfma_f32_16x16x32_bf16 a[60:63], a[252:255], v[204:207], a[60:63]\n\t"
    "v_xor_b32 v24, 0x4000, v24\n\t"
    "v_xor_b32 v25, 0x4000, v25\n\t"
    "s_waitcnt vmcnt(0)\n\t"
    "; ---- ghost2\n\t"
    "ghost2_%=:\n\t"
    "s_and_b32 s68, %0, 1\n\t"
    "s_lshl_b32 s84, s68, 13\n\t"
    "v_xor_b32 v38, 0x4000, v24\n\t"
    "v_add_u32 v38, s84, v38\n\t"
    "v_xor_b32 v36, 0x8000, v38\n\t"
    "s_lshl_b32 s84, s68, 15\n\t"
    "s_add_u32 s84, s84, 0x10000\n\t"
    "v_xor_b32 v37, 0x4000, v25\n\t"
    "v_and_b32 v37, 0x43ff, v37\n\t"
    "v_add_u32 v37, s84, v37\n\t"
    "s_cmp_lt_u32 %0, 2\n\t"
    "s_cselect_b64 s[94:95], -1, 0\n\t"
    "v_cndmask_b32 v39, v37, v26, s[94:95]\n\t"
    "v_cndmask_b32 v37, v26, v37, s[94:95]\n\t"
    "ds_write_b128 v36, v[28:31] offset:0\n\t"
    "ds_write_b128 v36, v[32:35] offset:1024\n\t"
    "s_waitcnt lgkmcnt(0)\n\t"
    "s_barrier\n\t"
    "ds_read_b128 v[160:163], v38 offset:0\n\t"
    "ds_read_b128 v[164:167], v38 offset:1024\n\t"
    "ds_write_b128 v37, a[64:67] offset:0\n\t"
    "ds_write_b128 v37, a[68:71] offset:1024\n\t"
    "ds_write_b128 v37, a[72:75] offset:2048\n\t"
    "ds_write_b128 v37, a[76:79] offset:3072\n\t"
    "ds_write_b128 v37, a[80:83] offset:4096\n\t"
    "ds_write_b128 v37, a[84:87] offset:5120\n\t"
    "ds_write_b128 v37, a[88:91] offset:6144\n\t"
    "ds_write_b128 v37, a[92:95] offset:7168\n\t"
    "ds_write_b128 v37, a[96:99] offset:8192\n\t"
    "ds_write_b128 v37, a[100:103] offset:9216\n\t"
    "ds_write_b128 v37, a[104:107] offset:10240\n\t"
    "ds_write_b128 v37, a[108:111] offset:11264\n\t"
    "ds_write_b128 v37, a[112:115] offset:12288\n\t"
    "ds_write_b128 v37, a[116:119] offset:13312\n\t"
    "ds_write_b128 v37, a[120:123] offset:14336\n\t"
    "ds_write_b128 v37, a[124:127] offset:15360\n\t"
    "v_log_f32 v40, v32\n\t"
    "s_waitcnt lgkmcnt(15)\n\t"
    "v_log_f32 v44, v164\n\t"
    "v_log_f32 v41, v33\n\t"
    "v_log_f32 v45, v165\n\t"
    "v_log_f32 v42, v34\n\t"
    "v_log_f32 v46, v166\n\t"
    "v_log_f32 v43, v35\n\t"
    "v_log_f32 v47, v167\n\t"
    "v_rcp_f32 v32, v32\n\t"
    "v_rcp_f32 v164, v164\n\t"
    "v_rcp_f32 v33, v33\n\t"
    "v_rcp_f32 v165, v165\n\t"
    "v_rcp_f32 v34, v34\n\t"
    "v_rcp_f32 v166, v166\n\t"
    "v_rcp_f32 v35, v35\n\t"
    "v_rcp_f32 v167, v167\n\t"
    "v_add_f32 v40, v40, v28\n\t"
    "v_add_f32 v44, v44, v160\n\t"
    "v_add_f32 v41, v41, v29\n\t"
    "v_add_f32 v45, v45, v161\n\t"
    "v_add_f32 v42, v42, v30\n\t"
    "v_add_f32 v46, v46, v162\n\t"
    "v_add_f32 v43, v43, v31\n\t"
    "v_add_f32 v47, v47, v163\n\t"
    "v_max_f32 v20, v40, v44\n\t"
    "v_max_f32 v21, v41, v45\n\t"
    "v_max_f32 v22, v42, v46\n\t"
    "v_max_f32 v23, v43, v47\n\t"
    "v_sub_f32 v168, v40, v20\n\t"
    "v_sub_f32 v172, v44, v20\n\t"
    "v_sub_f32 v169, v41, v21\n\t"
    "v_sub_f32 v173, v45, v21\n\t"
    "v_sub_f32 v170, v42, v22\n\t"
    "v_sub_f32 v174, v46, v22\n\t"
    "v_sub_f32 v171, v43, v23\n\t"
    "v_sub_f32 v175, v47, v23\n\t"
    "v_exp_f32 v168, v168\n\t"
    "v_exp_f32 v172, v172\n\t"
    "v_exp_f32 v169, v169\n\t"
    "v_exp_f32 v173, v173\n\t"
    "v_exp_f32 v170, v170\n\t"
    "v_exp_f32 v174, v174\n\t"
    "v_exp_f32 v171, v171\n\t"
    "v_exp_f32 v175, v175\n\t"
    "v_add_f32 v20, v168, v172\n\t"
    "v_add_f32 v21, v169, v173\n\t"
    "v_add_f32 v22, v170, v174\n\t"
    "v_add_f32 v23, v171, v175\n\t"
    "v_rcp_f32 v20, v20\n\t"
    "v_rcp_f32 v21, v21\n\t"
    "v_rcp_f32 v22, v22\n\t"
    "v_rcp_f32 v23, v23\n\t"
    "v_mul_f32 v168, v168, v32\n\t"
    "v_mul_f32 v172, v172, v164\n\t"
    "v_mul_f32 v169, v169, v33\n\t"
    "v_mul_f32 v173, v173, v165\n\t"
    "v_mul_f32 v170, v170, v34\n\t"
    "v_mul_f32 v174, v174, v166\n\t"
    "v_mul_f32 v171, v171, v35\n\t"
    "v_mul_f32 v175, v175, v167\n\t"
    "v_mul_f32 v176, v168, v20\n\t"
    "v_mul_f32 v184, v172, v20\n\t"
    "v_mul_f32 v178, v169, v21\n\t"
    "v_mul_f32 v186, v173, v21\n\t"
    "v_mul_f32 v180, v170, v22\n\t"
    "v_mul_f32 v188, v174, v22\n\t"
    "v_mul_f32 v182, v171, v23\n\t"
    "v_mul_f32 v190, v175, v23\n\t"
    "s_branch merge_%=\n\t"
    "; ---- empty\n\t"
    "empty_%=:\n\t"
    "v_mov_b32 v28, 0xf149f2ca\n\t"
    "v_mov_b32 v29, 0xf149f2ca\n\t"
    "v_mov_b32 v30, 0xf149f2ca\n\t"
    "v_mov_b32 v31, 0xf149f2ca\n\t"
    "v_mov_b32 v32, 1.0\n\t"
    "v_mov_b32 v33, 1.0\n\t"
    "v_mov_b32 v34, 1.0\n\t"
    "v_mov_b32 v35, 1.0\n\t"
    "v_xor_b32 v24, 0x4000, v24\n\t"
    "s_waitcnt vmcnt(0) lgkmcnt(0)\n\t"
    "s_barrier\n\t"
    "s_branch ghost2_%=\n\t"
    "; ---- merge0\n\t"
    "merge_%=:\n\t"
    "s_cmp_lg_u32 s81, 0\n\t"
    "s_cbranch_scc1 merge1_%=\n\t"
    "; ---- merge_os\n\t"
    "s_waitcnt vmcnt(9)\n\t"
    "v_pk_add_f32 v[64:65], v[64:65], v[68:69]\n\t"
    "v_pk_add_f32 v[66:67], v[66:67], v[70:71]\n\t"
    "v_pk_add_f32 v[72:73], v[72:73], v[76:77]\n\t"
    "v_pk_add_f32 v[74:75], v[74:75], v[78:79]\n\t"
    "v_pk_add_f32 v[80:81], v[80:81], v[84:85]\n\t"
    "v_pk_add_f32 v[82:83], v[82:83], v[86:87]\n\t"
    "v_pk_add_f32 v[88:89], v[88:89], v[92:93]\n\t"
    "v_pk_add_f32 v[90:91], v[90:91], v[94:95]\n\t"
    "v_pk_add_f32 v[64:65], v[64:65], v[72:73]\n\t"
    "v_pk_add_f32 v[66:67], v[66:67], v[74:75]\n\t"
    "v_pk_add_f32 v[80:81], v[80:81], v[88:89]\n\t"
    "v_pk_add_f32 v[82:83], v[82:83], v[90:91]\n\t"
    "v_pk_add_f32 v[64:65], v[64:65], v[80:81]\n\t"
    "v_pk_add_f32 v[66:67], v[66:67], v[82:83]\n\t"
    "v_cvt_pk_bf16_f32 v68, v64, v65\n\t"
    "v_cvt_pk_bf16_f32 v69, v66, v67\n\t"
    "v_pk_add_f32 v[96:97], v[96:97], v[100:101]\n\t"
    "v_pk_add_f32 v[98:99], v[98:99], v[102:103]\n\t"
    "v_pk_add_f32 v[104:105], v[104:105], v[108:109]\n\t"
    "v_pk_add_f32 v[106:107], v[106:107], v[110:111]\n\t"
    "v_pk_add_f32 v[112:113], v[112:113], v[116:117]\n\t"
    "v_pk_add_f32 v[114:115], v[114:115], v[118:119]\n\t"
    "s_waitcnt vmcnt(8)\n\t"
    "v_pk_add_f32 v[120:121], v[120:121], v[124:125]\n\t"
    "v_pk_add_f32 v[122:123], v[122:123], v[126:127]\n\t"
    "v_pk_add_f32 v[96:97], v[96:97], v[104:105]\n\t"
    "v_pk_add_f32 v[98:99], v[98:99], v[106:107]\n\t"
    "v_pk_add_f32 v[112:113], v[112:113], v[120:121]\n\t"
    "v_pk_add_f32 v[114:115], v[114:115], v[122:123]\n\t"
    "v_pk_add_f32 v[96:97], v[96:97], v[112:113]\n\t"
    "v_pk_add_f32 v[98:99], v[98:99], v[114:115]\n\t"
    "v_cvt_pk_bf16_f32 v100, v96, v97\n\t"
    "v_cvt_pk_bf16_f32 v101, v98, v99\n\t"
    "v_xor_b32 v36, 0x4000, v24\n\t"
    "v_and_b32 v36, 0x4000, v36\n\t"
    "v_add_u32 v36, 0x800, v36\n\t"
    "v_lshrrev_b32 v20, 1, v6\n\t"
    "v_add_u32 v20, v36, v20\n\t"
    "v_lshrrev_b32 v21, 1, v7\n\t"
    "v_add_u32 v21, v36, v21\n\t"
    "v_lshrrev_b32 v22, 1, %6\n\t"
    "v_add_u32 v36, v36, v22\n\t"
    "ds_write_b64 v20, v[68:69] offset:0\n\t"
    "ds_write_b64 v21, v[100:101] offset:0\n\t"
    "; ---- merge\n\t"
    "merge1_%=:\n\t"
    "v_accvgpr_read_b32 v192, a0\n\t"
    "v_accvgpr_read_b32 v193, a1\n\t"
    "v_accvgpr_read_b32 v194, a2\n\t"
    "v_accvgpr_read_b32 v195, a3\n\t"
    "v_accvgpr_read_b32 v196, a4\n\t"
    "v_accvgpr_read_b32 v197, a5\n\t"
    "v_accvgpr_read_b32 v198, a6\n\t"
    "v_accvgpr_read_b32 v199, a7\n\t"
    "v_accvgpr_read_b32 v200, a8\n\t"
    "v_accvgpr_read_b32 v201, a9\n\t"
    "v_accvgpr_read_b32 v202, a10\n\t"
    "v_accvgpr_read_b32 v203, a11\n\t"
    "v_accvgpr_read_b32 v204, a12\n\t"
    "v_accvgpr_read_b32 v205, a13\n\t"
    "v_accvgpr_read_b32 v206, a14\n\t"
    "v_accvgpr_read_b32 v207, a15\n\t"
    "v_accvgpr_read_b32 v208, a16\n\t"
    "v_accvgpr_read_b32 v209, a17\n\t"
    "v_accvgpr_read_b32 v210, a18\n\t"
    "v_accvgpr_read_b32 v211, a19\n\t"
    "v_accvgpr_read_b32 v212, a20\n\t"
    "v_accvgpr_read_b32 v213, a21\n\t"
    "v_accvgpr_read_b32 v214, a22\n\t"
    "v_accvgpr_read_b32 v215, a23\n\t"
    "v_accvgpr_read_b32 v216, a24\n\t"
    "v_accvgpr_read_b32 v217, a25\n\t"
    "v_accvgpr_read_b32 v218, a26\n\t"
    "v_accvgpr_read_b32 v219, a27\n\t"
    "v_accvgpr_read_b32 v220, a28\n\t"
    "v_accvgpr_read_b32 v221, a29\n\t"
    "v_accvgpr_read_b32 v222, a30\n\t"
    "v_accvgpr_read_b32 v223, a31\n\t"
    "s_waitcnt lgkmcnt(0)\n\t"
    "s_barrier\n\t"
    "ds_read_b64 v[48:49], v36 offset:0\n\t"
    "ds_read_b64 v[50:51], v36 offset:1024\n\t"
    "ds_read_b64 v[56:57], v36 offset:2048\n\t"
    "ds_read_b64 v[52:53], v36 offset:512\n\t"
    "ds_read_b64 v[54:55], v36 offset:1536\n\t"
    "ds_read_b64 v[60:61], v36 offset:2560\n\t"
    "s_waitcnt vmcnt(8)\n\t"
    "ds_read_b128 v[64:67], v39 offset:0\n\t"
    "ds_read_b128 v[80:83], v39 offset:4096\n\t"
    "ds_read_b128 v[68:71], v39 offset:1024\n\t"
    "ds_read_b128 v[84:87], v39 offset:5120\n\t"
    "ds_read_b128 v[72:75], v39 offset:2048\n\t"
    "ds_read_b128 v[88:91], v39 offset:6144\n\t"
    "ds_read_b128 v[76:79], v39 offset:3072\n\t"
    "ds_read_b128 v[92:95], v39 offset:7168\n\t"
    "ds_read_b128 v[96:99], v39 offset:8192\n\t"
    "ds_read_b128 v[112:115], v39 offset:12288\n\t"
    "ds_read_b128 v[100:103], v39 offset:9216\n\t"
    "ds_read_b128 v[116:119], v39 offset:13312\n\t"
    "ds_read_b128 v[104:107], v39 offset:10240\n\t"
    "ds_read_b128 v[120:123], v39 offset:14336\n\t"
    "ds_read_b128 v[108:111], v39 offset:11264\n\t"
    "ds_read_b128 v[124:127], v39 offset:15360\n\t"
    "v_accvgpr_read_b32 v224, a32\n\t"
    "v_accvgpr_read_b32 v225, a33\n\t"
    "v_accvgpr_read_b32 v226, a34\n\t"
    "v_accvgpr_read_b32 v227, a35\n\t"
    "v_accvgpr_read_b32 v228, a36\n\t"
    "v_accvgpr_read_b32 v229, a37\n\t"
    "v_accvgpr_read_b32 v230, a38\n\t"
    "v_accvgpr_read_b32 v231, a39\n\t"
    "v_accvgpr_read_b32 v232, a40\n\t"
    "v_accvgpr_read_b32 v233, a41\n\t"
    "v_accvgpr_read_b32 v234, a42\n\t"
    "v_accvgpr_read_b32 v235, a43\n\t"
    "v_accvgpr_read_b32 v236, a44\n\t"
    "v_accvgpr_read_b32 v237, a45\n\t"
    "v_accvgpr_read_b32 v238, a46\n\t"
    "v_accvgpr_read_b32 v239, a47\n\t"
    "v_accvgpr_read_b32 v240, a48\n\t"
    "v_accvgpr_read_b32 v241, a49\n\t"
    "v_accvgpr_read_b32 v242, a50\n\t"
    "v_accvgpr_read_b32 v243, a51\n\t"
    "v_accvgpr_read_b32 v244, a52\n\t"
    "v_accvgpr_read_b32 v245, a53\n\t"
    "v_accvgpr_read_b32 v246, a54\n\t"
    "v_accvgpr_read_b32 v247, a55\n\t"
    "v_accvgpr_read_b32 v248, a56\n\t"
    "v_accvgpr_read_b32 v249, a57\n\t"
    "v_accvgpr_read_b32 v250, a58\n\t"
    "v_accvgpr_read_b32 v251, a59\n\t"
    "v_accvgpr_read_b32 v252, a60\n\t"
    "v_accvgpr_read_b32 v253, a61\n\t"
    "v_accvgpr_read_b32 v254, a62\n\t"
    "v_accvgpr_read_b32 v255, a63\n\t"
    "s_mov_b32 s65, 0\n\t"
    "s_xor_b32 s66, s66, 0x4000\n\t"
    "s_cmp_eq_u32 s81, 0\n\t"
    "s_cbranch_scc0 merge3_%=\n\t"
    "; ---- merge2\n\t"
    "s_waitcnt lgkmcnt(0)\n\t"
    "s_barrier\n\t"
    "s_mov_b32 m0, s64\n\t"
    "v_pk_mul_f32 v[192:193], v[192:193], v[176:177] op_sel_hi:[1,0]\n\t"
    "v_pk_mul_f32 v[194:195], v[194:195], v[176:177] op_sel_hi:[1,0]\n\t"
    "v_pk_mul_f32 v[208:209], v[208:209], v[176:177] op_sel_hi:[1,0]\n\t"
    "v_pk_mul_f32 v[210:211], v[210:211], v[176:177] op_sel_hi:[1,0]\n\t"
    "buffer_load_dwordx4 %6, s[48:51], s70 offen offset:0 sc1 lds\n\t"
    "v_pk_fma_f32 v[192:193], v[64:65], v[184:185], v[192:193] op_sel_hi:[1,0,1]\n\t"
    "v_pk_fma_f32 v[194:195], v[66:67], v[184:185], v[194:195] op_sel_hi:[1,0,1]\n\t"
    "v_pk_fma_f32 v[208:209], v[80:81], v[184:185], v[208:209] op_sel_hi:[1,0,1]\n\t"
    "v_pk_fma_f32 v[210:211], v[82:83], v[184:185], v[210:211] op_sel_hi:[1,0,1]\n\t"
    "v_accvgpr_write_b32 a64, 0\n\t"
    "v_accvgpr_write_b32 a65, 0\n\t"
    "v_accvgpr_write_b32 a66, 0\n\t"
    "v_accvgpr_write_b32 a67, 0\n\t"
    "v_accvgpr_write_b32 a68, 0\n\t"
    "v_accvgpr_write_b32 a69, 0\n\t"
    "v_accvgpr_write_b32 a70, 0\n\t"
    "v_accvgpr_write_b32 a71, 0\n\t"
    "v_cvt_pk_bf16_f32 v40, v192, v193\n\t"
    "v_cvt_pk_bf16_f32 v41, v194, v195\n\t"
    "v_cvt_pk_bf16_f32 v42, v208, v209\n\t"
    "v_cvt_pk_bf16_f32 v43, v210, v211\n\t"
    "buffer_store_dwordx4 v[40:43], %6, s[56:59], 0 offen offset:0 sc1\n\t"
    "v_pk_mul_f32 v[196:197], v[196:197], v[178:179] op_sel_hi:[1,0]\n\t"
    "v_pk_mul_f32 v[198:199], v[198:199], v[178:179] op_sel_hi:[1,0]\n\t"
    "v_pk_mul_f32 v[212:213], v[212:213], v[178:179] op_sel_hi:[1,0]\n\t"
    "v_pk_mul_f32 v[214:215], v[214:215], v[178:179] op_sel_hi:[1,0]\n\t"
    "buffer_load_dwordx4 %6, s[48:51], s70 offen offset:1024 sc1 lds\n\t"
    "v_pk_fma_f32 v[196:197], v[68:69], v[186:187], v[196:197] op_sel_hi:[1,0,1]\n\t"
    "v_pk_fma_f32 v[198:199], v[70:71], v[186:187], v[198:199] op_sel_hi:[1,0,1]\n\t"
    "v_pk_fma_f32 v[212:213], v[84:85], v[186:187], v[212:213] op_sel_hi:[1,0,1]\n\t"
    "v_pk_fma_f32 v[214:215], v[86:87], v[186:187], v[214:215] op_sel_hi:[1,0,1]\n\t"
    "v_accvgpr_write_b32 a72, 0\n\t"
    "v_accvgpr_write_b32 a73, 0\n\t"
    "v_accvgpr_write_b32 a74, 0\n\t"
    "v_accvgpr_write_b32 a75, 0\n\t"
    "v_accvgpr_write_b32 a76, 0\n\t"
    "v_accvgpr_write_b32 a77, 0\n\t"
    "v_accvgpr_write_b32 a78, 0\n\t"
    "v_accvgpr_write_b32 a79, 0\n\t"
    "v_cvt_pk_bf16_f32 v44, v196, v197\n\t"
    "v_cvt_pk_bf16_f32 v45, v198, v199\n\t"
    "v_cvt_pk_bf16_f32 v46, v212, v213\n\t"
    "v_cvt_pk_bf16_f32 v47, v214, v215\n\t"
    "buffer_store_dwordx4 v[44:47], %6, s[56:59], 0 offen offset:2048 sc1\n\t"
    "v_pk_mul_f32 v[200:201], v[200:201], v[180:181] op_sel_hi:[1,0]\n\t"
    "v_pk_mul_f32 v[202:203], v[202:203], v[180:181] op_sel_hi:[1,0]\n\t"
    "v_pk_mul_f32 v[216:217], v[216:217], v[180:181] op_sel_hi:[1,0]\n\t"
    "v_pk_mul_f32 v[218:219], v[218:219], v[180:181] op_sel_hi:[1,0]\n\t"
    "buffer_load_dwordx4 %6, s[48:51], s70 offen offset:2048 sc1 lds\n\t"
    "v_pk_fma_f32 v[200:201], v[72:73], v[188:189], v[200:201] op_sel_hi:[1,0,1]\n\t"
    "v_pk_fma_f32 v[202:203], v[74:75], v[188:189], v[202:203] op_sel_hi:[1,0,1]\n\t"
    "v_pk_fma_f32 v[216:217], v[88:89], v[188:189], v[216:217] op_sel_hi:[1,0,1]\n\t"
    "v_pk_fma_f32 v[218:219], v[90:91], v[188:189], v[218:219] op_sel_hi:[1,0,1]\n\t"
    "v_accvgpr_write_b32 a80, 0\n\t"
    "v_accvgpr_write_b32 a81, 0\n\t"
    "v_accvgpr_write_b32 a82, 0\n\t"
    "v_accvgpr_write_b32 a83, 0\n\t"
    "v_accvgpr_write_b32 a84, 0\n\t"
    "v_accvgpr_write_b32 a85, 0\n\t"
    "v_accvgpr_write_b32 a86, 0\n\t"
    "v_accvgpr_write_b32 a87, 0\n\t"
    "v_cvt_pk_bf16_f32 v40, v200, v201\n\t"
    "v_cvt_pk_bf16_f32 v41, v202, v203\n\t"
    "v_cvt_pk_bf16_f32 v42, v216, v217\n\t"
    "v_cvt_pk_bf16_f32 v43, v218, v219\n\t"
    "buffer_store_dwordx4 v[40:43], %6, s[56:59], s85 offen offset:0 sc1\n\t"
    "v_pk_mul_f32 v[204:205], v[204:205], v[182:183] op_sel_hi:[1,0]\n\t"
    "v_pk_mul_f32 v[206:207], v[206:207], v[182:183] op_sel_hi:[1,0]\n\t"
    "v_pk_mul_f32 v[220:221], v[220:221], v[182:183] op_sel_hi:[1,0]\n\t"
    "v_pk_mul_f32 v[222:223], v[222:223], v[182:183] op_sel_hi:[1,0]\n\t"
    "buffer_load_dwordx4 %6, s[48:51], s70 offen offset:3072 sc1 lds\n\t"
    "v_pk_fma_f32 v[204:205], v[76:77], v[190:191], v[204:205] op_sel_hi:[1,0,1]\n\t"
    "v_pk_fma_f32 v[206:207], v[78:79], v[190:191], v[206:207] op_sel_hi:[1,0,1]\n\t"
    "v_pk_fma_f32 v[220:221], v[92:93], v[190:191], v[220:221] op_sel_hi:[1,0,1]\n\t"
    "v_pk_fma_f32 v[222:223], v[94:95], v[190:191], v[222:223] op_sel_hi:[1,0,1]\n\t"
    "v_accvgpr_write_b32 a88, 0\n\t"
    "v_accvgpr_write_b32 a89, 0\n\t"
    "v_accvgpr_write_b32 a90, 0\n\t"
    "v_accvgpr_write_b32 a91, 0\n\t"
    "v_accvgpr_write_b32 a92, 0\n\t"
    "v_accvgpr_write_b32 a93, 0\n\t"
    "v_accvgpr_write_b32 a94, 0\n\t"
    "v_accvgpr_write_b32 a95, 0\n\t"
    "v_cvt_pk_bf16_f32 v44, v204, v205\n\t"
    "v_cvt_pk_bf16_f32 v45, v206, v207\n\t"
    "v_cvt_pk_bf16_f32 v46, v220, v221\n\t"
    "v_cvt_pk_bf16_f32 v47, v222, v223\n\t"
    "buffer_store_dwordx4 v[44:47], %6, s[56:59], s85 offen offset:2048 sc1\n\t"
    "s_add_u32 m0, s64, 0x1000\n\t"
    "v_pk_mul_f32 v[224:225], v[224:225], v[176:177] op_sel_hi:[1,0]\n\t"
    "v_pk_mul_f32 v[226:227], v[226:227], v[176:177] op_sel_hi:[1,0]\n\t"
    "v_pk_mul_f32 v[240:241], v[240:241], v[176:177] op_sel_hi:[1,0]\n\t"
    "v_pk_mul_f32 v[242:243], v[242:243], v[176:177] op_sel_hi:[1,0]\n\t"
    "buffer_load_dwordx4 v27, s[48:51], s70 offen offset:0 sc1 lds\n\t"
    "v_pk_fma_f32 v[224:225], v[96:97], v[184:185], v[224:225] op_sel_hi:[1,0,1]\n\t"
    "v_pk_fma_f32 v[226:227], v[98:99], v[184:185], v[226:227] op_sel_hi:[1,0,1]\n\t"
    "v_pk_fma_f32 v[240:241], v[112:113], v[184:185], v[240:241] op_sel_hi:[1,0,1]\n\t"
    "v_pk_fma_f32 v[242:243], v[114:115], v[184:185], v[242:243] op_sel_hi:[1,0,1]\n\t"
    "v_accvgpr_write_b32 a96, 0\n\t"
    "v_accvgpr_write_b32 a97, 0\n\t"
    "v_accvgpr_write_b32 a98, 0\n\t"
    "v_accvgpr_write_b32 a99, 0\n\t"
    "v_accvgpr_write_b32 a100, 0\n\t"
    "v_accvgpr_write_b32 a101, 0\n\t"
    "v_accvgpr_write_b32 a102, 0\n\t"
    "v_accvgpr_write_b32 a103, 0\n\t"
    "v_cvt_pk_bf16_f32 v40, v224, v225\n\t"
    "v_cvt_pk_bf16_f32 v41, v226, v227\n\t"
    "v_cvt_pk_bf16_f32 v42, v240, v241\n\t"
    "v_cvt_pk_bf16_f32 v43, v242, v243\n\t"
    "buffer_store_dwordx4 v[40:43], %6, s[56:59], 0 offen offset:1024 sc1\n\t"
    "v_pk_mul_f32 v[228:229], v[228:229], v[178:179] op_sel_hi:[1,0]\n\t"
    "v_pk_mul_f32 v[230:231], v[230:231], v[178:179] op_sel_hi:[1,0]\n\t"
    "v_pk_mul_f32 v[244:245], v[244:245], v[178:179] op_sel_hi:[1,0]\n\t"
    "v_pk_mul_f32 v[246:247], v[246:247], v[178:179] op_sel_hi:[1,0]\n\t"
    "buffer_load_dwordx4 v27, s[48:51], s70 offen offset:1024 sc1 lds\n\t"
    "v_pk_fma_f32 v[228:229], v[100:101], v[186:187], v[228:229] op_sel_hi:[1,0,1]\n\t"
    "v_pk_fma_f32 v[230:231], v[102:103], v[186:187], v[230:231] op_sel_hi:[1,0,1]\n\t"
    "v_pk_fma_f32 v[244:245], v[116:117], v[186:187], v[244:245] op_sel_hi:[1,0,1]\n\t"
    "v_pk_fma_f32 v[246:247], v[118:119], v[186:187], v[246:247] op_sel_hi:[1,0,1]\n\t"
    "v_accvgpr_write_b32 a104, 0\n\t"
    "v_accvgpr_write_b32 a105, 0\n\t"
    "v_accvgpr_write_b32 a106, 0\n\t"
    "v_accvgpr_write_b32 a107, 0\n\t"
    "v_accvgpr_write_b32 a108, 0\n\t"
    "v_accvgpr_write_b32 a109, 0\n\t"
    "v_accvgpr_write_b32 a110, 0\n\t"
    "v_accvgpr_write_b32 a111, 0\n\t"
    "v_cvt_pk_bf16_f32 v44, v228, v229\n\t"
    "v_cvt_pk_bf16_f32 v45, v230, v231\n\t"
    "v_cvt_pk_bf16_f32 v46, v244, v245\n\t"
    "v_cvt_pk_bf16_f32 v47, v246, v247\n\t"
    "buffer_store_dwordx4 v[44:47], %6, s[56:59], 0 offen offset:3072 sc1\n\t"
    "v_pk_mul_f32 v[232:233], v[232:233], v[180:181] op_sel_hi:[1,0]\n\t"
    "v_pk_mul_f32 v[234:235], v[234:235], v[180:181] op_sel_hi:[1,0]\n\t"
    "v_pk_mul_f32 v[248:249], v[248:249], v[180:181] op_sel_hi:[1,0]\n\t"
    "v_pk_mul_f32 v[250:251], v[250:251], v[180:181] op_sel_hi:[1,0]\n\t"
    "buffer_load_dwordx4 v27, s[48:51], s70 offen offset:2048 sc1 lds\n\t"
    "v_pk_fma_f32 v[232:233], v[104:105], v[188:189], v[232:233] op_sel_hi:[1,0,1]\n\t"
    "v_pk_fma_f32 v[234:235], v[106:107], v[188:189], v[234:235] op_sel_hi:[1,0,1]\n\t"
    "v_pk_fma_f32 v[248:249], v[120:121], v[188:189], v[248:249] op_sel_hi:[1,0,1]\n\t"
    "v_pk_fma_f32 v[250:251], v[122:123], v[188:189], v[250:251] op_sel_hi:[1,0,1]\n\t"
    "v_accvgpr_write_b32 a112, 0\n\t"
    "v_accvgpr_write_b32 a113, 0\n\t"
    "v_accvgpr_write_b32 a114, 0\n\t"
    "v_accvgpr_write_b32 a115, 0\n\t"
    "v_accvgpr_write_b32 a116, 0\n\t"
    "v_accvgpr_write_b32 a117, 0\n\t"
    "v_accvgpr_write_b32 a118, 0\n\t"
    "v_accvgpr_write_b32 a119, 0\n\t"
    "v_cvt_pk_bf16_f32 v40, v232, v233\n\t"
    "v_cvt_pk_bf16_f32 v41, v234, v235\n\t"
    "v_cvt_pk_bf16_f32 v42, v248, v249\n\t"
    "v_cvt_pk_bf16_f32 v43, v250, v251\n\t"
    "buffer_store_dwordx4 v[40:43], %6, s[56:59], s85 offen offset:1024 sc1\n\t"
    "v_pk_mul_f32 v[236:237], v[236:237], v[182:183] op_sel_hi:[1,0]\n\t"
    "v_pk_mul_f32 v[238:239], v[238:239], v[182:183] op_sel_hi:[1,0]\n\t"
    "v_pk_mul_f32 v[252:253], v[252:253], v[182:183] op_sel_hi:[1,0]\n\t"
    "v_pk_mul_f32 v[254:255], v[254:255], v[182:183] op_sel_hi:[1,0]\n\t"
    "buffer_load_dwordx4 v27, s[48:51], s70 offen offset:3072 sc1 lds\n\t"
    "v_pk_fma_f32 v[236:237], v[108:109], v[190:191], v[236:237] op_sel_hi:[1,0,1]\n\t"
    "v_pk_fma_f32 v[238:239], v[110:111], v[190:191], v[238:239] op_sel_hi:[1,0,1]\n\t"
    "v_pk_fma_f32 v[252:253], v[124:125], v[190:191], v[252:253] op_sel_hi:[1,0,1]\n\t"
    "v_pk_fma_f32 v[254:255], v[126:127], v[190:191], v[254:255] op_sel_hi:[1,0,1]\n\t"
    "v_accvgpr_write_b32 a120, 0\n\t"
    "v_accvgpr_write_b32 a121, 0\n\t"
    "v_accvgpr_write_b32 a122, 0\n\t"
    "v_accvgpr_write_b32 a123, 0\n\t"
    "v_accvgpr_write_b32 a124, 0\n\t"
    "v_accvgpr_write_b32 a125, 0\n\t"
    "v_accvgpr_write_b32 a126, 0\n\t"
    "v_accvgpr_write_b32 a127, 0\n\t"
    "v_cvt_pk_bf16_f32 v44, v236, v237\n\t"
    "v_cvt_pk_bf16_f32 v45, v238, v239\n\t"
    "v_cvt_pk_bf16_f32 v46, v252, v253\n\t"
    "v_cvt_pk_bf16_f32 v47, v254, v255\n\t"
    "buffer_store_dwordx4 v[44:47], %6, s[56:59], s85 offen offset:3072 sc1\n\t"
    "s_add_u32 s70, s70, 0x10000\n\t"
    "s_xor_b32 s64, s64, 0x4000\n\t"
    "s_cmp_eq_u32 s70, s71\n\t"
    "s_cselect_b32 s70, 0, s70\n\t"
    "s_and_b32 s68, %15, 7\n\t"
    "s_lshl_b32 s68, s68, 1\n\t"
    "s_lshr_b32 s84, %0, 1\n\t"
    "s_add_u32 s68, s68, s84\n\t"
    "s_mul_i32 s68, s68, 0x24000\n\t"
    "s_and_b32 s84, %0, 1\n\t"
    "s_lshl_b32 s84, s84, 13\n\t"
    "s_add_u32 s68, s68, s84\n\t"
    "s_sub_u32 s68, 0xc00000, s68\n\t"
    "s_sub_u32 s76, s40, s68\n\t"
    "s_subb_u32 s77, s41, 0\n\t"
    "s_mov_b32 s78, 0x24000\n\t"
    "s_cmp_lg_u32 s61, 0\n\t"
    "s_cselect_b64 s[96:97], -1, 0\n\t"
    "v_mov_b32 v58, 0\n\t"
    "v_mov_b32 v59, 0\n\t"
    "v_mov_b32 v62, 0\n\t"
    "v_mov_b32 v63, 0\n\t"
    "v_cmp_gt_u32 vcc, 0x100, %6\n\t"
    "s_nop 0\n\t"
    "s_and_b64 s[94:95], vcc, s[96:97]\n\t"
    "s_waitcnt vmcnt(16)\n\t"
    "v_mfma_f32_16x16x32_bf16 v[64:67], v[48:51], v[128:131], 0\n\t"
    "v_mfma_f32_16x16x32_bf16 v[68:71], v[52:55], v[128:131], 0\n\t"
    "v_mfma_f32_16x16x32_bf16 v[72:75], v[48:51], v[132:135], 0\n\t"
    "v_mfma_f32_16x16x32_bf16 v[76:79], v[52:55], v[132:135], 0\n\t"
    "v_mfma_f32_16x16x32_bf16 v[80:83], v[48:51], v[136:139], 0\n\t"
    "v_mfma_f32_16x16x32_bf16 v[84:87], v[52:55], v[136:139], 0\n\t"
    "v_mfma_f32_16x16x32_bf16 v[88:91], v[48:51], v[140:143], 0\n\t"
    "v_mfma_f32_16x16x32_bf16 v[92:95], v[52:55], v[140:143], 0\n\t"
    "v_mfma_f32_16x16x32_bf16 v[64:67], v[56:59], v[144:147], v[64:67]\n\t"
    "v_mfma_f32_16x16x32_bf16 v[68:71], v[60:63], v[144:147], v[68:71]\n\t"
    "v_mfma_f32_16x16x32_bf16 v[72:75], v[56:59], v[148:151], v[72:75]\n\t"
    "v_mfma_f32_16x16x32_bf16 v[76:79], v[60:63], v[148:151], v[76:79]\n\t"
    "v_mfma_f32_16x16x32_bf16 v[80:83], v[56:59], v[152:155], v[80:83]\n\t"
    "v_mfma_f32_16x16x32_bf16 v[84:87], v[60:63], v[152:155], v[84:87]\n\t"
    "v_mfma_f32_16x16x32_bf16 v[88:91], v[56:59], v[156:159], v[88:91]\n\t"
    "v_mfma_f32_16x16x32_bf16 v[92:95], v[60:63], v[156:159], v[92:95]\n\t"
    "s_nop 1\n\t"
    "v_mov_b32 v96, v68\n\t"
    "v_mov_b32 v100, v68\n\t"
    "v_mov_b32 v97, v76\n\t"
    "v_mov_b32 v101, v76\n\t"
    "v_mov_b32 v98, v84\n\t"
    "v_mov_b32 v102, v84\n\t"
    "v_mov_b32 v99, v92\n\t"
    "v_mov_b32 v103, v92\n\t"
    "v_permlane16_swap_b32 v96, v100\n\t"
    "v_permlane16_swap_b32 v97, v101\n\t"
    "v_permlane16_swap_b32 v98, v102\n\t"
    "v_permlane16_swap_b32 v99, v103\n\t"
    "v_add_f32 v96, 0x358637bd, v100\n\t"
    "v_add_f32 v97, 0x358637bd, v101\n\t"
    "v_add_f32 v98, 0x358637bd, v102\n\t"
    "v_add_f32 v99, 0x358637bd, v103\n\t"
    "v_rcp_f32 v96, v96\n\t"
    "v_rcp_f32 v97, v97\n\t"
    "v_rcp_f32 v98, v98\n\t"
    "v_rcp_f32 v99, v99\n\t"
    "v_mul_f32 v64, v96, v64\n\t"
    "v_mul_f32 v65, v96, v65\n\t"
    "v_mul_f32 v66, v96, v66\n\t"
    "v_mul_f32 v67, v96, v67\n\t"
    "v_mul_f32 v68, v96, v68\n\t"
    "v_mul_f32 v69, v96, v69\n\t"
    "v_mul_f32 v70, v96, v70\n\t"
    "v_mul_f32 v71, v96, v71\n\t"
    "v_cvt_pk_bf16_f32 v64, v64, v65\n\t"
    "v_cvt_pk_bf16_f32 v65, v66, v67\n\t"
    "v_cvt_pk_bf16_f32 v68, v68, v69\n\t"
    "v_cvt_pk_bf16_f32 v69, v70, v71\n\t"
    "v_cndmask_b32 v66, v68, v65, s[96:97]\n\t"
    "v_cndmask_b32 v64, v64, v68, s[94:95]\n\t"
    "v_cndmask_b32 v65, v65, v69, s[94:95]\n\t"
    "s_mov_b32 s68, 0\n\t"
    "buffer_store_dwordx2 v[64:65], %13, s[76:79], s68 offen offset:0 sc1\n\t"
    "buffer_store_dword v66, %14, s[76:79], s68 offen offset:0 sc1\n\t"
    "v_mul_f32 v72, v97, v72\n\t"
    "v_mul_f32 v73, v97, v73\n\t"
    "v_mul_f32 v74, v97, v74\n\t"
    "v_mul_f32 v75, v97, v75\n\t"
    "v_mul_f32 v76, v97, v76\n\t"
    "v_mul_f32 v77, v97, v77\n\t"
    "v_mul_f32 v78, v97, v78\n\t"
    "v_mul_f32 v79, v97, v79\n\t"
    "v_cvt_pk_bf16_f32 v72, v72, v73\n\t"
    "v_cvt_pk_bf16_f32 v73, v74, v75\n\t"
    "v_cvt_pk_bf16_f32 v76, v76, v77\n\t"
    "v_cvt_pk_bf16_f32 v77, v78, v79\n\t"
    "v_cndmask_b32 v74, v76, v73, s[96:97]\n\t"
    "v_cndmask_b32 v72, v72, v76, s[94:95]\n\t"
    "v_cndmask_b32 v73, v73, v77, s[94:95]\n\t"
    "s_mov_b32 s68, 0x800\n\t"
    "buffer_store_dwordx2 v[72:73], %13, s[76:79], s68 offen offset:0 sc1\n\t"
    "buffer_store_dword v74, %14, s[76:79], s68 offen offset:0 sc1\n\t"
    "v_mul_f32 v80, v98, v80\n\t"
    "v_mul_f32 v81, v98, v81\n\t"
    "v_mul_f32 v82, v98, v82\n\t"
    "v_mul_f32 v83, v98, v83\n\t"
    "v_mul_f32 v84, v98, v84\n\t"
    "v_mul_f32 v85, v98, v85\n\t"
    "v_mul_f32 v86, v98, v86\n\t"
    "v_mul_f32 v87, v98, v87\n\t"
    "v_cvt_pk_bf16_f32 v80, v80, v81\n\t"
    "v_cvt_pk_bf16_f32 v81, v82, v83\n\t"
    "v_cvt_pk_bf16_f32 v84, v84, v85\n\t"
    "v_cvt_pk_bf16_f32 v85, v86, v87\n\t"
    "v_cndmask_b32 v82, v84, v81, s[96:97]\n\t"
    "v_cndmask_b32 v80, v80, v84, s[94:95]\n\t"
    "v_cndmask_b32 v81, v81, v85, s[94:95]\n\t"
    "s_mov_b32 s68, 0x1000\n\t"
    "buffer_store_dwordx2 v[80:81], %13, s[76:79], s68 offen offset:0 sc1\n\t"
    "buffer_store_dword v82, %14, s[76:79], s68 offen offset:0 sc1\n\t"
    "v_mul_f32 v88, v99, v88\n\t"
    "v_mul_f32 v89, v99, v89\n\t"
    "v_mul_f32 v90, v99, v90\n\t"
    "v_mul_f32 v91, v99, v91\n\t"
    "v_mul_f32 v92, v99, v92\n\t"
    "v_mul_f32 v93, v99, v93\n\t"
    "v_mul_f32 v94, v99, v94\n\t"
    "v_mul_f32 v95, v99, v95\n\t"
    "v_cvt_pk_bf16_f32 v88, v88, v89\n\t"
    "v_cvt_pk_bf16_f32 v89, v90, v91\n\t"
    "v_cvt_pk_bf16_f32 v92, v92, v93\n\t"
    "v_cvt_pk_bf16_f32 v93, v94, v95\n\t"
    "v_cndmask_b32 v90, v92, v89, s[96:97]\n\t"
    "v_cndmask_b32 v88, v88, v92, s[94:95]\n\t"
    "v_cndmask_b32 v89, v89, v93, s[94:95]\n\t"
    "s_mov_b32 s68, 0x1800\n\t"
    "buffer_store_dwordx2 v[88:89], %13, s[76:79], s68 offen offset:0 sc1\n\t"
    "buffer_store_dword v90, %14, s[76:79], s68 offen offset:0 sc1\n\t"
    "s_mov_b32 s74, 0\n\t"
    "s_mov_b32 s78, 0\n\t"
    "; ---- next\n\t"
    "s_getpc_b64 s[72:73]\n\t"
    "kmain_pf2:\n\t"
    "s_mov_b32 s74, kmain_code_end-kmain_pf2\n\t"
    "s_lshr_b32 s68, %15, 3\n\t"
    "s_lshl_b32 s68, s68, 12\n\t"
    "s_lshl_b32 s84, %0, 10\n\t"
    "s_add_u32 s68, s68, s84\n\t"
    "buffer_load_dword v13, %6, s[72:75], s68 offen\n\t"
    "s_mov_b32 s74, 0\n\t"
    "s_mov_b32 s81, 1\n\t"
    "s_mov_b32 s69, %5\n\t"
    "s_mov_b64 s[56:57], %3\n\t"
    "s_branch blk_start_%=\n\t"
    "; ---- merge3\n\t"
    "merge3_%=:\n\t"
    "v_pk_mul_f32 v[192:193], v[192:193], v[176:177] op_sel_hi:[1,0]\n\t"
    "v_pk_mul_f32 v[194:195], v[194:195], v[176:177] op_sel_hi:[1,0]\n\t"
    "v_pk_mul_f32 v[208:209], v[208:209], v[176:177] op_sel_hi:[1,0]\n\t"
    "v_pk_mul_f32 v[210:211], v[210:211], v[176:177] op_sel_hi:[1,0]\n\t"
    "s_waitcnt lgkmcnt(12)\n\t"
    "v_pk_fma_f32 v[192:193], v[64:65], v[184:185], v[192:193] op_sel_hi:[1,0,1]\n\t"
    "v_pk_fma_f32 v[194:195], v[66:67], v[184:185], v[194:195] op_sel_hi:[1,0,1]\n\t"
    "v_pk_fma_f32 v[208:209], v[80:81], v[184:185], v[208:209] op_sel_hi:[1,0,1]\n\t"
    "v_pk_fma_f32 v[210:211], v[82:83], v[184:185], v[210:211] op_sel_hi:[1,0,1]\n\t"
    "v_cvt_pk_bf16_f32 v40, v192, v193\n\t"
    "v_cvt_pk_bf16_f32 v41, v194, v195\n\t"
    "v_cvt_pk_bf16_f32 v42, v208, v209\n\t"
    "v_cvt_pk_bf16_f32 v43, v210, v211\n\t"
    "buffer_store_dwordx4 v[40:43], %6, s[56:59], 0 offen offset:0 sc1\n\t"
    "v_pk_mul_f32 v[196:197], v[196:197], v[178:179] op_sel_hi:[1,0]\n\t"
    "v_pk_mul_f32 v[198:199], v[198:199], v[178:179] op_sel_hi:[1,0]\n\t"
    "v_pk_mul_f32 v[212:213], v[212:213], v[178:179] op_sel_hi:[1,0]\n\t"
    "v_pk_mul_f32 v[214:215], v[214:215], v[178:179] op_sel_hi:[1,0]\n\t"
    "v_pk_fma_f32 v[196:197], v[68:69], v[186:187], v[196:197] op_sel_hi:[1,0,1]\n\t"
    "v_pk_fma_f32 v[198:199], v[70:71], v[186:187], v[198:199] op_sel_hi:[1,0,1]\n\t"
    "v_pk_fma_f32 v[212:213], v[84:85], v[186:187], v[212:213] op_sel_hi:[1,0,1]\n\t"
    "v_pk_fma_f32 v[214:215], v[86:87], v[186:187], v[214:215] op_sel_hi:[1,0,1]\n\t"
    "v_cvt_pk_bf16_f32 v44, v196, v197\n\t"
    "v_cvt_pk_bf16_f32 v45, v198, v199\n\t"
    "v_cvt_pk_bf16_f32 v46, v212, v213\n\t"
    "v_cvt_pk_bf16_f32 v47, v214, v215\n\t"
    "buffer_store_dwordx4 v[44:47], %6, s[56:59], 0 offen offset:2048 sc1\n\t"
    "v_pk_mul_f32 v[200:201], v[200:201], v[180:181] op_sel_hi:[1,0]\n\t"
    "v_pk_mul_f32 v[202:203], v[202:203], v[180:181] op_sel_hi:[1,0]\n\t"
    "v_pk_mul_f32 v[216:217], v[216:217], v[180:181] op_sel_hi:[1,0]\n\t"
    "v_pk_mul_f32 v[218:219], v[218:219], v[180:181] op_sel_hi:[1,0]\n\t"
    "s_waitcnt lgkmcnt(8)\n\t"
    "v_pk_fma_f32 v[200:201], v[72:73], v[188:189], v[200:201] op_sel_hi:[1,0,1]\n\t"
    "v_pk_fma_f32 v[202:203], v[74:75], v[188:189], v[202:203] op_sel_hi:[1,0,1]\n\t"
    "v_pk_fma_f32 v[216:217], v[88:89], v[188:189], v[216:217] op_sel_hi:[1,0,1]\n\t"
    "v_pk_fma_f32 v[218:219], v[90:91], v[188:189], v[218:219] op_sel_hi:[1,0,1]\n\t"
    "v_cvt_pk_bf16_f32 v40, v200, v201\n\t"
    "v_cvt_pk_bf16_f32 v41, v202, v203\n\t"
    "v_cvt_pk_bf16_f32 v42, v216, v217\n\t"
    "v_cvt_pk_bf16_f32 v43, v218, v219\n\t"
    "buffer_store_dwordx4 v[40:43], %6, s[56:59], s85 offen offset:0 sc1\n\t"
    "v_pk_mul_f32 v[204:205], v[204:205], v[182:183] op_sel_hi:[1,0]\n\t"
    "v_pk_mul_f32 v[206:207], v[206:207], v[182:183] op_sel_hi:[1,0]\n\t"
    "v_pk_mul_f32 v[220:221], v[220:221], v[182:183] op_sel_hi:[1,0]\n\t"
    "v_pk_mul_f32 v[222:223], v[222:223], v[182:183] op_sel_hi:[1,0]\n\t"
    "v_pk_fma_f32 v[204:205], v[76:77], v[190:191], v[204:205] op_sel_hi:[1,0,1]\n\t"
    "v_pk_fma_f32 v[206:207], v[78:79], v[190:191], v[206:207] op_sel_hi:[1,0,1]\n\t"
    "v_pk_fma_f32 v[220:221], v[92:93], v[190:191], v[220:221] op_sel_hi:[1,0,1]\n\t"
    "v_pk_fma_f32 v[222:223], v[94:95], v[190:191], v[222:223] op_sel_hi:[1,0,1]\n\t"
    "v_cvt_pk_bf16_f32 v44, v204, v205\n\t"
    "v_cvt_pk_bf16_f32 v45, v206, v207\n\t"
    "v_cvt_pk_bf16_f32 v46, v220, v221\n\t"
    "v_cvt_pk_bf16_f32 v47, v222, v223\n\t"
    "buffer_store_dwordx4 v[44:47], %6, s[56:59], s85 offen offset:2048 sc1\n\t"
    "v_pk_mul_f32 v[224:225], v[224:225], v[176:177] op_sel_hi:[1,0]\n\t"
    "v_pk_mul_f32 v[226:227], v[226:227], v[176:177] op_sel_hi:[1,0]\n\t"
    "v_pk_mul_f32 v[240:241], v[240:241], v[176:177] op_sel_hi:[1,0]\n\t"
    "v_pk_mul_f32 v[242:243], v[242:243], v[176:177] op_sel_hi:[1,0]\n\t"
    "s_waitcnt lgkmcnt(4)\n\t"
    "v_pk_fma_f32 v[224:225], v[96:97], v[184:185], v[224:225] op_sel_hi:[1,0,1]\n\t"
    "v_pk_fma_f32 v[226:227], v[98:99], v[184:185], v[226:227] op_sel_hi:[1,0,1]\n\t"
    "v_pk_fma_f32 v[240:241], v[112:113], v[184:185], v[240:241] op_sel_hi:[1,0,1]\n\t"
    "v_pk_fma_f32 v[242:243], v[114:115], v[184:185], v[242:243] op_sel_hi:[1,0,1]\n\t"
    "v_cvt_pk_bf16_f32 v40, v224, v225\n\t"
    "v_cvt_pk_bf16_f32 v41, v226, v227\n\t"
    "v_cvt_pk_bf16_f32 v42, v240, v241\n\t"
    "v_cvt_pk_bf16_f32 v43, v242, v243\n\t"
    "buffer_store_dwordx4 v[40:43], %6, s[56:59], 0 offen offset:1024 sc1\n\t"
    "v_pk_mul_f32 v[228:229], v[228:229], v[178:179] op_sel_hi:[1,0]\n\t"
    "v_pk_mul_f32 v[230:231], v[230:231], v[178:179] op_sel_hi:[1,0]\n\t"
    "v_pk_mul_f32 v[244:245], v[244:245], v[178:179] op_sel_hi:[1,0]\n\t"
    "v_pk_mul_f32 v[246:247], v[246:247], v[178:179] op_sel_hi:[1,0]\n\t"
    "v_pk_fma_f32 v[228:229], v[100:101], v[186:187], v[228:229] op_sel_hi:[1,0,1]\n\t"
    "v_pk_fma_f32 v[230:231], v[102:103], v[186:187], v[230:231] op_sel_hi:[1,0,1]\n\t"
    "v_pk_fma_f32 v[244:245], v[116:117], v[186:187], v[244:245] op_sel_hi:[1,0,1]\n\t"
    "v_pk_fma_f32 v[246:247], v[118:119], v[186:187], v[246:247] op_sel_hi:[1,0,1]\n\t"
    "v_cvt_pk_bf16_f32 v44, v228, v229\n\t"
    "v_cvt_pk_bf16_f32 v45, v230, v231\n\t"
    "v_cvt_pk_bf16_f32 v46, v244, v245\n\t"
    "v_cvt_pk_bf16_f32 v47, v246, v247\n\t"
    "buffer_store_dwordx4 v[44:47], %6, s[56:59], 0 offen offset:3072 sc1\n\t"
    "v_pk_mul_f32 v[232:233], v[232:233], v[180:181] op_sel_hi:[1,0]\n\t"
    "v_pk_mul_f32 v[234:235], v[234:235], v[180:181] op_sel_hi:[1,0]\n\t"
    "v_pk_mul_f32 v[248:249], v[248:249], v[180:181] op_sel_hi:[1,0]\n\t"
    "v_pk_mul_f32 v[250:251], v[250:251], v[180:181] op_sel_hi:[1,0]\n\t"
    "s_waitcnt lgkmcnt(0)\n\t"
    "v_pk_fma_f32 v[232:233], v[104:105], v[188:189], v[232:233] op_sel_hi:[1,0,1]\n\t"
    "v_pk_fma_f32 v[234:235], v[106:107], v[188:189], v[234:235] op_sel_hi:[1,0,1]\n\t"
    "v_pk_fma_f32 v[248:249], v[120:121], v[188:189], v[248:249] op_sel_hi:[1,0,1]\n\t"
    "v_pk_fma_f32 v[250:251], v[122:123], v[188:189], v[250:251] op_sel_hi:[1,0,1]\n\t"
    "v_cvt_pk_bf16_f32 v40, v232, v233\n\t"
    "v_cvt_pk_bf16_f32 v41, v234, v235\n\t"
    "v_cvt_pk_bf16_f32 v42, v248, v249\n\t"
    "v_cvt_pk_bf16_f32 v43, v250, v251\n\t"
    "buffer_store_dwordx4 v[40:43], %6, s[56:59], s85 offen offset:1024 sc1\n\t"
    "v_pk_mul_f32 v[236:237], v[236:237], v[182:183] op_sel_hi:[1,0]\n\t"
    "v_pk_mul_f32 v[238:239], v[238:239], v[182:183] op_sel_hi:[1,0]\n\t"
    "v_pk_mul_f32 v[252:253], v[252:253], v[182:183] op_sel_hi:[1,0]\n\t"
    "v_pk_mul_f32 v[254:255], v[254:255], v[182:183] op_sel_hi:[1,0]\n\t"
    "v_pk_fma_f32 v[236:237], v[108:109], v[190:191], v[236:237] op_sel_hi:[1,0,1]\n\t"
    "v_pk_fma_f32 v[238:239], v[110:111], v[190:191], v[238:239] op_sel_hi:[1,0,1]\n\t"
    "v_pk_fma_f32 v[252:253], v[124:125], v[190:191], v[252:253] op_sel_hi:[1,0,1]\n\t"
    "v_pk_fma_f32 v[254:255], v[126:127], v[190:191], v[254:255] op_sel_hi:[1,0,1]\n\t"
    "v_cvt_pk_bf16_f32 v44, v236, v237\n\t"
    "v_cvt_pk_bf16_f32 v45, v238, v239\n\t"
    "v_cvt_pk_bf16_f32 v46, v252, v253\n\t"
    "v_cvt_pk_bf16_f32 v47, v254, v255\n\t"
    "buffer_store_dwordx4 v[44:47], %6, s[56:59], s85 offen offset:3072 sc1\n\t"
    "s_branch att_end_%=\n\t"
    "; ---- rare_b1\n\t"
    "rare_b1_%=:\n\t"
    "s_mov_b32 s80, 0\n\t"
    "s_branch rare0_%=\n\t"
    "; ---- rare_bl\n\t"
    "rare_bl_%=:\n\t"
    "s_mov_b32 s80, 1\n\t"
    "s_branch rare0_%=\n\t"
    "; ---- rare0\n\t"
    "rare0_%=:\n\t"
    "v_max3_f32 v36, v64, v65, v66\n\t"
    "v_max3_f32 v37, v68, v69, v70\n\t"
    "v_max3_f32 v38, v72, v73, v74\n\t"
    "v_max3_f32 v39, v76, v77, v78\n\t"
    "v_max3_f32 v36, v36, v67, v80\n\t"
    "v_max3_f32 v37, v37, v71, v84\n\t"
    "v_max3_f32 v38, v38, v75, v88\n\t"
    "v_max3_f32 v39, v39, v79, v92\n\t"
    "v_max3_f32 v36, v36, v81, v82\n\t"
    "v_max3_f32 v37, v37, v85, v86\n\t"
    "v_max3_f32 v38, v38, v89, v90\n\t"
    "v_max3_f32 v39, v39, v93, v94\n\t"
    "v_max3_f32 v36, v36, v83, v96\n\t"
    "v_max3_f32 v37, v37, v87, v100\n\t"
    "v_max3_f32 v38, v38, v91, v104\n\t"
    "v_max3_f32 v39, v39, v95, v108\n\t"
    "v_max3_f32 v36, v36, v97, v98\n\t"
    "v_max3_f32 v37, v37, v101, v102\n\t"
    "v_max3_f32 v38, v38, v105, v106\n\t"
    "v_max3_f32 v39, v39, v109, v110\n\t"
    "v_max3_f32 v36, v36, v99, v112\n\t"
    "v_max3_f32 v37, v37, v103, v116\n\t"
    "v_max3_f32 v38, v38, v107, v120\n\t"
    "v_max3_f32 v39, v39, v111, v124\n\t"
    "v_max3_f32 v36, v36, v113, v114\n\t"
    "v_max3_f32 v37, v37, v117, v118\n\t"
    "v_max3_f32 v38, v38, v121, v122\n\t"
    "v_max3_f32 v39, v39, v125, v126\n\t"
    "v_max_f32 v36, v36, v115\n\t"
    "v_max_f32 v37, v37, v119\n\t"
    "v_max_f32 v38, v38, v123\n\t"
    "v_max_f32 v39, v39, v127\n\t"
    "v_mov_b32 v20, v36\n\t"
    "v_mov_b32 v21, v37\n\t"
    "v_mov_b32 v22, v38\n\t"
    "v_mov_b32 v23, v39\n\t"
    "v_permlane16_swap_b32 v36, v20\n\t"
    "v_permlane16_swap_b32 v37, v21\n\t"
    "v_permlane16_swap_b32 v38, v22\n\t"
    "v_permlane16_swap_b32 v39, v23\n\t"
    "v_max_f32 v36, v36, v20\n\t"
    "v_max_f32 v37, v37, v21\n\t"
    "v_max_f32 v38, v38, v22\n\t"
    "v_max_f32 v39, v39, v23\n\t"
    "v_mov_b32 v20, v36\n\t"
    "v_mov_b32 v21, v37\n\t"
    "v_mov_b32 v22, v38\n\t"
    "v_mov_b32 v23, v39\n\t"
    "v_permlane32_swap_b32 v36, v20\n\t"
    "v_permlane32_swap_b32 v37, v21\n\t"
    "v_permlane32_swap_b32 v38, v22\n\t"
    "v_permlane32_swap_b32 v39, v23\n\t"
    "v_max_f32 v36, v36, v20\n\t"
    "v_max_f32 v37, v37, v21\n\t"
    "v_max_f32 v38, v38, v22\n\t"
    "v_max_f32 v39, v39, v23\n\t"
    "v_add_f32 v36, 0x42c80000, v36\n\t"
    "v_add_f32 v37, 0x42c80000, v37\n\t"
    "v_add_f32 v38, 0x42c80000, v38\n\t"
    "v_add_f32 v39, 0x42c80000, v39\n\t"
    "v_ceil_f32 v36, v36\n\t"
    "v_ceil_f32 v37, v37\n\t"
    "v_ceil_f32 v38, v38\n\t"
    "v_ceil_f32 v39, v39\n\t"
    "v_max_f32 v36, 0, v36\n\t"
    "v_max_f32 v37, 0, v37\n\t"
    "v_max_f32 v38, 0, v38\n\t"
    "v_max_f32 v39, 0, v39\n\t"
    "v_cvt_i32_f32 v44, -v36\n\t"
    "v_cvt_i32_f32 v45, -v37\n\t"
    "v_cvt_i32_f32 v46, -v38\n\t"
    "v_cvt_i32_f32 v47, -v39\n\t"
    "v_add_f32 v28, v28, v36\n\t"
    "v_add_f32 v29, v29, v37\n\t"
    "v_add_f32 v30, v30, v38\n\t"
    "v_add_f32 v31, v31, v39\n\t"
    "v_sub_f32 v48, 0xc2c80000, v28\n\t"
    "v_sub_f32 v49, 0xc2c80000, v28\n\t"
    "v_sub_f32 v50, 0xc2c80000, v28\n\t"
    "v_sub_f32 v51, 0xc2c80000, v28\n\t"
    "v_sub_f32 v52, 0xc2c80000, v29\n\t"
    "v_sub_f32 v53, 0xc2c80000, v29\n\t"
    "v_sub_f32 v54, 0xc2c80000, v29\n\t"
    "v_sub_f32 v55, 0xc2c80000, v29\n\t"
    "v_sub_f32 v56, 0xc2c80000, v30\n\t"
    "v_sub_f32 v57, 0xc2c80000, v30\n\t"
    "v_sub_f32 v58, 0xc2c80000, v30\n\t"
    "v_sub_f32 v59, 0xc2c80000, v30\n\t"
    "v_sub_f32 v60, 0xc2c80000, v31\n\t"
    "v_sub_f32 v61, 0xc2c80000, v31\n\t"
    "v_sub_f32 v62, 0xc2c80000, v31\n\t"
    "v_sub_f32 v63, 0xc2c80000, v31\n\t"
    "v_ldexp_f32 v32, v32, v44\n\t"
    "v_ldexp_f32 v33, v33, v45\n\t"
    "v_ldexp_f32 v34, v34, v46\n\t"
    "v_ldexp_f32 v35, v35, v47\n\t"
    "v_accvgpr_read_b32 v40, a0\n\t"
    "v_accvgpr_read_b32 v41, a1\n\t"
    "v_accvgpr_read_b32 v42, a2\n\t"
    "v_accvgpr_read_b32 v43, a3\n\t"
    "v_ldexp_f32 v40, v40, v44\n\t"
    "v_ldexp_f32 v41, v41, v44\n\t"
    "v_ldexp_f32 v42, v42, v44\n\t"
    "v_ldexp_f32 v43, v43, v44\n\t"
    "v_accvgpr_write_b32 a0, v40\n\t"
    "v_accvgpr_write_b32 a1, v41\n\t"
    "v_accvgpr_write_b32 a2, v42\n\t"
    "v_accvgpr_write_b32 a3, v43\n\t"
    "v_accvgpr_read_b32 v40, a4\n\t"
    "v_accvgpr_read_b32 v41, a5\n\t"
    "v_accvgpr_read_b32 v42, a6\n\t"
    "v_accvgpr_read_b32 v43, a7\n\t"
    "v_ldexp_f32 v40, v40, v45\n\t"
    "v_ldexp_f32 v41, v41, v45\n\t"
    "v_ldexp_f32 v42, v42, v45\n\t"
    "v_ldexp_f32 v43, v43, v45\n\t"
    "v_accvgpr_write_b32 a4, v40\n\t"
    "v_accvgpr_write_b32 a5, v41\n\t"
    "v_accvgpr_write_b32 a6, v42\n\t"
    "v_accvgpr_write_b32 a7, v43\n\t"
    "v_accvgpr_read_b32 v40, a8\n\t"
    "v_accvgpr_read_b32 v41, a9\n\t"
    "v_accvgpr_read_b32 v42, a10\n\t"
    "v_accvgpr_read_b32 v43, a11\n\t"
    "v_ldexp_f32 v40, v40, v46\n\t"
    "v_ldexp_f32 v41, v41, v46\n\t"
    "v_ldexp_f32 v42, v42, v46\n\t"
    "v_ldexp_f32 v43, v43, v46\n\t"
    "v_accvgpr_write_b32 a8, v40\n\t"
    "v_accvgpr_write_b32 a9, v41\n\t"
    "v_accvgpr_write_b32 a10, v42\n\t"
    "v_accvgpr_write_b32 a11, v43\n\t"
    "v_accvgpr_read_b32 v40, a12\n\t"
    "v_accvgpr_read_b32 v41, a13\n\t"
    "v_accvgpr_read_b32 v42, a14\n\t"
    "v_accvgpr_read_b32 v43, a15\n\t"
    "v_ldexp_f32 v40, v40, v47\n\t"
    "v_ldexp_f32 v41, v41, v47\n\t"
    "v_ldexp_f32 v42, v42, v47\n\t"
    "v_ldexp_f32 v43, v43, v47\n\t"
    "v_accvgpr_write_b32 a12, v40\n\t"
    "v_accvgpr_write_b32 a13, v41\n\t"
    "v_accvgpr_write_b32 a14, v42\n\t"
    "v_accvgpr_write_b32 a15, v43\n\t"
    "v_accvgpr_read_b32 v40, a16\n\t"
    "v_accvgpr_read_b32 v41, a17\n\t"
    "v_accvgpr_read_b32 v42, a18\n\t"
    "v_accvgpr_read_b32 v43, a19\n\t"
    "v_ldexp_f32 v40, v40, v44\n\t"
    "v_ldexp_f32 v41, v41, v44\n\t"
    "v_ldexp_f32 v42, v42, v44\n\t"
    "v_ldexp_f32 v43, v43, v44\n\t"
    "v_accvgpr_write_b32 a16, v40\n\t"
    "v_accvgpr_write_b32 a17, v41\n\t"
    "v_accvgpr_write_b32 a18, v42\n\t"
    "v_accvgpr_write_b32 a19, v43\n\t"
    "v_accvgpr_read_b32 v40, a20\n\t"
    "v_accvgpr_read_b32 v41, a21\n\t"
    "v_accvgpr_read_b32 v42, a22\n\t"
    "v_accvgpr_read_b32 v43, a23\n\t"
    "v_ldexp_f32 v40, v40, v45\n\t"
    "v_ldexp_f32 v41, v41, v45\n\t"
    "v_ldexp_f32 v42, v42, v45\n\t"
    "v_ldexp_f32 v43, v43, v45\n\t"
    "v_accvgpr_write_b32 a20, v40\n\t"
    "v_accvgpr_write_b32 a21, v41\n\t"
    "v_accvgpr_write_b32 a22, v42\n\t"
    "v_accvgpr_write_b32 a23, v43\n\t"
    "v_accvgpr_read_b32 v40, a24\n\t"
    "v_accvgpr_read_b32 v41, a25\n\t"
    "v_accvgpr_read_b32 v42, a26\n\t"
    "v_accvgpr_read_b32 v43, a27\n\t"
    "v_ldexp_f32 v40, v40, v46\n\t"
    "v_ldexp_f32 v41, v41, v46\n\t"
    "v_ldexp_f32 v42, v42, v46\n\t"
    "v_ldexp_f32 v43, v43, v46\n\t"
    "v_accvgpr_write_b32 a24, v40\n\t"
    "v_accvgpr_write_b32 a25, v41\n\t"
    "v_accvgpr_write_b32 a26, v42\n\t"
    "v_accvgpr_write_b32 a27, v43\n\t"
    "v_accvgpr_read_b32 v40, a28\n\t"
    "v_accvgpr_read_b32 v41, a29\n\t"
    "v_accvgpr_read_b32 v42, a30\n\t"
    "v_accvgpr_read_b32 v43, a31\n\t"
    "v_ldexp_f32 v40, v40, v47\n\t"
    "v_ldexp_f32 v41, v41, v47\n\t"
    "v_ldexp_f32 v42, v42, v47\n\t"
    "v_ldexp_f32 v43, v43, v47\n\t"
    "v_accvgpr_write_b32 a28, v40\n\t"
    "v_accvgpr_write_b32 a29, v41\n\t"
    "v_accvgpr_write_b32 a30, v42\n\t"
    "v_accvgpr_write_b32 a31, v43\n\t"
    "v_accvgpr_read_b32 v40, a32\n\t"
    "v_accvgpr_read_b32 v41, a33\n\t"
    "v_accvgpr_read_b32 v42, a34\n\t"
    "v_accvgpr_read_b32 v43, a35\n\t"
    "v_ldexp_f32 v40, v40, v44\n\t"
    "v_ldexp_f32 v41, v41, v44\n\t"
    "v_ldexp_f32 v42, v42, v44\n\t"
    "v_ldexp_f32 v43, v43, v44\n\t"
    "v_accvgpr_write_b32 a32, v40\n\t"
    "v_accvgpr_write_b32 a33, v41\n\t"
    "v_accvgpr_write_b32 a34, v42\n\t"
    "v_accvgpr_write_b32 a35, v43\n\t"
    "v_accvgpr_read_b32 v40, a36\n\t"
    "v_accvgpr_read_b32 v41, a37\n\t"
    "v_accvgpr_read_b32 v42, a38\n\t"
    "v_accvgpr_read_b32 v43, a39\n\t"
    "v_ldexp_f32 v40, v40, v45\n\t"
    "v_ldexp_f32 v41, v41, v45\n\t"
    "v_ldexp_f32 v42, v42, v45\n\t"
    "v_ldexp_f32 v43, v43, v45\n\t"
    "v_accvgpr_write_b32 a36, v40\n\t"
    "v_accvgpr_write_b32 a37, v41\n\t"
    "v_accvgpr_write_b32 a38, v42\n\t"
    "v_accvgpr_write_b32 a39, v43\n\t"
    "v_accvgpr_read_b32 v40, a40\n\t"
    "v_accvgpr_read_b32 v41, a41\n\t"
    "v_accvgpr_read_b32 v42, a42\n\t"
    "v_accvgpr_read_b32 v43, a43\n\t"
    "v_ldexp_f32 v40, v40, v46\n\t"
    "v_ldexp_f32 v41, v41, v46\n\t"
    "v_ldexp_f32 v42, v42, v46\n\t"
    "v_ldexp_f32 v43, v43, v46\n\t"
    "v_accvgpr_write_b32 a40, v40\n\t"
    "v_accvgpr_write_b32 a41, v41\n\t"
    "v_accvgpr_write_b32 a42, v42\n\t"
    "v_accvgpr_write_b32 a43, v43\n\t"
    "v_accvgpr_read_b32 v40, a44\n\t"
    "v_accvgpr_read_b32 v41, a45\n\t"
    "v_accvgpr_read_b32 v42, a46\n\t"
    "v_accvgpr_read_b32 v43, a47\n\t"
    "v_ldexp_f32 v40, v40, v47\n\t"
    "v_ldexp_f32 v41, v41, v47\n\t"
    "v_ldexp_f32 v42, v42, v47\n\t"
    "v_ldexp_f32 v43, v43, v47\n\t"
    "v_accvgpr_write_b32 a44, v40\n\t"
    "v_accvgpr_write_b32 a45, v41\n\t"
    "v_accvgpr_write_b32 a46, v42\n\t"
    "v_accvgpr_write_b32 a47, v43\n\t"
    "v_accvgpr_read_b32 v40, a48\n\t"
    "v_accvgpr_read_b32 v41, a49\n\t"
    "v_accvgpr_read_b32 v42, a50\n\t"
    "v_accvgpr_read_b32 v43, a51\n\t"
    "v_ldexp_f32 v40, v40, v44\n\t"
    "v_ldexp_f32 v41, v41, v44\n\t"
    "v_ldexp_f32 v42, v42, v44\n\t"
    "v_ldexp_f32 v43, v43, v44\n\t"
    "v_accvgpr_write_b32 a48, v40\n\t"
    "v_accvgpr_write_b32 a49, v41\n\t"
    "v_accvgpr_write_b32 a50, v42\n\t"
    "v_accvgpr_write_b32 a51, v43\n\t"
    "v_accvgpr_read_b32 v40, a52\n\t"
    "v_accvgpr_read_b32 v41, a53\n\t"
    "v_accvgpr_read_b32 v42, a54\n\t"
    "v_accvgpr_read_b32 v43, a55\n\t"
    "v_ldexp_f32 v40, v40, v45\n\t"
    "v_ldexp_f32 v41, v41, v45\n\t"
    "v_ldexp_f32 v42, v42, v45\n\t"
    "v_ldexp_f32 v43, v43, v45\n\t"
    "v_accvgpr_write_b32 a52, v40\n\t"
    "v_accvgpr_write_b32 a53, v41\n\t"
    "v_accvgpr_write_b32 a54, v42\n\t"
    "v_accvgpr_write_b32 a55, v43\n\t"
    "v_accvgpr_read_b32 v40, a56\n\t"
    "v_accvgpr_read_b32 v41, a57\n\t"
    "v_accvgpr_read_b32 v42, a58\n\t"
    "v_accvgpr_read_b32 v43, a59\n\t"
    "v_ldexp_f32 v40, v40, v46\n\t"
    "v_ldexp_f32 v41, v41, v46\n\t"
    "v_ldexp_f32 v42, v42, v46\n\t"
    "v_ldexp_f32 v43, v43, v46\n\t"
    "v_accvgpr_write_b32 a56, v40\n\t"
    "v_accvgpr_write_b32 a57, v41\n\t"
    "v_accvgpr_write_b32 a58, v42\n\t"
    "v_accvgpr_write_b32 a59, v43\n\t"
    "v_accvgpr_read_b32 v40, a60\n\t"
    "v_accvgpr_read_b32 v41, a61\n\t"
    "v_accvgpr_read_b32 v42, a62\n\t"
    "v_accvgpr_read_b32 v43, a63\n\t"
    "v_ldexp_f32 v40, v40, v47\n\t"
    "v_ldexp_f32 v41, v41, v47\n\t"
    "v_ldexp_f32 v42, v42, v47\n\t"
    "v_ldexp_f32 v43, v43, v47\n\t"
    "v_accvgpr_write_b32 a60, v40\n\t"
    "v_accvgpr_write_b32 a61, v41\n\t"
    "v_accvgpr_write_b32 a62, v42\n\t"
    "v_accvgpr_write_b32 a63, v43\n\t"
    "v_accvgpr_read_b32 v40, a64\n\t"
    "v_accvgpr_read_b32 v41, a65\n\t"
    "v_accvgpr_read_b32 v42, a66\n\t"
    "v_accvgpr_read_b32 v43, a67\n\t"
    "v_ldexp_f32 v40, v40, v44\n\t"
    "v_ldexp_f32 v41, v41, v44\n\t"
    "v_ldexp_f32 v42, v42, v44\n\t"
    "v_ldexp_f32 v43, v43, v44\n\t"
    "v_accvgpr_write_b32 a64, v40\n\t"
    "v_accvgpr_write_b32 a65, v41\n\t"
    "v_accvgpr_write_b32 a66, v42\n\t"
    "v_accvgpr_write_b32 a67, v43\n\t"
    "v_accvgpr_read_b32 v40, a68\n\t"
    "v_accvgpr_read_b32 v41, a69\n\t"
    "v_accvgpr_read_b32 v42, a70\n\t"
    "v_accvgpr_read_b32 v43, a71\n\t"
    "v_ldexp_f32 v40, v40, v45\n\t"
    "v_ldexp_f32 v41, v41, v45\n\t"
    "v_ldexp_f32 v42, v42, v45\n\t"
    "v_ldexp_f32 v43, v43, v45\n\t"
    "v_accvgpr_write_b32 a68, v40\n\t"
    "v_accvgpr_write_b32 a69, v41\n\t"
    "v_accvgpr_write_b32 a70, v42\n\t"
    "v_accvgpr_write_b32 a71, v43\n\t"
    "v_accvgpr_read_b32 v40, a72\n\t"
    "v_accvgpr_read_b32 v41, a73\n\t"
    "v_accvgpr_read_b32 v42, a74\n\t"
    "v_accvgpr_read_b32 v43, a75\n\t"
    "v_ldexp_f32 v40, v40, v46\n\t"
    "v_ldexp_f32 v41, v41, v46\n\t"
    "v_ldexp_f32 v42, v42, v46\n\t"
    "v_ldexp_f32 v43, v43, v46\n\t"
    "v_accvgpr_write_b32 a72, v40\n\t"
    "v_accvgpr_write_b32 a73, v41\n\t"
    "v_accvgpr_write_b32 a74, v42\n\t"
    "v_accvgpr_write_b32 a75, v43\n\t"
    "v_accvgpr_read_b32 v40, a76\n\t"
    "v_accvgpr_read_b32 v41, a77\n\t"
    "v_accvgpr_read_b32 v42, a78\n\t"
    "v_accvgpr_read_b32 v43, a79\n\t"
    "v_ldexp_f32 v40, v40, v47\n\t"
    "v_ldexp_f32 v41, v41, v47\n\t"
    "v_ldexp_f32 v42, v42, v47\n\t"
    "v_ldexp_f32 v43, v43, v47\n\t"
    "v_accvgpr_write_b32 a76, v40\n\t"
    "v_accvgpr_write_b32 a77, v41\n\t"
    "v_accvgpr_write_b32 a78, v42\n\t"
    "v_accvgpr_write_b32 a79, v43\n\t"
    "v_accvgpr_read_b32 v40, a80\n\t"
    "v_accvgpr_read_b32 v41, a81\n\t"
    "v_accvgpr_read_b32 v42, a82\n\t"
    "v_accvgpr_read_b32 v43, a83\n\t"
    "v_ldexp_f32 v40, v40, v44\n\t"
    "v_ldexp_f32 v41, v41, v44\n\t"
    "v_ldexp_f32 v42, v42, v44\n\t"
    "v_ldexp_f32 v43, v43, v44\n\t"
    "v_accvgpr_write_b32 a80, v40\n\t"
    "v_accvgpr_write_b32 a81, v41\n\t"
    "v_accvgpr_write_b32 a82, v42\n\t"
    "v_accvgpr_write_b32 a83, v43\n\t"
    "v_accvgpr_read_b32 v40, a84\n\t"
    "v_accvgpr_read_b32 v41, a85\n\t"
    "v_accvgpr_read_b32 v42, a86\n\t"
    "v_accvgpr_read_b32 v43, a87\n\t"
    "v_ldexp_f32 v40, v40, v45\n\t"
    "v_ldexp_f32 v41, v41, v45\n\t"
    "v_ldexp_f32 v42, v42, v45\n\t"
    "v_ldexp_f32 v43, v43, v45\n\t"
    "v_accvgpr_write_b32 a84, v40\n\t"
    "v_accvgpr_write_b32 a85, v41\n\t"
    "v_accvgpr_write_b32 a86, v42\n\t"
    "v_accvgpr_write_b32 a87, v43\n\t"
    "v_accvgpr_read_b32 v40, a88\n\t"
    "v_accvgpr_read_b32 v41, a89\n\t"
    "v_accvgpr_read_b32 v42, a90\n\t"
    "v_accvgpr_read_b32 v43, a91\n\t"
    "v_ldexp_f32 v40, v40, v46\n\t"
    "v_ldexp_f32 v41, v41, v46\n\t"
    "v_ldexp_f32 v42, v42, v46\n\t"
    "v_ldexp_f32 v43, v43, v46\n\t"
    "v_accvgpr_write_b32 a88, v40\n\t"
    "v_accvgpr_write_b32 a89, v41\n\t"
    "v_accvgpr_write_b32 a90, v42\n\t"
    "v_accvgpr_write_b32 a91, v43\n\t"
    "v_accvgpr_read_b32 v40, a92\n\t"
    "v_accvgpr_read_b32 v41, a93\n\t"
    "v_accvgpr_read_b32 v42, a94\n\t"
    "v_accvgpr_read_b32 v43, a95\n\t"
    "v_ldexp_f32 v40, v40, v47\n\t"
    "v_ldexp_f32 v41, v41, v47\n\t"
    "v_ldexp_f32 v42, v42, v47\n\t"
    "v_ldexp_f32 v43, v43, v47\n\t"
    "v_accvgpr_write_b32 a92, v40\n\t"
    "v_accvgpr_write_b32 a93, v41\n\t"
    "v_accvgpr_write_b32 a94, v42\n\t"
    "v_accvgpr_write_b32 a95, v43\n\t"
    "v_accvgpr_read_b32 v40, a96\n\t"
    "v_accvgpr_read_b32 v41, a97\n\t"
    "v_accvgpr_read_b32 v42, a98\n\t"
    "v_accvgpr_read_b32 v43, a99\n\t"
    "v_ldexp_f32 v40, v40, v44\n\t"
    "v_ldexp_f32 v41, v41, v44\n\t"
    "v_ldexp_f32 v42, v42, v44\n\t"
    "v_ldexp_f32 v43, v43, v44\n\t"
    "v_accvgpr_write_b32 a96, v40\n\t"
    "v_accvgpr_write_b32 a97, v41\n\t"
    "v_accvgpr_write_b32 a98, v42\n\t"
    "v_accvgpr_write_b32 a99, v43\n\t"
    "v_accvgpr_read_b32 v40, a100\n\t"
    "v_accvgpr_read_b32 v41, a101\n\t"
    "v_accvgpr_read_b32 v42, a102\n\t"
    "v_accvgpr_read_b32 v43, a103\n\t"
    "v_ldexp_f32 v40, v40, v45\n\t"
    "v_ldexp_f32 v41, v41, v45\n\t"
    "v_ldexp_f32 v42, v42, v45\n\t"
    "v_ldexp_f32 v43, v43, v45\n\t"
    "v_accvgpr_write_b32 a100, v40\n\t"
    "v_accvgpr_write_b32 a101, v41\n\t"
    "v_accvgpr_write_b32 a102, v42\n\t"
    "v_accvgpr_write_b32 a103, v43\n\t"
    "v_accvgpr_read_b32 v40, a104\n\t"
    "v_accvgpr_read_b32 v41, a105\n\t"
    "v_accvgpr_read_b32 v42, a106\n\t"
    "v_accvgpr_read_b32 v43, a107\n\t"
    "v_ldexp_f32 v40, v40, v46\n\t"
    "v_ldexp_f32 v41, v41, v46\n\t"
    "v_ldexp_f32 v42, v42, v46\n\t"
    "v_ldexp_f32 v43, v43, v46\n\t"
    "v_accvgpr_write_b32 a104, v40\n\t"
    "v_accvgpr_write_b32 a105, v41\n\t"
    "v_accvgpr_write_b32 a106, v42\n\t"
    "v_accvgpr_write_b32 a107, v43\n\t"
    "v_accvgpr_read_b32 v40, a108\n\t"
    "v_accvgpr_read_b32 v41, a109\n\t"
    "v_accvgpr_read_b32 v42, a110\n\t"
    "v_accvgpr_read_b32 v43, a111\n\t"
    "v_ldexp_f32 v40, v40, v47\n\t"
    "v_ldexp_f32 v41, v41, v47\n\t"
    "v_ldexp_f32 v42, v42, v47\n\t"
    "v_ldexp_f32 v43, v43, v47\n\t"
    "v_accvgpr_write_b32 a108, v40\n\t"
    "v_accvgpr_write_b32 a109, v41\n\t"
    "v_accvgpr_write_b32 a110, v42\n\t"
    "v_accvgpr_write_b32 a111, v43\n\t"
    "v_accvgpr_read_b32 v40, a112\n\t"
    "v_accvgpr_read_b32 v41, a113\n\t"
    "v_accvgpr_read_b32 v42, a114\n\t"
    "v_accvgpr_read_b32 v43, a115\n\t"
    "v_ldexp_f32 v40, v40, v44\n\t"
    "v_ldexp_f32 v41, v41, v44\n\t"
    "v_ldexp_f32 v42, v42, v44\n\t"
    "v_ldexp_f32 v43, v43, v44\n\t"
    "v_accvgpr_write_b32 a112, v40\n\t"
    "v_accvgpr_write_b32 a113, v41\n\t"
    "v_accvgpr_write_b32 a114, v42\n\t"
    "v_accvgpr_write_b32 a115, v43\n\t"
    "v_accvgpr_read_b32 v40, a116\n\t"
    "v_accvgpr_read_b32 v41, a117\n\t"
    "v_accvgpr_read_b32 v42, a118\n\t"
    "v_accvgpr_read_b32 v43, a119\n\t"
    "v_ldexp_f32 v40, v40, v45\n\t"
    "v_ldexp_f32 v41, v41, v45\n\t"
    "v_ldexp_f32 v42, v42, v45\n\t"
    "v_ldexp_f32 v43, v43, v45\n\t"
    "v_accvgpr_write_b32 a116, v40\n\t"
    "v_accvgpr_write_b32 a117, v41\n\t"
    "v_accvgpr_write_b32 a118, v42\n\t"
    "v_accvgpr_write_b32 a119, v43\n\t"
    "v_accvgpr_read_b32 v40, a120\n\t"
    "v_accvgpr_read_b32 v41, a121\n\t"
    "v_accvgpr_read_b32 v42, a122\n\t"
    "v_accvgpr_read_b32 v43, a123\n\t"
    "v_ldexp_f32 v40, v40, v46\n\t"
    "v_ldexp_f32 v41, v41, v46\n\t"
    "v_ldexp_f32 v42, v42, v46\n\t"
    "v_ldexp_f32 v43, v43, v46\n\t"
    "v_accvgpr_write_b32 a120, v40\n\t"
    "v_accvgpr_write_b32 a121, v41\n\t"
    "v_accvgpr_write_b32 a122, v42\n\t"
    "v_accvgpr_write_b32 a123, v43\n\t"
    "v_accvgpr_read_b32 v40, a124\n\t"
    "v_accvgpr_read_b32 v41, a125\n\t"
    "v_accvgpr_read_b32 v42, a126\n\t"
    "v_accvgpr_read_b32 v43, a127\n\t"
    "v_ldexp_f32 v40, v40, v47\n\t"
    "v_ldexp_f32 v41, v41, v47\n\t"
    "v_ldexp_f32 v42, v42, v47\n\t"
    "v_ldexp_f32 v43, v43, v47\n\t"
    "v_accvgpr_write_b32 a124, v40\n\t"
    "v_accvgpr_write_b32 a125, v41\n\t"
    "v_accvgpr_write_b32 a126, v42\n\t"
    "v_accvgpr_write_b32 a127, v43\n\t"
    "v_sub_f32 v64, v64, v36\n\t"
    "v_sub_f32 v65, v65, v36\n\t"
    "v_sub_f32 v66, v66, v36\n\t"
    "v_sub_f32 v67, v67, v36\n\t"
    "v_sub_f32 v68, v68, v37\n\t"
    "v_sub_f32 v69, v69, v37\n\t"
    "v_sub_f32 v70, v70, v37\n\t"
    "v_sub_f32 v71, v71, v37\n\t"
    "v_sub_f32 v72, v72, v38\n\t"
    "v_sub_f32 v73, v73, v38\n\t"
    "v_sub_f32 v74, v74, v38\n\t"
    "v_sub_f32 v75, v75, v38\n\t"
    "v_sub_f32 v76, v76, v39\n\t"
    "v_sub_f32 v77, v77, v39\n\t"
    "v_sub_f32 v78, v78, v39\n\t"
    "v_sub_f32 v79, v79, v39\n\t"
    "v_sub_f32 v80, v80, v36\n\t"
    "v_sub_f32 v81, v81, v36\n\t"
    "v_sub_f32 v82, v82, v36\n\t"
    "v_sub_f32 v83, v83, v36\n\t"
    "v_sub_f32 v84, v84, v37\n\t"
    "v_sub_f32 v85, v85, v37\n\t"
    "v_sub_f32 v86, v86, v37\n\t"
    "v_sub_f32 v87, v87, v37\n\t"
    "v_sub_f32 v88, v88, v38\n\t"
    "v_sub_f32 v89, v89, v38\n\t"
    "v_sub_f32 v90, v90, v38\n\t"
    "v_sub_f32 v91, v91, v38\n\t"
    "v_sub_f32 v92, v92, v39\n\t"
    "v_sub_f32 v93, v93, v39\n\t"
    "v_sub_f32 v94, v94, v39\n\t"
    "v_sub_f32 v95, v95, v39\n\t"
    "v_sub_f32 v96, v96, v36\n\t"
    "v_sub_f32 v97, v97, v36\n\t"
    "v_sub_f32 v98, v98, v36\n\t"
    "v_sub_f32 v99, v99, v36\n\t"
    "v_sub_f32 v100, v100, v37\n\t"
    "v_sub_f32 v101, v101, v37\n\t"
    "v_sub_f32 v102, v102, v37\n\t"
    "v_sub_f32 v103, v103, v37\n\t"
    "v_sub_f32 v104, v104, v38\n\t"
    "v_sub_f32 v105, v105, v38\n\t"
    "v_sub_f32 v106, v106, v38\n\t"
    "v_sub_f32 v107, v107, v38\n\t"
    "v_sub_f32 v108, v108, v39\n\t"
    "v_sub_f32 v109, v109, v39\n\t"
    "v_sub_f32 v110, v110, v39\n\t"
    "v_sub_f32 v111, v111, v39\n\t"
    "v_sub_f32 v112, v112, v36\n\t"
    "v_sub_f32 v113, v113, v36\n\t"
    "v_sub_f32 v114, v114, v36\n\t"
    "v_sub_f32 v115, v115, v36\n\t"
    "v_sub_f32 v116, v116, v37\n\t"
    "v_sub_f32 v117, v117, v37\n\t"
    "v_sub_f32 v118, v118, v37\n\t"
    "v_sub_f32 v119, v119, v37\n\t"
    "v_sub_f32 v120, v120, v38\n\t"
    "v_sub_f32 v121, v121, v38\n\t"
    "v_sub_f32 v122, v122, v38\n\t"
    "v_sub_f32 v123, v123, v38\n\t"
    "v_sub_f32 v124, v124, v39\n\t"
    "v_sub_f32 v125, v125, v39\n\t"
    "v_sub_f32 v126, v126, v39\n\t"
    "v_sub_f32 v127, v127, v39\n\t"
    "v_sub_f32 v128, v128, v36\n\t"
    "v_sub_f32 v129, v129, v36\n\t"
    "v_sub_f32 v130, v130, v36\n\t"
    "v_sub_f32 v131, v131, v36\n\t"
    "v_sub_f32 v132, v132, v37\n\t"
    "v_sub_f32 v133, v133, v37\n\t"
    "v_sub_f32 v134, v134, v37\n\t"
    "v_sub_f32 v135, v135, v37\n\t"
    "v_sub_f32 v136, v136, v38\n\t"
    "v_sub_f32 v137, v137, v38\n\t"
    "v_sub_f32 v138, v138, v38\n\t"
    "v_sub_f32 v139, v139, v38\n\t"
    "v_sub_f32 v140, v140, v39\n\t"
    "v_sub_f32 v141, v141, v39\n\t"
    "v_sub_f32 v142, v142, v39\n\t"
    "v_sub_f32 v143, v143, v39\n\t"
    "v_sub_f32 v144, v144, v36\n\t"
    "v_sub_f32 v145, v145, v36\n\t"
    "v_sub_f32 v146, v146, v36\n\t"
    "v_sub_f32 v147, v147, v36\n\t"
    "v_sub_f32 v148, v148, v37\n\t"
    "v_sub_f32 v149, v149, v37\n\t"
    "v_sub_f32 v150, v150, v37\n\t"
    "v_sub_f32 v151, v151, v37\n\t"
    "v_sub_f32 v152, v152, v38\n\t"
    "v_sub_f32 v153, v153, v38\n\t"
    "v_sub_f32 v154, v154, v38\n\t"
    "v_sub_f32 v155, v155, v38\n\t"
    "v_sub_f32 v156, v156, v39\n\t"
    "v_sub_f32 v157, v157, v39\n\t"
    "v_sub_f32 v158, v158, v39\n\t"
    "v_sub_f32 v159, v159, v39\n\t"
    "v_sub_f32 v160, v160, v36\n\t"
    "v_sub_f32 v161, v161, v36\n\t"
    "v_sub_f32 v162, v162, v36\n\t"
    "v_sub_f32 v163, v163, v36\n\t"
    "v_sub_f32 v164, v164, v37\n\t"
    "v_sub_f32 v165, v165, v37\n\t"
    "v_sub_f32 v166, v166, v37\n\t"
    "v_sub_f32 v167, v167, v37\n\t"
    "v_sub_f32 v168, v168, v38\n\t"
    "v_sub_f32 v169, v169, v38\n\t"
    "v_sub_f32 v170, v170, v38\n\t"
    "v_sub_f32 v171, v171, v38\n\t"
    "v_sub_f32 v172, v172, v39\n\t"
    "v_sub_f32 v173, v173, v39\n\t"
    "v_sub_f32 v174, v174, v39\n\t"
    "v_sub_f32 v175, v175, v39\n\t"
    "v_sub_f32 v176, v176, v36\n\t"
    "v_sub_f32 v177, v177, v36\n\t"
    "v_sub_f32 v178, v178, v36\n\t"
    "v_sub_f32 v179, v179, v36\n\t"
    "v_sub_f32 v180, v180, v37\n\t"
    "v_sub_f32 v181, v181, v37\n\t"
    "v_sub_f32 v182, v182, v37\n\t"
    "v_sub_f32 v183, v183, v37\n\t"
    "v_sub_f32 v184, v184, v38\n\t"
    "v_sub_f32 v185, v185, v38\n\t"
    "v_sub_f32 v186, v186, v38\n\t"
    "v_sub_f32 v187, v187, v38\n\t"
    "v_sub_f32 v188, v188, v39\n\t"
    "v_sub_f32 v189, v189, v39\n\t"
    "v_sub_f32 v190, v190, v39\n\t"
    "v_sub_f32 v191, v191, v39\n\t"
    "v_exp_f32 v40, v64\n\t"
    "v_exp_f32 v41, v65\n\t"
    "v_exp_f32 v42, v66\n\t"
    "v_exp_f32 v43, v67\n\t"
    "v_exp_f32 v44, v68\n\t"
    "v_exp_f32 v45, v69\n\t"
    "v_exp_f32 v46, v70\n\t"
    "v_exp_f32 v47, v71\n\t"
    "v_cvt_pk_bf16_f32 v192, v40, v41\n\t"
    "v_cvt_pk_bf16_f32 v193, v42, v43\n\t"
    "v_cvt_pk_bf16_f32 v196, v44, v45\n\t"
    "v_cvt_pk_bf16_f32 v197, v46, v47\n\t"
    "v_exp_f32 v40, v72\n\t"
    "v_exp_f32 v41, v73\n\t"
    "v_exp_f32 v42, v74\n\t"
    "v_exp_f32 v43, v75\n\t"
    "v_exp_f32 v44, v76\n\t"
    "v_exp_f32 v45, v77\n\t"
    "v_exp_f32 v46, v78\n\t"
    "v_exp_f32 v47, v79\n\t"
    "v_cvt_pk_bf16_f32 v200, v40, v41\n\t"
    "v_cvt_pk_bf16_f32 v201, v42, v43\n\t"
    "v_cvt_pk_bf16_f32 v204, v44, v45\n\t"
    "v_cvt_pk_bf16_f32 v205, v46, v47\n\t"
    "v_exp_f32 v40, v80\n\t"
    "v_exp_f32 v41, v81\n\t"
    "v_exp_f32 v42, v82\n\t"
    "v_exp_f32 v43, v83\n\t"
    "v_exp_f32 v44, v84\n\t"
    "v_exp_f32 v45, v85\n\t"
    "v_exp_f32 v46, v86\n\t"
    "v_exp_f32 v47, v87\n\t"
    "v_cvt_pk_bf16_f32 v194, v40, v41\n\t"
    "v_cvt_pk_bf16_f32 v195, v42, v43\n\t"
    "v_cvt_pk_bf16_f32 v198, v44, v45\n\t"
    "v_cvt_pk_bf16_f32 v199, v46, v47\n\t"
    "v_exp_f32 v40, v88\n\t"
    "v_exp_f32 v41, v89\n\t"
    "v_exp_f32 v42, v90\n\t"
    "v_exp_f32 v43, v91\n\t"
    "v_exp_f32 v44, v92\n\t"
    "v_exp_f32 v45, v93\n\t"
    "v_exp_f32 v46, v94\n\t"
    "v_exp_f32 v47, v95\n\t"
    "v_cvt_pk_bf16_f32 v202, v40, v41\n\t"
    "v_cvt_pk_bf16_f32 v203, v42, v43\n\t"
    "v_cvt_pk_bf16_f32 v206, v44, v45\n\t"
    "v_cvt_pk_bf16_f32 v207, v46, v47\n\t"
    "v_exp_f32 v40, v96\n\t"
    "v_exp_f32 v41, v97\n\t"
    "v_exp_f32 v42, v98\n\t"
    "v_exp_f32 v43, v99\n\t"
    "v_exp_f32 v44, v100\n\t"
    "v_exp_f32 v45, v101\n\t"
    "v_exp_f32 v46, v102\n\t"
    "v_exp_f32 v47, v103\n\t"
    "v_cvt_pk_bf16_f32 v224, v40, v41\n\t"
    "v_cvt_pk_bf16_f32 v225, v42, v43\n\t"
    "v_cvt_pk_bf16_f32 v228, v44, v45\n\t"
    "v_cvt_pk_bf16_f32 v229, v46, v47\n\t"
    "v_exp_f32 v40, v104\n\t"
    "v_exp_f32 v41, v105\n\t"
    "v_exp_f32 v42, v106\n\t"
    "v_exp_f32 v43, v107\n\t"
    "v_exp_f32 v44, v108\n\t"
    "v_exp_f32 v45, v109\n\t"
    "v_exp_f32 v46, v110\n\t"
    "v_exp_f32 v47, v111\n\t"
    "v_cvt_pk_bf16_f32 v232, v40, v41\n\t"
    "v_cvt_pk_bf16_f32 v233, v42, v43\n\t"
    "v_cvt_pk_bf16_f32 v236, v44, v45\n\t"
    "v_cvt_pk_bf16_f32 v237, v46, v47\n\t"
    "v_exp_f32 v40, v112\n\t"
    "v_exp_f32 v41, v113\n\t"
    "v_exp_f32 v42, v114\n\t"
    "v_exp_f32 v43, v115\n\t"
    "v_exp_f32 v44, v116\n\t"
    "v_exp_f32 v45, v117\n\t"
    "v_exp_f32 v46, v118\n\t"
    "v_exp_f32 v47, v119\n\t"
    "v_cvt_pk_bf16_f32 v226, v40, v41\n\t"
    "v_cvt_pk_bf16_f32 v227, v42, v43\n\t"
    "v_cvt_pk_bf16_f32 v230, v44, v45\n\t"
    "v_cvt_pk_bf16_f32 v231, v46, v47\n\t"
    "v_exp_f32 v40, v120\n\t"
    "v_exp_f32 v41, v121\n\t"
    "v_exp_f32 v42, v122\n\t"
    "v_exp_f32 v43, v123\n\t"
    "v_exp_f32 v44, v124\n\t"
    "v_exp_f32 v45, v125\n\t"
    "v_exp_f32 v46, v126\n\t"
    "v_exp_f32 v47, v127\n\t"
    "v_cvt_pk_bf16_f32 v234, v40, v41\n\t"
    "v_cvt_pk_bf16_f32 v235, v42, v43\n\t"
    "v_cvt_pk_bf16_f32 v238, v44, v45\n\t"
    "v_cvt_pk_bf16_f32 v239, v46, v47\n\t"
    "v_mfma_f32_16x16x32_bf16 v[240:243], v[16:19], v[192:195], 0\n\t"
    "v_mfma_f32_16x16x32_bf16 v[244:247], v[16:19], v[196:199], 0\n\t"
    "v_mfma_f32_16x16x32_bf16 v[248:251], v[16:19], v[200:203], 0\n\t"
    "v_mfma_f32_16x16x32_bf16 v[252:255], v[16:19], v[204:207], 0\n\t"
    "v_mfma_f32_16x16x32_bf16 v[240:243], v[16:19], v[224:227], v[240:243]\n\t"
    "v_mfma_f32_16x16x32_bf16 v[244:247], v[16:19], v[228:231], v[244:247]\n\t"
    "v_mfma_f32_16x16x32_bf16 v[248:251], v[16:19], v[232:235], v[248:251]\n\t"
    "v_mfma_f32_16x16x32_bf16 v[252:255], v[16:19], v[236:239], v[252:255]\n\t"
    "s_nop 7\n\t"
    "s_cmp_eq_u32 s80, 0\n\t"
    "s_cbranch_scc1 cont_b1_%=\n\t"
    "s_branch cont_bl_%=\n\t"
    "; ---- rare_b0\n\t"
    "rare_b0_%=:\n\t"
    "s_mov_b32 s80, 0\n\t"
    "s_branch rare1_%=\n\t"
    "; ---- rare1\n\t"
    "rare1_%=:\n\t"
    "v_max3_f32 v36, v128, v129, v130\n\t"
    "v_max3_f32 v37, v132, v133, v134\n\t"
    "v_max3_f32 v38, v136, v137, v138\n\t"
    "v_max3_f32 v39, v140, v141, v142\n\t"
    "v_max3_f32 v36, v36, v131, v144\n\t"
    "v_max3_f32 v37, v37, v135, v148\n\t"
    "v_max3_f32 v38, v38, v139, v152\n\t"
    "v_max3_f32 v39, v39, v143, v156\n\t"
    "v_max3_f32 v36, v36, v145, v146\n\t"
    "v_max3_f32 v37, v37, v149, v150\n\t"
    "v_max3_f32 v38, v38, v153, v154\n\t"
    "v_max3_f32 v39, v39, v157, v158\n\t"
    "v_max3_f32 v36, v36, v147, v160\n\t"
    "v_max3_f32 v37, v37, v151, v164\n\t"
    "v_max3_f32 v38, v38, v155, v168\n\t"
    "v_max3_f32 v39, v39, v159, v172\n\t"
    "v_max3_f32 v36, v36, v161, v162\n\t"
    "v_max3_f32 v37, v37, v165, v166\n\t"
    "v_max3_f32 v38, v38, v169, v170\n\t"
    "v_max3_f32 v39, v39, v173, v174\n\t"
    "v_max3_f32 v36, v36, v163, v176\n\t"
    "v_max3_f32 v37, v37, v167, v180\n\t"
    "v_max3_f32 v38, v38, v171, v184\n\t"
    "v_max3_f32 v39, v39, v175, v188\n\t"
    "v_max3_f32 v36, v36, v177, v178\n\t"
    "v_max3_f32 v37, v37, v181, v182\n\t"
    "v_max3_f32 v38, v38, v185, v186\n\t"
    "v_max3_f32 v39, v39, v189, v190\n\t"
    "v_max_f32 v36, v36, v179\n\t"
    "v_max_f32 v37, v37, v183\n\t"
    "v_max_f32 v38, v38, v187\n\t"
    "v_max_f32 v39, v39, v191\n\t"
    "v_mov_b32 v20, v36\n\t"
    "v_mov_b32 v21, v37\n\t"
    "v_mov_b32 v22, v38\n\t"
    "v_mov_b32 v23, v39\n\t"
    "v_permlane16_swap_b32 v36, v20\n\t"
    "v_permlane16_swap_b32 v37, v21\n\t"
    "v_permlane16_swap_b32 v38, v22\n\t"
    "v_permlane16_swap_b32 v39, v23\n\t"
    "v_max_f32 v36, v36, v20\n\t"
    "v_max_f32 v37, v37, v21\n\t"
    "v_max_f32 v38, v38, v22\n\t"
    "v_max_f32 v39, v39, v23\n\t"
    "v_mov_b32 v20, v36\n\t"
    "v_mov_b32 v21, v37\n\t"
    "v_mov_b32 v22, v38\n\t"
    "v_mov_b32 v23, v39\n\t"
    "v_permlane32_swap_b32 v36, v20\n\t"
    "v_permlane32_swap_b32 v37, v21\n\t"
    "v_permlane32_swap_b32 v38, v22\n\t"
    "v_permlane32_swap_b32 v39, v23\n\t"
    "v_max_f32 v36, v36, v20\n\t"
    "v_max_f32 v37, v37, v21\n\t"
    "v_max_f32 v38, v38, v22\n\t"
    "v_max_f32 v39, v39, v23\n\t"
    "v_add_f32 v36, 0x42c80000, v36\n\t"
    "v_add_f32 v37, 0x42c80000, v37\n\t"
    "v_add_f32 v38, 0x42c80000, v38\n\t"
    "v_add_f32 v39, 0x42c80000, v39\n\t"
    "v_ceil_f32 v36, v36\n\t"
    "v_ceil_f32 v37, v37\n\t"
    "v_ceil_f32 v38, v38\n\t"
    "v_ceil_f32 v39, v39\n\t"
    "v_max_f32 v36, 0, v36\n\t"
    "v_max_f32 v37, 0, v37\n\t"
    "v_max_f32 v38, 0, v38\n\t"
    "v_max_f32 v39, 0, v39\n\t"
    "v_cvt_i32_f32 v44, -v36\n\t"
    "v_cvt_i32_f32 v45, -v37\n\t"
    "v_cvt_i32_f32 v46, -v38\n\t"
    "v_cvt_i32_f32 v47, -v39\n\t"
    "v_add_f32 v28, v28, v36\n\t"
    "v_add_f32 v29, v29, v37\n\t"
    "v_add_f32 v30, v30, v38\n\t"
    "v_add_f32 v31, v31, v39\n\t"
    "v_sub_f32 v48, 0xc2c80000, v28\n\t"
    "v_sub_f32 v49, 0xc2c80000, v28\n\t"
    "v_sub_f32 v50, 0xc2c80000, v28\n\t"
    "v_sub_f32 v51, 0xc2c80000, v28\n\t"
    "v_sub_f32 v52, 0xc2c80000, v29\n\t"
    "v_sub_f32 v53, 0xc2c80000, v29\n\t"
    "v_sub_f32 v54, 0xc2c80000, v29\n\t"
    "v_sub_f32 v55, 0xc2c80000, v29\n\t"
    "v_sub_f32 v56, 0xc2c80000, v30\n\t"
    "v_sub_f32 v57, 0xc2c80000, v30\n\t"
    "v_sub_f32 v58, 0xc2c80000, v30\n\t"
    "v_sub_f32 v59, 0xc2c80000, v30\n\t"
    "v_sub_f32 v60, 0xc2c80000, v31\n\t"
    "v_sub_f32 v61, 0xc2c80000, v31\n\t"
    "v_sub_f32 v62, 0xc2c80000, v31\n\t"
    "v_sub_f32 v63, 0xc2c80000, v31\n\t"
    "v_ldexp_f32 v32, v32, v44\n\t"
    "v_ldexp_f32 v33, v33, v45\n\t"
    "v_ldexp_f32 v34, v34, v46\n\t"
    "v_ldexp_f32 v35, v35, v47\n\t"
    "v_accvgpr_read_b32 v40, a0\n\t"
    "v_accvgpr_read_b32 v41, a1\n\t"
    "v_accvgpr_read_b32 v42, a2\n\t"
    "v_accvgpr_read_b32 v43, a3\n\t"
    "v_ldexp_f32 v40, v40, v44\n\t"
    "v_ldexp_f32 v41, v41, v44\n\t"
    "v_ldexp_f32 v42, v42, v44\n\t"
    "v_ldexp_f32 v43, v43, v44\n\t"
    "v_accvgpr_write_b32 a0, v40\n\t"
    "v_accvgpr_write_b32 a1, v41\n\t"
    "v_accvgpr_write_b32 a2, v42\n\t"
    "v_accvgpr_write_b32 a3, v43\n\t"
    "v_accvgpr_read_b32 v40, a4\n\t"
    "v_accvgpr_read_b32 v41, a5\n\t"
    "v_accvgpr_read_b32 v42, a6\n\t"
    "v_accvgpr_read_b32 v43, a7\n\t"
    "v_ldexp_f32 v40, v40, v45\n\t"
    "v_ldexp_f32 v41, v41, v45\n\t"
    "v_ldexp_f32 v42, v42, v45\n\t"
    "v_ldexp_f32 v43, v43, v45\n\t"
    "v_accvgpr_write_b32 a4, v40\n\t"
    "v_accvgpr_write_b32 a5, v41\n\t"
    "v_accvgpr_write_b32 a6, v42\n\t"
    "v_accvgpr_write_b32 a7, v43\n\t"
    "v_accvgpr_read_b32 v40, a8\n\t"
    "v_accvgpr_read_b32 v41, a9\n\t"
    "v_accvgpr_read_b32 v42, a10\n\t"
    "v_accvgpr_read_b32 v43, a11\n\t"
    "v_ldexp_f32 v40, v40, v46\n\t"
    "v_ldexp_f32 v41, v41, v46\n\t"
    "v_ldexp_f32 v42, v42, v46\n\t"
    "v_ldexp_f32 v43, v43, v46\n\t"
    "v_accvgpr_write_b32 a8, v40\n\t"
    "v_accvgpr_write_b32 a9, v41\n\t"
    "v_accvgpr_write_b32 a10, v42\n\t"
    "v_accvgpr_write_b32 a11, v43\n\t"
    "v_accvgpr_read_b32 v40, a12\n\t"
    "v_accvgpr_read_b32 v41, a13\n\t"
    "v_accvgpr_read_b32 v42, a14\n\t"
    "v_accvgpr_read_b32 v43, a15\n\t"
    "v_ldexp_f32 v40, v40, v47\n\t"
    "v_ldexp_f32 v41, v41, v47\n\t"
    "v_ldexp_f32 v42, v42, v47\n\t"
    "v_ldexp_f32 v43, v43, v47\n\t"
    "v_accvgpr_write_b32 a12, v40\n\t"
    "v_accvgpr_write_b32 a13, v41\n\t"
    "v_accvgpr_write_b32 a14, v42\n\t"
    "v_accvgpr_write_b32 a15, v43\n\t"
    "v_accvgpr_read_b32 v40, a16\n\t"
    "v_accvgpr_read_b32 v41, a17\n\t"
    "v_accvgpr_read_b32 v42, a18\n\t"
    "v_accvgpr_read_b32 v43, a19\n\t"
    "v_ldexp_f32 v40, v40, v44\n\t"
    "v_ldexp_f32 v41, v41, v44\n\t"
    "v_ldexp_f32 v42, v42, v44\n\t"
    "v_ldexp_f32 v43, v43, v44\n\t"
    "v_accvgpr_write_b32 a16, v40\n\t"
    "v_accvgpr_write_b32 a17, v41\n\t"
    "v_accvgpr_write_b32 a18, v42\n\t"
    "v_accvgpr_write_b32 a19, v43\n\t"
    "v_accvgpr_read_b32 v40, a20\n\t"
    "v_accvgpr_read_b32 v41, a21\n\t"
    "v_accvgpr_read_b32 v42, a22\n\t"
    "v_accvgpr_read_b32 v43, a23\n\t"
    "v_ldexp_f32 v40, v40, v45\n\t"
    "v_ldexp_f32 v41, v41, v45\n\t"
    "v_ldexp_f32 v42, v42, v45\n\t"
    "v_ldexp_f32 v43, v43, v45\n\t"
    "v_accvgpr_write_b32 a20, v40\n\t"
    "v_accvgpr_write_b32 a21, v41\n\t"
    "v_accvgpr_write_b32 a22, v42\n\t"
    "v_accvgpr_write_b32 a23, v43\n\t"
    "v_accvgpr_read_b32 v40, a24\n\t"
    "v_accvgpr_read_b32 v41, a25\n\t"
    "v_accvgpr_read_b32 v42, a26\n\t"
    "v_accvgpr_read_b32 v43, a27\n\t"
    "v_ldexp_f32 v40, v40, v46\n\t"
    "v_ldexp_f32 v41, v41, v46\n\t"
    "v_ldexp_f32 v42, v42, v46\n\t"
    "v_ldexp_f32 v43, v43, v46\n\t"
    "v_accvgpr_write_b32 a24, v40\n\t"
    "v_accvgpr_write_b32 a25, v41\n\t"
    "v_accvgpr_write_b32 a26, v42\n\t"
    "v_accvgpr_write_b32 a27, v43\n\t"
    "v_accvgpr_read_b32 v40, a28\n\t"
    "v_accvgpr_read_b32 v41, a29\n\t"
    "v_accvgpr_read_b32 v42, a30\n\t"
    "v_accvgpr_read_b32 v43, a31\n\t"
    "v_ldexp_f32 v40, v40, v47\n\t"
    "v_ldexp_f32 v41, v41, v47\n\t"
    "v_ldexp_f32 v42, v42, v47\n\t"
    "v_ldexp_f32 v43, v43, v47\n\t"
    "v_accvgpr_write_b32 a28, v40\n\t"
    "v_accvgpr_write_b32 a29, v41\n\t"
    "v_accvgpr_write_b32 a30, v42\n\t"
    "v_accvgpr_write_b32 a31, v43\n\t"
    "v_accvgpr_read_b32 v40, a32\n\t"
    "v_accvgpr_read_b32 v41, a33\n\t"
    "v_accvgpr_read_b32 v42, a34\n\t"
    "v_accvgpr_read_b32 v43, a35\n\t"
    "v_ldexp_f32 v40, v40, v44\n\t"
    "v_ldexp_f32 v41, v41, v44\n\t"
    "v_ldexp_f32 v42, v42, v44\n\t"
    "v_ldexp_f32 v43, v43, v44\n\t"
    "v_accvgpr_write_b32 a32, v40\n\t"
    "v_accvgpr_write_b32 a33, v41\n\t"
    "v_accvgpr_write_b32 a34, v42\n\t"
    "v_accvgpr_write_b32 a35, v43\n\t"
    "v_accvgpr_read_b32 v40, a36\n\t"
    "v_accvgpr_read_b32 v41, a37\n\t"
    "v_accvgpr_read_b32 v42, a38\n\t"
    "v_accvgpr_read_b32 v43, a39\n\t"
    "v_ldexp_f32 v40, v40, v45\n\t"
    "v_ldexp_f32 v41, v41, v45\n\t"
    "v_ldexp_f32 v42, v42, v45\n\t"
    "v_ldexp_f32 v43, v43, v45\n\t"
    "v_accvgpr_write_b32 a36, v40\n\t"
    "v_accvgpr_write_b32 a37, v41\n\t"
    "v_accvgpr_write_b32 a38, v42\n\t"
    "v_accvgpr_write_b32 a39, v43\n\t"
    "v_accvgpr_read_b32 v40, a40\n\t"
    "v_accvgpr_read_b32 v41, a41\n\t"
    "v_accvgpr_read_b32 v42, a42\n\t"
    "v_accvgpr_read_b32 v43, a43\n\t"
    "v_ldexp_f32 v40, v40, v46\n\t"
    "v_ldexp_f32 v41, v41, v46\n\t"
    "v_ldexp_f32 v42, v42, v46\n\t"
    "v_ldexp_f32 v43, v43, v46\n\t"
    "v_accvgpr_write_b32 a40, v40\n\t"
    "v_accvgpr_write_b32 a41, v41\n\t"
    "v_accvgpr_write_b32 a42, v42\n\t"
    "v_accvgpr_write_b32 a43, v43\n\t"
    "v_accvgpr_read_b32 v40, a44\n\t"
    "v_accvgpr_read_b32 v41, a45\n\t"
    "v_accvgpr_read_b32 v42, a46\n\t"
    "v_accvgpr_read_b32 v43, a47\n\t"
    "v_ldexp_f32 v40, v40, v47\n\t"
    "v_ldexp_f32 v41, v41, v47\n\t"
    "v_ldexp_f32 v42, v42, v47\n\t"
    "v_ldexp_f32 v43, v43, v47\n\t"
    "v_accvgpr_write_b32 a44, v40\n\t"
    "v_accvgpr_write_b32 a45, v41\n\t"
    "v_accvgpr_write_b32 a46, v42\n\t"
    "v_accvgpr_write_b32 a47, v43\n\t"
    "v_accvgpr_read_b32 v40, a48\n\t"
    "v_accvgpr_read_b32 v41, a49\n\t"
    "v_accvgpr_read_b32 v42, a50\n\t"
    "v_accvgpr_read_b32 v43, a51\n\t"
    "v_ldexp_f32 v40, v40, v44\n\t"
    "v_ldexp_f32 v41, v41, v44\n\t"
    "v_ldexp_f32 v42, v42, v44\n\t"
    "v_ldexp_f32 v43, v43, v44\n\t"
    "v_accvgpr_write_b32 a48, v40\n\t"
    "v_accvgpr_write_b32 a49, v41\n\t"
    "v_accvgpr_write_b32 a50, v42\n\t"
    "v_accvgpr_write_b32 a51, v43\n\t"
    "v_accvgpr_read_b32 v40, a52\n\t"
    "v_accvgpr_read_b32 v41, a53\n\t"
    "v_accvgpr_read_b32 v42, a54\n\t"
    "v_accvgpr_read_b32 v43, a55\n\t"
    "v_ldexp_f32 v40, v40, v45\n\t"
    "v_ldexp_f32 v41, v41, v45\n\t"
    "v_ldexp_f32 v42, v42, v45\n\t"
    "v_ldexp_f32 v43, v43, v45\n\t"
    "v_accvgpr_write_b32 a52, v40\n\t"
    "v_accvgpr_write_b32 a53, v41\n\t"
    "v_accvgpr_write_b32 a54, v42\n\t"
    "v_accvgpr_write_b32 a55, v43\n\t"
    "v_accvgpr_read_b32 v40, a56\n\t"
    "v_accvgpr_read_b32 v41, a57\n\t"
    "v_accvgpr_read_b32 v42, a58\n\t"
    "v_accvgpr_read_b32 v43, a59\n\t"
    "v_ldexp_f32 v40, v40, v46\n\t"
    "v_ldexp_f32 v41, v41, v46\n\t"
    "v_ldexp_f32 v42, v42, v46\n\t"
    "v_ldexp_f32 v43, v43, v46\n\t"
    "v_accvgpr_write_b32 a56, v40\n\t"
    "v_accvgpr_write_b32 a57, v41\n\t"
    "v_accvgpr_write_b32 a58, v42\n\t"
    "v_accvgpr_write_b32 a59, v43\n\t"
    "v_accvgpr_read_b32 v40, a60\n\t"
    "v_accvgpr_read_b32 v41, a61\n\t"
    "v_accvgpr_read_b32 v42, a62\n\t"
    "v_accvgpr_read_b32 v43, a63\n\t"
    "v_ldexp_f32 v40, v40, v47\n\t"
    "v_ldexp_f32 v41, v41, v47\n\t"
    "v_ldexp_f32 v42, v42, v47\n\t"
    "v_ldexp_f32 v43, v43, v47\n\t"
    "v_accvgpr_write_b32 a60, v40\n\t"
    "v_accvgpr_write_b32 a61, v41\n\t"
    "v_accvgpr_write_b32 a62, v42\n\t"
    "v_accvgpr_write_b32 a63, v43\n\t"
    "v_accvgpr_read_b32 v40, a64\n\t"
    "v_accvgpr_read_b32 v41, a65\n\t"
    "v_accvgpr_read_b32 v42, a66\n\t"
    "v_accvgpr_read_b32 v43, a67\n\t"
    "v_ldexp_f32 v40, v40, v44\n\t"
    "v_ldexp_f32 v41, v41, v44\n\t"
    "v_ldexp_f32 v42, v42, v44\n\t"
    "v_ldexp_f32 v43, v43, v44\n\t"
    "v_accvgpr_write_b32 a64, v40\n\t"
    "v_accvgpr_write_b32 a65, v41\n\t"
    "v_accvgpr_write_b32 a66, v42\n\t"
    "v_accvgpr_write_b32 a67, v43\n\t"
    "v_accvgpr_read_b32 v40, a68\n\t"
    "v_accvgpr_read_b32 v41, a69\n\t"
    "v_accvgpr_read_b32 v42, a70\n\t"
    "v_accvgpr_read_b32 v43, a71\n\t"
    "v_ldexp_f32 v40, v40, v45\n\t"
    "v_ldexp_f32 v41, v41, v45\n\t"
    "v_ldexp_f32 v42, v42, v45\n\t"
    "v_ldexp_f32 v43, v43, v45\n\t"
    "v_accvgpr_write_b32 a68, v40\n\t"
    "v_accvgpr_write_b32 a69, v41\n\t"
    "v_accvgpr_write_b32 a70, v42\n\t"
    "v_accvgpr_write_b32 a71, v43\n\t"
    "v_accvgpr_read_b32 v40, a72\n\t"
    "v_accvgpr_read_b32 v41, a73\n\t"
    "v_accvgpr_read_b32 v42, a74\n\t"
    "v_accvgpr_read_b32 v43, a75\n\t"
    "v_ldexp_f32 v40, v40, v46\n\t"
    "v_ldexp_f32 v41, v41, v46\n\t"
    "v_ldexp_f32 v42, v42, v46\n\t"
    "v_ldexp_f32 v43, v43, v46\n\t"
    "v_accvgpr_write_b32 a72, v40\n\t"
    "v_accvgpr_write_b32 a73, v41\n\t"
    "v_accvgpr_write_b32 a74, v42\n\t"
    "v_accvgpr_write_b32 a75, v43\n\t"
    "v_accvgpr_read_b32 v40, a76\n\t"
    "v_accvgpr_read_b32 v41, a77\n\t"
    "v_accvgpr_read_b32 v42, a78\n\t"
    "v_accvgpr_read_b32 v43, a79\n\t"
    "v_ldexp_f32 v40, v40, v47\n\t"
    "v_ldexp_f32 v41, v41, v47\n\t"
    "v_ldexp_f32 v42, v42, v47\n\t"
    "v_ldexp_f32 v43, v43, v47\n\t"
    "v_accvgpr_write_b32 a76, v40\n\t"
    "v_accvgpr_write_b32 a77, v41\n\t"
    "v_accvgpr_write_b32 a78, v42\n\t"
    "v_accvgpr_write_b32 a79, v43\n\t"
    "v_accvgpr_read_b32 v40, a80\n\t"
    "v_accvgpr_read_b32 v41, a81\n\t"
    "v_accvgpr_read_b32 v42, a82\n\t"
    "v_accvgpr_read_b32 v43, a83\n\t"
    "v_ldexp_f32 v40, v40, v44\n\t"
    "v_ldexp_f32 v41, v41, v44\n\t"
    "v_ldexp_f32 v42, v42, v44\n\t"
    "v_ldexp_f32 v43, v43, v44\n\t"
    "v_accvgpr_write_b32 a80, v40\n\t"
    "v_accvgpr_write_b32 a81, v41\n\t"
    "v_accvgpr_write_b32 a82, v42\n\t"
    "v_accvgpr_write_b32 a83, v43\n\t"
    "v_accvgpr_read_b32 v40, a84\n\t"
    "v_accvgpr_read_b32 v41, a85\n\t"
    "v_accvgpr_read_b32 v42, a86\n\t"
    "v_accvgpr_read_b32 v43, a87\n\t"
    "v_ldexp_f32 v40, v40, v45\n\t"
    "v_ldexp_f32 v41, v41, v45\n\t"
    "v_ldexp_f32 v42, v42, v45\n\t"
    "v_ldexp_f32 v43, v43, v45\n\t"
    "v_accvgpr_write_b32 a84, v40\n\t"
    "v_accvgpr_write_b32 a85, v41\n\t"
    "v_accvgpr_write_b32 a86, v42\n\t"
    "v_accvgpr_write_b32 a87, v43\n\t"
    "v_accvgpr_read_b32 v40, a88\n\t"
    "v_accvgpr_read_b32 v41, a89\n\t"
    "v_accvgpr_read_b32 v42, a90\n\t"
    "v_accvgpr_read_b32 v43, a91\n\t"
    "v_ldexp_f32 v40, v40, v46\n\t"
    "v_ldexp_f32 v41, v41, v46\n\t"
    "v_ldexp_f32 v42, v42, v46\n\t"
    "v_ldexp_f32 v43, v43, v46\n\t"
    "v_accvgpr_write_b32 a88, v40\n\t"
    "v_accvgpr_write_b32 a89, v41\n\t"
    "v_accvgpr_write_b32 a90, v42\n\t"
    "v_accvgpr_write_b32 a91, v43\n\t"
    "v_accvgpr_read_b32 v40, a92\n\t"
    "v_accvgpr_read_b32 v41, a93\n\t"
    "v_accvgpr_read_b32 v42, a94\n\t"
    "v_accvgpr_read_b32 v43, a95\n\t"
    "v_ldexp_f32 v40, v40, v47\n\t"
    "v_ldexp_f32 v41, v41, v47\n\t"
    "v_ldexp_f32 v42, v42, v47\n\t"
    "v_ldexp_f32 v43, v43, v47\n\t"
    "v_accvgpr_write_b32 a92, v40\n\t"
    "v_accvgpr_write_b32 a93, v41\n\t"
    "v_accvgpr_write_b32 a94, v42\n\t"
    "v_accvgpr_write_b32 a95, v43\n\t"
    "v_accvgpr_read_b32 v40, a96\n\t"
    "v_accvgpr_read_b32 v41, a97\n\t"
    "v_accvgpr_read_b32 v42, a98\n\t"
    "v_accvgpr_read_b32 v43, a99\n\t"
    "v_ldexp_f32 v40, v40, v44\n\t"
    "v_ldexp_f32 v41, v41, v44\n\t"
    "v_ldexp_f32 v42, v42, v44\n\t"
    "v_ldexp_f32 v43, v43, v44\n\t"
    "v_accvgpr_write_b32 a96, v40\n\t"
    "v_accvgpr_write_b32 a97, v41\n\t"
    "v_accvgpr_write_b32 a98, v42\n\t"
    "v_accvgpr_write_b32 a99, v43\n\t"
    "v_accvgpr_read_b32 v40, a100\n\t"
    "v_accvgpr_read_b32 v41, a101\n\t"
    "v_accvgpr_read_b32 v42, a102\n\t"
    "v_accvgpr_read_b32 v43, a103\n\t"
    "v_ldexp_f32 v40, v40, v45\n\t"
    "v_ldexp_f32 v41, v41, v45\n\t"
    "v_ldexp_f32 v42, v42, v45\n\t"
    "v_ldexp_f32 v43, v43, v45\n\t"
    "v_accvgpr_write_b32 a100, v40\n\t"
    "v_accvgpr_write_b32 a101, v41\n\t"
    "v_accvgpr_write_b32 a102, v42\n\t"
    "v_accvgpr_write_b32 a103, v43\n\t"
    "v_accvgpr_read_b32 v40, a104\n\t"
    "v_accvgpr_read_b32 v41, a105\n\t"
    "v_accvgpr_read_b32 v42, a106\n\t"
    "v_accvgpr_read_b32 v43, a107\n\t"
    "v_ldexp_f32 v40, v40, v46\n\t"
    "v_ldexp_f32 v41, v41, v46\n\t"
    "v_ldexp_f32 v42, v42, v46\n\t"
    "v_ldexp_f32 v43, v43, v46\n\t"
    "v_accvgpr_write_b32 a104, v40\n\t"
    "v_accvgpr_write_b32 a105, v41\n\t"
    "v_accvgpr_write_b32 a106, v42\n\t"
    "v_accvgpr_write_b32 a107, v43\n\t"
    "v_accvgpr_read_b32 v40, a108\n\t"
    "v_accvgpr_read_b32 v41, a109\n\t"
    "v_accvgpr_read_b32 v42, a110\n\t"
    "v_accvgpr_read_b32 v43, a111\n\t"
    "v_ldexp_f32 v40, v40, v47\n\t"
    "v_ldexp_f32 v41, v41, v47\n\t"
    "v_ldexp_f32 v42, v42, v47\n\t"
    "v_ldexp_f32 v43, v43, v47\n\t"
    "v_accvgpr_write_b32 a108, v40\n\t"
    "v_accvgpr_write_b32 a109, v41\n\t"
    "v_accvgpr_write_b32 a110, v42\n\t"
    "v_accvgpr_write_b32 a111, v43\n\t"
    "v_accvgpr_read_b32 v40, a112\n\t"
    "v_accvgpr_read_b32 v41, a113\n\t"
    "v_accvgpr_read_b32 v42, a114\n\t"
    "v_accvgpr_read_b32 v43, a115\n\t"
    "v_ldexp_f32 v40, v40, v44\n\t"
    "v_ldexp_f32 v41, v41, v44\n\t"
    "v_ldexp_f32 v42, v42, v44\n\t"
    "v_ldexp_f32 v43, v43, v44\n\t"
    "v_accvgpr_write_b32 a112, v40\n\t"
    "v_accvgpr_write_b32 a113, v41\n\t"
    "v_accvgpr_write_b32 a114, v42\n\t"
    "v_accvgpr_write_b32 a115, v43\n\t"
    "v_accvgpr_read_b32 v40, a116\n\t"
    "v_accvgpr_read_b32 v41, a117\n\t"
    "v_accvgpr_read_b32 v42, a118\n\t"
    "v_accvgpr_read_b32 v43, a119\n\t"
    "v_ldexp_f32 v40, v40, v45\n\t"
    "v_ldexp_f32 v41, v41, v45\n\t"
    "v_ldexp_f32 v42, v42, v45\n\t"
    "v_ldexp_f32 v43, v43, v45\n\t"
    "v_accvgpr_write_b32 a116, v40\n\t"
    "v_accvgpr_write_b32 a117, v41\n\t"
    "v_accvgpr_write_b32 a118, v42\n\t"
    "v_accvgpr_write_b32 a119, v43\n\t"
    "v_accvgpr_read_b32 v40, a120\n\t"
    "v_accvgpr_read_b32 v41, a121\n\t"
    "v_accvgpr_read_b32 v42, a122\n\t"
    "v_accvgpr_read_b32 v43, a123\n\t"
    "v_ldexp_f32 v40, v40, v46\n\t"
    "v_ldexp_f32 v41, v41, v46\n\t"
    "v_ldexp_f32 v42, v42, v46\n\t"
    "v_ldexp_f32 v43, v43, v46\n\t"
    "v_accvgpr_write_b32 a120, v40\n\t"
    "v_accvgpr_write_b32 a121, v41\n\t"
    "v_accvgpr_write_b32 a122, v42\n\t"
    "v_accvgpr_write_b32 a123, v43\n\t"
    "v_accvgpr_read_b32 v40, a124\n\t"
    "v_accvgpr_read_b32 v41, a125\n\t"
    "v_accvgpr_read_b32 v42, a126\n\t"
    "v_accvgpr_read_b32 v43, a127\n\t"
    "v_ldexp_f32 v40, v40, v47\n\t"
    "v_ldexp_f32 v41, v41, v47\n\t"
    "v_ldexp_f32 v42, v42, v47\n\t"
    "v_ldexp_f32 v43, v43, v47\n\t"
    "v_accvgpr_write_b32 a124, v40\n\t"
    "v_accvgpr_write_b32 a125, v41\n\t"
    "v_accvgpr_write_b32 a126, v42\n\t"
    "v_accvgpr_write_b32 a127, v43\n\t"
    "v_sub_f32 v64, v64, v36\n\t"
    "v_sub_f32 v65, v65, v36\n\t"
    "v_sub_f32 v66, v66, v36\n\t"
    "v_sub_f32 v67, v67, v36\n\t"
    "v_sub_f32 v68, v68, v37\n\t"
    "v_sub_f32 v69, v69, v37\n\t"
    "v_sub_f32 v70, v70, v37\n\t"
    "v_sub_f32 v71, v71, v37\n\t"
    "v_sub_f32 v72, v72, v38\n\t"
    "v_sub_f32 v73, v73, v38\n\t"
    "v_sub_f32 v74, v74, v38\n\t"
    "v_sub_f32 v75, v75, v38\n\t"
    "v_sub_f32 v76, v76, v39\n\t"
    "v_sub_f32 v77, v77, v39\n\t"
    "v_sub_f32 v78, v78, v39\n\t"
    "v_sub_f32 v79, v79, v39\n\t"
    "v_sub_f32 v80, v80, v36\n\t"
    "v_sub_f32 v81, v81, v36\n\t"
    "v_sub_f32 v82, v82, v36\n\t"
    "v_sub_f32 v83, v83, v36\n\t"
    "v_sub_f32 v84, v84, v37\n\t"
    "v_sub_f32 v85, v85, v37\n\t"
    "v_sub_f32 v86, v86, v37\n\t"
    "v_sub_f32 v87, v87, v37\n\t"
    "v_sub_f32 v88, v88, v38\n\t"
    "v_sub_f32 v89, v89, v38\n\t"
    "v_sub_f32 v90, v90, v38\n\t"
    "v_sub_f32 v91, v91, v38\n\t"
    "v_sub_f32 v92, v92, v39\n\t"
    "v_sub_f32 v93, v93, v39\n\t"
    "v_sub_f32 v94, v94, v39\n\t"
    "v_sub_f32 v95, v95, v39\n\t"
    "v_sub_f32 v96, v96, v36\n\t"
    "v_sub_f32 v97, v97, v36\n\t"
    "v_sub_f32 v98, v98, v36\n\t"
    "v_sub_f32 v99, v99, v36\n\t"
    "v_sub_f32 v100, v100, v37\n\t"
    "v_sub_f32 v101, v101, v37\n\t"
    "v_sub_f32 v102, v102, v37\n\t"
    "v_sub_f32 v103, v103, v37\n\t"
    "v_sub_f32 v104, v104, v38\n\t"
    "v_sub_f32 v105, v105, v38\n\t"
    "v_sub_f32 v106, v106, v38\n\t"
    "v_sub_f32 v107, v107, v38\n\t"
    "v_sub_f32 v108, v108, v39\n\t"
    "v_sub_f32 v109, v109, v39\n\t"
    "v_sub_f32 v110, v110, v39\n\t"
    "v_sub_f32 v111, v111, v39\n\t"
    "v_sub_f32 v112, v112, v36\n\t"
    "v_sub_f32 v113, v113, v36\n\t"
    "v_sub_f32 v114, v114, v36\n\t"
    "v_sub_f32 v115, v115, v36\n\t"
    "v_sub_f32 v116, v116, v37\n\t"
    "v_sub_f32 v117, v117, v37\n\t"
    "v_sub_f32 v118, v118, v37\n\t"
    "v_sub_f32 v119, v119, v37\n\t"
    "v_sub_f32 v120, v120, v38\n\t"
    "v_sub_f32 v121, v121, v38\n\t"
    "v_sub_f32 v122, v122, v38\n\t"
    "v_sub_f32 v123, v123, v38\n\t"
    "v_sub_f32 v124, v124, v39\n\t"
    "v_sub_f32 v125, v125, v39\n\t"
    "v_sub_f32 v126, v126, v39\n\t"
    "v_sub_f32 v127, v127, v39\n\t"
    "v_sub_f32 v128, v128, v36\n\t"
    "v_sub_f32 v129, v129, v36\n\t"
    "v_sub_f32 v130, v130, v36\n\t"
    "v_sub_f32 v131, v131, v36\n\t"
    "v_sub_f32 v132, v132, v37\n\t"
    "v_sub_f32 v133, v133, v37\n\t"
    "v_sub_f32 v134, v134, v37\n\t"
    "v_sub_f32 v135, v135, v37\n\t"
    "v_sub_f32 v136, v136, v38\n\t"
    "v_sub_f32 v137, v137, v38\n\t"
    "v_sub_f32 v138, v138, v38\n\t"
    "v_sub_f32 v139, v139, v38\n\t"
    "v_sub_f32 v140, v140, v39\n\t"
    "v_sub_f32 v141, v141, v39\n\t"
    "v_sub_f32 v142, v142, v39\n\t"
    "v_sub_f32 v143, v143, v39\n\t"
    "v_sub_f32 v144, v144, v36\n\t"
    "v_sub_f32 v145, v145, v36\n\t"
    "v_sub_f32 v146, v146, v36\n\t"
    "v_sub_f32 v147, v147, v36\n\t"
    "v_sub_f32 v148, v148, v37\n\t"
    "v_sub_f32 v149, v149, v37\n\t"
    "v_sub_f32 v150, v150, v37\n\t"
    "v_sub_f32 v151, v151, v37\n\t"
    "v_sub_f32 v152, v152, v38\n\t"
    "v_sub_f32 v153, v153, v38\n\t"
    "v_sub_f32 v154, v154, v38\n\t"
    "v_sub_f32 v155, v155, v38\n\t"
    "v_sub_f32 v156, v156, v39\n\t"
    "v_sub_f32 v157, v157, v39\n\t"
    "v_sub_f32 v158, v158, v39\n\t"
    "v_sub_f32 v159, v159, v39\n\t"
    "v_sub_f32 v160, v160, v36\n\t"
    "v_sub_f32 v161, v161, v36\n\t"
    "v_sub_f32 v162, v162, v36\n\t"
    "v_sub_f32 v163, v163, v36\n\t"
    "v_sub_f32 v164, v164, v37\n\t"
    "v_sub_f32 v165, v165, v37\n\t"
    "v_sub_f32 v166, v166, v37\n\t"
    "v_sub_f32 v167, v167, v37\n\t"
    "v_sub_f32 v168, v168, v38\n\t"
    "v_sub_f32 v169, v169, v38\n\t"
    "v_sub_f32 v170, v170, v38\n\t"
    "v_sub_f32 v171, v171, v38\n\t"
    "v_sub_f32 v172, v172, v39\n\t"
    "v_sub_f32 v173, v173, v39\n\t"
    "v_sub_f32 v174, v174, v39\n\t"
    "v_sub_f32 v175, v175, v39\n\t"
    "v_sub_f32 v176, v176, v36\n\t"
    "v_sub_f32 v177, v177, v36\n\t"
    "v_sub_f32 v178, v178, v36\n\t"
    "v_sub_f32 v179, v179, v36\n\t"
    "v_sub_f32 v180, v180, v37\n\t"
    "v_sub_f32 v181, v181, v37\n\t"
    "v_sub_f32 v182, v182, v37\n\t"
    "v_sub_f32 v183, v183, v37\n\t"
    "v_sub_f32 v184, v184, v38\n\t"
    "v_sub_f32 v185, v185, v38\n\t"
    "v_sub_f32 v186, v186, v38\n\t"
    "v_sub_f32 v187, v187, v38\n\t"
    "v_sub_f32 v188, v188, v39\n\t"
    "v_sub_f32 v189, v189, v39\n\t"
    "v_sub_f32 v190, v190, v39\n\t"
    "v_sub_f32 v191, v191, v39\n\t"
    "v_exp_f32 v40, v128\n\t"
    "v_exp_f32 v41, v129\n\t"
    "v_exp_f32 v42, v130\n\t"
    "v_exp_f32 v43, v131\n\t"
    "v_exp_f32 v44, v132\n\t"
    "v_exp_f32 v45, v133\n\t"
    "v_exp_f32 v46, v134\n\t"
    "v_exp_f32 v47, v135\n\t"
    "v_cvt_pk_bf16_f32 v208, v40, v41\n\t"
    "v_cvt_pk_bf16_f32 v209, v42, v43\n\t"
    "v_cvt_pk_bf16_f32 v212, v44, v45\n\t"
    "v_cvt_pk_bf16_f32 v213, v46, v47\n\t"
    "v_exp_f32 v40, v136\n\t"
    "v_exp_f32 v41, v137\n\t"
    "v_exp_f32 v42, v138\n\t"
    "v_exp_f32 v43, v139\n\t"
    "v_exp_f32 v44, v140\n\t"
    "v_exp_f32 v45, v141\n\t"
    "v_exp_f32 v46, v142\n\t"
    "v_exp_f32 v47, v143\n\t"
    "v_cvt_pk_bf16_f32 v216, v40, v41\n\t"
    "v_cvt_pk_bf16_f32 v217, v42, v43\n\t"
    "v_cvt_pk_bf16_f32 v220, v44, v45\n\t"
    "v_cvt_pk_bf16_f32 v221, v46, v47\n\t"
    "v_exp_f32 v40, v144\n\t"
    "v_exp_f32 v41, v145\n\t"
    "v_exp_f32 v42, v146\n\t"
    "v_exp_f32 v43, v147\n\t"
    "v_exp_f32 v44, v148\n\t"
    "v_exp_f32 v45, v149\n\t"
    "v_exp_f32 v46, v150\n\t"
    "v_exp_f32 v47, v151\n\t"
    "v_cvt_pk_bf16_f32 v210, v40, v41\n\t"
    "v_cvt_pk_bf16_f32 v211, v42, v43\n\t"
    "v_cvt_pk_bf16_f32 v214, v44, v45\n\t"
    "v_cvt_pk_bf16_f32 v215, v46, v47\n\t"
    "v_exp_f32 v40, v152\n\t"
    "v_exp_f32 v41, v153\n\t"
    "v_exp_f32 v42, v154\n\t"
    "v_exp_f32 v43, v155\n\t"
    "v_exp_f32 v44, v156\n\t"
    "v_exp_f32 v45, v157\n\t"
    "v_exp_f32 v46, v158\n\t"
    "v_exp_f32 v47, v159\n\t"
    "v_cvt_pk_bf16_f32 v218, v40, v41\n\t"
    "v_cvt_pk_bf16_f32 v219, v42, v43\n\t"
    "v_cvt_pk_bf16_f32 v222, v44, v45\n\t"
    "v_cvt_pk_bf16_f32 v223, v46, v47\n\t"
    "v_exp_f32 v40, v160\n\t"
    "v_exp_f32 v41, v161\n\t"
    "v_exp_f32 v42, v162\n\t"
    "v_exp_f32 v43, v163\n\t"
    "v_exp_f32 v44, v164\n\t"
    "v_exp_f32 v45, v165\n\t"
    "v_exp_f32 v46, v166\n\t"
    "v_exp_f32 v47, v167\n\t"
    "v_cvt_pk_bf16_f32 v224, v40, v41\n\t"
    "v_cvt_pk_bf16_f32 v225, v42, v43\n\t"
    "v_cvt_pk_bf16_f32 v228, v44, v45\n\t"
    "v_cvt_pk_bf16_f32 v229, v46, v47\n\t"
    "v_exp_f32 v40, v168\n\t"
    "v_exp_f32 v41, v169\n\t"
    "v_exp_f32 v42, v170\n\t"
    "v_exp_f32 v43, v171\n\t"
    "v_exp_f32 v44, v172\n\t"
    "v_exp_f32 v45, v173\n\t"
    "v_exp_f32 v46, v174\n\t"
    "v_exp_f32 v47, v175\n\t"
    "v_cvt_pk_bf16_f32 v232, v40, v41\n\t"
    "v_cvt_pk_bf16_f32 v233, v42, v43\n\t"
    "v_cvt_pk_bf16_f32 v236, v44, v45\n\t"
    "v_cvt_pk_bf16_f32 v237, v46, v47\n\t"
    "v_exp_f32 v40, v176\n\t"
    "v_exp_f32 v41, v177\n\t"
    "v_exp_f32 v42, v178\n\t"
    "v_exp_f32 v43, v179\n\t"
    "v_exp_f32 v44, v180\n\t"
    "v_exp_f32 v45, v181\n\t"
    "v_exp_f32 v46, v182\n\t"
    "v_exp_f32 v47, v183\n\t"
    "v_cvt_pk_bf16_f32 v226, v40, v41\n\t"
    "v_cvt_pk_bf16_f32 v227, v42, v43\n\t"
    "v_cvt_pk_bf16_f32 v230, v44, v45\n\t"
    "v_cvt_pk_bf16_f32 v231, v46, v47\n\t"
    "v_exp_f32 v40, v184\n\t"
    "v_exp_f32 v41, v185\n\t"
    "v_exp_f32 v42, v186\n\t"
    "v_exp_f32 v43, v187\n\t"
    "v_exp_f32 v44, v188\n\t"
    "v_exp_f32 v45, v189\n\t"
    "v_exp_f32 v46, v190\n\t"
    "v_exp_f32 v47, v191\n\t"
    "v_cvt_pk_bf16_f32 v234, v40, v41\n\t"
    "v_cvt_pk_bf16_f32 v235, v42, v43\n\t"
    "v_cvt_pk_bf16_f32 v238, v44, v45\n\t"
    "v_cvt_pk_bf16_f32 v239, v46, v47\n\t"
    "v_mfma_f32_16x16x32_bf16 v[240:243], v[16:19], v[208:211], 0\n\t"
    "v_mfma_f32_16x16x32_bf16 v[244:247], v[16:19], v[212:215], 0\n\t"
    "v_mfma_f32_16x16x32_bf16 v[248:251], v[16:19], v[216:219], 0\n\t"
    "v_mfma_f32_16x16x32_bf16 v[252:255], v[16:19], v[220:223], 0\n\t"
    "v_mfma_f32_16x16x32_bf16 v[240:243], v[16:19], v[224:227], v[240:243]\n\t"
    "v_mfma_f32_16x16x32_bf16 v[244:247], v[16:19], v[228:231], v[244:247]\n\t"
    "v_mfma_f32_16x16x32_bf16 v[248:251], v[16:19], v[232:235], v[248:251]\n\t"
    "v_mfma_f32_16x16x32_bf16 v[252:255], v[16:19], v[236:239], v[252:255]\n\t"
    "s_nop 7\n\t"
    "s_cmp_eq_u32 s80, 0\n\t"
    "s_cbranch_scc1 cont_b0_%=\n\t"
    "s_branch cont_b0_%=\n\t"
    "; ---- rare_pv\n\t"
    "rare_pv_%=:\n\t"
    "s_mov_b32 s80, 0\n\t"
    "s_branch rare2_%=\n\t"
    "; ---- rare_ps\n\t"
    "rare_ps_%=:\n\t"
    "s_mov_b32 s80, 1\n\t"
    "s_branch rare2_%=\n\t"
    "; ---- rare2\n\t"
    "rare2_%=:\n\t"
    "v_max3_f32 v36, v128, v129, v130\n\t"
    "v_max3_f32 v37, v132, v133, v134\n\t"
    "v_max3_f32 v38, v136, v137, v138\n\t"
    "v_max3_f32 v39, v140, v141, v142\n\t"
    "v_max3_f32 v36, v36, v131, v144\n\t"
    "v_max3_f32 v37, v37, v135, v148\n\t"
    "v_max3_f32 v38, v38, v139, v152\n\t"
    "v_max3_f32 v39, v39, v143, v156\n\t"
    "v_max3_f32 v36, v36, v145, v146\n\t"
    "v_max3_f32 v37, v37, v149, v150\n\t"
    "v_max3_f32 v38, v38, v153, v154\n\t"
    "v_max3_f32 v39, v39, v157, v158\n\t"
    "v_max3_f32 v36, v36, v147, v160\n\t"
    "v_max3_f32 v37, v37, v151, v164\n\t"
    "v_max3_f32 v38, v38, v155, v168\n\t"
    "v_max3_f32 v39, v39, v159, v172\n\t"
    "v_max3_f32 v36, v36, v161, v162\n\t"
    "v_max3_f32 v37, v37, v165, v166\n\t"
    "v_max3_f32 v38, v38, v169, v170\n\t"
    "v_max3_f32 v39, v39, v173, v174\n\t"
    "v_max3_f32 v36, v36, v163, v176\n\t"
    "v_max3_f32 v37, v37, v167, v180\n\t"
    "v_max3_f32 v38, v38, v171, v184\n\t"
    "v_max3_f32 v39, v39, v175, v188\n\t"
    "v_max3_f32 v36, v36, v177, v178\n\t"
    "v_max3_f32 v37, v37, v181, v182\n\t"
    "v_max3_f32 v38, v38, v185, v186\n\t"
    "v_max3_f32 v39, v39, v189, v190\n\t"
    "v_max_f32 v36, v36, v179\n\t"
    "v_max_f32 v37, v37, v183\n\t"
    "v_max_f32 v38, v38, v187\n\t"
    "v_max_f32 v39, v39, v191\n\t"
    "v_mov_b32 v20, v36\n\t"
    "v_mov_b32 v21, v37\n\t"
    "v_mov_b32 v22, v38\n\t"
    "v_mov_b32 v23, v39\n\t"
    "v_permlane16_swap_b32 v36, v20\n\t"
    "v_permlane16_swap_b32 v37, v21\n\t"
    "v_permlane16_swap_b32 v38, v22\n\t"
    "v_permlane16_swap_b32 v39, v23\n\t"
    "v_max_f32 v36, v36, v20\n\t"
    "v_max_f32 v37, v37, v21\n\t"
    "v_max_f32 v38, v38, v22\n\t"
    "v_max_f32 v39, v39, v23\n\t"
    "v_mov_b32 v20, v36\n\t"
    "v_mov_b32 v21, v37\n\t"
    "v_mov_b32 v22, v38\n\t"
    "v_mov_b32 v23, v39\n\t"
    "v_permlane32_swap_b32 v36, v20\n\t"
    "v_permlane32_swap_b32 v37, v21\n\t"
    "v_permlane32_swap_b32 v38, v22\n\t"
    "v_permlane32_swap_b32 v39, v23\n\t"
    "v_max_f32 v36, v36, v20\n\t"
    "v_max_f32 v37, v37, v21\n\t"
    "v_max_f32 v38, v38, v22\n\t"
    "v_max_f32 v39, v39, v23\n\t"
    "v_add_f32 v36, 0x42c80000, v36\n\t"
    "v_add_f32 v37, 0x42c80000, v37\n\t"
    "v_add_f32 v38, 0x42c80000, v38\n\t"
    "v_add_f32 v39, 0x42c80000, v39\n\t"
    "v_ceil_f32 v36, v36\n\t"
    "v_ceil_f32 v37, v37\n\t"
    "v_ceil_f32 v38, v38\n\t"
    "v_ceil_f32 v39, v39\n\t"
    "v_max_f32 v36, 0, v36\n\t"
    "v_max_f32 v37, 0, v37\n\t"
    "v_max_f32 v38, 0, v38\n\t"
    "v_max_f32 v39, 0, v39\n\t"
    "v_cvt_i32_f32 v44, -v36\n\t"
    "v_cvt_i32_f32 v45, -v37\n\t"
    "v_cvt_i32_f32 v46, -v38\n\t"
    "v_cvt_i32_f32 v47, -v39\n\t"
    "v_add_f32 v28, v28, v36\n\t"
    "v_add_f32 v29, v29, v37\n\t"
    "v_add_f32 v30, v30, v38\n\t"
    "v_add_f32 v31, v31, v39\n\t"
    "v_sub_f32 v48, 0xc2c80000, v28\n\t"
    "v_sub_f32 v49, 0xc2c80000, v28\n\t"
    "v_sub_f32 v50, 0xc2c80000, v28\n\t"
    "v_sub_f32 v51, 0xc2c80000, v28\n\t"
    "v_sub_f32 v52, 0xc2c80000, v29\n\t"
    "v_sub_f32 v53, 0xc2c80000, v29\n\t"
    "v_sub_f32 v54, 0xc2c80000, v29\n\t"
    "v_sub_f32 v55, 0xc2c80000, v29\n\t"
    "v_sub_f32 v56, 0xc2c80000, v30\n\t"
    "v_sub_f32 v57, 0xc2c80000, v30\n\t"
    "v_sub_f32 v58, 0xc2c80000, v30\n\t"
    "v_sub_f32 v59, 0xc2c80000, v30\n\t"
    "v_sub_f32 v60, 0xc2c80000, v31\n\t"
    "v_sub_f32 v61, 0xc2c80000, v31\n\t"
    "v_sub_f32 v62, 0xc2c80000, v31\n\t"
    "v_sub_f32 v63, 0xc2c80000, v31\n\t"
    "v_ldexp_f32 v32, v32, v44\n\t"
    "v_ldexp_f32 v33, v33, v45\n\t"
    "v_ldexp_f32 v34, v34, v46\n\t"
    "v_ldexp_f32 v35, v35, v47\n\t"
    "v_accvgpr_read_b32 v40, a0\n\t"
    "v_accvgpr_read_b32 v41, a1\n\t"
    "v_accvgpr_read_b32 v42, a2\n\t"
    "v_accvgpr_read_b32 v43, a3\n\t"
    "v_ldexp_f32 v40, v40, v44\n\t"
    "v_ldexp_f32 v41, v41, v44\n\t"
    "v_ldexp_f32 v42, v42, v44\n\t"
    "v_ldexp_f32 v43, v43, v44\n\t"
    "v_accvgpr_write_b32 a0, v40\n\t"
    "v_accvgpr_write_b32 a1, v41\n\t"
    "v_accvgpr_write_b32 a2, v42\n\t"
    "v_accvgpr_write_b32 a3, v43\n\t"
    "v_accvgpr_read_b32 v40, a4\n\t"
    "v_accvgpr_read_b32 v41, a5\n\t"
    "v_accvgpr_read_b32 v42, a6\n\t"
    "v_accvgpr_read_b32 v43, a7\n\t"
    "v_ldexp_f32 v40, v40, v45\n\t"
    "v_ldexp_f32 v41, v41, v45\n\t"
    "v_ldexp_f32 v42, v42, v45\n\t"
    "v_ldexp_f32 v43, v43, v45\n\t"
    "v_accvgpr_write_b32 a4, v40\n\t"
    "v_accvgpr_write_b32 a5, v41\n\t"
    "v_accvgpr_write_b32 a6, v42\n\t"
    "v_accvgpr_write_b32 a7, v43\n\t"
    "v_accvgpr_read_b32 v40, a8\n\t"
    "v_accvgpr_read_b32 v41, a9\n\t"
    "v_accvgpr_read_b32 v42, a10\n\t"
    "v_accvgpr_read_b32 v43, a11\n\t"
    "v_ldexp_f32 v40, v40, v46\n\t"
    "v_ldexp_f32 v41, v41, v46\n\t"
    "v_ldexp_f32 v42, v42, v46\n\t"
    "v_ldexp_f32 v43, v43, v46\n\t"
    "v_accvgpr_write_b32 a8, v40\n\t"
    "v_accvgpr_write_b32 a9, v41\n\t"
    "v_accvgpr_write_b32 a10, v42\n\t"
    "v_accvgpr_write_b32 a11, v43\n\t"
    "v_accvgpr_read_b32 v40, a12\n\t"
    "v_accvgpr_read_b32 v41, a13\n\t"
    "v_accvgpr_read_b32 v42, a14\n\t"
    "v_accvgpr_read_b32 v43, a15\n\t"
    "v_ldexp_f32 v40, v40, v47\n\t"
    "v_ldexp_f32 v41, v41, v47\n\t"
    "v_ldexp_f32 v42, v42, v47\n\t"
    "v_ldexp_f32 v43, v43, v47\n\t"
    "v_accvgpr_write_b32 a12, v40\n\t"
    "v_accvgpr_write_b32 a13, v41\n\t"
    "v_accvgpr_write_b32 a14, v42\n\t"
    "v_accvgpr_write_b32 a15, v43\n\t"
    "v_accvgpr_read_b32 v40, a16\n\t"
    "v_accvgpr_read_b32 v41, a17\n\t"
    "v_accvgpr_read_b32 v42, a18\n\t"
    "v_accvgpr_read_b32 v43, a19\n\t"
    "v_ldexp_f32 v40, v40, v44\n\t"
    "v_ldexp_f32 v41, v41, v44\n\t"
    "v_ldexp_f32 v42, v42, v44\n\t"
    "v_ldexp_f32 v43, v43, v44\n\t"
    "v_accvgpr_write_b32 a16, v40\n\t"
    "v_accvgpr_write_b32 a17, v41\n\t"
    "v_accvgpr_write_b32 a18, v42\n\t"
    "v_accvgpr_write_b32 a19, v43\n\t"
    "v_accvgpr_read_b32 v40, a20\n\t"
    "v_accvgpr_read_b32 v41, a21\n\t"
    "v_accvgpr_read_b32 v42, a22\n\t"
    "v_accvgpr_read_b32 v43, a23\n\t"
    "v_ldexp_f32 v40, v40, v45\n\t"
    "v_ldexp_f32 v41, v41, v45\n\t"
    "v_ldexp_f32 v42, v42, v45\n\t"
    "v_ldexp_f32 v43, v43, v45\n\t"
    "v_accvgpr_write_b32 a20, v40\n\t"
    "v_accvgpr_write_b32 a21, v41\n\t"
    "v_accvgpr_write_b32 a22, v42\n\t"
    "v_accvgpr_write_b32 a23, v43\n\t"
    "v_accvgpr_read_b32 v40, a24\n\t"
    "v_accvgpr_read_b32 v41, a25\n\t"
    "v_accvgpr_read_b32 v42, a26\n\t"
    "v_accvgpr_read_b32 v43, a27\n\t"
    "v_ldexp_f32 v40, v40, v46\n\t"
    "v_ldexp_f32 v41, v41, v46\n\t"
    "v_ldexp_f32 v42, v42, v46\n\t"
    "v_ldexp_f32 v43, v43, v46\n\t"
    "v_accvgpr_write_b32 a24, v40\n\t"
    "v_accvgpr_write_b32 a25, v41\n\t"
    "v_accvgpr_write_b32 a26, v42\n\t"
    "v_accvgpr_write_b32 a27, v43\n\t"
    "v_accvgpr_read_b32 v40, a28\n\t"
    "v_accvgpr_read_b32 v41, a29\n\t"
    "v_accvgpr_read_b32 v42, a30\n\t"
    "v_accvgpr_read_b32 v43, a31\n\t"
    "v_ldexp_f32 v40, v40, v47\n\t"
    "v_ldexp_f32 v41, v41, v47\n\t"
    "v_ldexp_f32 v42, v42, v47\n\t"
    "v_ldexp_f32 v43, v43, v47\n\t"
    "v_accvgpr_write_b32 a28, v40\n\t"
    "v_accvgpr_write_b32 a29, v41\n\t"
    "v_accvgpr_write_b32 a30, v42\n\t"
    "v_accvgpr_write_b32 a31, v43\n\t"
    "v_accvgpr_read_b32 v40, a32\n\t"
    "v_accvgpr_read_b32 v41, a33\n\t"
    "v_accvgpr_read_b32 v42, a34\n\t"
    "v_accvgpr_read_b32 v43, a35\n\t"
    "v_ldexp_f32 v40, v40, v44\n\t"
    "v_ldexp_f32 v41, v41, v44\n\t"
    "v_ldexp_f32 v42, v42, v44\n\t"
    "v_ldexp_f32 v43, v43, v44\n\t"
    "v_accvgpr_write_b32 a32, v40\n\t"
    "v_accvgpr_write_b32 a33, v41\n\t"
    "v_accvgpr_write_b32 a34, v42\n\t"
    "v_accvgpr_write_b32 a35, v43\n\t"
    "v_accvgpr_read_b32 v40, a36\n\t"
    "v_accvgpr_read_b32 v41, a37\n\t"
    "v_accvgpr_read_b32 v42, a38\n\t"
    "v_accvgpr_read_b32 v43, a39\n\t"
    "v_ldexp_f32 v40, v40, v45\n\t"
    "v_ldexp_f32 v41, v41, v45\n\t"
    "v_ldexp_f32 v42, v42, v45\n\t"
    "v_ldexp_f32 v43, v43, v45\n\t"
    "v_accvgpr_write_b32 a36, v40\n\t"
    "v_accvgpr_write_b32 a37, v41\n\t"
    "v_accvgpr_write_b32 a38, v42\n\t"
    "v_accvgpr_write_b32 a39, v43\n\t"
    "v_accvgpr_read_b32 v40, a40\n\t"
    "v_accvgpr_read_b32 v41, a41\n\t"
    "v_accvgpr_read_b32 v42, a42\n\t"
    "v_accvgpr_read_b32 v43, a43\n\t"
    "v_ldexp_f32 v40, v40, v46\n\t"
    "v_ldexp_f32 v41, v41, v46\n\t"
    "v_ldexp_f32 v42, v42, v46\n\t"
    "v_ldexp_f32 v43, v43, v46\n\t"
    "v_accvgpr_write_b32 a40, v40\n\t"
    "v_accvgpr_write_b32 a41, v41\n\t"
    "v_accvgpr_write_b32 a42, v42\n\t"
    "v_accvgpr_write_b32 a43, v43\n\t"
    "v_accvgpr_read_b32 v40, a44\n\t"
    "v_accvgpr_read_b32 v41, a45\n\t"
    "v_accvgpr_read_b32 v42, a46\n\t"
    "v_accvgpr_read_b32 v43, a47\n\t"
    "v_ldexp_f32 v40, v40, v47\n\t"
    "v_ldexp_f32 v41, v41, v47\n\t"
    "v_ldexp_f32 v42, v42, v47\n\t"
    "v_ldexp_f32 v43, v43, v47\n\t"
    "v_accvgpr_write_b32 a44, v40\n\t"
    "v_accvgpr_write_b32 a45, v41\n\t"
    "v_accvgpr_write_b32 a46, v42\n\t"
    "v_accvgpr_write_b32 a47, v43\n\t"
    "v_accvgpr_read_b32 v40, a48\n\t"
    "v_accvgpr_read_b32 v41, a49\n\t"
    "v_accvgpr_read_b32 v42, a50\n\t"
    "v_accvgpr_read_b32 v43, a51\n\t"
    "v_ldexp_f32 v40, v40, v44\n\t"
    "v_ldexp_f32 v41, v41, v44\n\t"
    "v_ldexp_f32 v42, v42, v44\n\t"
    "v_ldexp_f32 v43, v43, v44\n\t"
    "v_accvgpr_write_b32 a48, v40\n\t"
    "v_accvgpr_write_b32 a49, v41\n\t"
    "v_accvgpr_write_b32 a50, v42\n\t"
    "v_accvgpr_write_b32 a51, v43\n\t"
    "v_accvgpr_read_b32 v40, a52\n\t"
    "v_accvgpr_read_b32 v41, a53\n\t"
    "v_accvgpr_read_b32 v42, a54\n\t"
    "v_accvgpr_read_b32 v43, a55\n\t"
    "v_ldexp_f32 v40, v40, v45\n\t"
    "v_ldexp_f32 v41, v41, v45\n\t"
    "v_ldexp_f32 v42, v42, v45\n\t"
    "v_ldexp_f32 v43, v43, v45\n\t"
    "v_accvgpr_write_b32 a52, v40\n\t"
    "v_accvgpr_write_b32 a53, v41\n\t"
    "v_accvgpr_write_b32 a54, v42\n\t"
    "v_accvgpr_write_b32 a55, v43\n\t"
    "v_accvgpr_read_b32 v40, a56\n\t"
    "v_accvgpr_read_b32 v41, a57\n\t"
    "v_accvgpr_read_b32 v42, a58\n\t"
    "v_accvgpr_read_b32 v43, a59\n\t"
    "v_ldexp_f32 v40, v40, v46\n\t"
    "v_ldexp_f32 v41, v41, v46\n\t"
    "v_ldexp_f32 v42, v42, v46\n\t"
    "v_ldexp_f32 v43, v43, v46\n\t"
    "v_accvgpr_write_b32 a56, v40\n\t"
    "v_accvgpr_write_b32 a57, v41\n\t"
    "v_accvgpr_write_b32 a58, v42\n\t"
    "v_accvgpr_write_b32 a59, v43\n\t"
    "v_accvgpr_read_b32 v40, a60\n\t"
    "v_accvgpr_read_b32 v41, a61\n\t"
    "v_accvgpr_read_b32 v42, a62\n\t"
    "v_accvgpr_read_b32 v43, a63\n\t"
    "v_ldexp_f32 v40, v40, v47\n\t"
    "v_ldexp_f32 v41, v41, v47\n\t"
    "v_ldexp_f32 v42, v42, v47\n\t"
    "v_ldexp_f32 v43, v43, v47\n\t"
    "v_accvgpr_write_b32 a60, v40\n\t"
    "v_accvgpr_write_b32 a61, v41\n\t"
    "v_accvgpr_write_b32 a62, v42\n\t"
    "v_accvgpr_write_b32 a63, v43\n\t"
    "v_accvgpr_read_b32 v40, a64\n\t"
    "v_accvgpr_read_b32 v41, a65\n\t"
    "v_accvgpr_read_b32 v42, a66\n\t"
    "v_accvgpr_read_b32 v43, a67\n\t"
    "v_ldexp_f32 v40, v40, v44\n\t"
    "v_ldexp_f32 v41, v41, v44\n\t"
    "v_ldexp_f32 v42, v42, v44\n\t"
    "v_ldexp_f32 v43, v43, v44\n\t"
    "v_accvgpr_write_b32 a64, v40\n\t"
    "v_accvgpr_write_b32 a65, v41\n\t"
    "v_accvgpr_write_b32 a66, v42\n\t"
    "v_accvgpr_write_b32 a67, v43\n\t"
    "v_accvgpr_read_b32 v40, a68\n\t"
    "v_accvgpr_read_b32 v41, a69\n\t"
    "v_accvgpr_read_b32 v42, a70\n\t"
    "v_accvgpr_read_b32 v43, a71\n\t"
    "v_ldexp_f32 v40, v40, v45\n\t"
    "v_ldexp_f32 v41, v41, v45\n\t"
    "v_ldexp_f32 v42, v42, v45\n\t"
    "v_ldexp_f32 v43, v43, v45\n\t"
    "v_accvgpr_write_b32 a68, v40\n\t"
    "v_accvgpr_write_b32 a69, v41\n\t"
    "v_accvgpr_write_b32 a70, v42\n\t"
    "v_accvgpr_write_b32 a71, v43\n\t"
    "v_accvgpr_read_b32 v40, a72\n\t"
    "v_accvgpr_read_b32 v41, a73\n\t"
    "v_accvgpr_read_b32 v42, a74\n\t"
    "v_accvgpr_read_b32 v43, a75\n\t"
    "v_ldexp_f32 v40, v40, v46\n\t"
    "v_ldexp_f32 v41, v41, v46\n\t"
    "v_ldexp_f32 v42, v42, v46\n\t"
    "v_ldexp_f32 v43, v43, v46\n\t"
    "v_accvgpr_write_b32 a72, v40\n\t"
    "v_accvgpr_write_b32 a73, v41\n\t"
    "v_accvgpr_write_b32 a74, v42\n\t"
    "v_accvgpr_write_b32 a75, v43\n\t"
    "v_accvgpr_read_b32 v40, a76\n\t"
    "v_accvgpr_read_b32 v41, a77\n\t"
    "v_accvgpr_read_b32 v42, a78\n\t"
    "v_accvgpr_read_b32 v43, a79\n\t"
    "v_ldexp_f32 v40, v40, v47\n\t"
    "v_ldexp_f32 v41, v41, v47\n\t"
    "v_ldexp_f32 v42, v42, v47\n\t"
    "v_ldexp_f32 v43, v43, v47\n\t"
    "v_accvgpr_write_b32 a76, v40\n\t"
    "v_accvgpr_write_b32 a77, v41\n\t"
    "v_accvgpr_write_b32 a78, v42\n\t"
    "v_accvgpr_write_b32 a79, v43\n\t"
    "v_accvgpr_read_b32 v40, a80\n\t"
    "v_accvgpr_read_b32 v41, a81\n\t"
    "v_accvgpr_read_b32 v42, a82\n\t"
    "v_accvgpr_read_b32 v43, a83\n\t"
    "v_ldexp_f32 v40, v40, v44\n\t"
    "v_ldexp_f32 v41, v41, v44\n\t"
    "v_ldexp_f32 v42, v42, v44\n\t"
    "v_ldexp_f32 v43, v43, v44\n\t"
    "v_accvgpr_write_b32 a80, v40\n\t"
    "v_accvgpr_write_b32 a81, v41\n\t"
    "v_accvgpr_write_b32 a82, v42\n\t"
    "v_accvgpr_write_b32 a83, v43\n\t"
    "v_accvgpr_read_b32 v40, a84\n\t"
    "v_accvgpr_read_b32 v41, a85\n\t"
    "v_accvgpr_read_b32 v42, a86\n\t"
    "v_accvgpr_read_b32 v43, a87\n\t"
    "v_ldexp_f32 v40, v40, v45\n\t"
    "v_ldexp_f32 v41, v41, v45\n\t"
    "v_ldexp_f32 v42, v42, v45\n\t"
    "v_ldexp_f32 v43, v43, v45\n\t"
    "v_accvgpr_write_b32 a84, v40\n\t"
    "v_accvgpr_write_b32 a85, v41\n\t"
    "v_accvgpr_write_b32 a86, v42\n\t"
    "v_accvgpr_write_b32 a87, v43\n\t"
    "v_accvgpr_read_b32 v40, a88\n\t"
    "v_accvgpr_read_b32 v41, a89\n\t"
    "v_accvgpr_read_b32 v42, a90\n\t"
    "v_accvgpr_read_b32 v43, a91\n\t"
    "v_ldexp_f32 v40, v40, v46\n\t"
    "v_ldexp_f32 v41, v41, v46\n\t"
    "v_ldexp_f32 v42, v42, v46\n\t"
    "v_ldexp_f32 v43, v43, v46\n\t"
    "v_accvgpr_write_b32 a88, v40\n\t"
    "v_accvgpr_write_b32 a89, v41\n\t"
    "v_accvgpr_write_b32 a90, v42\n\t"
    "v_accvgpr_write_b32 a91, v43\n\t"
    "v_accvgpr_read_b32 v40, a92\n\t"
    "v_accvgpr_read_b32 v41, a93\n\t"
    "v_accvgpr_read_b32 v42, a94\n\t"
    "v_accvgpr_read_b32 v43, a95\n\t"
    "v_ldexp_f32 v40, v40, v47\n\t"
    "v_ldexp_f32 v41, v41, v47\n\t"
    "v_ldexp_f32 v42, v42, v47\n\t"
    "v_ldexp_f32 v43, v43, v47\n\t"
    "v_accvgpr_write_b32 a92, v40\n\t"
    "v_accvgpr_write_b32 a93, v41\n\t"
    "v_accvgpr_write_b32 a94, v42\n\t"
    "v_accvgpr_write_b32 a95, v43\n\t"
    "v_accvgpr_read_b32 v40, a96\n\t"
    "v_accvgpr_read_b32 v41, a97\n\t"
    "v_accvgpr_read_b32 v42, a98\n\t"
    "v_accvgpr_read_b32 v43, a99\n\t"
    "v_ldexp_f32 v40, v40, v44\n\t"
    "v_ldexp_f32 v41, v41, v44\n\t"
    "v_ldexp_f32 v42, v42, v44\n\t"
    "v_ldexp_f32 v43, v43, v44\n\t"
    "v_accvgpr_write_b32 a96, v40\n\t"
    "v_accvgpr_write_b32 a97, v41\n\t"
    "v_accvgpr_write_b32 a98, v42\n\t"
    "v_accvgpr_write_b32 a99, v43\n\t"
    "v_accvgpr_read_b32 v40, a100\n\t"
    "v_accvgpr_read_b32 v41, a101\n\t"
    "v_accvgpr_read_b32 v42, a102\n\t"
    "v_accvgpr_read_b32 v43, a103\n\t"
    "v_ldexp_f32 v40, v40, v45\n\t"
    "v_ldexp_f32 v41, v41, v45\n\t"
    "v_ldexp_f32 v42, v42, v45\n\t"
    "v_ldexp_f32 v43, v43, v45\n\t"
    "v_accvgpr_write_b32 a100, v40\n\t"
    "v_accvgpr_write_b32 a101, v41\n\t"
    "v_accvgpr_write_b32 a102, v42\n\t"
    "v_accvgpr_write_b32 a103, v43\n\t"
    "v_accvgpr_read_b32 v40, a104\n\t"
    "v_accvgpr_read_b32 v41, a105\n\t"
    "v_accvgpr_read_b32 v42, a106\n\t"
    "v_accvgpr_read_b32 v43, a107\n\t"
    "v_ldexp_f32 v40, v40, v46\n\t"
    "v_ldexp_f32 v41, v41, v46\n\t"
    "v_ldexp_f32 v42, v42, v46\n\t"
    "v_ldexp_f32 v43, v43, v46\n\t"
    "v_accvgpr_write_b32 a104, v40\n\t"
    "v_accvgpr_write_b32 a105, v41\n\t"
    "v_accvgpr_write_b32 a106, v42\n\t"
    "v_accvgpr_write_b32 a107, v43\n\t"
    "v_accvgpr_read_b32 v40, a108\n\t"
    "v_accvgpr_read_b32 v41, a109\n\t"
    "v_accvgpr_read_b32 v42, a110\n\t"
    "v_accvgpr_read_b32 v43, a111\n\t"
    "v_ldexp_f32 v40, v40, v47\n\t"
    "v_ldexp_f32 v41, v41, v47\n\t"
    "v_ldexp_f32 v42, v42, v47\n\t"
    "v_ldexp_f32 v43, v43, v47\n\t"
    "v_accvgpr_write_b32 a108, v40\n\t"
    "v_accvgpr_write_b32 a109, v41\n\t"
    "v_accvgpr_write_b32 a110, v42\n\t"
    "v_accvgpr_write_b32 a111, v43\n\t"
    "v_accvgpr_read_b32 v40, a112\n\t"
    "v_accvgpr_read_b32 v41, a113\n\t"
    "v_accvgpr_read_b32 v42, a114\n\t"
    "v_accvgpr_read_b32 v43, a115\n\t"
    "v_ldexp_f32 v40, v40, v44\n\t"
    "v_ldexp_f32 v41, v41, v44\n\t"
    "v_ldexp_f32 v42, v42, v44\n\t"
    "v_ldexp_f32 v43, v43, v44\n\t"
    "v_accvgpr_write_b32 a112, v40\n\t"
    "v_accvgpr_write_b32 a113, v41\n\t"
    "v_accvgpr_write_b32 a114, v42\n\t"
    "v_accvgpr_write_b32 a115, v43\n\t"
    "v_accvgpr_read_b32 v40, a116\n\t"
    "v_accvgpr_read_b32 v41, a117\n\t"
    "v_accvgpr_read_b32 v42, a118\n\t"
    "v_accvgpr_read_b32 v43, a119\n\t"
    "v_ldexp_f32 v40, v40, v45\n\t"
    "v_ldexp_f32 v41, v41, v45\n\t"
    "v_ldexp_f32 v42, v42, v45\n\t"
    "v_ldexp_f32 v43, v43, v45\n\t"
    "v_accvgpr_write_b32 a116, v40\n\t"
    "v_accvgpr_write_b32 a117, v41\n\t"
    "v_accvgpr_write_b32 a118, v42\n\t"
    "v_accvgpr_write_b32 a119, v43\n\t"
    "v_accvgpr_read_b32 v40, a120\n\t"
    "v_accvgpr_read_b32 v41, a121\n\t"
    "v_accvgpr_read_b32 v42, a122\n\t"
    "v_accvgpr_read_b32 v43, a123\n\t"
    "v_ldexp_f32 v40, v40, v46\n\t"
    "v_ldexp_f32 v41, v41, v46\n\t"
    "v_ldexp_f32 v42, v42, v46\n\t"
    "v_ldexp_f32 v43, v43, v46\n\t"
    "v_accvgpr_write_b32 a120, v40\n\t"
    "v_accvgpr_write_b32 a121, v41\n\t"
    "v_accvgpr_write_b32 a122, v42\n\t"
    "v_accvgpr_write_b32 a123, v43\n\t"
    "v_accvgpr_read_b32 v40, a124\n\t"
    "v_accvgpr_read_b32 v41, a125\n\t"
    "v_accvgpr_read_b32 v42, a126\n\t"
    "v_accvgpr_read_b32 v43, a127\n\t"
    "v_ldexp_f32 v40, v40, v47\n\t"
    "v_ldexp_f32 v41, v41, v47\n\t"
    "v_ldexp_f32 v42, v42, v47\n\t"
    "v_ldexp_f32 v43, v43, v47\n\t"
    "v_accvgpr_write_b32 a124, v40\n\t"
    "v_accvgpr_write_b32 a125, v41\n\t"
    "v_accvgpr_write_b32 a126, v42\n\t"
    "v_accvgpr_write_b32 a127, v43\n\t"
    "v_sub_f32 v128, v128, v36\n\t"
    "v_sub_f32 v129, v129, v36\n\t"
    "v_sub_f32 v130, v130, v36\n\t"
    "v_sub_f32 v131, v131, v36\n\t"
    "v_sub_f32 v132, v132, v37\n\t"
    "v_sub_f32 v133, v133, v37\n\t"
    "v_sub_f32 v134, v134, v37\n\t"
    "v_sub_f32 v135, v135, v37\n\t"
    "v_sub_f32 v136, v136, v38\n\t"
    "v_sub_f32 v137, v137, v38\n\t"
    "v_sub_f32 v138, v138, v38\n\t"
    "v_sub_f32 v139, v139, v38\n\t"
    "v_sub_f32 v140, v140, v39\n\t"
    "v_sub_f32 v141, v141, v39\n\t"
    "v_sub_f32 v142, v142, v39\n\t"
    "v_sub_f32 v143, v143, v39\n\t"
    "v_sub_f32 v144, v144, v36\n\t"
    "v_sub_f32 v145, v145, v36\n\t"
    "v_sub_f32 v146, v146, v36\n\t"
    "v_sub_f32 v147, v147, v36\n\t"
    "v_sub_f32 v148, v148, v37\n\t"
    "v_sub_f32 v149, v149, v37\n\t"
    "v_sub_f32 v150, v150, v37\n\t"
    "v_sub_f32 v151, v151, v37\n\t"
    "v_sub_f32 v152, v152, v38\n\t"
    "v_sub_f32 v153, v153, v38\n\t"
    "v_sub_f32 v154, v154, v38\n\t"
    "v_sub_f32 v155, v155, v38\n\t"
    "v_sub_f32 v156, v156, v39\n\t"
    "v_sub_f32 v157, v157, v39\n\t"
    "v_sub_f32 v158, v158, v39\n\t"
    "v_sub_f32 v159, v159, v39\n\t"
    "v_sub_f32 v160, v160, v36\n\t"
    "v_sub_f32 v161, v161, v36\n\t"
    "v_sub_f32 v162, v162, v36\n\t"
    "v_sub_f32 v163, v163, v36\n\t"
    "v_sub_f32 v164, v164, v37\n\t"
    "v_sub_f32 v165, v165, v37\n\t"
    "v_sub_f32 v166, v166, v37\n\t"
    "v_sub_f32 v167, v167, v37\n\t"
    "v_sub_f32 v168, v168, v38\n\t"
    "v_sub_f32 v169, v169, v38\n\t"
    "v_sub_f32 v170, v170, v38\n\t"
    "v_sub_f32 v171, v171, v38\n\t"
    "v_sub_f32 v172, v172, v39\n\t"
    "v_sub_f32 v173, v173, v39\n\t"
    "v_sub_f32 v174, v174, v39\n\t"
    "v_sub_f32 v175, v175, v39\n\t"
    "v_sub_f32 v176, v176, v36\n\t"
    "v_sub_f32 v177, v177, v36\n\t"
    "v_sub_f32 v178, v178, v36\n\t"
    "v_sub_f32 v179, v179, v36\n\t"
    "v_sub_f32 v180, v180, v37\n\t"
    "v_sub_f32 v181, v181, v37\n\t"
    "v_sub_f32 v182, v182, v37\n\t"
    "v_sub_f32 v183, v183, v37\n\t"
    "v_sub_f32 v184, v184, v38\n\t"
    "v_sub_f32 v185, v185, v38\n\t"
    "v_sub_f32 v186, v186, v38\n\t"
    "v_sub_f32 v187, v187, v38\n\t"
    "v_sub_f32 v188, v188, v39\n\t"
    "v_sub_f32 v189, v189, v39\n\t"
    "v_sub_f32 v190, v190, v39\n\t"
    "v_sub_f32 v191, v191, v39\n\t"
    "v_exp_f32 v40, v128\n\t"
    "v_exp_f32 v41, v129\n\t"
    "v_exp_f32 v42, v130\n\t"
    "v_exp_f32 v43, v131\n\t"
    "v_exp_f32 v44, v132\n\t"
    "v_exp_f32 v45, v133\n\t"
    "v_exp_f32 v46, v134\n\t"
    "v_exp_f32 v47, v135\n\t"
    "v_cvt_pk_bf16_f32 v208, v40, v41\n\t"
    "v_cvt_pk_bf16_f32 v209, v42, v43\n\t"
    "v_cvt_pk_bf16_f32 v212, v44, v45\n\t"
    "v_cvt_pk_bf16_f32 v213, v46, v47\n\t"
    "v_exp_f32 v40, v136\n\t"
    "v_exp_f32 v41, v137\n\t"
    "v_exp_f32 v42, v138\n\t"
    "v_exp_f32 v43, v139\n\t"
    "v_exp_f32 v44, v140\n\t"
    "v_exp_f32 v45, v141\n\t"
    "v_exp_f32 v46, v142\n\t"
    "v_exp_f32 v47, v143\n\t"
    "v_cvt_pk_bf16_f32 v216, v40, v41\n\t"
    "v_cvt_pk_bf16_f32 v217, v42, v43\n\t"
    "v_cvt_pk_bf16_f32 v220, v44, v45\n\t"
    "v_cvt_pk_bf16_f32 v221, v46, v47\n\t"
    "v_exp_f32 v40, v144\n\t"
    "v_exp_f32 v41, v145\n\t"
    "v_exp_f32 v42, v146\n\t"
    "v_exp_f32 v43, v147\n\t"
    "v_exp_f32 v44, v148\n\t"
    "v_exp_f32 v45, v149\n\t"
    "v_exp_f32 v46, v150\n\t"
    "v_exp_f32 v47, v151\n\t"
    "v_cvt_pk_bf16_f32 v210, v40, v41\n\t"
    "v_cvt_pk_bf16_f32 v211, v42, v43\n\t"
    "v_cvt_pk_bf16_f32 v214, v44, v45\n\t"
    "v_cvt_pk_bf16_f32 v215, v46, v47\n\t"
    "v_exp_f32 v40, v152\n\t"
    "v_exp_f32 v41, v153\n\t"
    "v_exp_f32 v42, v154\n\t"
    "v_exp_f32 v43, v155\n\t"
    "v_exp_f32 v44, v156\n\t"
    "v_exp_f32 v45, v157\n\t"
    "v_exp_f32 v46, v158\n\t"
    "v_exp_f32 v47, v159\n\t"
    "v_cvt_pk_bf16_f32 v218, v40, v41\n\t"
    "v_cvt_pk_bf16_f32 v219, v42, v43\n\t"
    "v_cvt_pk_bf16_f32 v222, v44, v45\n\t"
    "v_cvt_pk_bf16_f32 v223, v46, v47\n\t"
    "v_exp_f32 v40, v160\n\t"
    "v_exp_f32 v41, v161\n\t"
    "v_exp_f32 v42, v162\n\t"
    "v_exp_f32 v43, v163\n\t"
    "v_exp_f32 v44, v164\n\t"
    "v_exp_f32 v45, v165\n\t"
    "v_exp_f32 v46, v166\n\t"
    "v_exp_f32 v47, v167\n\t"
    "v_cvt_pk_bf16_f32 v192, v40, v41\n\t"
    "v_cvt_pk_bf16_f32 v193, v42, v43\n\t"
    "v_cvt_pk_bf16_f32 v196, v44, v45\n\t"
    "v_cvt_pk_bf16_f32 v197, v46, v47\n\t"
    "v_exp_f32 v40, v168\n\t"
    "v_exp_f32 v41, v169\n\t"
    "v_exp_f32 v42, v170\n\t"
    "v_exp_f32 v43, v171\n\t"
    "v_exp_f32 v44, v172\n\t"
    "v_exp_f32 v45, v173\n\t"
    "v_exp_f32 v46, v174\n\t"
    "v_exp_f32 v47, v175\n\t"
    "v_cvt_pk_bf16_f32 v200, v40, v41\n\t"
    "v_cvt_pk_bf16_f32 v201, v42, v43\n\t"
    "v_cvt_pk_bf16_f32 v204, v44, v45\n\t"
    "v_cvt_pk_bf16_f32 v205, v46, v47\n\t"
    "v_exp_f32 v40, v176\n\t"
    "v_exp_f32 v41, v177\n\t"
    "v_exp_f32 v42, v178\n\t"
    "v_exp_f32 v43, v179\n\t"
    "v_exp_f32 v44, v180\n\t"
    "v_exp_f32 v45, v181\n\t"
    "v_exp_f32 v46, v182\n\t"
    "v_exp_f32 v47, v183\n\t"
    "v_cvt_pk_bf16_f32 v194, v40, v41\n\t"
    "v_cvt_pk_bf16_f32 v195, v42, v43\n\t"
    "v_cvt_pk_bf16_f32 v198, v44, v45\n\t"
    "v_cvt_pk_bf16_f32 v199, v46, v47\n\t"
    "v_exp_f32 v40, v184\n\t"
    "v_exp_f32 v41, v185\n\t"
    "v_exp_f32 v42, v186\n\t"
    "v_exp_f32 v43, v187\n\t"
    "v_exp_f32 v44, v188\n\t"
    "v_exp_f32 v45, v189\n\t"
    "v_exp_f32 v46, v190\n\t"
    "v_exp_f32 v47, v191\n\t"
    "v_cvt_pk_bf16_f32 v202, v40, v41\n\t"
    "v_cvt_pk_bf16_f32 v203, v42, v43\n\t"
    "v_cvt_pk_bf16_f32 v206, v44, v45\n\t"
    "v_cvt_pk_bf16_f32 v207, v46, v47\n\t"
    "v_mfma_f32_16x16x32_bf16 v[240:243], v[16:19], v[208:211], 0\n\t"
    "v_mfma_f32_16x16x32_bf16 v[244:247], v[16:19], v[212:215], 0\n\t"
    "v_mfma_f32_16x16x32_bf16 v[248:251], v[16:19], v[216:219], 0\n\t"
    "v_mfma_f32_16x16x32_bf16 v[252:255], v[16:19], v[220:223], 0\n\t"
    "v_mfma_f32_16x16x32_bf16 v[240:243], v[16:19], v[192:195], v[240:243]\n\t"
    "v_mfma_f32_16x16x32_bf16 v[244:247], v[16:19], v[196:199], v[244:247]\n\t"
    "v_mfma_f32_16x16x32_bf16 v[248:251], v[16:19], v[200:203], v[248:251]\n\t"
    "v_mfma_f32_16x16x32_bf16 v[252:255], v[16:19], v[204:207], v[252:255]\n\t"
    "s_nop 7\n\t"
    "s_cmp_eq_u32 s80, 0\n\t"
    "s_cbranch_scc1 cont_pv_%=\n\t"
    "s_branch cont_ps_%=\n\t"
    "; ---- att_end\n\t"
    "att_end_%=:\n\t"
    "s_nop 1\n\t"
      :: "s"(w), "s"(pKV), "s"(uni64(ws + WS_CTX + (size_t)((b1 * 64 + 2 * h + g) * 8 + qh * 4) * 2048)), "s"(uni64(ws + WS_CTX + (size_t)((b2 * 64 + 2 * h + g) * 8 + qh * 4) * 2048)),
         "s"(b1 + 1), "s"(b2 + 1), "v"(l * 16), "s"(0), "s"(uni64(ws + WS_FLAG)), "s"(FL_B + bid * 128), "s"(tag),
         "s"(uni64(bid < 219 ? pf : wo)), "s"(bid < 219 ? 24576u : 0u), "v"(o8), "v"(o4), "s"(bid), "v"(FL_B + ((bid & ~7) + (l & 7)) * 128) DBGOP(1)
      : "v6", "v7", "v8", "v9", "v10", "v11", "v12", "v13", "v14", "v15", "v16", "v17", "v18", "v19", "v20", "v21", "v22", "v23", "v24", "v25", "v26", "v27", "v28", "v29", "v30", "v31", "v32", "v33", "v34", "v35", "v36", "v37", "v38", "v39", "v40", "v41", "v42", "v43", "v44", "v45", "v46", "v47", "v48", "v49", "v50", "v51", "v52", "v53", "v54", "v55", "v56", "v57", "v58", "v59", "v60", "v61", "v62", "v63", "v64", "v65", "v66", "v67", "v68", "v69", "v70", "v71", "v72", "v73", "v74", "v75", "v76", "v77", "v78", "v79", "v80", "v81", "v82", "v83", "v84", "v85", "v86", "v87", "v88", "v89", "v90", "v91", "v92", "v93", "v94", "v95", "v96", "v97", "v98", "v99", "v100", "v101", "v102", "v103", "v104", "v105", "v106", "v107", "v108", "v109", "v110", "v111", "v112", "v113", "v114", "v115", "v116", "v117", "v118", "v119", "v120", "v121", "v122", "v123", "v124", "v125", "v126", "v127", "v128", "v129", "v130", "v131", "v132", "v133", "v134", "v135", "v136", "v137", "v138", "v139", "v140", "v141", "v142", "v143", "v144", "v145", "v146", "v147", "v148", "v149", "v150", "v151", "v152", "v153", "v154", "v155", "v156", "v157", "v158", "v159", "v160", "v161", "v162", "v163", "v164", "v165", "v166", "v167", "v168", "v169", "v170", "v171", "v172", "v173", "v174", "v175", "v176", "v177", "v178", "v179", "v180", "v181", "v182", "v183", "v184", "v185", "v186", "v187", "v188", "v189", "v190", "v191", "v192", "v193", "v194", "v195", "v196", "v197", "v198", "v199", "v200", "v201", "v202", "v203", "v204", "v205", "v206", "v207", "v208", "v209", "v210", "v211", "v212", "v213", "v214", "v215", "v216", "v217", "v218", "v219", "v220", "v221", "v222", "v223", "v224", "v225", "v226", "v227", "v228", "v229", "v230", "v231", "v232", "v233", "v234", "v235", "v236", "v237", "v238", "v239", "v240", "v241", "v242", "v243", "v244", "v245", "v246", "v247", "v248", "v249", "v250", "v251", "v252", "v253", "v254", "v255", "a0", "a1", "a2", "a3", "a4", "a5", "a6", "a7", "a8", "a9", "a10", "a11", "a12", "a13", "a14", "a15", "a16", "a17", "a18", "a19", "a20", "a21", "a22", "a23", "a24", "a25", "a26", "a27", "a28", "a29", "a30", "a31", "a32", "a33", "a34", "a35", "a36", "a37", "a38", "a39", "a40", "a41", "a42", "a43", "a44", "a45", "a46", "a47", "a48", "a49", "a50", "a51", "a52", "a53", "a54", "a55", "a56", "a57", "a58", "a59", "a60", "a61", "a62", "a63", "a64", "a65", "a66", "a67", "a68", "a69", "a70", "a71", "a72", "a73", "a74", "a75", "a76", "a77", "a78", "a79", "a80", "a81", "a82", "a83", "a84", "a85", "a86", "a87", "a88", "a89", "a90", "a91", "a92", "a93", "a94", "a95", "a96", "a97", "a98", "a99", "a100", "a101", "a102", "a103", "a104", "a105", "a106", "a107", "a108", "a109", "a110", "a111", "a112", "a113", "a114", "a115", "a116", "a117", "a118", "a119", "a120", "a121", "a122", "a123", "a124", "a125", "a126", "a127", "a128", "a129", "a130", "a131", "a132", "a133", "a134", "a135", "a136", "a137", "a138", "a139", "a140", "a141", "a142", "a143", "a144", "a145", "a146", "a147", "a148", "a149", "a150", "a151", "a152", "a153", "a154", "a155", "a156", "a157", "a158", "a159", "a160", "a161", "a162", "a163", "a164", "a165", "a166", "a167", "a168", "a169", "a170", "a171", "a172", "a173", "a174", "a175", "a176", "a177", "a178", "a179", "a180", "a181", "a182", "a183", "a184", "a185", "a186", "a187", "a188", "a189", "a190", "a191", "a192", "a193", "a194", "a195", "a196", "a197", "a198", "a199", "a200", "a201", "a202", "a203", "a204", "a205", "a206", "a207", "a208", "a209", "a210", "a211", "a212", "a213", "a214", "a215", "a216", "a217", "a218", "a219", "a220", "a221", "a222", "a223", "a224", "a225", "a226", "a227", "a228", "a229", "a230", "a231", "a232", "a233", "a234", "a235", "a236", "a237", "a238", "a239", "a240", "a241", "a242", "a243", "a244", "a245", "a246", "a247", "a248", "a249", "a250", "a251", "a252", "a253", "a254", "a255", "s40", "s41", "s42", "s43", "s44", "s45", "s46", "s47", "s48", "s49", "s50", "s51", "s52", "s53", "s54", "s55", "s56", "s57", "s58", "s59", "s60", "s61", "s62", "s63", "s64", "s65", "s66", "s67", "s68", "s69", "s70", "s71", "s72", "s73", "s74", "s75", "s76", "s77", "s78", "s79", "s80", "s81", "s82", "s83", "s84", "s85", "s86", "s87", "s88", "s89", "s90", "s91", "s92", "s93", "s94", "s95", "s96", "s97", "vcc", "scc", "memory");
  }
  const int sk = bid & 3, rt = ((bid >> 2) & 1) * 8 + ((bid >> 3) & 7), ct = bid >> 6;
  const int k0 = 19 * sk, nst = sk == 3 ? 7 : 19;
  const unsigned offT = k0 * 64u * 1152u + ct * 288u;
  { const int l = fresh_lane();
    unsigned vo[5];
#pragma unroll
    for (int j = 0; j < 5; ++j) { const unsigned p = 5 * w + j, n = 64 * p + l; vo[j] = ((n / 18) * 1152 + (n % 18) * 16 - 1024 * (j & 3)) | (p < 18 ? 0u : 0x7ff00000u); }
    const int hp = min(k0 / 2 + l, (k0 + nst - 1) / 2), xr = rt < 8 ? rt : 15 - rt;
    CSTAMP(1, 27);
    asm volatile(
    "; ---- setup\n\t"
    "s_mov_b64 s[72:73], %5\n\t"
    "s_add_u32 s72, s72, 0x5000000\n\t"
    "s_addc_u32 s73, s73, 0\n\t"
    "s_mov_b32 s74, 0x40000\n\t"
    "s_mov_b32 s75, 0x20000\n\t"
    "s_waitcnt lgkmcnt(0)\n\t"
    "s_barrier\n\t"
    "s_mov_b64 s[44:45], %2\n\t"
    "s_mov_b32 s46, %3\n\t"
    "s_mov_b32 s47, 0x20000\n\t"
    "s_lshl_b32 s69, %0, 12\n\t"
    "s_mul_i32 s70, %0, 0x1400\n\t"
    "s_add_u32 s70, s70, 0x4000\n\t"
    "s_mov_b32 s57, 0\n\t"
    "s_mov_b32 s56, 0\n\t"
    "s_mov_b32 s62, 0\n\t"
    "s_add_u32 m0, s62, s70\n\t"
    "s_nop 0\n\t"
    "buffer_load_dwordx4 %13, s[44:47], s57 offen offset:0 lds\n\t"
    "buffer_load_dwordx4 %14, s[44:47], s57 offen offset:1024 lds\n\t"
    "buffer_load_dwordx4 %15, s[44:47], s57 offen offset:2048 lds\n\t"
    "buffer_load_dwordx4 %16, s[44:47], s57 offen offset:3072 lds\n\t"
    "s_add_u32 m0, m0, 0x1000\n\t"
    "s_nop 0\n\t"
    "buffer_load_dwordx4 %17, s[44:47], s57 offen offset:0 lds\n\t"
    "s_mov_b32 s57, 0x12000\n\t"
    "s_mov_b32 s56, 0x4000\n\t"
    "s_mov_b32 s62, 0x9000\n\t"
    "s_add_u32 m0, s62, s70\n\t"
    "s_nop 0\n\t"
    "buffer_load_dwordx4 %13, s[44:47], s57 offen offset:0 lds\n\t"
    "buffer_load_dwordx4 %14, s[44:47], s57 offen offset:1024 lds\n\t"
    "buffer_load_dwordx4 %15, s[44:47], s57 offen offset:2048 lds\n\t"
    "buffer_load_dwordx4 %16, s[44:47], s57 offen offset:3072 lds\n\t"
    "s_add_u32 m0, m0, 0x1000\n\t"
    "s_nop 0\n\t"
    "buffer_load_dwordx4 %17, s[44:47], s57 offen offset:0 lds\n\t"
    "s_lshl_b32 s64, %1, 1\n\t"
    "s_and_b32 s64, s64, 8\n\t"
    "s_lshr_b32 s65, %1, 3\n\t"
    "s_and_b32 s65, s65, 7\n\t"
    "s_add_u32 s64, s64, s65\n\t"
    "s_lshl_b32 s65, s64, 6\n\t"
    "s_add_u32 s65, s65, %9\n\t"
    "s_lshl_b32 s65, s65, 14\n\t"
    "s_add_u32 s65, s65, s69\n\t"
    "s_add_u32 s65, s65, 0x2000000\n\t"
    "s_mov_b64 s[40:41], %5\n\t"
    "s_add_u32 s40, s40, s65\n\t"
    "s_addc_u32 s41, s41, 0\n\t"
    "s_mov_b32 s42, 0x1000000\n\t"
    "s_mov_b32 s43, 0x20000\n\t"
    "s_mul_i32 s65, s64, 0x24000\n\t"
    "s_add_u32 s65, s65, s69\n\t"
    "s_add_u32 s65, s65, 0x4400000\n\t"
    "s_mov_b64 s[60:61], %5\n\t"
    "s_add_u32 s60, s60, s65\n\t"
    "s_addc_u32 s61, s61, 0\n\t"
    "s_lshl_b32 s64, s64, 2\n\t"
    "s_lshr_b32 s65, %1, 6\n\t"
    "s_add_u32 s64, s64, s65\n\t"
    "s_mul_i32 s64, s64, 0x2d000\n\t"
    "s_add_u32 s64, s64, 0x3000000\n\t"
    "s_mov_b64 s[48:49], %5\n\t"
    "s_add_u32 s48, s48, s64\n\t"
    "s_addc_u32 s49, s49, 0\n\t"
    "s_mov_b32 s50, 0x2d000\n\t"
    "s_mov_b32 s51, 0x20000\n\t"
    "s_lshl_b32 s59, %1, 7\n\t"
    "s_add_u32 s78, s59, 0x10000\n\t"
    "s_waitcnt vmcnt(0)\n\t"
    "s_barrier\n\t"
    "s_cmp_lg_u32 %0, 0\n\t"
    "s_cbranch_scc1 pubc_%=\n\t"
    "v_mov_b64 v[40:41], %19\n\t"
    "v_mov_b32 v42, 0\n\t"
    "s_mov_b64 s[76:77], exec\n\t"
    "s_mov_b64 exec, 1\n\t"
    "buffer_store_dwordx2 v[40:41], v42, s[72:75], s78 offen offset:0 sc1\n\t"
    "s_mov_b64 exec, s[76:77]\n\t"
    "pubc_%=:\n\t"
    "buffer_load_dwordx2 v[38:39], %20, s[72:75], 0 offen offset:0 sc1\n\t"
    "; ---- setup2\n\t"
    "s_mov_b32 s67, 0x40000\n\t"
    "ctxw_chk_%=:\n\t"
    "s_waitcnt vmcnt(0)\n\t"
    "v_cmp_ne_u64 vcc, %19, v[38:39]\n\t"
    "s_nop 0\n\t"
    "s_cbranch_vccz ctxw_ok_%=\n\t"
    "s_sleep 2\n\t"
    "buffer_load_dwordx2 v[38:39], %20, s[72:75], 0 offen offset:0 sc1\n\t"
    "s_waitcnt vmcnt(0)\n\t"
    "s_sub_u32 s67, s67, 1\n\t"
    "s_cmp_lg_u32 s67, 0\n\t"
    "s_cbranch_scc1 ctxw_chk_%=\n\t"
    "ctxw_ok_%=:\n\t"
    "s_mov_b32 s57, 0\n\t"
    "s_mov_b32 s56, 0\n\t"
    "s_mov_b32 s62, 0\n\t"
    "s_add_u32 m0, s62, s69\n\t"
    "s_nop 0\n\t"
    "buffer_load_dwordx4 %11, s[40:43], s56 offen offset:0 sc1 lds\n\t"
    "buffer_load_dwordx4 %11, s[40:43], s56 offen offset:1024 sc1 lds\n\t"
    "buffer_load_dwordx4 %11, s[40:43], s56 offen offset:2048 sc1 lds\n\t"
    "buffer_load_dwordx4 %11, s[40:43], s56 offen offset:3072 sc1 lds\n\t"
    "s_mov_b32 s57, 0x12000\n\t"
    "s_mov_b32 s56, 0x4000\n\t"
    "s_mov_b32 s62, 0x9000\n\t"
    "s_add_u32 m0, s62, s69\n\t"
    "s_nop 0\n\t"
    "buffer_load_dwordx4 %11, s[40:43], s56 offen offset:0 sc1 lds\n\t"
    "buffer_load_dwordx4 %11, s[40:43], s56 offen offset:1024 sc1 lds\n\t"
    "buffer_load_dwordx4 %11, s[40:43], s56 offen offset:2048 sc1 lds\n\t"
    "buffer_load_dwordx4 %11, s[40:43], s56 offen offset:3072 sc1 lds\n\t"
    "s_mov_b64 s[52:53], %10\n\t"
    "s_mov_b32 s54, 0x12000\n\t"
    "s_mov_b32 s55, 0x20000\n\t"
    "s_and_b32 s64, %0, 1\n\t"
    "s_lshl_b32 s64, s64, 10\n\t"
    "s_add_u32 s64, s64, s69\n\t"
    "v_add_u32 v35, s64, %11\n\t"
    "v_xor_b32 v36, 0x1000, v35\n\t"
    "v_mov_b32 v32, v35\n\t"
    "v_mov_b32 v33, v36\n\t"
    "v_mov_b32 v34, %12\n\t"
    "v_and_b32 v37, 8, %18\n\t"
    "v_mul_u32_u24 v37, 3, v37\n\t"
    "v_add_u32 v37, v37, %18\n\t"
    "s_mul_i32 s64, %0, 0x4800\n\t"
    "v_add_u32 v48, s64, %11\n\t"
    "s_xor_b32 s65, %0, 1\n\t"
    "s_mul_i32 s65, s65, 0x4800\n\t"
    "v_add_u32 v49, s65, %11\n\t"
    "s_sub_u32 s58, %4, 4\n\t"
    "s_mov_b32 s63, 0x9000\n\t"
    "s_mov_b32 s68, 0\n\t"
    "s_sub_u32 s66, %4, 1\n\t"
    "s_waitcnt vmcnt(4)\n\t"
    "s_barrier\n\t"
    "ds_read_b128 a[0:3], v32 offset:0\n\t"
    "ds_read_b64_tr_b16 a[16:17], v34 offset:0\n\t"
    "ds_read_b64_tr_b16 a[18:19], v34 offset:4608\n\t"
    "ds_read_b128 a[4:7], v32 offset:2048\n\t"
    "ds_read_b64_tr_b16 a[20:21], v34 offset:32\n\t"
    "ds_read_b64_tr_b16 a[22:23], v34 offset:4640\n\t"
    "ds_read_b128 a[8:11], v33 offset:0\n\t"
    "ds_read_b64_tr_b16 a[24:25], v34 offset:64\n\t"
    "ds_read_b64_tr_b16 a[26:27], v34 offset:4672\n\t"
    "ds_read_b128 a[12:15], v33 offset:2048\n\t"
    "ds_read_b64_tr_b16 a[28:29], v34 offset:96\n\t"
    "ds_read_b64_tr_b16 a[30:31], v34 offset:4704\n\t"
    "ds_read_b64_tr_b16 a[32:33], v34 offset:128\n\t"
    "ds_read_b64_tr_b16 a[34:35], v34 offset:4736\n\t"
    "ds_read_b64_tr_b16 a[36:37], v34 offset:160\n\t"
    "ds_read_b64_tr_b16 a[38:39], v34 offset:4768\n\t"
    "ds_read_b64_tr_b16 a[40:41], v34 offset:192\n\t"
    "ds_read_b64_tr_b16 a[42:43], v34 offset:4800\n\t"
    "ds_read_b64_tr_b16 a[44:45], v34 offset:224\n\t"
    "ds_read_b64_tr_b16 a[46:47], v34 offset:4832\n\t"
    "ds_read_b64_tr_b16 a[48:49], v34 offset:256\n\t"
    "ds_read_b64_tr_b16 a[50:51], v34 offset:4864\n\t"
    "v_add_u32 v32, s63, v35\n\t"
    "v_add_u32 v33, s63, v36\n\t"
    "v_add_u32 v34, s63, %12\n\t"
    "s_add_u32 s63, s63, 0x9000\n\t"
    "s_cmp_eq_u32 s63, 0x24000\n\t"
    "s_cselect_b32 s63, 0, s63\n\t"
    "; ---- first0\n\t"
    "s_waitcnt vmcnt(0) lgkmcnt(0)\n\t"
    "s_barrier\n\t"
    "v_mfma_f32_16x16x32_bf16 v[64:67], a[16:19], a[0:3], 0\n\t"
    "ds_read_b128 a[52:55], v32 offset:0\n\t"
    "ds_read_b64_tr_b16 a[68:69], v34 offset:0\n\t"
    "ds_read_b64_tr_b16 a[70:71], v34 offset:4608\n\t"
    "v_mfma_f32_16x16x32_bf16 v[100:103], a[16:19], a[4:7], 0\n\t"
    "ds_read_b128 a[56:59], v32 offset:2048\n\t"
    "s_mov_b32 s57, 0x24000\n\t"
    "s_mov_b32 s56, 0x8000\n\t"
    "s_mov_b32 s62, 0x12000\n\t"
    "s_add_u32 m0, s62, s70\n\t"
    "v_mfma_f32_16x16x32_bf16 v[136:139], a[16:19], a[8:11], 0\n\t"
    "buffer_load_dwordx4 %13, s[44:47], s57 offen offset:0 lds\n\t"
    "v_mfma_f32_16x16x32_bf16 v[172:175], a[16:19], a[12:15], 0\n\t"
    "buffer_load_dwordx4 %14, s[44:47], s57 offen offset:1024 lds\n\t"
    "v_mfma_f32_16x16x32_bf16 v[68:71], a[20:23], a[0:3], 0\n\t"
    "buffer_load_dwordx4 %15, s[44:47], s57 offen offset:2048 lds\n\t"
    "v_mfma_f32_16x16x32_bf16 v[104:107], a[20:23], a[4:7], 0\n\t"
    "buffer_load_dwordx4 %16, s[44:47], s57 offen offset:3072 lds\n\t"
    "v_mfma_f32_16x16x32_bf16 v[140:143], a[20:23], a[8:11], 0\n\t"
    "ds_read_b64_tr_b16 a[72:73], v34 offset:32\n\t"
    "ds_read_b64_tr_b16 a[74:75], v34 offset:4640\n\t"
    "ds_read_b128 a[60:63], v33 offset:0\n\t"
    "ds_read_b64_tr_b16 a[76:77], v34 offset:64\n\t"
    "s_add_u32 m0, m0, 0x1000\n\t"
    "v_mfma_f32_16x16x32_bf16 v[176:179], a[20:23], a[12:15], 0\n\t"
    "buffer_load_dwordx4 %17, s[44:47], s57 offen offset:0 lds\n\t"
    "v_mfma_f32_16x16x32_bf16 v[72:75], a[24:27], a[0:3], 0\n\t"
    "ds_read_b64_tr_b16 a[78:79], v34 offset:4672\n\t"
    "ds_read_b128 a[64:67], v33 offset:2048\n\t"
    "ds_read_b64_tr_b16 a[80:81], v34 offset:96\n\t"
    "s_add_u32 m0, s62, s69\n\t"
    "v_mfma_f32_16x16x32_bf16 v[108:111], a[24:27], a[4:7], 0\n\t"
    "buffer_load_dwordx4 %11, s[40:43], s56 offen offset:0 sc1 lds\n\t"
    "v_mfma_f32_16x16x32_bf16 v[144:147], a[24:27], a[8:11], 0\n\t"
    "buffer_load_dwordx4 %11, s[40:43], s56 offen offset:1024 sc1 lds\n\t"
    "v_mfma_f32_16x16x32_bf16 v[180:183], a[24:27], a[12:15], 0\n\t"
    "buffer_load_dwordx4 %11, s[40:43], s56 offen offset:2048 sc1 lds\n\t"
    "v_mfma_f32_16x16x32_bf16 v[76:79], a[28:31], a[0:3], 0\n\t"
    "buffer_load_dwordx4 %11, s[40:43], s56 offen offset:3072 sc1 lds\n\t"
    "v_mfma_f32_16x16x32_bf16 v[112:115], a[28:31], a[4:7], 0\n\t"
    "ds_read_b64_tr_b16 a[82:83], v34 offset:4704\n\t"
    "ds_read_b64_tr_b16 a[84:85], v34 offset:128\n\t"
    "s_mov_b32 s57, 0x36000\n\t"
    "s_mov_b32 s56, 0xc000\n\t"
    "s_mov_b32 s62, 0x1b000\n\t"
    "s_add_u32 m0, s62, s70\n\t"
    "v_mfma_f32_16x16x32_bf16 v[148:151], a[28:31], a[8:11], 0\n\t"
    "buffer_load_dwordx4 %13, s[44:47], s57 offen offset:0 lds\n\t"
    "v_mfma_f32_16x16x32_bf16 v[184:187], a[28:31], a[12:15], 0\n\t"
    "buffer_load_dwordx4 %14, s[44:47], s57 offen offset:1024 lds\n\t"
    "v_mfma_f32_16x16x32_bf16 v[80:83], a[32:35], a[0:3], 0\n\t"
    "buffer_load_dwordx4 %15, s[44:47], s57 offen offset:2048 lds\n\t"
    "v_mfma_f32_16x16x32_bf16 v[116:119], a[32:35], a[4:7], 0\n\t"
    "buffer_load_dwordx4 %16, s[44:47], s57 offen offset:3072 lds\n\t"
    "v_mfma_f32_16x16x32_bf16 v[152:155], a[32:35], a[8:11], 0\n\t"
    "ds_read_b64_tr_b16 a[86:87], v34 offset:4736\n\t"
    "ds_read_b64_tr_b16 a[88:89], v34 offset:160\n\t"
    "ds_read_b64_tr_b16 a[90:91], v34 offset:4768\n\t"
    "ds_read_b64_tr_b16 a[92:93], v34 offset:192\n\t"
    "ds_read_b64_tr_b16 a[94:95], v34 offset:4800\n\t"
    "ds_read_b64_tr_b16 a[96:97], v34 offset:224\n\t"
    "s_add_u32 m0, m0, 0x1000\n\t"
    "v_mfma_f32_16x16x32_bf16 v[188:191], a[32:35], a[12:15], 0\n\t"
    "buffer_load_dwordx4 %17, s[44:47], s57 offen offset:0 lds\n\t"
    "v_mfma_f32_16x16x32_bf16 v[84:87], a[36:39], a[0:3], 0\n\t"
    "ds_read_b64_tr_b16 a[98:99], v34 offset:4832\n\t"
    "ds_read_b64_tr_b16 a[100:101], v34 offset:256\n\t"
    "ds_read_b64_tr_b16 a[102:103], v34 offset:4864\n\t"
    "s_add_u32 m0, s62, s69\n\t"
    "v_mfma_f32_16x16x32_bf16 v[120:123], a[36:39], a[4:7], 0\n\t"
    "buffer_load_dwordx4 %11, s[40:43], s56 offen offset:0 sc1 lds\n\t"
    "v_mfma_f32_16x16x32_bf16 v[156:159], a[36:39], a[8:11], 0\n\t"
    "buffer_load_dwordx4 %11, s[40:43], s56 offen offset:1024 sc1 lds\n\t"
    "v_mfma_f32_16x16x32_bf16 v[192:195], a[36:39], a[12:15], 0\n\t"
    "buffer_load_dwordx4 %11, s[40:43], s56 offen offset:2048 sc1 lds\n\t"
    "v_mfma_f32_16x16x32_bf16 v[88:91], a[40:43], a[0:3], 0\n\t"
    "buffer_load_dwordx4 %11, s[40:43], s56 offen offset:3072 sc1 lds\n\t"
    "v_mfma_f32_16x16x32_bf16 v[124:127], a[40:43], a[4:7], 0\n\t"
    "s_mov_b32 s57, 0x48000\n\t"
    "s_mov_b32 s56, 0x10000\n\t"
    "s_mov_b32 s62, 0\n\t"
    "s_add_u32 m0, s62, s70\n\t"
    "v_mfma_f32_16x16x32_bf16 v[160:163], a[40:43], a[8:11], 0\n\t"
    "buffer_load_dwordx4 %13, s[44:47], s57 offen offset:0 lds\n\t"
    "v_mfma_f32_16x16x32_bf16 v[196:199], a[40:43], a[12:15], 0\n\t"
    "buffer_load_dwordx4 %14, s[44:47], s57 offen offset:1024 lds\n\t"
    "v_mfma_f32_16x16x32_bf16 v[92:95], a[44:47], a[0:3], 0\n\t"
    "buffer_load_dwordx4 %15, s[44:47], s57 offen offset:2048 lds\n\t"
    "v_mfma_f32_16x16x32_bf16 v[128:131], a[44:47], a[4:7], 0\n\t"
    "buffer_load_dwordx4 %16, s[44:47], s57 offen offset:3072 lds\n\t"
    "v_mfma_f32_16x16x32_bf16 v[164:167], a[44:47], a[8:11], 0\n\t"
    "s_add_u32 m0, m0, 0x1000\n\t"
    "v_add_u32 v32, s63, v35\n\t"
    "v_add_u32 v33, s63, v36\n\t"
    "v_add_u32 v34, s63, %12\n\t"
    "s_add_u32 s63, s63, 0x9000\n\t"
    "s_cmp_eq_u32 s63, 0x24000\n\t"
    "s_cselect_b32 s63, 0, s63\n\t"
    "v_mfma_f32_16x16x32_bf16 v[200:203], a[44:47], a[12:15], 0\n\t"
    "buffer_load_dwordx4 %17, s[44:47], s57 offen offset:0 lds\n\t"
    "v_mfma_f32_16x16x32_bf16 v[96:99], a[48:51], a[0:3], 0\n\t"
    "s_add_u32 m0, s62, s69\n\t"
    "v_mfma_f32_16x16x32_bf16 v[132:135], a[48:51], a[4:7], 0\n\t"
    "buffer_load_dwordx4 %11, s[40:43], s56 offen offset:0 sc1 lds\n\t"
    "v_mfma_f32_16x16x32_bf16 v[168:171], a[48:51], a[8:11], 0\n\t"
    "buffer_load_dwordx4 %11, s[40:43], s56 offen offset:1024 sc1 lds\n\t"
    "v_mfma_f32_16x16x32_bf16 v[204:207], a[48:51], a[12:15], 0\n\t"
    "buffer_load_dwordx4 %11, s[40:43], s56 offen offset:2048 sc1 lds\n\t"
    "buffer_load_dwordx4 %11, s[40:43], s56 offen offset:3072 sc1 lds\n\t"
    "s_add_u32 s62, s62, 0x9000\n\t"
    "s_cmp_eq_u32 s62, 0x24000\n\t"
    "s_cselect_b32 s62, 0, s62\n\t"
    "s_add_u32 s56, s56, 0x4000\n\t"
    "s_add_u32 s57, s57, 0x12000\n\t"
    "s_sub_u32 s58, s58, 1\n\t"
    "; ---- b1\n\t"
    "o_b1_%=:\n\t"
    "s_waitcnt vmcnt(18) lgkmcnt(0)\n\t"
    "s_barrier\n\t"
    "v_mfma_f32_16x16x32_bf16 v[64:67], a[68:71], a[52:55], v[64:67]\n\t"
    "ds_read_b128 a[0:3], v32 offset:0\n\t"
    "s_add_u32 m0, s62, s70\n\t"
    "v_mfma_f32_16x16x32_bf16 v[100:103], a[68:71], a[56:59], v[100:103]\n\t"
    "buffer_load_dwordx4 %13, s[44:47], s57 offen offset:0 lds\n\t"
    "v_mfma_f32_16x16x32_bf16 v[136:139], a[68:71], a[60:63], v[136:139]\n\t"
    "ds_read_b64_tr_b16 a[16:17], v34 offset:0\n\t"
    "ds_read_b64_tr_b16 a[18:19], v34 offset:4608\n\t"
    "v_mfma_f32_16x16x32_bf16 v[172:175], a[68:71], a[64:67], v[172:175]\n\t"
    "ds_read_b128 a[4:7], v32 offset:2048\n\t"
    "ds_read_b64_tr_b16 a[20:21], v34 offset:32\n\t"
    "v_mfma_f32_16x16x32_bf16 v[68:71], a[72:75], a[52:55], v[68:71]\n\t"
    "buffer_load_dwordx4 %14, s[44:47], s57 offen offset:1024 lds\n\t"
    "v_mfma_f32_16x16x32_bf16 v[104:107], a[72:75], a[56:59], v[104:107]\n\t"
    "ds_read_b64_tr_b16 a[22:23], v34 offset:4640\n\t"
    "ds_read_b128 a[8:11], v33 offset:0\n\t"
    "v_mfma_f32_16x16x32_bf16 v[140:143], a[72:75], a[60:63], v[140:143]\n\t"
    "ds_read_b64_tr_b16 a[24:25], v34 offset:64\n\t"
    "ds_read_b64_tr_b16 a[26:27], v34 offset:4672\n\t"
    "v_mfma_f32_16x16x32_bf16 v[176:179], a[72:75], a[64:67], v[176:179]\n\t"
    "buffer_load_dwordx4 %15, s[44:47], s57 offen offset:2048 lds\n\t"
    "v_mfma_f32_16x16x32_bf16 v[72:75], a[76:79], a[52:55], v[72:75]\n\t"
    "ds_read_b128 a[12:15], v33 offset:2048\n\t"
    "ds_read_b64_tr_b16 a[28:29], v34 offset:96\n\t"
    "v_mfma_f32_16x16x32_bf16 v[108:111], a[76:79], a[56:59], v[108:111]\n\t"
    "v_mfma_f32_16x16x32_bf16 v[144:147], a[76:79], a[60:63], v[144:147]\n\t"
    "buffer_load_dwordx4 %16, s[44:47], s57 offen offset:3072 lds\n\t"
    "v_mfma_f32_16x16x32_bf16 v[180:183], a[76:79], a[64:67], v[180:183]\n\t"
    "ds_read_b64_tr_b16 a[30:31], v34 offset:4704\n\t"
    "s_add_u32 m0, m0, 0x1000\n\t"
    "v_mfma_f32_16x16x32_bf16 v[76:79], a[80:83], a[52:55], v[76:79]\n\t"
    "ds_read_b64_tr_b16 a[32:33], v34 offset:128\n\t"
    "ds_read_b64_tr_b16 a[34:35], v34 offset:4736\n\t"
    "v_mfma_f32_16x16x32_bf16 v[112:115], a[80:83], a[56:59], v[112:115]\n\t"
    "buffer_load_dwordx4 %17, s[44:47], s57 offen offset:0 lds\n\t"
    "v_mfma_f32_16x16x32_bf16 v[148:151], a[80:83], a[60:63], v[148:151]\n\t"
    "ds_read_b64_tr_b16 a[36:37], v34 offset:160\n\t"
    "s_add_u32 m0, s62, s69\n\t"
    "v_mfma_f32_16x16x32_bf16 v[184:187], a[80:83], a[64:67], v[184:187]\n\t"
    "ds_read_b64_tr_b16 a[38:39], v34 offset:4768\n\t"
    "ds_read_b64_tr_b16 a[40:41], v34 offset:192\n\t"
    "v_mfma_f32_16x16x32_bf16 v[80:83], a[84:87], a[52:55], v[80:83]\n\t"
    "buffer_load_dwordx4 %11, s[40:43], s56 offen offset:0 sc1 lds\n\t"
    "v_mfma_f32_16x16x32_bf16 v[116:119], a[84:87], a[56:59], v[116:119]\n\t"
    "ds_read_b64_tr_b16 a[42:43], v34 offset:4800\n\t"
    "ds_read_b64_tr_b16 a[44:45], v34 offset:224\n\t"
    "v_mfma_f32_16x16x32_bf16 v[152:155], a[84:87], a[60:63], v[152:155]\n\t"
    "ds_read_b64_tr_b16 a[46:47], v34 offset:4832\n\t"
    "v_mfma_f32_16x16x32_bf16 v[188:191], a[84:87], a[64:67], v[188:191]\n\t"
    "buffer_load_dwordx4 %11, s[40:43], s56 offen offset:1024 sc1 lds\n\t"
    "v_mfma_f32_16x16x32_bf16 v[84:87], a[88:91], a[52:55], v[84:87]\n\t"
    "ds_read_b64_tr_b16 a[48:49], v34 offset:256\n\t"
    "ds_read_b64_tr_b16 a[50:51], v34 offset:4864\n\t"
    "v_mfma_f32_16x16x32_bf16 v[120:123], a[88:91], a[56:59], v[120:123]\n\t"
    "v_mfma_f32_16x16x32_bf16 v[156:159], a[88:91], a[60:63], v[156:159]\n\t"
    "buffer_load_dwordx4 %11, s[40:43], s56 offen offset:2048 sc1 lds\n\t"
    "v_mfma_f32_16x16x32_bf16 v[192:195], a[88:91], a[64:67], v[192:195]\n\t"
    "v_mfma_f32_16x16x32_bf16 v[88:91], a[92:95], a[52:55], v[88:91]\n\t"
    "v_mfma_f32_16x16x32_bf16 v[124:127], a[92:95], a[56:59], v[124:127]\n\t"
    "buffer_load_dwordx4 %11, s[40:43], s56 offen offset:3072 sc1 lds\n\t"
    "v_mfma_f32_16x16x32_bf16 v[160:163], a[92:95], a[60:63], v[160:163]\n\t"
    "v_add_u32 v32, s63, v35\n\t"
    "v_mfma_f32_16x16x32_bf16 v[196:199], a[92:95], a[64:67], v[196:199]\n\t"
    "v_add_u32 v33, s63, v36\n\t"
    "v_mfma_f32_16x16x32_bf16 v[92:95], a[96:99], a[52:55], v[92:95]\n\t"
    "v_add_u32 v34, s63, %12\n\t"
    "s_add_u32 s62, s62, 0x9000\n\t"
    "s_cmp_eq_u32 s62, 0x24000\n\t"
    "s_cselect_b32 s62, 0, s62\n\t"
    "v_mfma_f32_16x16x32_bf16 v[128:131], a[96:99], a[56:59], v[128:131]\n\t"
    "s_add_u32 s56, s56, 0x4000\n\t"
    "s_add_u32 s57, s57, 0x12000\n\t"
    "v_mfma_f32_16x16x32_bf16 v[164:167], a[96:99], a[60:63], v[164:167]\n\t"
    "s_add_u32 s63, s63, 0x9000\n\t"
    "s_cmp_eq_u32 s63, 0x24000\n\t"
    "s_cselect_b32 s63, 0, s63\n\t"
    "s_sub_u32 s58, s58, 1\n\t"
    "v_mfma_f32_16x16x32_bf16 v[200:203], a[96:99], a[64:67], v[200:203]\n\t"
    "v_mfma_f32_16x16x32_bf16 v[96:99], a[100:103], a[52:55], v[96:99]\n\t"
    "v_mfma_f32_16x16x32_bf16 v[132:135], a[100:103], a[56:59], v[132:135]\n\t"
    "v_mfma_f32_16x16x32_bf16 v[168:171], a[100:103], a[60:63], v[168:171]\n\t"
    "v_mfma_f32_16x16x32_bf16 v[204:207], a[100:103], a[64:67], v[204:207]\n\t"
    "s_cmp_eq_u32 s58, 0\n\t"
    "s_mov_b32 s79, 1\n\t"
    "s_cbranch_scc1 o_wrap_%=\n\t"
    "; ---- b1x\n\t"
    "o_back1_%=:\n\t"
    "s_sub_u32 s66, s66, 1\n\t"
    "s_cmp_eq_u32 s66, 0\n\t"
    "s_cbranch_scc1 o_done_%=\n\t"
    "; ---- b0\n\t"
    "o_b0_%=:\n\t"
    "s_waitcnt vmcnt(18) lgkmcnt(0)\n\t"
    "s_barrier\n\t"
    "v_mfma_f32_16x16x32_bf16 v[64:67], a[16:19], a[0:3], v[64:67]\n\t"
    "ds_read_b128 a[52:55], v32 offset:0\n\t"
    "s_add_u32 m0, s62, s70\n\t"
    "v_mfma_f32_16x16x32_bf16 v[100:103], a[16:19], a[4:7], v[100:103]\n\t"
    "buffer_load_dwordx4 %13, s[44:47], s57 offen offset:0 lds\n\t"
    "v_mfma_f32_16x16x32_bf16 v[136:139], a[16:19], a[8:11], v[136:139]\n\t"
    "ds_read_b64_tr_b16 a[68:69], v34 offset:0\n\t"
    "ds_read_b64_tr_b16 a[70:71], v34 offset:4608\n\t"
    "v_mfma_f32_16x16x32_bf16 v[172:175], a[16:19], a[12:15], v[172:175]\n\t"
    "ds_read_b128 a[56:59], v32 offset:2048\n\t"
    "ds_read_b64_tr_b16 a[72:73], v34 offset:32\n\t"
    "v_mfma_f32_16x16x32_bf16 v[68:71], a[20:23], a[0:3], v[68:71]\n\t"
    "buffer_load_dwordx4 %14, s[44:47], s57 offen offset:1024 lds\n\t"
    "v_mfma_f32_16x16x32_bf16 v[104:107], a[20:23], a[4:7], v[104:107]\n\t"
    "ds_read_b64_tr_b16 a[74:75], v34 offset:4640\n\t"
    "ds_read_b128 a[60:63], v33 offset:0\n\t"
    "v_mfma_f32_16x16x32_bf16 v[140:143], a[20:23], a[8:11], v[140:143]\n\t"
    "ds_read_b64_tr_b16 a[76:77], v34 offset:64\n\t"
    "ds_read_b64_tr_b16 a[78:79], v34 offset:4672\n\t"
    "v_mfma_f32_16x16x32_bf16 v[176:179], a[20:23], a[12:15], v[176:179]\n\t"
    "buffer_load_dwordx4 %15, s[44:47], s57 offen offset:2048 lds\n\t"
    "v_mfma_f32_16x16x32_bf16 v[72:75], a[24:27], a[0:3], v[72:75]\n\t"
    "ds_read_b128 a[64:67], v33 offset:2048\n\t"
    "ds_read_b64_tr_b16 a[80:81], v34 offset:96\n\t"
    "v_mfma_f32_16x16x32_bf16 v[108:111], a[24:27], a[4:7], v[108:111]\n\t"
    "v_mfma_f32_16x16x32_bf16 v[144:147], a[24:27], a[8:11], v[144:147]\n\t"
    "buffer_load_dwordx4 %16, s[44:47], s57 offen offset:3072 lds\n\t"
    "v_mfma_f32_16x16x32_bf16 v[180:183], a[24:27], a[12:15], v[180:183]\n\t"
    "ds_read_b64_tr_b16 a[82:83], v34 offset:4704\n\t"
    "s_add_u32 m0, m0, 0x1000\n\t"
    "v_mfma_f32_16x16x32_bf16 v[76:79], a[28:31], a[0:3], v[76:79]\n\t"
    "ds_read_b64_tr_b16 a[84:85], v34 offset:128\n\t"
    "ds_read_b64_tr_b16 a[86:87], v34 offset:4736\n\t"
    "v_mfma_f32_16x16x32_bf16 v[112:115], a[28:31], a[4:7], v[112:115]\n\t"
    "buffer_load_dwordx4 %17, s[44:47], s57 offen offset:0 lds\n\t"
    "v_mfma_f32_16x16x32_bf16 v[148:151], a[28:31], a[8:11], v[148:151]\n\t"
    "ds_read_b64_tr_b16 a[88:89], v34 offset:160\n\t"
    "s_add_u32 m0, s62, s69\n\t"
    "v_mfma_f32_16x16x32_bf16 v[184:187], a[28:31], a[12:15], v[184:187]\n\t"
    "ds_read_b64_tr_b16 a[90:91], v34 offset:4768\n\t"
    "ds_read_b64_tr_b16 a[92:93], v34 offset:192\n\t"
    "v_mfma_f32_16x16x32_bf16 v[80:83], a[32:35], a[0:3], v[80:83]\n\t"
    "buffer_load_dwordx4 %11, s[40:43], s56 offen offset:0 sc1 lds\n\t"
    "v_mfma_f32_16x16x32_bf16 v[116:119], a[32:35], a[4:7], v[116:119]\n\t"
    "ds_read_b64_tr_b16 a[94:95], v34 offset:4800\n\t"
    "ds_read_b64_tr_b16 a[96:97], v34 offset:224\n\t"
    "v_mfma_f32_16x16x32_bf16 v[152:155], a[32:35], a[8:11], v[152:155]\n\t"
    "ds_read_b64_tr_b16 a[98:99], v34 offset:4832\n\t"
    "v_mfma_f32_16x16x32_bf16 v[188:191], a[32:35], a[12:15], v[188:191]\n\t"
    "buffer_load_dwordx4 %11, s[40:43], s56 offen offset:1024 sc1 lds\n\t"
    "v_mfma_f32_16x16x32_bf16 v[84:87], a[36:39], a[0:3], v[84:87]\n\t"
    "ds_read_b64_tr_b16 a[100:101], v34 offset:256\n\t"
    "ds_read_b64_tr_b16 a[102:103], v34 offset:4864\n\t"
    "v_mfma_f32_16x16x32_bf16 v[120:123], a[36:39], a[4:7], v[120:123]\n\t"
    "v_mfma_f32_16x16x32_bf16 v[156:159], a[36:39], a[8:11], v[156:159]\n\t"
    "buffer_load_dwordx4 %11, s[40:43], s56 offen offset:2048 sc1 lds\n\t"
    "v_mfma_f32_16x16x32_bf16 v[192:195], a[36:39], a[12:15], v[192:195]\n\t"
    "v_mfma_f32_16x16x32_bf16 v[88:91], a[40:43], a[0:3], v[88:91]\n\t"
    "v_mfma_f32_16x16x32_bf16 v[124:127], a[40:43], a[4:7], v[124:127]\n\t"
    "buffer_load_dwordx4 %11, s[40:43], s56 offen offset:3072 sc1 lds\n\t"
    "v_mfma_f32_16x16x32_bf16 v[160:163], a[40:43], a[8:11], v[160:163]\n\t"
    "v_add_u32 v32, s63, v35\n\t"
    "v_mfma_f32_16x16x32_bf16 v[196:199], a[40:43], a[12:15], v[196:199]\n\t"
    "v_add_u32 v33, s63, v36\n\t"
    "v_mfma_f32_16x16x32_bf16 v[92:95], a[44:47], a[0:3], v[92:95]\n\t"
    "v_add_u32 v34, s63, %12\n\t"
    "s_add_u32 s62, s62, 0x9000\n\t"
    "s_cmp_eq_u32 s62, 0x24000\n\t"
    "s_cselect_b32 s62, 0, s62\n\t"
    "v_mfma_f32_16x16x32_bf16 v[128:131], a[44:47], a[4:7], v[128:131]\n\t"
    "s_add_u32 s56, s56, 0x4000\n\t"
    "s_add_u32 s57, s57, 0x12000\n\t"
    "v_mfma_f32_16x16x32_bf16 v[164:167], a[44:47], a[8:11], v[164:167]\n\t"
    "s_add_u32 s63, s63, 0x9000\n\t"
    "s_cmp_eq_u32 s63, 0x24000\n\t"
    "s_cselect_b32 s63, 0, s63\n\t"
    "s_sub_u32 s58, s58, 1\n\t"
    "v_mfma_f32_16x16x32_bf16 v[200:203], a[44:47], a[12:15], v[200:203]\n\t"
    "v_mfma_f32_16x16x32_bf16 v[96:99], a[48:51], a[0:3], v[96:99]\n\t"
    "v_mfma_f32_16x16x32_bf16 v[132:135], a[48:51], a[4:7], v[132:135]\n\t"
    "v_mfma_f32_16x16x32_bf16 v[168:171], a[48:51], a[8:11], v[168:171]\n\t"
    "v_mfma_f32_16x16x32_bf16 v[204:207], a[48:51], a[12:15], v[204:207]\n\t"
    "s_cmp_eq_u32 s58, 0\n\t"
    "s_mov_b32 s79, 2\n\t"
    "s_cbranch_scc1 o_wrap_%=\n\t"
    "; ---- b0x\n\t"
    "o_back2_%=:\n\t"
    "s_sub_u32 s66, s66, 1\n\t"
    "s_cmp_eq_u32 s66, 0\n\t"
    "s_cbranch_scc0 o_b1_%=\n\t"
    "; ---- done\n\t"
    "o_done_%=:\n\t"
    "; ---- st3\n\t"
    "s_and_b32 s64, %0, 1\n\t"
    "s_add_u32 s64, s64, 3\n\t"
    "s_mul_i32 s64, s64, 0x9000\n\t"
    "s_mul_i32 s65, %0, 0x2400\n\t"
    "s_add_u32 s71, s64, s65\n\t"
    "s_xor_b32 s65, %0, 1\n\t"
    "s_mul_i32 s65, s65, 0x2400\n\t"
    "s_add_u32 s64, s64, s65\n\t"
    "v_cvt_pk_bf16_f32 v64, v64, v65\n\t"
    "v_cvt_pk_bf16_f32 v65, v66, v67\n\t"
    "v_cvt_pk_bf16_f32 v66, v100, v101\n\t"
    "v_cvt_pk_bf16_f32 v67, v102, v103\n\t"
    "s_add_u32 s67, s71, 0\n\t"
    "buffer_store_dwordx4 v[64:67], %11, s[48:51], s67 offen offset:0 sc1\n\t"
    "v_cvt_pk_bf16_f32 v68, v68, v69\n\t"
    "v_cvt_pk_bf16_f32 v69, v70, v71\n\t"
    "v_cvt_pk_bf16_f32 v70, v104, v105\n\t"
    "v_cvt_pk_bf16_f32 v71, v106, v107\n\t"
    "s_add_u32 s67, s71, 0\n\t"
    "buffer_store_dwordx4 v[68:71], %11, s[48:51], s67 offen offset:1024 sc1\n\t"
    "v_cvt_pk_bf16_f32 v72, v72, v73\n\t"
    "v_cvt_pk_bf16_f32 v73, v74, v75\n\t"
    "v_cvt_pk_bf16_f32 v74, v108, v109\n\t"
    "v_cvt_pk_bf16_f32 v75, v110, v111\n\t"
    "s_add_u32 s67, s71, 0\n\t"
    "buffer_store_dwordx4 v[72:75], %11, s[48:51], s67 offen offset:2048 sc1\n\t"
    "v_cvt_pk_bf16_f32 v76, v76, v77\n\t"
    "v_cvt_pk_bf16_f32 v77, v78, v79\n\t"
    "v_cvt_pk_bf16_f32 v78, v112, v113\n\t"
    "v_cvt_pk_bf16_f32 v79, v114, v115\n\t"
    "s_add_u32 s67, s71, 0\n\t"
    "buffer_store_dwordx4 v[76:79], %11, s[48:51], s67 offen offset:3072 sc1\n\t"
    "v_cvt_pk_bf16_f32 v80, v80, v81\n\t"
    "v_cvt_pk_bf16_f32 v81, v82, v83\n\t"
    "v_cvt_pk_bf16_f32 v82, v116, v117\n\t"
    "v_cvt_pk_bf16_f32 v83, v118, v119\n\t"
    "s_add_u32 s67, s71, 0x1000\n\t"
    "buffer_store_dwordx4 v[80:83], %11, s[48:51], s67 offen offset:0 sc1\n\t"
    "v_cvt_pk_bf16_f32 v84, v84, v85\n\t"
    "v_cvt_pk_bf16_f32 v85, v86, v87\n\t"
    "v_cvt_pk_bf16_f32 v86, v120, v121\n\t"
    "v_cvt_pk_bf16_f32 v87, v122, v123\n\t"
    "s_add_u32 s67, s71, 0x1000\n\t"
    "buffer_store_dwordx4 v[84:87], %11, s[48:51], s67 offen offset:1024 sc1\n\t"
    "v_cvt_pk_bf16_f32 v88, v88, v89\n\t"
    "v_cvt_pk_bf16_f32 v89, v90, v91\n\t"
    "v_cvt_pk_bf16_f32 v90, v124, v125\n\t"
    "v_cvt_pk_bf16_f32 v91, v126, v127\n\t"
    "s_add_u32 s67, s71, 0x1000\n\t"
    "buffer_store_dwordx4 v[88:91], %11, s[48:51], s67 offen offset:2048 sc1\n\t"
    "v_cvt_pk_bf16_f32 v92, v92, v93\n\t"
    "v_cvt_pk_bf16_f32 v93, v94, v95\n\t"
    "v_cvt_pk_bf16_f32 v94, v128, v129\n\t"
    "v_cvt_pk_bf16_f32 v95, v130, v131\n\t"
    "s_add_u32 s67, s71, 0x1000\n\t"
    "buffer_store_dwordx4 v[92:95], %11, s[48:51], s67 offen offset:3072 sc1\n\t"
    "v_cvt_pk_bf16_f32 v96, v96, v97\n\t"
    "v_cvt_pk_bf16_f32 v97, v98, v99\n\t"
    "v_cvt_pk_bf16_f32 v98, v132, v133\n\t"
    "v_cvt_pk_bf16_f32 v99, v134, v135\n\t"
    "s_add_u32 s67, s71, 0x2000\n\t"
    "buffer_store_dwordx4 v[96:99], %11, s[48:51], s67 offen offset:0 sc1\n\t"
    "v_cvt_pk_bf16_f32 v136, v136, v137\n\t"
    "v_cvt_pk_bf16_f32 v137, v138, v139\n\t"
    "v_cvt_pk_bf16_f32 v138, v172, v173\n\t"
    "v_cvt_pk_bf16_f32 v139, v174, v175\n\t"
    "s_add_u32 s67, s64, 0\n\t"
    "buffer_store_dwordx4 v[136:139], %11, s[48:51], s67 offen offset:0 sc1\n\t"
    "v_cvt_pk_bf16_f32 v140, v140, v141\n\t"
    "v_cvt_pk_bf16_f32 v141, v142, v143\n\t"
    "v_cvt_pk_bf16_f32 v142, v176, v177\n\t"
    "v_cvt_pk_bf16_f32 v143, v178, v179\n\t"
    "s_add_u32 s67, s64, 0\n\t"
    "buffer_store_dwordx4 v[140:143], %11, s[48:51], s67 offen offset:1024 sc1\n\t"
    "v_cvt_pk_bf16_f32 v144, v144, v145\n\t"
    "v_cvt_pk_bf16_f32 v145, v146, v147\n\t"
    "v_cvt_pk_bf16_f32 v146, v180, v181\n\t"
    "v_cvt_pk_bf16_f32 v147, v182, v183\n\t"
    "s_add_u32 s67, s64, 0\n\t"
    "buffer_store_dwordx4 v[144:147], %11, s[48:51], s67 offen offset:2048 sc1\n\t"
    "v_cvt_pk_bf16_f32 v148, v148, v149\n\t"
    "v_cvt_pk_bf16_f32 v149, v150, v151\n\t"
    "v_cvt_pk_bf16_f32 v150, v184, v185\n\t"
    "v_cvt_pk_bf16_f32 v151, v186, v187\n\t"
    "s_add_u32 s67, s64, 0\n\t"
    "buffer_store_dwordx4 v[148:151], %11, s[48:51], s67 offen offset:3072 sc1\n\t"
    "v_cvt_pk_bf16_f32 v152, v152, v153\n\t"
    "v_cvt_pk_bf16_f32 v153, v154, v155\n\t"
    "v_cvt_pk_bf16_f32 v154, v188, v189\n\t"
    "v_cvt_pk_bf16_f32 v155, v190, v191\n\t"
    "s_add_u32 s67, s64, 0x1000\n\t"
    "buffer_store_dwordx4 v[152:155], %11, s[48:51], s67 offen offset:0 sc1\n\t"
    "v_cvt_pk_bf16_f32 v156, v156, v157\n\t"
    "v_cvt_pk_bf16_f32 v157, v158, v159\n\t"
    "v_cvt_pk_bf16_f32 v158, v192, v193\n\t"
    "v_cvt_pk_bf16_f32 v159, v194, v195\n\t"
    "s_add_u32 s67, s64, 0x1000\n\t"
    "buffer_store_dwordx4 v[156:159], %11, s[48:51], s67 offen offset:1024 sc1\n\t"
    "v_cvt_pk_bf16_f32 v160, v160, v161\n\t"
    "v_cvt_pk_bf16_f32 v161, v162, v163\n\t"
    "v_cvt_pk_bf16_f32 v162, v196, v197\n\t"
    "v_cvt_pk_bf16_f32 v163, v198, v199\n\t"
    "s_add_u32 s67, s64, 0x1000\n\t"
    "buffer_store_dwordx4 v[160:163], %11, s[48:51], s67 offen offset:2048 sc1\n\t"
    "v_cvt_pk_bf16_f32 v164, v164, v165\n\t"
    "v_cvt_pk_bf16_f32 v165, v166, v167\n\t"
    "v_cvt_pk_bf16_f32 v166, v200, v201\n\t"
    "v_cvt_pk_bf16_f32 v167, v202, v203\n\t"
    "s_add_u32 s67, s64, 0x1000\n\t"
    "buffer_store_dwordx4 v[164:167], %11, s[48:51], s67 offen offset:3072 sc1\n\t"
    "v_cvt_pk_bf16_f32 v168, v168, v169\n\t"
    "v_cvt_pk_bf16_f32 v169, v170, v171\n\t"
    "v_cvt_pk_bf16_f32 v170, v204, v205\n\t"
    "v_cvt_pk_bf16_f32 v171, v206, v207\n\t"
    "s_add_u32 s67, s64, 0x2000\n\t"
    "buffer_store_dwordx4 v[168:171], %11, s[48:51], s67 offen offset:0 sc1\n\t"
    "s_mov_b32 s68, 1\n\t"
    "s_sub_u32 s66, %8, 2\n\t"
    "s_add_u32 s78, s59, 0x20000\n\t"
    "s_waitcnt vmcnt(36) lgkmcnt(0)\n\t"
    "s_barrier\n\t"
    "v_mfma_f32_16x16x32_bf16 v[64:67], a[68:71], a[52:55], 0\n\t"
    "ds_read_b128 a[0:3], v32 offset:0\n\t"
    "s_add_u32 m0, s62, s70\n\t"
    "v_mfma_f32_16x16x32_bf16 v[100:103], a[68:71], a[56:59], 0\n\t"
    "buffer_load_dwordx4 %13, s[44:47], s57 offen offset:0 lds\n\t"
    "v_mfma_f32_16x16x32_bf16 v[136:139], a[68:71], a[60:63], 0\n\t"
    "ds_read_b64_tr_b16 a[16:17], v34 offset:0\n\t"
    "ds_read_b64_tr_b16 a[18:19], v34 offset:4608\n\t"
    "v_mfma_f32_16x16x32_bf16 v[172:175], a[68:71], a[64:67], 0\n\t"
    "ds_read_b128 a[4:7], v32 offset:2048\n\t"
    "ds_read_b64_tr_b16 a[20:21], v34 offset:32\n\t"
    "v_mfma_f32_16x16x32_bf16 v[68:71], a[72:75], a[52:55], 0\n\t"
    "buffer_load_dwordx4 %14, s[44:47], s57 offen offset:1024 lds\n\t"
    "v_mfma_f32_16x16x32_bf16 v[104:107], a[72:75], a[56:59], 0\n\t"
    "ds_read_b64_tr_b16 a[22:23], v34 offset:4640\n\t"
    "ds_read_b128 a[8:11], v33 offset:0\n\t"
    "v_mfma_f32_16x16x32_bf16 v[140:143], a[72:75], a[60:63], 0\n\t"
    "ds_read_b64_tr_b16 a[24:25], v34 offset:64\n\t"
    "ds_read_b64_tr_b16 a[26:27], v34 offset:4672\n\t"
    "v_mfma_f32_16x16x32_bf16 v[176:179], a[72:75], a[64:67], 0\n\t"
    "buffer_load_dwordx4 %15, s[44:47], s57 offen offset:2048 lds\n\t"
    "v_mfma_f32_16x16x32_bf16 v[72:75], a[76:79], a[52:55], 0\n\t"
    "ds_read_b128 a[12:15], v33 offset:2048\n\t"
    "ds_read_b64_tr_b16 a[28:29], v34 offset:96\n\t"
    "v_mfma_f32_16x16x32_bf16 v[108:111], a[76:79], a[56:59], 0\n\t"
    "v_mfma_f32_16x16x32_bf16 v[144:147], a[76:79], a[60:63], 0\n\t"
    "buffer_load_dwordx4 %16, s[44:47], s57 offen offset:3072 lds\n\t"
    "v_mfma_f32_16x16x32_bf16 v[180:183], a[76:79], a[64:67], 0\n\t"
    "ds_read_b64_tr_b16 a[30:31], v34 offset:4704\n\t"
    "s_add_u32 m0, m0, 0x1000\n\t"
    "v_mfma_f32_16x16x32_bf16 v[76:79], a[80:83], a[52:55], 0\n\t"
    "ds_read_b64_tr_b16 a[32:33], v34 offset:128\n\t"
    "ds_read_b64_tr_b16 a[34:35], v34 offset:4736\n\t"
    "v_mfma_f32_16x16x32_bf16 v[112:115], a[80:83], a[56:59], 0\n\t"
    "buffer_load_dwordx4 %17, s[44:47], s57 offen offset:0 lds\n\t"
    "v_mfma_f32_16x16x32_bf16 v[148:151], a[80:83], a[60:63], 0\n\t"
    "ds_read_b64_tr_b16 a[36:37], v34 offset:160\n\t"
    "s_add_u32 m0, s62, s69\n\t"
    "v_mfma_f32_16x16x32_bf16 v[184:187], a[80:83], a[64:67], 0\n\t"
    "ds_read_b64_tr_b16 a[38:39], v34 offset:4768\n\t"
    "ds_read_b64_tr_b16 a[40:41], v34 offset:192\n\t"
    "v_mfma_f32_16x16x32_bf16 v[80:83], a[84:87], a[52:55], 0\n\t"
    "buffer_load_dwordx4 %11, s[40:43], s56 offen offset:0 sc1 lds\n\t"
    "v_mfma_f32_16x16x32_bf16 v[116:119], a[84:87], a[56:59], 0\n\t"
    "ds_read_b64_tr_b16 a[42:43], v34 offset:4800\n\t"
    "ds_read_b64_tr_b16 a[44:45], v34 offset:224\n\t"
    "v_mfma_f32_16x16x32_bf16 v[152:155], a[84:87], a[60:63], 0\n\t"
    "ds_read_b64_tr_b16 a[46:47], v34 offset:4832\n\t"
    "v_mfma_f32_16x16x32_bf16 v[188:191], a[84:87], a[64:67], 0\n\t"
    "buffer_load_dwordx4 %11, s[40:43], s56 offen offset:1024 sc1 lds\n\t"
    "v_mfma_f32_16x16x32_bf16 v[84:87], a[88:91], a[52:55], 0\n\t"
    "ds_read_b64_tr_b16 a[48:49], v34 offset:256\n\t"
    "ds_read_b64_tr_b16 a[50:51], v34 offset:4864\n\t"
    "v_mfma_f32_16x16x32_bf16 v[120:123], a[88:91], a[56:59], 0\n\t"
    "v_mfma_f32_16x16x32_bf16 v[156:159], a[88:91], a[60:63], 0\n\t"
    "buffer_load_dwordx4 %11, s[40:43], s56 offen offset:2048 sc1 lds\n\t"
    "v_mfma_f32_16x16x32_bf16 v[192:195], a[88:91], a[64:67], 0\n\t"
    "v_mfma_f32_16x16x32_bf16 v[88:91], a[92:95], a[52:55], 0\n\t"
    "v_mfma_f32_16x16x32_bf16 v[124:127], a[92:95], a[56:59], 0\n\t"
    "buffer_load_dwordx4 %11, s[40:43], s56 offen offset:3072 sc1 lds\n\t"
    "v_mfma_f32_16x16x32_bf16 v[160:163], a[92:95], a[60:63], 0\n\t"
    "v_add_u32 v32, s63, v35\n\t"
    "v_mfma_f32_16x16x32_bf16 v[196:199], a[92:95], a[64:67], 0\n\t"
    "v_add_u32 v33, s63, v36\n\t"
    "v_mfma_f32_16x16x32_bf16 v[92:95], a[96:99], a[52:55], 0\n\t"
    "v_add_u32 v34, s63, %12\n\t"
    "s_add_u32 s62, s62, 0x9000\n\t"
    "s_cmp_eq_u32 s62, 0x24000\n\t"
    "s_cselect_b32 s62, 0, s62\n\t"
    "v_mfma_f32_16x16x32_bf16 v[128:131], a[96:99], a[56:59], 0\n\t"
    "s_add_u32 s56, s56, 0x4000\n\t"
    "s_add_u32 s57, s57, 0x12000\n\t"
    "v_mfma_f32_16x16x32_bf16 v[164:167], a[96:99], a[60:63], 0\n\t"
    "s_add_u32 s63, s63, 0x9000\n\t"
    "s_cmp_eq_u32 s63, 0x24000\n\t"
    "s_cselect_b32 s63, 0, s63\n\t"
    "s_sub_u32 s58, s58, 1\n\t"
    "v_mfma_f32_16x16x32_bf16 v[200:203], a[96:99], a[64:67], 0\n\t"
    "v_mfma_f32_16x16x32_bf16 v[96:99], a[100:103], a[52:55], 0\n\t"
    "v_mfma_f32_16x16x32_bf16 v[132:135], a[100:103], a[56:59], 0\n\t"
    "v_mfma_f32_16x16x32_bf16 v[168:171], a[100:103], a[60:63], 0\n\t"
    "v_mfma_f32_16x16x32_bf16 v[204:207], a[100:103], a[64:67], 0\n\t"
    "s_waitcnt vmcnt(0) lgkmcnt(0)\n\t"
    "s_barrier\n\t"
    "s_cmp_lg_u32 %0, 0\n\t"
    "s_cbranch_scc1 pubp3_%=\n\t"
    "v_mov_b64 v[40:41], %19\n\t"
    "v_mov_b32 v42, 0\n\t"
    "s_mov_b64 s[76:77], exec\n\t"
    "s_mov_b64 exec, 1\n\t"
    "buffer_store_dwordx2 v[40:41], v42, s[72:75], s78 offen offset:0 sc1\n\t"
    "s_mov_b64 exec, s[76:77]\n\t"
    "pubp3_%=:\n\t"
    "v_mfma_f32_16x16x32_bf16 v[64:67], a[16:19], a[0:3], v[64:67]\n\t"
    "ds_read_b128 a[52:55], v32 offset:0\n\t"
    "s_add_u32 m0, s62, s70\n\t"
    "v_mfma_f32_16x16x32_bf16 v[100:103], a[16:19], a[4:7], v[100:103]\n\t"
    "buffer_load_dwordx4 %13, s[44:47], s57 offen offset:0 lds\n\t"
    "v_mfma_f32_16x16x32_bf16 v[136:139], a[16:19], a[8:11], v[136:139]\n\t"
    "ds_read_b64_tr_b16 a[68:69], v34 offset:0\n\t"
    "ds_read_b64_tr_b16 a[70:71], v34 offset:4608\n\t"
    "v_mfma_f32_16x16x32_bf16 v[172:175], a[16:19], a[12:15], v[172:175]\n\t"
    "ds_read_b128 a[56:59], v32 offset:2048\n\t"
    "ds_read_b64_tr_b16 a[72:73], v34 offset:32\n\t"
    "v_mfma_f32_16x16x32_bf16 v[68:71], a[20:23], a[0:3], v[68:71]\n\t"
    "buffer_load_dwordx4 %14, s[44:47], s57 offen offset:1024 lds\n\t"
    "v_mfma_f32_16x16x32_bf16 v[104:107], a[20:23], a[4:7], v[104:107]\n\t"
    "ds_read_b64_tr_b16 a[74:75], v34 offset:4640\n\t"
    "ds_read_b128 a[60:63], v33 offset:0\n\t"
    "v_mfma_f32_16x16x32_bf16 v[140:143], a[20:23], a[8:11], v[140:143]\n\t"
    "ds_read_b64_tr_b16 a[76:77], v34 offset:64\n\t"
    "ds_read_b64_tr_b16 a[78:79], v34 offset:4672\n\t"
    "v_mfma_f32_16x16x32_bf16 v[176:179], a[20:23], a[12:15], v[176:179]\n\t"
    "buffer_load_dwordx4 %15, s[44:47], s57 offen offset:2048 lds\n\t"
    "v_mfma_f32_16x16x32_bf16 v[72:75], a[24:27], a[0:3], v[72:75]\n\t"
    "ds_read_b128 a[64:67], v33 offset:2048\n\t"
    "ds_read_b64_tr_b16 a[80:81], v34 offset:96\n\t"
    "v_mfma_f32_16x16x32_bf16 v[108:111], a[24:27], a[4:7], v[108:111]\n\t"
    "v_mfma_f32_16x16x32_bf16 v[144:147], a[24:27], a[8:11], v[144:147]\n\t"
    "buffer_load_dwordx4 %16, s[44:47], s57 offen offset:3072 lds\n\t"
    "v_mfma_f32_16x16x32_bf16 v[180:183], a[24:27], a[12:15], v[180:183]\n\t"
    "ds_read_b64_tr_b16 a[82:83], v34 offset:4704\n\t"
    "s_add_u32 m0, m0, 0x1000\n\t"
    "v_mfma_f32_16x16x32_bf16 v[76:79], a[28:31], a[0:3], v[76:79]\n\t"
    "ds_read_b64_tr_b16 a[84:85], v34 offset:128\n\t"
    "ds_read_b64_tr_b16 a[86:87], v34 offset:4736\n\t"
    "v_mfma_f32_16x16x32_bf16 v[112:115], a[28:31], a[4:7], v[112:115]\n\t"
    "buffer_load_dwordx4 %17, s[44:47], s57 offen offset:0 lds\n\t"
    "v_mfma_f32_16x16x32_bf16 v[148:151], a[28:31], a[8:11], v[148:151]\n\t"
    "ds_read_b64_tr_b16 a[88:89], v34 offset:160\n\t"
    "s_add_u32 m0, s62, s69\n\t"
    "v_mfma_f32_16x16x32_bf16 v[184:187], a[28:31], a[12:15], v[184:187]\n\t"
    "ds_read_b64_tr_b16 a[90:91], v34 offset:4768\n\t"
    "ds_read_b64_tr_b16 a[92:93], v34 offset:192\n\t"
    "v_mfma_f32_16x16x32_bf16 v[80:83], a[32:35], a[0:3], v[80:83]\n\t"
    "buffer_load_dwordx4 %11, s[40:43], s56 offen offset:0 sc1 lds\n\t"
    "v_mfma_f32_16x16x32_bf16 v[116:119], a[32:35], a[4:7], v[116:119]\n\t"
    "ds_read_b64_tr_b16 a[94:95], v34 offset:4800\n\t"
    "ds_read_b64_tr_b16 a[96:97], v34 offset:224\n\t"
    "v_mfma_f32_16x16x32_bf16 v[152:155], a[32:35], a[8:11], v[152:155]\n\t"
    "ds_read_b64_tr_b16 a[98:99], v34 offset:4832\n\t"
    "v_mfma_f32_16x16x32_bf16 v[188:191], a[32:35], a[12:15], v[188:191]\n\t"
    "buffer_load_dwordx4 %11, s[40:43], s56 offen offset:1024 sc1 lds\n\t"
    "v_mfma_f32_16x16x32_bf16 v[84:87], a[36:39], a[0:3], v[84:87]\n\t"
    "ds_read_b64_tr_b16 a[100:101], v34 offset:256\n\t"
    "ds_read_b64_tr_b16 a[102:103], v34 offset:4864\n\t"
    "v_mfma_f32_16x16x32_bf16 v[120:123], a[36:39], a[4:7], v[120:123]\n\t"
    "v_mfma_f32_16x16x32_bf16 v[156:159], a[36:39], a[8:11], v[156:159]\n\t"
    "buffer_load_dwordx4 %11, s[40:43], s56 offen offset:2048 sc1 lds\n\t"
    "v_mfma_f32_16x16x32_bf16 v[192:195], a[36:39], a[12:15], v[192:195]\n\t"
    "v_mfma_f32_16x16x32_bf16 v[88:91], a[40:43], a[0:3], v[88:91]\n\t"
    "v_mfma_f32_16x16x32_bf16 v[124:127], a[40:43], a[4:7], v[124:127]\n\t"
    "buffer_load_dwordx4 %11, s[40:43], s56 offen offset:3072 sc1 lds\n\t"
    "v_mfma_f32_16x16x32_bf16 v[160:163], a[40:43], a[8:11], v[160:163]\n\t"
    "v_add_u32 v32, s63, v35\n\t"
    "v_mfma_f32_16x16x32_bf16 v[196:199], a[40:43], a[12:15], v[196:199]\n\t"
    "v_add_u32 v33, s63, v36\n\t"
    "v_mfma_f32_16x16x32_bf16 v[92:95], a[44:47], a[0:3], v[92:95]\n\t"
    "v_add_u32 v34, s63, %12\n\t"
    "s_add_u32 s62, s62, 0x9000\n\t"
    "s_cmp_eq_u32 s62, 0x24000\n\t"
    "s_cselect_b32 s62, 0, s62\n\t"
    "v_mfma_f32_16x16x32_bf16 v[128:131], a[44:47], a[4:7], v[128:131]\n\t"
    "s_add_u32 s56, s56, 0x4000\n\t"
    "s_add_u32 s57, s57, 0x12000\n\t"
    "v_mfma_f32_16x16x32_bf16 v[164:167], a[44:47], a[8:11], v[164:167]\n\t"
    "s_add_u32 s63, s63, 0x9000\n\t"
    "s_cmp_eq_u32 s63, 0x24000\n\t"
    "s_cselect_b32 s63, 0, s63\n\t"
    "s_sub_u32 s58, s58, 1\n\t"
    "v_mfma_f32_16x16x32_bf16 v[200:203], a[44:47], a[12:15], v[200:203]\n\t"
    "v_mfma_f32_16x16x32_bf16 v[96:99], a[48:51], a[0:3], v[96:99]\n\t"
    "v_mfma_f32_16x16x32_bf16 v[132:135], a[48:51], a[4:7], v[132:135]\n\t"
    "v_mfma_f32_16x16x32_bf16 v[168:171], a[48:51], a[8:11], v[168:171]\n\t"
    "v_mfma_f32_16x16x32_bf16 v[204:207], a[48:51], a[12:15], v[204:207]\n\t"
    "s_branch o_b1_%=\n\t"
    "; ---- wrap\n\t"
    "o_wrap_%=:\n\t"
    "s_cmp_eq_u32 %8, s68\n\t"
    "s_cbranch_scc1 o_tailA_%=\n\t"
    "s_cmp_lg_u32 s68, 0\n\t"
    "s_cbranch_scc1 o_tailB_%=\n\t"
    "s_mov_b32 s56, 0\n\t"
    "s_mov_b32 s57, 0\n\t"
    "; ---- wrap2\n\t"
    "s_mov_b64 s[40:41], s[60:61]\n\t"
    "s_mov_b32 s42, 0x1000000\n\t"
    "s_mov_b64 s[44:45], %6\n\t"
    "s_mov_b32 s46, %7\n\t"
    "s_mov_b32 s58, %8\n\t"
    "buffer_load_dwordx2 v[38:39], %21, s[72:75], 0 offen offset:0 sc1\n\t"
    "s_mov_b32 s67, 0x40000\n\t"
    "ow_chk_%=:\n\t"
    "s_waitcnt vmcnt(0)\n\t"
    "v_cmp_ne_u64 vcc, %19, v[38:39]\n\t"
    "s_nop 0\n\t"
    "s_cbranch_vccz ow_ok_%=\n\t"
    "s_sleep 2\n\t"
    "buffer_load_dwordx2 v[38:39], %21, s[72:75], 0 offen offset:0 sc1\n\t"
    "s_waitcnt vmcnt(0)\n\t"
    "s_sub_u32 s67, s67, 1\n\t"
    "s_cmp_lg_u32 s67, 0\n\t"
    "s_cbranch_scc1 ow_chk_%=\n\t"
    "ow_ok_%=:\n\t"
    "; ---- wret\n\t"
    "o_wret_%=:\n\t"
    "s_cmp_eq_u32 s79, 1\n\t"
    "s_cbranch_scc1 o_back1_%=\n\t"
    "s_branch o_back2_%=\n\t"
    "; ---- tailB\n\t"
    "o_tailB_%=:\n\t"
    "s_waitcnt vmcnt(18) lgkmcnt(0)\n\t"
    "s_barrier\n\t"
    "v_mfma_f32_16x16x32_bf16 v[64:67], a[16:19], a[0:3], v[64:67]\n\t"
    "ds_read_b128 a[52:55], v32 offset:0\n\t"
    "ds_read_b64_tr_b16 a[68:69], v34 offset:0\n\t"
    "v_mfma_f32_16x16x32_bf16 v[100:103], a[16:19], a[4:7], v[100:103]\n\t"
    "ds_read_b64_tr_b16 a[70:71], v34 offset:4608\n\t"
    "ds_read_b128 a[56:59], v32 offset:2048\n\t"
    "v_mfma_f32_16x16x32_bf16 v[136:139], a[16:19], a[8:11], v[136:139]\n\t"
    "ds_read_b64_tr_b16 a[72:73], v34 offset:32\n\t"
    "v_mfma_f32_16x16x32_bf16 v[172:175], a[16:19], a[12:15], v[172:175]\n\t"
    "ds_read_b64_tr_b16 a[74:75], v34 offset:4640\n\t"
    "v_mfma_f32_16x16x32_bf16 v[68:71], a[20:23], a[0:3], v[68:71]\n\t"
    "ds_read_b128 a[60:63], v33 offset:0\n\t"
    "v_mfma_f32_16x16x32_bf16 v[104:107], a[20:23], a[4:7], v[104:107]\n\t"
    "ds_read_b64_tr_b16 a[76:77], v34 offset:64\n\t"
    "v_mfma_f32_16x16x32_bf16 v[140:143], a[20:23], a[8:11], v[140:143]\n\t"
    "ds_read_b64_tr_b16 a[78:79], v34 offset:4672\n\t"
    "v_mfma_f32_16x16x32_bf16 v[176:179], a[20:23], a[12:15], v[176:179]\n\t"
    "ds_read_b128 a[64:67], v33 offset:2048\n\t"
    "v_mfma_f32_16x16x32_bf16 v[72:75], a[24:27], a[0:3], v[72:75]\n\t"
    "ds_read_b64_tr_b16 a[80:81], v34 offset:96\n\t"
    "v_mfma_f32_16x16x32_bf16 v[108:111], a[24:27], a[4:7], v[108:111]\n\t"
    "v_mfma_f32_16x16x32_bf16 v[144:147], a[24:27], a[8:11], v[144:147]\n\t"
    "ds_read_b64_tr_b16 a[82:83], v34 offset:4704\n\t"
    "v_mfma_f32_16x16x32_bf16 v[180:183], a[24:27], a[12:15], v[180:183]\n\t"
    "ds_read_b64_tr_b16 a[84:85], v34 offset:128\n\t"
    "v_mfma_f32_16x16x32_bf16 v[76:79], a[28:31], a[0:3], v[76:79]\n\t"
    "ds_read_b64_tr_b16 a[86:87], v34 offset:4736\n\t"
    "v_mfma_f32_16x16x32_bf16 v[112:115], a[28:31], a[4:7], v[112:115]\n\t"
    "ds_read_b64_tr_b16 a[88:89], v34 offset:160\n\t"
    "v_mfma_f32_16x16x32_bf16 v[148:151], a[28:31], a[8:11], v[148:151]\n\t"
    "ds_read_b64_tr_b16 a[90:91], v34 offset:4768\n\t"
    "v_mfma_f32_16x16x32_bf16 v[184:187], a[28:31], a[12:15], v[184:187]\n\t"
    "ds_read_b64_tr_b16 a[92:93], v34 offset:192\n\t"
    "v_mfma_f32_16x16x32_bf16 v[80:83], a[32:35], a[0:3], v[80:83]\n\t"
    "ds_read_b64_tr_b16 a[94:95], v34 offset:4800\n\t"
    "v_mfma_f32_16x16x32_bf16 v[116:119], a[32:35], a[4:7], v[116:119]\n\t"
    "ds_read_b64_tr_b16 a[96:97], v34 offset:224\n\t"
    "v_mfma_f32_16x16x32_bf16 v[152:155], a[32:35], a[8:11], v[152:155]\n\t"
    "ds_read_b64_tr_b16 a[98:99], v34 offset:4832\n\t"
    "v_mfma_f32_16x16x32_bf16 v[188:191], a[32:35], a[12:15], v[188:191]\n\t"
    "ds_read_b64_tr_b16 a[100:101], v34 offset:256\n\t"
    "v_mfma_f32_16x16x32_bf16 v[84:87], a[36:39], a[0:3], v[84:87]\n\t"
    "ds_read_b64_tr_b16 a[102:103], v34 offset:4864\n\t"
    "v_mfma_f32_16x16x32_bf16 v[120:123], a[36:39], a[4:7], v[120:123]\n\t"
    "v_mfma_f32_16x16x32_bf16 v[156:159], a[36:39], a[8:11], v[156:159]\n\t"
    "v_mfma_f32_16x16x32_bf16 v[192:195], a[36:39], a[12:15], v[192:195]\n\t"
    "v_mfma_f32_16x16x32_bf16 v[88:91], a[40:43], a[0:3], v[88:91]\n\t"
    "v_mfma_f32_16x16x32_bf16 v[124:127], a[40:43], a[4:7], v[124:127]\n\t"
    "v_mfma_f32_16x16x32_bf16 v[160:163], a[40:43], a[8:11], v[160:163]\n\t"
    "v_add_u32 v32, s63, v35\n\t"
    "v_mfma_f32_16x16x32_bf16 v[196:199], a[40:43], a[12:15], v[196:199]\n\t"
    "v_add_u32 v33, s63, v36\n\t"
    "v_mfma_f32_16x16x32_bf16 v[92:95], a[44:47], a[0:3], v[92:95]\n\t"
    "v_add_u32 v34, s63, %12\n\t"
    "v_mfma_f32_16x16x32_bf16 v[128:131], a[44:47], a[4:7], v[128:131]\n\t"
    "v_mfma_f32_16x16x32_bf16 v[164:167], a[44:47], a[8:11], v[164:167]\n\t"
    "s_add_u32 s63, s63, 0x9000\n\t"
    "s_cmp_eq_u32 s63, 0x24000\n\t"
    "s_cselect_b32 s63, 0, s63\n\t"
    "v_mfma_f32_16x16x32_bf16 v[200:203], a[44:47], a[12:15], v[200:203]\n\t"
    "v_mfma_f32_16x16x32_bf16 v[96:99], a[48:51], a[0:3], v[96:99]\n\t"
    "v_mfma_f32_16x16x32_bf16 v[132:135], a[48:51], a[4:7], v[132:135]\n\t"
    "v_mfma_f32_16x16x32_bf16 v[168:171], a[48:51], a[8:11], v[168:171]\n\t"
    "v_mfma_f32_16x16x32_bf16 v[204:207], a[48:51], a[12:15], v[204:207]\n\t"
    "; ---- tailA\n\t"
    "o_tailA_%=:\n\t"
    "s_waitcnt vmcnt(9) lgkmcnt(0)\n\t"
    "s_barrier\n\t"
    "v_mfma_f32_16x16x32_bf16 v[64:67], a[68:71], a[52:55], v[64:67]\n\t"
    "ds_read_b128 a[0:3], v32 offset:0\n\t"
    "ds_read_b64_tr_b16 a[16:17], v34 offset:0\n\t"
    "v_mfma_f32_16x16x32_bf16 v[100:103], a[68:71], a[56:59], v[100:103]\n\t"
    "ds_read_b64_tr_b16 a[18:19], v34 offset:4608\n\t"
    "ds_read_b128 a[4:7], v32 offset:2048\n\t"
    "v_mfma_f32_16x16x32_bf16 v[136:139], a[68:71], a[60:63], v[136:139]\n\t"
    "ds_read_b64_tr_b16 a[20:21], v34 offset:32\n\t"
    "v_mfma_f32_16x16x32_bf16 v[172:175], a[68:71], a[64:67], v[172:175]\n\t"
    "ds_read_b64_tr_b16 a[22:23], v34 offset:4640\n\t"
    "v_mfma_f32_16x16x32_bf16 v[68:71], a[72:75], a[52:55], v[68:71]\n\t"
    "ds_read_b128 a[8:11], v33 offset:0\n\t"
    "v_mfma_f32_16x16x32_bf16 v[104:107], a[72:75], a[56:59], v[104:107]\n\t"
    "ds_read_b64_tr_b16 a[24:25], v34 offset:64\n\t"
    "v_mfma_f32_16x16x32_bf16 v[140:143], a[72:75], a[60:63], v[140:143]\n\t"
    "ds_read_b64_tr_b16 a[26:27], v34 offset:4672\n\t"
    "v_mfma_f32_16x16x32_bf16 v[176:179], a[72:75], a[64:67], v[176:179]\n\t"
    "ds_read_b128 a[12:15], v33 offset:2048\n\t"
    "v_mfma_f32_16x16x32_bf16 v[72:75], a[76:79], a[52:55], v[72:75]\n\t"
    "ds_read_b64_tr_b16 a[28:29], v34 offset:96\n\t"
    "v_mfma_f32_16x16x32_bf16 v[108:111], a[76:79], a[56:59], v[108:111]\n\t"
    "v_mfma_f32_16x16x32_bf16 v[144:147], a[76:79], a[60:63], v[144:147]\n\t"
    "ds_read_b64_tr_b16 a[30:31], v34 offset:4704\n\t"
    "v_mfma_f32_16x16x32_bf16 v[180:183], a[76:79], a[64:67], v[180:183]\n\t"
    "ds_read_b64_tr_b16 a[32:33], v34 offset:128\n\t"
    "v_mfma_f32_16x16x32_bf16 v[76:79], a[80:83], a[52:55], v[76:79]\n\t"
    "ds_read_b64_tr_b16 a[34:35], v34 offset:4736\n\t"
    "v_mfma_f32_16x16x32_bf16 v[112:115], a[80:83], a[56:59], v[112:115]\n\t"
    "ds_read_b64_tr_b16 a[36:37], v34 offset:160\n\t"
    "v_mfma_f32_16x16x32_bf16 v[148:151], a[80:83], a[60:63], v[148:151]\n\t"
    "ds_read_b64_tr_b16 a[38:39], v34 offset:4768\n\t"
    "v_mfma_f32_16x16x32_bf16 v[184:187], a[80:83], a[64:67], v[184:187]\n\t"
    "ds_read_b64_tr_b16 a[40:41], v34 offset:192\n\t"
    "v_mfma_f32_16x16x32_bf16 v[80:83], a[84:87], a[52:55], v[80:83]\n\t"
    "ds_read_b64_tr_b16 a[42:43], v34 offset:4800\n\t"
    "v_mfma_f32_16x16x32_bf16 v[116:119], a[84:87], a[56:59], v[116:119]\n\t"
    "ds_read_b64_tr_b16 a[44:45], v34 offset:224\n\t"
    "v_mfma_f32_16x16x32_bf16 v[152:155], a[84:87], a[60:63], v[152:155]\n\t"
    "ds_read_b64_tr_b16 a[46:47], v34 offset:4832\n\t"
    "v_mfma_f32_16x16x32_bf16 v[188:191], a[84:87], a[64:67], v[188:191]\n\t"
    "ds_read_b64_tr_b16 a[48:49], v34 offset:256\n\t"
    "v_mfma_f32_16x16x32_bf16 v[84:87], a[88:91], a[52:55], v[84:87]\n\t"
    "ds_read_b64_tr_b16 a[50:51], v34 offset:4864\n\t"
    "v_mfma_f32_16x16x32_bf16 v[120:123], a[88:91], a[56:59], v[120:123]\n\t"
    "v_mfma_f32_16x16x32_bf16 v[156:159], a[88:91], a[60:63], v[156:159]\n\t"
    "v_mfma_f32_16x16x32_bf16 v[192:195], a[88:91], a[64:67], v[192:195]\n\t"
    "v_mfma_f32_16x16x32_bf16 v[88:91], a[92:95], a[52:55], v[88:91]\n\t"
    "v_mfma_f32_16x16x32_bf16 v[124:127], a[92:95], a[56:59], v[124:127]\n\t"
    "v_mfma_f32_16x16x32_bf16 v[160:163], a[92:95], a[60:63], v[160:163]\n\t"
    "v_add_u32 v32, s63, v35\n\t"
    "v_mfma_f32_16x16x32_bf16 v[196:199], a[92:95], a[64:67], v[196:199]\n\t"
    "v_add_u32 v33, s63, v36\n\t"
    "v_mfma_f32_16x16x32_bf16 v[92:95], a[96:99], a[52:55], v[92:95]\n\t"
    "v_add_u32 v34, s63, %12\n\t"
    "v_mfma_f32_16x16x32_bf16 v[128:131], a[96:99], a[56:59], v[128:131]\n\t"
    "v_mfma_f32_16x16x32_bf16 v[164:167], a[96:99], a[60:63], v[164:167]\n\t"
    "s_add_u32 s63, s63, 0x9000\n\t"
    "s_cmp_eq_u32 s63, 0x24000\n\t"
    "s_cselect_b32 s63, 0, s63\n\t"
    "v_mfma_f32_16x16x32_bf16 v[200:203], a[96:99], a[64:67], v[200:203]\n\t"
    "v_mfma_f32_16x16x32_bf16 v[96:99], a[100:103], a[52:55], v[96:99]\n\t"
    "v_mfma_f32_16x16x32_bf16 v[132:135], a[100:103], a[56:59], v[132:135]\n\t"
    "v_mfma_f32_16x16x32_bf16 v[168:171], a[100:103], a[60:63], v[168:171]\n\t"
    "v_mfma_f32_16x16x32_bf16 v[204:207], a[100:103], a[64:67], v[204:207]\n\t"
    "s_waitcnt vmcnt(0) lgkmcnt(0)\n\t"
    "s_barrier\n\t"
    "v_mfma_f32_16x16x32_bf16 v[64:67], a[16:19], a[0:3], v[64:67]\n\t"
    "ds_read_b128 a[52:55], v32 offset:0\n\t"
    "ds_read_b64_tr_b16 a[68:69], v34 offset:0\n\t"
    "v_mfma_f32_16x16x32_bf16 v[100:103], a[16:19], a[4:7], v[100:103]\n\t"
    "ds_read_b64_tr_b16 a[70:71], v34 offset:4608\n\t"
    "ds_read_b128 a[56:59], v32 offset:2048\n\t"
    "v_mfma_f32_16x16x32_bf16 v[136:139], a[16:19], a[8:11], v[136:139]\n\t"
    "ds_read_b64_tr_b16 a[72:73], v34 offset:32\n\t"
    "v_mfma_f32_16x16x32_bf16 v[172:175], a[16:19], a[12:15], v[172:175]\n\t"
    "ds_read_b64_tr_b16 a[74:75], v34 offset:4640\n\t"
    "v_mfma_f32_16x16x32_bf16 v[68:71], a[20:23], a[0:3], v[68:71]\n\t"
    "ds_read_b128 a[60:63], v33 offset:0\n\t"
    "v_mfma_f32_16x16x32_bf16 v[104:107], a[20:23], a[4:7], v[104:107]\n\t"
    "ds_read_b64_tr_b16 a[76:77], v34 offset:64\n\t"
    "v_mfma_f32_16x16x32_bf16 v[140:143], a[20:23], a[8:11], v[140:143]\n\t"
    "ds_read_b64_tr_b16 a[78:79], v34 offset:4672\n\t"
    "v_mfma_f32_16x16x32_bf16 v[176:179], a[20:23], a[12:15], v[176:179]\n\t"
    "ds_read_b128 a[64:67], v33 offset:2048\n\t"
    "v_mfma_f32_16x16x32_bf16 v[72:75], a[24:27], a[0:3], v[72:75]\n\t"
    "ds_read_b64_tr_b16 a[80:81], v34 offset:96\n\t"
    "v_mfma_f32_16x16x32_bf16 v[108:111], a[24:27], a[4:7], v[108:111]\n\t"
    "v_mfma_f32_16x16x32_bf16 v[144:147], a[24:27], a[8:11], v[144:147]\n\t"
    "ds_read_b64_tr_b16 a[82:83], v34 offset:4704\n\t"
    "v_mfma_f32_16x16x32_bf16 v[180:183], a[24:27], a[12:15], v[180:183]\n\t"
    "ds_read_b64_tr_b16 a[84:85], v34 offset:128\n\t"
    "v_mfma_f32_16x16x32_bf16 v[76:79], a[28:31], a[0:3], v[76:79]\n\t"
    "ds_read_b64_tr_b16 a[86:87], v34 offset:4736\n\t"
    "v_mfma_f32_16x16x32_bf16 v[112:115], a[28:31], a[4:7], v[112:115]\n\t"
    "ds_read_b64_tr_b16 a[88:89], v34 offset:160\n\t"
    "v_mfma_f32_16x16x32_bf16 v[148:151], a[28:31], a[8:11], v[148:151]\n\t"
    "ds_read_b64_tr_b16 a[90:91], v34 offset:4768\n\t"
    "v_mfma_f32_16x16x32_bf16 v[184:187], a[28:31], a[12:15], v[184:187]\n\t"
    "ds_read_b64_tr_b16 a[92:93], v34 offset:192\n\t"
    "v_mfma_f32_16x16x32_bf16 v[80:83], a[32:35], a[0:3], v[80:83]\n\t"
    "ds_read_b64_tr_b16 a[94:95], v34 offset:4800\n\t"
    "v_mfma_f32_16x16x32_bf16 v[116:119], a[32:35], a[4:7], v[116:119]\n\t"
    "ds_read_b64_tr_b16 a[96:97], v34 offset:224\n\t"
    "v_mfma_f32_16x16x32_bf16 v[152:155], a[32:35], a[8:11], v[152:155]\n\t"
    "ds_read_b64_tr_b16 a[98:99], v34 offset:4832\n\t"
    "v_mfma_f32_16x16x32_bf16 v[188:191], a[32:35], a[12:15], v[188:191]\n\t"
    "ds_read_b64_tr_b16 a[100:101], v34 offset:256\n\t"
    "v_mfma_f32_16x16x32_bf16 v[84:87], a[36:39], a[0:3], v[84:87]\n\t"
    "ds_read_b64_tr_b16 a[102:103], v34 offset:4864\n\t"
    "v_mfma_f32_16x16x32_bf16 v[120:123], a[36:39], a[4:7], v[120:123]\n\t"
    "v_mfma_f32_16x16x32_bf16 v[156:159], a[36:39], a[8:11], v[156:159]\n\t"
    "v_mfma_f32_16x16x32_bf16 v[192:195], a[36:39], a[12:15], v[192:195]\n\t"
    "v_mfma_f32_16x16x32_bf16 v[88:91], a[40:43], a[0:3], v[88:91]\n\t"
    "v_mfma_f32_16x16x32_bf16 v[124:127], a[40:43], a[4:7], v[124:127]\n\t"
    "v_mfma_f32_16x16x32_bf16 v[160:163], a[40:43], a[8:11], v[160:163]\n\t"
    "v_add_u32 v32, s63, v35\n\t"
    "v_mfma_f32_16x16x32_bf16 v[196:199], a[40:43], a[12:15], v[196:199]\n\t"
    "v_add_u32 v33, s63, v36\n\t"
    "v_mfma_f32_16x16x32_bf16 v[92:95], a[44:47], a[0:3], v[92:95]\n\t"
    "v_add_u32 v34, s63, %12\n\t"
    "v_mfma_f32_16x16x32_bf16 v[128:131], a[44:47], a[4:7], v[128:131]\n\t"
    "v_mfma_f32_16x16x32_bf16 v[164:167], a[44:47], a[8:11], v[164:167]\n\t"
    "s_add_u32 s63, s63, 0x9000\n\t"
    "s_cmp_eq_u32 s63, 0x24000\n\t"
    "s_cselect_b32 s63, 0, s63\n\t"
    "v_mfma_f32_16x16x32_bf16 v[200:203], a[44:47], a[12:15], v[200:203]\n\t"
    "v_mfma_f32_16x16x32_bf16 v[96:99], a[48:51], a[0:3], v[96:99]\n\t"
    "v_mfma_f32_16x16x32_bf16 v[132:135], a[48:51], a[4:7], v[132:135]\n\t"
    "v_mfma_f32_16x16x32_bf16 v[168:171], a[48:51], a[8:11], v[168:171]\n\t"
    "v_mfma_f32_16x16x32_bf16 v[204:207], a[48:51], a[12:15], v[204:207]\n\t"
    "s_waitcnt vmcnt(0) lgkmcnt(0)\n\t"
    "s_barrier\n\t"
    "v_mfma_f32_16x16x32_bf16 v[64:67], a[68:71], a[52:55], v[64:67]\n\t"
    "ds_read_b128 a[0:3], v32 offset:0\n\t"
    "ds_read_b64_tr_b16 a[16:17], v34 offset:0\n\t"
    "v_mfma_f32_16x16x32_bf16 v[100:103], a[68:71], a[56:59], v[100:103]\n\t"
    "ds_read_b64_tr_b16 a[18:19], v34 offset:4608\n\t"
    "ds_read_b128 a[4:7], v32 offset:2048\n\t"
    "v_mfma_f32_16x16x32_bf16 v[136:139], a[68:71], a[60:63], v[136:139]\n\t"
    "ds_read_b64_tr_b16 a[20:21], v34 offset:32\n\t"
    "v_mfma_f32_16x16x32_bf16 v[172:175], a[68:71], a[64:67], v[172:175]\n\t"
    "ds_read_b64_tr_b16 a[22:23], v34 offset:4640\n\t"
    "v_mfma_f32_16x16x32_bf16 v[68:71], a[72:75], a[52:55], v[68:71]\n\t"
    "ds_read_b128 a[8:11], v33 offset:0\n\t"
    "v_mfma_f32_16x16x32_bf16 v[104:107], a[72:75], a[56:59], v[104:107]\n\t"
    "ds_read_b64_tr_b16 a[24:25], v34 offset:64\n\t"
    "v_mfma_f32_16x16x32_bf16 v[140:143], a[72:75], a[60:63], v[140:143]\n\t"
    "ds_read_b64_tr_b16 a[26:27], v34 offset:4672\n\t"
    "v_mfma_f32_16x16x32_bf16 v[176:179], a[72:75], a[64:67], v[176:179]\n\t"
    "ds_read_b128 a[12:15], v33 offset:2048\n\t"
    "v_mfma_f32_16x16x32_bf16 v[72:75], a[76:79], a[52:55], v[72:75]\n\t"
    "ds_read_b64_tr_b16 a[28:29], v34 offset:96\n\t"
    "v_mfma_f32_16x16x32_bf16 v[108:111], a[76:79], a[56:59], v[108:111]\n\t"
    "v_mfma_f32_16x16x32_bf16 v[144:147], a[76:79], a[60:63], v[144:147]\n\t"
    "ds_read_b64_tr_b16 a[30:31], v34 offset:4704\n\t"
    "v_mfma_f32_16x16x32_bf16 v[180:183], a[76:79], a[64:67], v[180:183]\n\t"
    "ds_read_b64_tr_b16 a[32:33], v34 offset:128\n\t"
    "v_mfma_f32_16x16x32_bf16 v[76:79], a[80:83], a[52:55], v[76:79]\n\t"
    "ds_read_b64_tr_b16 a[34:35], v34 offset:4736\n\t"
    "v_mfma_f32_16x16x32_bf16 v[112:115], a[80:83], a[56:59], v[112:115]\n\t"
    "ds_read_b64_tr_b16 a[36:37], v34 offset:160\n\t"
    "v_mfma_f32_16x16x32_bf16 v[148:151], a[80:83], a[60:63], v[148:151]\n\t"
    "ds_read_b64_tr_b16 a[38:39], v34 offset:4768\n\t"
    "v_mfma_f32_16x16x32_bf16 v[184:187], a[80:83], a[64:67], v[184:187]\n\t"
    "ds_read_b64_tr_b16 a[40:41], v34 offset:192\n\t"
    "v_mfma_f32_16x16x32_bf16 v[80:83], a[84:87], a[52:55], v[80:83]\n\t"
    "ds_read_b64_tr_b16 a[42:43], v34 offset:4800\n\t"
    "v_mfma_f32_16x16x32_bf16 v[116:119], a[84:87], a[56:59], v[116:119]\n\t"
    "ds_read_b64_tr_b16 a[44:45], v34 offset:224\n\t"
    "v_mfma_f32_16x16x32_bf16 v[152:155], a[84:87], a[60:63], v[152:155]\n\t"
    "ds_read_b64_tr_b16 a[46:47], v34 offset:4832\n\t"
    "v_mfma_f32_16x16x32_bf16 v[188:191], a[84:87], a[64:67], v[188:191]\n\t"
    "ds_read_b64_tr_b16 a[48:49], v34 offset:256\n\t"
    "v_mfma_f32_16x16x32_bf16 v[84:87], a[88:91], a[52:55], v[84:87]\n\t"
    "ds_read_b64_tr_b16 a[50:51], v34 offset:4864\n\t"
    "v_mfma_f32_16x16x32_bf16 v[120:123], a[88:91], a[56:59], v[120:123]\n\t"
    "v_mfma_f32_16x16x32_bf16 v[156:159], a[88:91], a[60:63], v[156:159]\n\t"
    "v_mfma_f32_16x16x32_bf16 v[192:195], a[88:91], a[64:67], v[192:195]\n\t"
    "v_mfma_f32_16x16x32_bf16 v[88:91], a[92:95], a[52:55], v[88:91]\n\t"
    "v_mfma_f32_16x16x32_bf16 v[124:127], a[92:95], a[56:59], v[124:127]\n\t"
    "v_mfma_f32_16x16x32_bf16 v[160:163], a[92:95], a[60:63], v[160:163]\n\t"
    "v_add_u32 v32, s63, v35\n\t"
    "v_mfma_f32_16x16x32_bf16 v[196:199], a[92:95], a[64:67], v[196:199]\n\t"
    "v_add_u32 v33, s63, v36\n\t"
    "v_mfma_f32_16x16x32_bf16 v[92:95], a[96:99], a[52:55], v[92:95]\n\t"
    "v_add_u32 v34, s63, %12\n\t"
    "v_mfma_f32_16x16x32_bf16 v[128:131], a[96:99], a[56:59], v[128:131]\n\t"
    "v_mfma_f32_16x16x32_bf16 v[164:167], a[96:99], a[60:63], v[164:167]\n\t"
    "s_add_u32 s63, s63, 0x9000\n\t"
    "s_cmp_eq_u32 s63, 0x24000\n\t"
    "s_cselect_b32 s63, 0, s63\n\t"
    "v_mfma_f32_16x16x32_bf16 v[200:203], a[96:99], a[64:67], v[200:203]\n\t"
    "v_mfma_f32_16x16x32_bf16 v[96:99], a[100:103], a[52:55], v[96:99]\n\t"
    "v_mfma_f32_16x16x32_bf16 v[132:135], a[100:103], a[56:59], v[132:135]\n\t"
    "v_mfma_f32_16x16x32_bf16 v[168:171], a[100:103], a[60:63], v[168:171]\n\t"
    "v_mfma_f32_16x16x32_bf16 v[204:207], a[100:103], a[64:67], v[204:207]\n\t"
    "s_cmp_lg_u32 s68, 0\n\t"
    "s_cbranch_scc1 o_xch_%=\n\t"
    "; ---- tailA2\n\t"
    "s_waitcnt vmcnt(0) lgkmcnt(0)\n\t"
    "s_barrier\n\t"
    "v_mfma_f32_16x16x32_bf16 v[64:67], a[16:19], a[0:3], v[64:67]\n\t"
    "ds_read_b128 a[52:55], v32 offset:0\n\t"
    "ds_read_b64_tr_b16 a[68:69], v34 offset:0\n\t"
    "v_mfma_f32_16x16x32_bf16 v[100:103], a[16:19], a[4:7], v[100:103]\n\t"
    "ds_read_b64_tr_b16 a[70:71], v34 offset:4608\n\t"
    "ds_read_b128 a[56:59], v32 offset:2048\n\t"
    "v_mfma_f32_16x16x32_bf16 v[136:139], a[16:19], a[8:11], v[136:139]\n\t"
    "ds_read_b64_tr_b16 a[72:73], v34 offset:32\n\t"
    "v_mfma_f32_16x16x32_bf16 v[172:175], a[16:19], a[12:15], v[172:175]\n\t"
    "ds_read_b64_tr_b16 a[74:75], v34 offset:4640\n\t"
    "v_mfma_f32_16x16x32_bf16 v[68:71], a[20:23], a[0:3], v[68:71]\n\t"
    "ds_read_b128 a[60:63], v33 offset:0\n\t"
    "v_mfma_f32_16x16x32_bf16 v[104:107], a[20:23], a[4:7], v[104:107]\n\t"
    "ds_read_b64_tr_b16 a[76:77], v34 offset:64\n\t"
    "v_mfma_f32_16x16x32_bf16 v[140:143], a[20:23], a[8:11], v[140:143]\n\t"
    "ds_read_b64_tr_b16 a[78:79], v34 offset:4672\n\t"
    "v_mfma_f32_16x16x32_bf16 v[176:179], a[20:23], a[12:15], v[176:179]\n\t"
    "ds_read_b128 a[64:67], v33 offset:2048\n\t"
    "v_mfma_f32_16x16x32_bf16 v[72:75], a[24:27], a[0:3], v[72:75]\n\t"
    "ds_read_b64_tr_b16 a[80:81], v34 offset:96\n\t"
    "v_mfma_f32_16x16x32_bf16 v[108:111], a[24:27], a[4:7], v[108:111]\n\t"
    "v_mfma_f32_16x16x32_bf16 v[144:147], a[24:27], a[8:11], v[144:147]\n\t"
    "ds_read_b64_tr_b16 a[82:83], v34 offset:4704\n\t"
    "v_mfma_f32_16x16x32_bf16 v[180:183], a[24:27], a[12:15], v[180:183]\n\t"
    "ds_read_b64_tr_b16 a[84:85], v34 offset:128\n\t"
    "v_mfma_f32_16x16x32_bf16 v[76:79], a[28:31], a[0:3], v[76:79]\n\t"
    "ds_read_b64_tr_b16 a[86:87], v34 offset:4736\n\t"
    "v_mfma_f32_16x16x32_bf16 v[112:115], a[28:31], a[4:7], v[112:115]\n\t"
    "ds_read_b64_tr_b16 a[88:89], v34 offset:160\n\t"
    "v_mfma_f32_16x16x32_bf16 v[148:151], a[28:31], a[8:11], v[148:151]\n\t"
    "ds_read_b64_tr_b16 a[90:91], v34 offset:4768\n\t"
    "v_mfma_f32_16x16x32_bf16 v[184:187], a[28:31], a[12:15], v[184:187]\n\t"
    "ds_read_b64_tr_b16 a[92:93], v34 offset:192\n\t"
    "v_mfma_f32_16x16x32_bf16 v[80:83], a[32:35], a[0:3], v[80:83]\n\t"
    "ds_read_b64_tr_b16 a[94:95], v34 offset:4800\n\t"
    "v_mfma_f32_16x16x32_bf16 v[116:119], a[32:35], a[4:7], v[116:119]\n\t"
    "ds_read_b64_tr_b16 a[96:97], v34 offset:224\n\t"
    "v_mfma_f32_16x16x32_bf16 v[152:155], a[32:35], a[8:11], v[152:155]\n\t"
    "ds_read_b64_tr_b16 a[98:99], v34 offset:4832\n\t"
    "v_mfma_f32_16x16x32_bf16 v[188:191], a[32:35], a[12:15], v[188:191]\n\t"
    "ds_read_b64_tr_b16 a[100:101], v34 offset:256\n\t"
    "v_mfma_f32_16x16x32_bf16 v[84:87], a[36:39], a[0:3], v[84:87]\n\t"
    "ds_read_b64_tr_b16 a[102:103], v34 offset:4864\n\t"
    "v_mfma_f32_16x16x32_bf16 v[120:123], a[36:39], a[4:7], v[120:123]\n\t"
    "v_mfma_f32_16x16x32_bf16 v[156:159], a[36:39], a[8:11], v[156:159]\n\t"
    "v_mfma_f32_16x16x32_bf16 v[192:195], a[36:39], a[12:15], v[192:195]\n\t"
    "v_mfma_f32_16x16x32_bf16 v[88:91], a[40:43], a[0:3], v[88:91]\n\t"
    "v_mfma_f32_16x16x32_bf16 v[124:127], a[40:43], a[4:7], v[124:127]\n\t"
    "v_mfma_f32_16x16x32_bf16 v[160:163], a[40:43], a[8:11], v[160:163]\n\t"
    "v_add_u32 v32, s63, v35\n\t"
    "v_mfma_f32_16x16x32_bf16 v[196:199], a[40:43], a[12:15], v[196:199]\n\t"
    "v_add_u32 v33, s63, v36\n\t"
    "v_mfma_f32_16x16x32_bf16 v[92:95], a[44:47], a[0:3], v[92:95]\n\t"
    "v_add_u32 v34, s63, %12\n\t"
    "v_mfma_f32_16x16x32_bf16 v[128:131], a[44:47], a[4:7], v[128:131]\n\t"
    "v_mfma_f32_16x16x32_bf16 v[164:167], a[44:47], a[8:11], v[164:167]\n\t"
    "s_add_u32 s63, s63, 0x9000\n\t"
    "s_cmp_eq_u32 s63, 0x24000\n\t"
    "s_cselect_b32 s63, 0, s63\n\t"
    "v_mfma_f32_16x16x32_bf16 v[200:203], a[44:47], a[12:15], v[200:203]\n\t"
    "v_mfma_f32_16x16x32_bf16 v[96:99], a[48:51], a[0:3], v[96:99]\n\t"
    "v_mfma_f32_16x16x32_bf16 v[132:135], a[48:51], a[4:7], v[132:135]\n\t"
    "v_mfma_f32_16x16x32_bf16 v[168:171], a[48:51], a[8:11], v[168:171]\n\t"
    "v_mfma_f32_16x16x32_bf16 v[204:207], a[48:51], a[12:15], v[204:207]\n\t"
    "; ---- xch\n\t"
    "o_xch_%=:\n\t"
    "s_waitcnt lgkmcnt(0)\n\t"
    "s_barrier\n\t"
    "ds_write_b128 v48, v[136:139] offset:0\n\t"
    "ds_write_b128 v48, v[140:143] offset:1024\n\t"
    "ds_write_b128 v48, v[144:147] offset:2048\n\t"
    "ds_write_b128 v48, v[148:151] offset:3072\n\t"
    "ds_write_b128 v48, v[152:155] offset:4096\n\t"
    "ds_write_b128 v48, v[156:159] offset:5120\n\t"
    "ds_write_b128 v48, v[160:163] offset:6144\n\t"
    "ds_write_b128 v48, v[164:167] offset:7168\n\t"
    "ds_write_b128 v48, v[168:171] offset:8192\n\t"
    "ds_write_b128 v48, v[172:175] offset:9216\n\t"
    "ds_write_b128 v48, v[176:179] offset:10240\n\t"
    "ds_write_b128 v48, v[180:183] offset:11264\n\t"
    "ds_write_b128 v48, v[184:187] offset:12288\n\t"
    "ds_write_b128 v48, v[188:191] offset:13312\n\t"
    "ds_write_b128 v48, v[192:195] offset:14336\n\t"
    "ds_write_b128 v48, v[196:199] offset:15360\n\t"
    "ds_write_b128 v48, v[200:203] offset:16384\n\t"
    "ds_write_b128 v48, v[204:207] offset:17408\n\t"
    "s_waitcnt lgkmcnt(0)\n\t"
    "s_barrier\n\t"
    "ds_read_b128 v[136:139], v49 offset:0\n\t"
    "ds_read_b128 v[140:143], v49 offset:1024\n\t"
    "ds_read_b128 v[144:147], v49 offset:2048\n\t"
    "ds_read_b128 v[148:151], v49 offset:3072\n\t"
    "ds_read_b128 v[152:155], v49 offset:4096\n\t"
    "ds_read_b128 v[156:159], v49 offset:5120\n\t"
    "ds_read_b128 v[160:163], v49 offset:6144\n\t"
    "ds_read_b128 v[164:167], v49 offset:7168\n\t"
    "ds_read_b128 v[168:171], v49 offset:8192\n\t"
    "ds_read_b128 v[172:175], v49 offset:9216\n\t"
    "ds_read_b128 v[176:179], v49 offset:10240\n\t"
    "ds_read_b128 v[180:183], v49 offset:11264\n\t"
    "ds_read_b128 v[184:187], v49 offset:12288\n\t"
    "ds_read_b128 v[188:191], v49 offset:13312\n\t"
    "ds_read_b128 v[192:195], v49 offset:14336\n\t"
    "ds_read_b128 v[196:199], v49 offset:15360\n\t"
    "ds_read_b128 v[200:203], v49 offset:16384\n\t"
    "ds_read_b128 v[204:207], v49 offset:17408\n\t"
    "s_cmp_lg_u32 s68, 0\n\t"
    "s_cbranch_scc1 o_epiB_%=\n\t"
    "s_mul_i32 s71, %23, 0x9000\n\t"
    "s_mul_i32 s65, %0, 0x2400\n\t"
    "s_add_u32 s71, s71, s65\n\t"
    "s_cmp_eq_u32 %23, 1\n\t"
    "s_cbranch_scc1 o_st1_%=\n\t"
    "s_cmp_eq_u32 %23, 2\n\t"
    "s_cbranch_scc1 o_st2_%=\n\t"
    "; ---- st0\n\t"
    "o_st0_%=:\n\t"
    "s_waitcnt lgkmcnt(13)\n\t"
    "v_pk_add_f32 v[76:77], v[76:77], v[148:149]\n\t"
    "v_pk_add_f32 v[78:79], v[78:79], v[150:151]\n\t"
    "s_waitcnt lgkmcnt(4)\n\t"
    "v_pk_add_f32 v[112:113], v[112:113], v[184:185]\n\t"
    "v_pk_add_f32 v[114:115], v[114:115], v[186:187]\n\t"
    "v_cvt_pk_bf16_f32 v76, v76, v77\n\t"
    "v_cvt_pk_bf16_f32 v77, v78, v79\n\t"
    "v_cvt_pk_bf16_f32 v78, v112, v113\n\t"
    "v_cvt_pk_bf16_f32 v79, v114, v115\n\t"
    "s_add_u32 s67, s71, 0\n\t"
    "buffer_store_dwordx4 v[76:79], %11, s[48:51], s67 offen offset:3072 sc1\n\t"
    "v_pk_add_f32 v[80:81], v[80:81], v[152:153]\n\t"
    "v_pk_add_f32 v[82:83], v[82:83], v[154:155]\n\t"
    "v_pk_add_f32 v[116:117], v[116:117], v[188:189]\n\t"
    "v_pk_add_f32 v[118:119], v[118:119], v[190:191]\n\t"
    "v_cvt_pk_bf16_f32 v80, v80, v81\n\t"
    "v_cvt_pk_bf16_f32 v81, v82, v83\n\t"
    "v_cvt_pk_bf16_f32 v82, v116, v117\n\t"
    "v_cvt_pk_bf16_f32 v83, v118, v119\n\t"
    "s_add_u32 s67, s71, 0x1000\n\t"
    "buffer_store_dwordx4 v[80:83], %11, s[48:51], s67 offen offset:0 sc1\n\t"
    "v_pk_add_f32 v[84:85], v[84:85], v[156:157]\n\t"
    "v_pk_add_f32 v[86:87], v[86:87], v[158:159]\n\t"
    "s_waitcnt lgkmcnt(2)\n\t"
    "v_pk_add_f32 v[120:121], v[120:121], v[192:193]\n\t"
    "v_pk_add_f32 v[122:123], v[122:123], v[194:195]\n\t"
    "v_cvt_pk_bf16_f32 v84, v84, v85\n\t"
    "v_cvt_pk_bf16_f32 v85, v86, v87\n\t"
    "v_cvt_pk_bf16_f32 v86, v120, v121\n\t"
    "v_cvt_pk_bf16_f32 v87, v122, v123\n\t"
    "s_add_u32 s67, s71, 0x1000\n\t"
    "buffer_store_dwordx4 v[84:87], %11, s[48:51], s67 offen offset:1024 sc1\n\t"
    "v_pk_add_f32 v[88:89], v[88:89], v[160:161]\n\t"
    "v_pk_add_f32 v[90:91], v[90:91], v[162:163]\n\t"
    "v_pk_add_f32 v[124:125], v[124:125], v[196:197]\n\t"
    "v_pk_add_f32 v[126:127], v[126:127], v[198:199]\n\t"
    "v_cvt_pk_bf16_f32 v88, v88, v89\n\t"
    "v_cvt_pk_bf16_f32 v89, v90, v91\n\t"
    "v_cvt_pk_bf16_f32 v90, v124, v125\n\t"
    "v_cvt_pk_bf16_f32 v91, v126, v127\n\t"
    "s_add_u32 s67, s71, 0x1000\n\t"
    "buffer_store_dwordx4 v[88:91], %11, s[48:51], s67 offen offset:2048 sc1\n\t"
    "v_pk_add_f32 v[92:93], v[92:93], v[164:165]\n\t"
    "v_pk_add_f32 v[94:95], v[94:95], v[166:167]\n\t"
    "s_waitcnt lgkmcnt(0)\n\t"
    "v_pk_add_f32 v[128:129], v[128:129], v[200:201]\n\t"
    "v_pk_add_f32 v[130:131], v[130:131], v[202:203]\n\t"
    "v_cvt_pk_bf16_f32 v92, v92, v93\n\t"
    "v_cvt_pk_bf16_f32 v93, v94, v95\n\t"
    "v_cvt_pk_bf16_f32 v94, v128, v129\n\t"
    "v_cvt_pk_bf16_f32 v95, v130, v131\n\t"
    "s_add_u32 s67, s71, 0x1000\n\t"
    "buffer_store_dwordx4 v[92:95], %11, s[48:51], s67 offen offset:3072 sc1\n\t"
    "v_pk_add_f32 v[96:97], v[96:97], v[168:169]\n\t"
    "v_pk_add_f32 v[98:99], v[98:99], v[170:171]\n\t"
    "v_pk_add_f32 v[132:133], v[132:133], v[204:205]\n\t"
    "v_pk_add_f32 v[134:135], v[134:135], v[206:207]\n\t"
    "v_cvt_pk_bf16_f32 v96, v96, v97\n\t"
    "v_cvt_pk_bf16_f32 v97, v98, v99\n\t"
    "v_cvt_pk_bf16_f32 v98, v132, v133\n\t"
    "v_cvt_pk_bf16_f32 v99, v134, v135\n\t"
    "s_add_u32 s67, s71, 0x2000\n\t"
    "buffer_store_dwordx4 v[96:99], %11, s[48:51], s67 offen offset:0 sc1\n\t"
    "v_pk_add_f32 v[64:65], v[64:65], v[136:137]\n\t"
    "v_pk_add_f32 v[66:67], v[66:67], v[138:139]\n\t"
    "v_pk_add_f32 v[100:101], v[100:101], v[172:173]\n\t"
    "v_pk_add_f32 v[102:103], v[102:103], v[174:175]\n\t"
    "v_pk_add_f32 v[68:69], v[68:69], v[140:141]\n\t"
    "v_pk_add_f32 v[70:71], v[70:71], v[142:143]\n\t"
    "v_pk_add_f32 v[104:105], v[104:105], v[176:177]\n\t"
    "v_pk_add_f32 v[106:107], v[106:107], v[178:179]\n\t"
    "v_pk_add_f32 v[72:73], v[72:73], v[144:145]\n\t"
    "v_pk_add_f32 v[74:75], v[74:75], v[146:147]\n\t"
    "v_pk_add_f32 v[108:109], v[108:109], v[180:181]\n\t"
    "v_pk_add_f32 v[110:111], v[110:111], v[182:183]\n\t"
    "s_add_u32 s78, s59, 0x20000\n\t"
    "s_waitcnt vmcnt(0)\n\t"
    "s_barrier\n\t"
    "s_cmp_lg_u32 %0, 0\n\t"
    "s_cbranch_scc1 pubp0_%=\n\t"
    "v_mov_b64 v[40:41], %19\n\t"
    "v_mov_b32 v42, 0\n\t"
    "s_mov_b64 s[76:77], exec\n\t"
    "s_mov_b64 exec, 1\n\t"
    "buffer_store_dwordx2 v[40:41], v42, s[72:75], s78 offen offset:0 sc1\n\t"
    "s_mov_b64 exec, s[76:77]\n\t"
    "pubp0_%=:\n\t"
    "buffer_load_dwordx2 v[38:39], %22, s[72:75], 0 offen offset:0 sc1\n\t"
    "s_mov_b32 s67, 0x40000\n\t"
    "pw0_chk_%=:\n\t"
    "s_waitcnt vmcnt(0)\n\t"
    "v_cmp_ne_u64 vcc, %19, v[38:39]\n\t"
    "s_nop 0\n\t"
    "s_cbranch_vccz pw0_ok_%=\n\t"
    "s_sleep 2\n\t"
    "buffer_load_dwordx2 v[38:39], %22, s[72:75], 0 offen offset:0 sc1\n\t"
    "s_waitcnt vmcnt(0)\n\t"
    "s_sub_u32 s67, s67, 1\n\t"
    "s_cmp_lg_u32 s67, 0\n\t"
    "s_cbranch_scc1 pw0_chk_%=\n\t"
    "pw0_ok_%=:\n\t"
    "s_add_u32 s67, s65, 0x9000\n\t"
    "buffer_load_dwordx4 v[136:139], %11, s[48:51], s67 offen offset:0 sc1\n\t"
    "s_add_u32 s67, s65, 0x12000\n\t"
    "buffer_load_dwordx4 v[140:143], %11, s[48:51], s67 offen offset:0 sc1\n\t"
    "s_add_u32 s67, s65, 0x1b000\n\t"
    "buffer_load_dwordx4 v[144:147], %11, s[48:51], s67 offen offset:0 sc1\n\t"
    "s_add_u32 s67, s65, 0x24000\n\t"
    "buffer_load_dwordx4 v[148:151], %11, s[48:51], s67 offen offset:0 sc1\n\t"
    "s_add_u32 s67, s65, 0x9000\n\t"
    "buffer_load_dwordx4 v[152:155], %11, s[48:51], s67 offen offset:1024 sc1\n\t"
    "s_add_u32 s67, s65, 0x12000\n\t"
    "buffer_load_dwordx4 v[156:159], %11, s[48:51], s67 offen offset:1024 sc1\n\t"
    "s_add_u32 s67, s65, 0x1b000\n\t"
    "buffer_load_dwordx4 v[160:163], %11, s[48:51], s67 offen offset:1024 sc1\n\t"
    "s_add_u32 s67, s65, 0x24000\n\t"
    "buffer_load_dwordx4 v[164:167], %11, s[48:51], s67 offen offset:1024 sc1\n\t"
    "s_add_u32 s67, s65, 0x9000\n\t"
    "buffer_load_dwordx4 v[168:171], %11, s[48:51], s67 offen offset:2048 sc1\n\t"
    "s_add_u32 s67, s65, 0x12000\n\t"
    "buffer_load_dwordx4 v[172:175], %11, s[48:51], s67 offen offset:2048 sc1\n\t"
    "s_add_u32 s67, s65, 0x1b000\n\t"
    "buffer_load_dwordx4 v[176:179], %11, s[48:51], s67 offen offset:2048 sc1\n\t"
    "s_add_u32 s67, s65, 0x24000\n\t"
    "buffer_load_dwordx4 v[180:183], %11, s[48:51], s67 offen offset:2048 sc1\n\t"
    "s_mov_b32 s71, 0x9000\n\t"
    "s_waitcnt vmcnt(11)\n\t"
    "v_lshlrev_b32 v52, 16, v136\n\t"
    "v_and_b32 v53, 0xffff0000, v136\n\t"
    "v_lshlrev_b32 v54, 16, v137\n\t"
    "v_and_b32 v55, 0xffff0000, v137\n\t"
    "v_lshlrev_b32 v56, 16, v138\n\t"
    "v_and_b32 v57, 0xffff0000, v138\n\t"
    "v_lshlrev_b32 v58, 16, v139\n\t"
    "v_and_b32 v59, 0xffff0000, v139\n\t"
    "v_pk_add_f32 v[64:65], v[64:65], v[52:53]\n\t"
    "v_pk_add_f32 v[66:67], v[66:67], v[54:55]\n\t"
    "v_pk_add_f32 v[100:101], v[100:101], v[56:57]\n\t"
    "v_pk_add_f32 v[102:103], v[102:103], v[58:59]\n\t"
    "s_waitcnt vmcnt(10)\n\t"
    "v_lshlrev_b32 v52, 16, v140\n\t"
    "v_and_b32 v53, 0xffff0000, v140\n\t"
    "v_lshlrev_b32 v54, 16, v141\n\t"
    "v_and_b32 v55, 0xffff0000, v141\n\t"
    "v_lshlrev_b32 v56, 16, v142\n\t"
    "v_and_b32 v57, 0xffff0000, v142\n\t"
    "v_lshlrev_b32 v58, 16, v143\n\t"
    "v_and_b32 v59, 0xffff0000, v143\n\t"
    "v_pk_add_f32 v[64:65], v[64:65], v[52:53]\n\t"
    "v_pk_add_f32 v[66:67], v[66:67], v[54:55]\n\t"
    "v_pk_add_f32 v[100:101], v[100:101], v[56:57]\n\t"
    "v_pk_add_f32 v[102:103], v[102:103], v[58:59]\n\t"
    "s_waitcnt vmcnt(9)\n\t"
    "v_lshlrev_b32 v52, 16, v144\n\t"
    "v_and_b32 v53, 0xffff0000, v144\n\t"
    "v_lshlrev_b32 v54, 16, v145\n\t"
    "v_and_b32 v55, 0xffff0000, v145\n\t"
    "v_lshlrev_b32 v56, 16, v146\n\t"
    "v_and_b32 v57, 0xffff0000, v146\n\t"
    "v_lshlrev_b32 v58, 16, v147\n\t"
    "v_and_b32 v59, 0xffff0000, v147\n\t"
    "v_pk_add_f32 v[64:65], v[64:65], v[52:53]\n\t"
    "v_pk_add_f32 v[66:67], v[66:67], v[54:55]\n\t"
    "v_pk_add_f32 v[100:101], v[100:101], v[56:57]\n\t"
    "v_pk_add_f32 v[102:103], v[102:103], v[58:59]\n\t"
    "s_waitcnt vmcnt(8)\n\t"
    "v_lshlrev_b32 v52, 16, v148\n\t"
    "v_and_b32 v53, 0xffff0000, v148\n\t"
    "v_lshlrev_b32 v54, 16, v149\n\t"
    "v_and_b32 v55, 0xffff0000, v149\n\t"
    "v_lshlrev_b32 v56, 16, v150\n\t"
    "v_and_b32 v57, 0xffff0000, v150\n\t"
    "v_lshlrev_b32 v58, 16, v151\n\t"
    "v_and_b32 v59, 0xffff0000, v151\n\t"
    "v_pk_add_f32 v[64:65], v[64:65], v[52:53]\n\t"
    "v_pk_add_f32 v[66:67], v[66:67], v[54:55]\n\t"
    "v_pk_add_f32 v[100:101], v[100:101], v[56:57]\n\t"
    "v_pk_add_f32 v[102:103], v[102:103], v[58:59]\n\t"
    "s_waitcnt vmcnt(7)\n\t"
    "v_lshlrev_b32 v52, 16, v152\n\t"
    "v_and_b32 v53, 0xffff0000, v152\n\t"
    "v_lshlrev_b32 v54, 16, v153\n\t"
    "v_and_b32 v55, 0xffff0000, v153\n\t"
    "v_lshlrev_b32 v56, 16, v154\n\t"
    "v_and_b32 v57, 0xffff0000, v154\n\t"
    "v_lshlrev_b32 v58, 16, v155\n\t"
    "v_and_b32 v59, 0xffff0000, v155\n\t"
    "v_pk_add_f32 v[68:69], v[68:69], v[52:53]\n\t"
    "v_pk_add_f32 v[70:71], v[70:71], v[54:55]\n\t"
    "v_pk_add_f32 v[104:105], v[104:105], v[56:57]\n\t"
    "v_pk_add_f32 v[106:107], v[106:107], v[58:59]\n\t"
    "s_waitcnt vmcnt(6)\n\t"
    "v_lshlrev_b32 v52, 16, v156\n\t"
    "v_and_b32 v53, 0xffff0000, v156\n\t"
    "v_lshlrev_b32 v54, 16, v157\n\t"
    "v_and_b32 v55, 0xffff0000, v157\n\t"
    "v_lshlrev_b32 v56, 16, v158\n\t"
    "v_and_b32 v57, 0xffff0000, v158\n\t"
    "v_lshlrev_b32 v58, 16, v159\n\t"
    "v_and_b32 v59, 0xffff0000, v159\n\t"
    "v_pk_add_f32 v[68:69], v[68:69], v[52:53]\n\t"
    "v_pk_add_f32 v[70:71], v[70:71], v[54:55]\n\t"
    "v_pk_add_f32 v[104:105], v[104:105], v[56:57]\n\t"
    "v_pk_add_f32 v[106:107], v[106:107], v[58:59]\n\t"
    "s_waitcnt vmcnt(5)\n\t"
    "v_lshlrev_b32 v52, 16, v160\n\t"
    "v_and_b32 v53, 0xffff0000, v160\n\t"
    "v_lshlrev_b32 v54, 16, v161\n\t"
    "v_and_b32 v55, 0xffff0000, v161\n\t"
    "v_lshlrev_b32 v56, 16, v162\n\t"
    "v_and_b32 v57, 0xffff0000, v162\n\t"
    "v_lshlrev_b32 v58, 16, v163\n\t"
    "v_and_b32 v59, 0xffff0000, v163\n\t"
    "v_pk_add_f32 v[68:69], v[68:69], v[52:53]\n\t"
    "v_pk_add_f32 v[70:71], v[70:71], v[54:55]\n\t"
    "v_pk_add_f32 v[104:105], v[104:105], v[56:57]\n\t"
    "v_pk_add_f32 v[106:107], v[106:107], v[58:59]\n\t"
    "s_waitcnt vmcnt(4)\n\t"
    "v_lshlrev_b32 v52, 16, v164\n\t"
    "v_and_b32 v53, 0xffff0000, v164\n\t"
    "v_lshlrev_b32 v54, 16, v165\n\t"
    "v_and_b32 v55, 0xffff0000, v165\n\t"
    "v_lshlrev_b32 v56, 16, v166\n\t"
    "v_and_b32 v57, 0xffff0000, v166\n\t"
    "v_lshlrev_b32 v58, 16, v167\n\t"
    "v_and_b32 v59, 0xffff0000, v167\n\t"
    "v_pk_add_f32 v[68:69], v[68:69], v[52:53]\n\t"
    "v_pk_add_f32 v[70:71], v[70:71], v[54:55]\n\t"
    "v_pk_add_f32 v[104:105], v[104:105], v[56:57]\n\t"
    "v_pk_add_f32 v[106:107], v[106:107], v[58:59]\n\t"
    "s_waitcnt vmcnt(3)\n\t"
    "v_lshlrev_b32 v52, 16, v168\n\t"
    "v_and_b32 v53, 0xffff0000, v168\n\t"
    "v_lshlrev_b32 v54, 16, v169\n\t"
    "v_and_b32 v55, 0xffff0000, v169\n\t"
    "v_lshlrev_b32 v56, 16, v170\n\t"
    "v_and_b32 v57, 0xffff0000, v170\n\t"
    "v_lshlrev_b32 v58, 16, v171\n\t"
    "v_and_b32 v59, 0xffff0000, v171\n\t"
    "v_pk_add_f32 v[72:73], v[72:73], v[52:53]\n\t"
    "v_pk_add_f32 v[74:75], v[74:75], v[54:55]\n\t"
    "v_pk_add_f32 v[108:109], v[108:109], v[56:57]\n\t"
    "v_pk_add_f32 v[110:111], v[110:111], v[58:59]\n\t"
    "s_waitcnt vmcnt(2)\n\t"
    "v_lshlrev_b32 v52, 16, v172\n\t"
    "v_and_b32 v53, 0xffff0000, v172\n\t"
    "v_lshlrev_b32 v54, 16, v173\n\t"
    "v_and_b32 v55, 0xffff0000, v173\n\t"
    "v_lshlrev_b32 v56, 16, v174\n\t"
    "v_and_b32 v57, 0xffff0000, v174\n\t"
    "v_lshlrev_b32 v58, 16, v175\n\t"
    "v_and_b32 v59, 0xffff0000, v175\n\t"
    "v_pk_add_f32 v[72:73], v[72:73], v[52:53]\n\t"
    "v_pk_add_f32 v[74:75], v[74:75], v[54:55]\n\t"
    "v_pk_add_f32 v[108:109], v[108:109], v[56:57]\n\t"
    "v_pk_add_f32 v[110:111], v[110:111], v[58:59]\n\t"
    "s_waitcnt vmcnt(1)\n\t"
    "v_lshlrev_b32 v52, 16, v176\n\t"
    "v_and_b32 v53, 0xffff0000, v176\n\t"
    "v_lshlrev_b32 v54, 16, v177\n\t"
    "v_and_b32 v55, 0xffff0000, v177\n\t"
    "v_lshlrev_b32 v56, 16, v178\n\t"
    "v_and_b32 v57, 0xffff0000, v178\n\t"
    "v_lshlrev_b32 v58, 16, v179\n\t"
    "v_and_b32 v59, 0xffff0000, v179\n\t"
    "v_pk_add_f32 v[72:73], v[72:73], v[52:53]\n\t"
    "v_pk_add_f32 v[74:75], v[74:75], v[54:55]\n\t"
    "v_pk_add_f32 v[108:109], v[108:109], v[56:57]\n\t"
    "v_pk_add_f32 v[110:111], v[110:111], v[58:59]\n\t"
    "s_waitcnt vmcnt(0)\n\t"
    "v_lshlrev_b32 v52, 16, v180\n\t"
    "v_and_b32 v53, 0xffff0000, v180\n\t"
    "v_lshlrev_b32 v54, 16, v181\n\t"
    "v_and_b32 v55, 0xffff0000, v181\n\t"
    "v_lshlrev_b32 v56, 16, v182\n\t"
    "v_and_b32 v57, 0xffff0000, v182\n\t"
    "v_lshlrev_b32 v58, 16, v183\n\t"
    "v_and_b32 v59, 0xffff0000, v183\n\t"
    "v_pk_add_f32 v[72:73], v[72:73], v[52:53]\n\t"
    "v_pk_add_f32 v[74:75], v[74:75], v[54:55]\n\t"
    "v_pk_add_f32 v[108:109], v[108:109], v[56:57]\n\t"
    "v_pk_add_f32 v[110:111], v[110:111], v[58:59]\n\t"
    "v_cvt_pk_bf16_f32 v40, v64, v65\n\t"
    "v_cvt_pk_bf16_f32 v41, v66, v67\n\t"
    "v_cvt_pk_bf16_f32 v42, v68, v69\n\t"
    "v_cvt_pk_bf16_f32 v43, v70, v71\n\t"
    "s_nop 0\n\t"
    "v_permlane16_swap_b32 v40, v42\n\t"
    "v_permlane16_swap_b32 v41, v43\n\t"
    "buffer_store_dwordx4 v[40:43], v37, s[52:55], 0 offen offset:0\n\t"
    "v_cvt_pk_bf16_f32 v44, v72, v73\n\t"
    "v_cvt_pk_bf16_f32 v45, v74, v75\n\t"
    "buffer_store_dwordx2 v[44:45], %18, s[52:55], 0 offen offset:64\n\t"
    "v_cvt_pk_bf16_f32 v44, v100, v101\n\t"
    "v_cvt_pk_bf16_f32 v45, v102, v103\n\t"
    "v_cvt_pk_bf16_f32 v46, v104, v105\n\t"
    "v_cvt_pk_bf16_f32 v47, v106, v107\n\t"
    "s_nop 0\n\t"
    "v_permlane16_swap_b32 v44, v46\n\t"
    "v_permlane16_swap_b32 v45, v47\n\t"
    "buffer_store_dwordx4 v[44:47], v37, s[52:55], s71 offen offset:0\n\t"
    "v_cvt_pk_bf16_f32 v40, v108, v109\n\t"
    "v_cvt_pk_bf16_f32 v41, v110, v111\n\t"
    "buffer_store_dwordx2 v[40:41], %18, s[52:55], s71 offen offset:64\n\t"
    "s_branch o_end_%=\n\t"
    "; ---- st1\n\t"
    "o_st1_%=:\n\t"
    "s_waitcnt lgkmcnt(8)\n\t"
    "v_pk_add_f32 v[64:65], v[64:65], v[136:137]\n\t"
    "v_pk_add_f32 v[66:67], v[66:67], v[138:139]\n\t"
    "v_pk_add_f32 v[100:101], v[100:101], v[172:173]\n\t"
    "v_pk_add_f32 v[102:103], v[102:103], v[174:175]\n\t"
    "v_cvt_pk_bf16_f32 v64, v64, v65\n\t"
    "v_cvt_pk_bf16_f32 v65, v66, v67\n\t"
    "v_cvt_pk_bf16_f32 v66, v100, v101\n\t"
    "v_cvt_pk_bf16_f32 v67, v102, v103\n\t"
    "s_add_u32 s67, s71, 0\n\t"
    "buffer_store_dwordx4 v[64:67], %11, s[48:51], s67 offen offset:0 sc1\n\t"
    "v_pk_add_f32 v[68:69], v[68:69], v[140:141]\n\t"
    "v_pk_add_f32 v[70:71], v[70:71], v[142:143]\n\t"
    "s_waitcnt lgkmcnt(6)\n\t"
    "v_pk_add_f32 v[104:105], v[104:105], v[176:177]\n\t"
    "v_pk_add_f32 v[106:107], v[106:107], v[178:179]\n\t"
    "v_cvt_pk_bf16_f32 v68, v68, v69\n\t"
    "v_cvt_pk_bf16_f32 v69, v70, v71\n\t"
    "v_cvt_pk_bf16_f32 v70, v104, v105\n\t"
    "v_cvt_pk_bf16_f32 v71, v106, v107\n\t"
    "s_add_u32 s67, s71, 0\n\t"
    "buffer_store_dwordx4 v[68:71], %11, s[48:51], s67 offen offset:1024 sc1\n\t"
    "v_pk_add_f32 v[72:73], v[72:73], v[144:145]\n\t"
    "v_pk_add_f32 v[74:75], v[74:75], v[146:147]\n\t"
    "v_pk_add_f32 v[108:109], v[108:109], v[180:181]\n\t"
    "v_pk_add_f32 v[110:111], v[110:111], v[182:183]\n\t"
    "v_cvt_pk_bf16_f32 v72, v72, v73\n\t"
    "v_cvt_pk_bf16_f32 v73, v74, v75\n\t"
    "v_cvt_pk_bf16_f32 v74, v108, v109\n\t"
    "v_cvt_pk_bf16_f32 v75, v110, v111\n\t"
    "s_add_u32 s67, s71, 0\n\t"
    "buffer_store_dwordx4 v[72:75], %11, s[48:51], s67 offen offset:2048 sc1\n\t"
    "v_pk_add_f32 v[88:89], v[88:89], v[160:161]\n\t"
    "v_pk_add_f32 v[90:91], v[90:91], v[162:163]\n\t"
    "s_waitcnt lgkmcnt(1)\n\t"
    "v_pk_add_f32 v[124:125], v[124:125], v[196:197]\n\t"
    "v_pk_add_f32 v[126:127], v[126:127], v[198:199]\n\t"
    "v_cvt_pk_bf16_f32 v88, v88, v89\n\t"
    "v_cvt_pk_bf16_f32 v89, v90, v91\n\t"
    "v_cvt_pk_bf16_f32 v90, v124, v125\n\t"
    "v_cvt_pk_bf16_f32 v91, v126, v127\n\t"
    "s_add_u32 s67, s71, 0x1000\n\t"
    "buffer_store_dwordx4 v[88:91], %11, s[48:51], s67 offen offset:2048 sc1\n\t"
    "v_pk_add_f32 v[92:93], v[92:93], v[164:165]\n\t"
    "v_pk_add_f32 v[94:95], v[94:95], v[166:167]\n\t"
    "v_pk_add_f32 v[128:129], v[128:129], v[200:201]\n\t"
    "v_pk_add_f32 v[130:131], v[130:131], v[202:203]\n\t"
    "v_cvt_pk_bf16_f32 v92, v92, v93\n\t"
    "v_cvt_pk_bf16_f32 v93, v94, v95\n\t"
    "v_cvt_pk_bf16_f32 v94, v128, v129\n\t"
    "v_cvt_pk_bf16_f32 v95, v130, v131\n\t"
    "s_add_u32 s67, s71, 0x1000\n\t"
    "buffer_store_dwordx4 v[92:95], %11, s[48:51], s67 offen offset:3072 sc1\n\t"
    "v_pk_add_f32 v[96:97], v[96:97], v[168:169]\n\t"
    "v_pk_add_f32 v[98:99], v[98:99], v[170:171]\n\t"
    "s_waitcnt lgkmcnt(0)\n\t"
    "v_pk_add_f32 v[132:133], v[132:133], v[204:205]\n\t"
    "v_pk_add_f32 v[134:135], v[134:135], v[206:207]\n\t"
    "v_cvt_pk_bf16_f32 v96, v96, v97\n\t"
    "v_cvt_pk_bf16_f32 v97, v98, v99\n\t"
    "v_cvt_pk_bf16_f32 v98, v132, v133\n\t"
    "v_cvt_pk_bf16_f32 v99, v134, v135\n\t"
    "s_add_u32 s67, s71, 0x2000\n\t"
    "buffer_store_dwordx4 v[96:99], %11, s[48:51], s67 offen offset:0 sc1\n\t"
    "v_pk_add_f32 v[76:77], v[76:77], v[148:149]\n\t"
    "v_pk_add_f32 v[78:79], v[78:79], v[150:151]\n\t"
    "v_pk_add_f32 v[112:113], v[112:113], v[184:185]\n\t"
    "v_pk_add_f32 v[114:115], v[114:115], v[186:187]\n\t"
    "v_pk_add_f32 v[80:81], v[80:81], v[152:153]\n\t"
    "v_pk_add_f32 v[82:83], v[82:83], v[154:155]\n\t"
    "v_pk_add_f32 v[116:117], v[116:117], v[188:189]\n\t"
    "v_pk_add_f32 v[118:119], v[118:119], v[190:191]\n\t"
    "v_pk_add_f32 v[84:85], v[84:85], v[156:157]\n\t"
    "v_pk_add_f32 v[86:87], v[86:87], v[158:159]\n\t"
    "v_pk_add_f32 v[120:121], v[120:121], v[192:193]\n\t"
    "v_pk_add_f32 v[122:123], v[122:123], v[194:195]\n\t"
    "s_add_u32 s78, s59, 0x20000\n\t"
    "s_waitcnt vmcnt(0)\n\t"
    "s_barrier\n\t"
    "s_cmp_lg_u32 %0, 0\n\t"
    "s_cbranch_scc1 pubp1_%=\n\t"
    "v_mov_b64 v[40:41], %19\n\t"
    "v_mov_b32 v42, 0\n\t"
    "s_mov_b64 s[76:77], exec\n\t"
    "s_mov_b64 exec, 1\n\t"
    "buffer_store_dwordx2 v[40:41], v42, s[72:75], s78 offen offset:0 sc1\n\t"
    "s_mov_b64 exec, s[76:77]\n\t"
    "pubp1_%=:\n\t"
    "buffer_load_dwordx2 v[38:39], %22, s[72:75], 0 offen offset:0 sc1\n\t"
    "s_mov_b32 s67, 0x40000\n\t"
    "pw1_chk_%=:\n\t"
    "s_waitcnt vmcnt(0)\n\t"
    "v_cmp_ne_u64 vcc, %19, v[38:39]\n\t"
    "s_nop 0\n\t"
    "s_cbranch_vccz pw1_ok_%=\n\t"
    "s_sleep 2\n\t"
    "buffer_load_dwordx2 v[38:39], %22, s[72:75], 0 offen offset:0 sc1\n\t"
    "s_waitcnt vmcnt(0)\n\t"
    "s_sub_u32 s67, s67, 1\n\t"
    "s_cmp_lg_u32 s67, 0\n\t"
    "s_cbranch_scc1 pw1_chk_%=\n\t"
    "pw1_ok_%=:\n\t"
    "s_add_u32 s67, s65, 0\n\t"
    "buffer_load_dwordx4 v[136:139], %11, s[48:51], s67 offen offset:3072 sc1\n\t"
    "s_add_u32 s67, s65, 0x12000\n\t"
    "buffer_load_dwordx4 v[140:143], %11, s[48:51], s67 offen offset:3072 sc1\n\t"
    "s_add_u32 s67, s65, 0x1b000\n\t"
    "buffer_load_dwordx4 v[144:147], %11, s[48:51], s67 offen offset:3072 sc1\n\t"
    "s_add_u32 s67, s65, 0x24000\n\t"
    "buffer_load_dwordx4 v[148:151], %11, s[48:51], s67 offen offset:3072 sc1\n\t"
    "s_add_u32 s67, s65, 0x1000\n\t"
    "buffer_load_dwordx4 v[152:155], %11, s[48:51], s67 offen offset:0 sc1\n\t"
    "s_add_u32 s67, s65, 0x13000\n\t"
    "buffer_load_dwordx4 v[156:159], %11, s[48:51], s67 offen offset:0 sc1\n\t"
    "s_add_u32 s67, s65, 0x1c000\n\t"
    "buffer_load_dwordx4 v[160:163], %11, s[48:51], s67 offen offset:0 sc1\n\t"
    "s_add_u32 s67, s65, 0x25000\n\t"
    "buffer_load_dwordx4 v[164:167], %11, s[48:51], s67 offen offset:0 sc1\n\t"
    "s_add_u32 s67, s65, 0x1000\n\t"
    "buffer_load_dwordx4 v[168:171], %11, s[48:51], s67 offen offset:1024 sc1\n\t"
    "s_add_u32 s67, s65, 0x13000\n\t"
    "buffer_load_dwordx4 v[172:175], %11, s[48:51], s67 offen offset:1024 sc1\n\t"
    "s_add_u32 s67, s65, 0x1c000\n\t"
    "buffer_load_dwordx4 v[176:179], %11, s[48:51], s67 offen offset:1024 sc1\n\t"
    "s_add_u32 s67, s65, 0x25000\n\t"
    "buffer_load_dwordx4 v[180:183], %11, s[48:51], s67 offen offset:1024 sc1\n\t"
    "s_mov_b32 s71, 0x9000\n\t"
    "s_waitcnt vmcnt(11)\n\t"
    "v_lshlrev_b32 v52, 16, v136\n\t"
    "v_and_b32 v53, 0xffff0000, v136\n\t"
    "v_lshlrev_b32 v54, 16, v137\n\t"
    "v_and_b32 v55, 0xffff0000, v137\n\t"
    "v_lshlrev_b32 v56, 16, v138\n\t"
    "v_and_b32 v57, 0xffff0000, v138\n\t"
    "v_lshlrev_b32 v58, 16, v139\n\t"
    "v_and_b32 v59, 0xffff0000, v139\n\t"
    "v_pk_add_f32 v[76:77], v[76:77], v[52:53]\n\t"
    "v_pk_add_f32 v[78:79], v[78:79], v[54:55]\n\t"
    "v_pk_add_f32 v[112:113], v[112:113], v[56:57]\n\t"
    "v_pk_add_f32 v[114:115], v[114:115], v[58:59]\n\t"
    "s_waitcnt vmcnt(10)\n\t"
    "v_lshlrev_b32 v52, 16, v140\n\t"
    "v_and_b32 v53, 0xffff0000, v140\n\t"
    "v_lshlrev_b32 v54, 16, v141\n\t"
    "v_and_b32 v55, 0xffff0000, v141\n\t"
    "v_lshlrev_b32 v56, 16, v142\n\t"
    "v_and_b32 v57, 0xffff0000, v142\n\t"
    "v_lshlrev_b32 v58, 16, v143\n\t"
    "v_and_b32 v59, 0xffff0000, v143\n\t"
    "v_pk_add_f32 v[76:77], v[76:77], v[52:53]\n\t"
    "v_pk_add_f32 v[78:79], v[78:79], v[54:55]\n\t"
    "v_pk_add_f32 v[112:113], v[112:113], v[56:57]\n\t"
    "v_pk_add_f32 v[114:115], v[114:115], v[58:59]\n\t"
    "s_waitcnt vmcnt(9)\n\t"
    "v_lshlrev_b32 v52, 16, v144\n\t"
    "v_and_b32 v53, 0xffff0000, v144\n\t"
    "v_lshlrev_b32 v54, 16, v145\n\t"
    "v_and_b32 v55, 0xffff0000, v145\n\t"
    "v_lshlrev_b32 v56, 16, v146\n\t"
    "v_and_b32 v57, 0xffff0000, v146\n\t"
    "v_lshlrev_b32 v58, 16, v147\n\t"
    "v_and_b32 v59, 0xffff0000, v147\n\t"
    "v_pk_add_f32 v[76:77], v[76:77], v[52:53]\n\t"
    "v_pk_add_f32 v[78:79], v[78:79], v[54:55]\n\t"
    "v_pk_add_f32 v[112:113], v[112:113], v[56:57]\n\t"
    "v_pk_add_f32 v[114:115], v[114:115], v[58:59]\n\t"
    "s_waitcnt vmcnt(8)\n\t"
    "v_lshlrev_b32 v52, 16, v148\n\t"
    "v_and_b32 v53, 0xffff0000, v148\n\t"
    "v_lshlrev_b32 v54, 16, v149\n\t"
    "v_and_b32 v55, 0xffff0000, v149\n\t"
    "v_lshlrev_b32 v56, 16, v150\n\t"
    "v_and_b32 v57, 0xffff0000, v150\n\t"
    "v_lshlrev_b32 v58, 16, v151\n\t"
    "v_and_b32 v59, 0xffff0000, v151\n\t"
    "v_pk_add_f32 v[76:77], v[76:77], v[52:53]\n\t"
    "v_pk_add_f32 v[78:79], v[78:79], v[54:55]\n\t"
    "v_pk_add_f32 v[112:113], v[112:113], v[56:57]\n\t"
    "v_pk_add_f32 v[114:115], v[114:115], v[58:59]\n\t"
    "s_waitcnt vmcnt(7)\n\t"
    "v_lshlrev_b32 v52, 16, v152\n\t"
    "v_and_b32 v53, 0xffff0000, v152\n\t"
    "v_lshlrev_b32 v54, 16, v153\n\t"
    "v_and_b32 v55, 0xffff0000, v153\n\t"
    "v_lshlrev_b32 v56, 16, v154\n\t"
    "v_and_b32 v57, 0xffff0000, v154\n\t"
    "v_lshlrev_b32 v58, 16, v155\n\t"
    "v_and_b32 v59, 0xffff0000, v155\n\t"
    "v_pk_add_f32 v[80:81], v[80:81], v[52:53]\n\t"
    "v_pk_add_f32 v[82:83], v[82:83], v[54:55]\n\t"
    "v_pk_add_f32 v[116:117], v[116:117], v[56:57]\n\t"
    "v_pk_add_f32 v[118:119], v[118:119], v[58:59]\n\t"
    "s_waitcnt vmcnt(6)\n\t"
    "v_lshlrev_b32 v52, 16, v156\n\t"
    "v_and_b32 v53, 0xffff0000, v156\n\t"
    "v_lshlrev_b32 v54, 16, v157\n\t"
    "v_and_b32 v55, 0xffff0000, v157\n\t"
    "v_lshlrev_b32 v56, 16, v158\n\t"
    "v_and_b32 v57, 0xffff0000, v158\n\t"
    "v_lshlrev_b32 v58, 16, v159\n\t"
    "v_and_b32 v59, 0xffff0000, v159\n\t"
    "v_pk_add_f32 v[80:81], v[80:81], v[52:53]\n\t"
    "v_pk_add_f32 v[82:83], v[82:83], v[54:55]\n\t"
    "v_pk_add_f32 v[116:117], v[116:117], v[56:57]\n\t"
    "v_pk_add_f32 v[118:119], v[118:119], v[58:59]\n\t"
    "s_waitcnt vmcnt(5)\n\t"
    "v_lshlrev_b32 v52, 16, v160\n\t"
    "v_and_b32 v53, 0xffff0000, v160\n\t"
    "v_lshlrev_b32 v54, 16, v161\n\t"
    "v_and_b32 v55, 0xffff0000, v161\n\t"
    "v_lshlrev_b32 v56, 16, v162\n\t"
    "v_and_b32 v57, 0xffff0000, v162\n\t"
    "v_lshlrev_b32 v58, 16, v163\n\t"
    "v_and_b32 v59, 0xffff0000, v163\n\t"
    "v_pk_add_f32 v[80:81], v[80:81], v[52:53]\n\t"
    "v_pk_add_f32 v[82:83], v[82:83], v[54:55]\n\t"
    "v_pk_add_f32 v[116:117], v[116:117], v[56:57]\n\t"
    "v_pk_add_f32 v[118:119], v[118:119], v[58:59]\n\t"
    "s_waitcnt vmcnt(4)\n\t"
    "v_lshlrev_b32 v52, 16, v164\n\t"
    "v_and_b32 v53, 0xffff0000, v164\n\t"
    "v_lshlrev_b32 v54, 16, v165\n\t"
    "v_and_b32 v55, 0xffff0000, v165\n\t"
    "v_lshlrev_b32 v56, 16, v166\n\t"
    "v_and_b32 v57, 0xffff0000, v166\n\t"
    "v_lshlrev_b32 v58, 16, v167\n\t"
    "v_and_b32 v59, 0xffff0000, v167\n\t"
    "v_pk_add_f32 v[80:81], v[80:81], v[52:53]\n\t"
    "v_pk_add_f32 v[82:83], v[82:83], v[54:55]\n\t"
    "v_pk_add_f32 v[116:117], v[116:117], v[56:57]\n\t"
    "v_pk_add_f32 v[118:119], v[118:119], v[58:59]\n\t"
    "s_waitcnt vmcnt(3)\n\t"
    "v_lshlrev_b32 v52, 16, v168\n\t"
    "v_and_b32 v53, 0xffff0000, v168\n\t"
    "v_lshlrev_b32 v54, 16, v169\n\t"
    "v_and_b32 v55, 0xffff0000, v169\n\t"
    "v_lshlrev_b32 v56, 16, v170\n\t"
    "v_and_b32 v57, 0xffff0000, v170\n\t"
    "v_lshlrev_b32 v58, 16, v171\n\t"
    "v_and_b32 v59, 0xffff0000, v171\n\t"
    "v_pk_add_f32 v[84:85], v[84:85], v[52:53]\n\t"
    "v_pk_add_f32 v[86:87], v[86:87], v[54:55]\n\t"
    "v_pk_add_f32 v[120:121], v[120:121], v[56:57]\n\t"
    "v_pk_add_f32 v[122:123], v[122:123], v[58:59]\n\t"
    "s_waitcnt vmcnt(2)\n\t"
    "v_lshlrev_b32 v52, 16, v172\n\t"
    "v_and_b32 v53, 0xffff0000, v172\n\t"
    "v_lshlrev_b32 v54, 16, v173\n\t"
    "v_and_b32 v55, 0xffff0000, v173\n\t"
    "v_lshlrev_b32 v56, 16, v174\n\t"
    "v_and_b32 v57, 0xffff0000, v174\n\t"
    "v_lshlrev_b32 v58, 16, v175\n\t"
    "v_and_b32 v59, 0xffff0000, v175\n\t"
    "v_pk_add_f32 v[84:85], v[84:85], v[52:53]\n\t"
    "v_pk_add_f32 v[86:87], v[86:87], v[54:55]\n\t"
    "v_pk_add_f32 v[120:121], v[120:121], v[56:57]\n\t"
    "v_pk_add_f32 v[122:123], v[122:123], v[58:59]\n\t"
    "s_waitcnt vmcnt(1)\n\t"
    "v_lshlrev_b32 v52, 16, v176\n\t"
    "v_and_b32 v53, 0xffff0000, v176\n\t"
    "v_lshlrev_b32 v54, 16, v177\n\t"
    "v_and_b32 v55, 0xffff0000, v177\n\t"
    "v_lshlrev_b32 v56, 16, v178\n\t"
    "v_and_b32 v57, 0xffff0000, v178\n\t"
    "v_lshlrev_b32 v58, 16, v179\n\t"
    "v_and_b32 v59, 0xffff0000, v179\n\t"
    "v_pk_add_f32 v[84:85], v[84:85], v[52:53]\n\t"
    "v_pk_add_f32 v[86:87], v[86:87], v[54:55]\n\t"
    "v_pk_add_f32 v[120:121], v[120:121], v[56:57]\n\t"
    "v_pk_add_f32 v[122:123], v[122:123], v[58:59]\n\t"
    "s_waitcnt vmcnt(0)\n\t"
    "v_lshlrev_b32 v52, 16, v180\n\t"
    "v_and_b32 v53, 0xffff0000, v180\n\t"
    "v_lshlrev_b32 v54, 16, v181\n\t"
    "v_and_b32 v55, 0xffff0000, v181\n\t"
    "v_lshlrev_b32 v56, 16, v182\n\t"
    "v_and_b32 v57, 0xffff0000, v182\n\t"
    "v_lshlrev_b32 v58, 16, v183\n\t"
    "v_and_b32 v59, 0xffff0000, v183\n\t"
    "v_pk_add_f32 v[84:85], v[84:85], v[52:53]\n\t"
    "v_pk_add_f32 v[86:87], v[86:87], v[54:55]\n\t"
    "v_pk_add_f32 v[120:121], v[120:121], v[56:57]\n\t"
    "v_pk_add_f32 v[122:123], v[122:123], v[58:59]\n\t"
    "v_cvt_pk_bf16_f32 v40, v80, v81\n\t"
    "v_cvt_pk_bf16_f32 v41, v82, v83\n\t"
    "v_cvt_pk_bf16_f32 v42, v84, v85\n\t"
    "v_cvt_pk_bf16_f32 v43, v86, v87\n\t"
    "s_nop 0\n\t"
    "v_permlane16_swap_b32 v40, v42\n\t"
    "v_permlane16_swap_b32 v41, v43\n\t"
    "buffer_store_dwordx4 v[40:43], v37, s[52:55], 0 offen offset:128\n\t"
    "v_cvt_pk_bf16_f32 v44, v76, v77\n\t"
    "v_cvt_pk_bf16_f32 v45, v78, v79\n\t"
    "buffer_store_dwordx2 v[44:45], %18, s[52:55], 0 offen offset:96\n\t"
    "v_cvt_pk_bf16_f32 v44, v116, v117\n\t"
    "v_cvt_pk_bf16_f32 v45, v118, v119\n\t"
    "v_cvt_pk_bf16_f32 v46, v120, v121\n\t"
    "v_cvt_pk_bf16_f32 v47, v122, v123\n\t"
    "s_nop 0\n\t"
    "v_permlane16_swap_b32 v44, v46\n\t"
    "v_permlane16_swap_b32 v45, v47\n\t"
    "buffer_store_dwordx4 v[44:47], v37, s[52:55], s71 offen offset:128\n\t"
    "v_cvt_pk_bf16_f32 v40, v112, v113\n\t"
    "v_cvt_pk_bf16_f32 v41, v114, v115\n\t"
    "buffer_store_dwordx2 v[40:41], %18, s[52:55], s71 offen offset:96\n\t"
    "s_branch o_end_%=\n\t"
    "; ---- st2\n\t"
    "o_st2_%=:\n\t"
    "s_waitcnt lgkmcnt(8)\n\t"
    "v_pk_add_f32 v[64:65], v[64:65], v[136:137]\n\t"
    "v_pk_add_f32 v[66:67], v[66:67], v[138:139]\n\t"
    "v_pk_add_f32 v[100:101], v[100:101], v[172:173]\n\t"
    "v_pk_add_f32 v[102:103], v[102:103], v[174:175]\n\t"
    "v_cvt_pk_bf16_f32 v64, v64, v65\n\t"
    "v_cvt_pk_bf16_f32 v65, v66, v67\n\t"
    "v_cvt_pk_bf16_f32 v66, v100, v101\n\t"
    "v_cvt_pk_bf16_f32 v67, v102, v103\n\t"
    "s_add_u32 s67, s71, 0\n\t"
    "buffer_store_dwordx4 v[64:67], %11, s[48:51], s67 offen offset:0 sc1\n\t"
    "v_pk_add_f32 v[68:69], v[68:69], v[140:141]\n\t"
    "v_pk_add_f32 v[70:71], v[70:71], v[142:143]\n\t"
    "s_waitcnt lgkmcnt(6)\n\t"
    "v_pk_add_f32 v[104:105], v[104:105], v[176:177]\n\t"
    "v_pk_add_f32 v[106:107], v[106:107], v[178:179]\n\t"
    "v_cvt_pk_bf16_f32 v68, v68, v69\n\t"
    "v_cvt_pk_bf16_f32 v69, v70, v71\n\t"
    "v_cvt_pk_bf16_f32 v70, v104, v105\n\t"
    "v_cvt_pk_bf16_f32 v71, v106, v107\n\t"
    "s_add_u32 s67, s71, 0\n\t"
    "buffer_store_dwordx4 v[68:71], %11, s[48:51], s67 offen offset:1024 sc1\n\t"
    "v_pk_add_f32 v[72:73], v[72:73], v[144:145]\n\t"
    "v_pk_add_f32 v[74:75], v[74:75], v[146:147]\n\t"
    "v_pk_add_f32 v[108:109], v[108:109], v[180:181]\n\t"
    "v_pk_add_f32 v[110:111], v[110:111], v[182:183]\n\t"
    "v_cvt_pk_bf16_f32 v72, v72, v73\n\t"
    "v_cvt_pk_bf16_f32 v73, v74, v75\n\t"
    "v_cvt_pk_bf16_f32 v74, v108, v109\n\t"
    "v_cvt_pk_bf16_f32 v75, v110, v111\n\t"
    "s_add_u32 s67, s71, 0\n\t"
    "buffer_store_dwordx4 v[72:75], %11, s[48:51], s67 offen offset:2048 sc1\n\t"
    "v_pk_add_f32 v[76:77], v[76:77], v[148:149]\n\t"
    "v_pk_add_f32 v[78:79], v[78:79], v[150:151]\n\t"
    "s_waitcnt lgkmcnt(4)\n\t"
    "v_pk_add_f32 v[112:113], v[112:113], v[184:185]\n\t"
    "v_pk_add_f32 v[114:115], v[114:115], v[186:187]\n\t"
    "v_cvt_pk_bf16_f32 v76, v76, v77\n\t"
    "v_cvt_pk_bf16_f32 v77, v78, v79\n\t"
    "v_cvt_pk_bf16_f32 v78, v112, v113\n\t"
    "v_cvt_pk_bf16_f32 v79, v114, v115\n\t"
    "s_add_u32 s67, s71, 0\n\t"
    "buffer_store_dwordx4 v[76:79], %11, s[48:51], s67 offen offset:3072 sc1\n\t"
    "v_pk_add_f32 v[80:81], v[80:81], v[152:153]\n\t"
    "v_pk_add_f32 v[82:83], v[82:83], v[154:155]\n\t"
    "v_pk_add_f32 v[116:117], v[116:117], v[188:189]\n\t"
    "v_pk_add_f32 v[118:119], v[118:119], v[190:191]\n\t"
    "v_cvt_pk_bf16_f32 v80, v80, v81\n\t"
    "v_cvt_pk_bf16_f32 v81, v82, v83\n\t"
    "v_cvt_pk_bf16_f32 v82, v116, v117\n\t"
    "v_cvt_pk_bf16_f32 v83, v118, v119\n\t"
    "s_add_u32 s67, s71, 0x1000\n\t"
    "buffer_store_dwordx4 v[80:83], %11, s[48:51], s67 offen offset:0 sc1\n\t"
    "v_pk_add_f32 v[84:85], v[84:85], v[156:157]\n\t"
    "v_pk_add_f32 v[86:87], v[86:87], v[158:159]\n\t"
    "s_waitcnt lgkmcnt(2)\n\t"
    "v_pk_add_f32 v[120:121], v[120:121], v[192:193]\n\t"
    "v_pk_add_f32 v[122:123], v[122:123], v[194:195]\n\t"
    "v_cvt_pk_bf16_f32 v84, v84, v85\n\t"
    "v_cvt_pk_bf16_f32 v85, v86, v87\n\t"
    "v_cvt_pk_bf16_f32 v86, v120, v121\n\t"
    "v_cvt_pk_bf16_f32 v87, v122, v123\n\t"
    "s_add_u32 s67, s71, 0x1000\n\t"
    "buffer_store_dwordx4 v[84:87], %11, s[48:51], s67 offen offset:1024 sc1\n\t"
    "v_pk_add_f32 v[88:89], v[88:89], v[160:161]\n\t"
    "v_pk_add_f32 v[90:91], v[90:91], v[162:163]\n\t"
    "v_pk_add_f32 v[124:125], v[124:125], v[196:197]\n\t"
    "v_pk_add_f32 v[126:127], v[126:127], v[198:199]\n\t"
    "v_pk_add_f32 v[92:93], v[92:93], v[164:165]\n\t"
    "v_pk_add_f32 v[94:95], v[94:95], v[166:167]\n\t"
    "s_waitcnt lgkmcnt(0)\n\t"
    "v_pk_add_f32 v[128:129], v[128:129], v[200:201]\n\t"
    "v_pk_add_f32 v[130:131], v[130:131], v[202:203]\n\t"
    "v_pk_add_f32 v[96:97], v[96:97], v[168:169]\n\t"
    "v_pk_add_f32 v[98:99], v[98:99], v[170:171]\n\t"
    "v_pk_add_f32 v[132:133], v[132:133], v[204:205]\n\t"
    "v_pk_add_f32 v[134:135], v[134:135], v[206:207]\n\t"
    "s_add_u32 s78, s59, 0x20000\n\t"
    "s_waitcnt vmcnt(0)\n\t"
    "s_barrier\n\t"
    "s_cmp_lg_u32 %0, 0\n\t"
    "s_cbranch_scc1 pubp2_%=\n\t"
    "v_mov_b64 v[40:41], %19\n\t"
    "v_mov_b32 v42, 0\n\t"
    "s_mov_b64 s[76:77], exec\n\t"
    "s_mov_b64 exec, 1\n\t"
    "buffer_store_dwordx2 v[40:41], v42, s[72:75], s78 offen offset:0 sc1\n\t"
    "s_mov_b64 exec, s[76:77]\n\t"
    "pubp2_%=:\n\t"
    "buffer_load_dwordx2 v[38:39], %22, s[72:75], 0 offen offset:0 sc1\n\t"
    "s_mov_b32 s67, 0x40000\n\t"
    "pw2_chk_%=:\n\t"
    "s_waitcnt vmcnt(0)\n\t"
    "v_cmp_ne_u64 vcc, %19, v[38:39]\n\t"
    "s_nop 0\n\t"
    "s_cbranch_vccz pw2_ok_%=\n\t"
    "s_sleep 2\n\t"
    "buffer_load_dwordx2 v[38:39], %22, s[72:75], 0 offen offset:0 sc1\n\t"
    "s_waitcnt vmcnt(0)\n\t"
    "s_sub_u32 s67, s67, 1\n\t"
    "s_cmp_lg_u32 s67, 0\n\t"
    "s_cbranch_scc1 pw2_chk_%=\n\t"
    "pw2_ok_%=:\n\t"
    "s_add_u32 s67, s65, 0x1000\n\t"
    "buffer_load_dwordx4 v[136:139], %11, s[48:51], s67 offen offset:2048 sc1\n\t"
    "s_add_u32 s67, s65, 0xa000\n\t"
    "buffer_load_dwordx4 v[140:143], %11, s[48:51], s67 offen offset:2048 sc1\n\t"
    "s_add_u32 s67, s65, 0x1c000\n\t"
    "buffer_load_dwordx4 v[144:147], %11, s[48:51], s67 offen offset:2048 sc1\n\t"
    "s_add_u32 s67, s65, 0x25000\n\t"
    "buffer_load_dwordx4 v[148:151], %11, s[48:51], s67 offen offset:2048 sc1\n\t"
    "s_add_u32 s67, s65, 0x1000\n\t"
    "buffer_load_dwordx4 v[152:155], %11, s[48:51], s67 offen offset:3072 sc1\n\t"
    "s_add_u32 s67, s65, 0xa000\n\t"
    "buffer_load_dwordx4 v[156:159], %11, s[48:51], s67 offen offset:3072 sc1\n\t"
    "s_add_u32 s67, s65, 0x1c000\n\t"
    "buffer_load_dwordx4 v[160:163], %11, s[48:51], s67 offen offset:3072 sc1\n\t"
    "s_add_u32 s67, s65, 0x25000\n\t"
    "buffer_load_dwordx4 v[164:167], %11, s[48:51], s67 offen offset:3072 sc1\n\t"
    "s_add_u32 s67, s65, 0x2000\n\t"
    "buffer_load_dwordx4 v[168:171], %11, s[48:51], s67 offen offset:0 sc1\n\t"
    "s_add_u32 s67, s65, 0xb000\n\t"
    "buffer_load_dwordx4 v[172:175], %11, s[48:51], s67 offen offset:0 sc1\n\t"
    "s_add_u32 s67, s65, 0x1d000\n\t"
    "buffer_load_dwordx4 v[176:179], %11, s[48:51], s67 offen offset:0 sc1\n\t"
    "s_add_u32 s67, s65, 0x26000\n\t"
    "buffer_load_dwordx4 v[180:183], %11, s[48:51], s67 offen offset:0 sc1\n\t"
    "s_mov_b32 s71, 0x9000\n\t"
    "s_waitcnt vmcnt(11)\n\t"
    "v_lshlrev_b32 v52, 16, v136\n\t"
    "v_and_b32 v53, 0xffff0000, v136\n\t"
    "v_lshlrev_b32 v54, 16, v137\n\t"
    "v_and_b32 v55, 0xffff0000, v137\n\t"
    "v_lshlrev_b32 v56, 16, v138\n\t"
    "v_and_b32 v57, 0xffff0000, v138\n\t"
    "v_lshlrev_b32 v58, 16, v139\n\t"
    "v_and_b32 v59, 0xffff0000, v139\n\t"
    "v_pk_add_f32 v[88:89], v[88:89], v[52:53]\n\t"
    "v_pk_add_f32 v[90:91], v[90:91], v[54:55]\n\t"
    "v_pk_add_f32 v[124:125], v[124:125], v[56:57]\n\t"
    "v_pk_add_f32 v[126:127], v[126:127], v[58:59]\n\t"
    "s_waitcnt vmcnt(10)\n\t"
    "v_lshlrev_b32 v52, 16, v140\n\t"
    "v_and_b32 v53, 0xffff0000, v140\n\t"
    "v_lshlrev_b32 v54, 16, v141\n\t"
    "v_and_b32 v55, 0xffff0000, v141\n\t"
    "v_lshlrev_b32 v56, 16, v142\n\t"
    "v_and_b32 v57, 0xffff0000, v142\n\t"
    "v_lshlrev_b32 v58, 16, v143\n\t"
    "v_and_b32 v59, 0xffff0000, v143\n\t"
    "v_pk_add_f32 v[88:89], v[88:89], v[52:53]\n\t"
    "v_pk_add_f32 v[90:91], v[90:91], v[54:55]\n\t"
    "v_pk_add_f32 v[124:125], v[124:125], v[56:57]\n\t"
    "v_pk_add_f32 v[126:127], v[126:127], v[58:59]\n\t"
    "s_waitcnt vmcnt(9)\n\t"
    "v_lshlrev_b32 v52, 16, v144\n\t"
    "v_and_b32 v53, 0xffff0000, v144\n\t"
    "v_lshlrev_b32 v54, 16, v145\n\t"
    "v_and_b32 v55, 0xffff0000, v145\n\t"
    "v_lshlrev_b32 v56, 16, v146\n\t"
    "v_and_b32 v57, 0xffff0000, v146\n\t"
    "v_lshlrev_b32 v58, 16, v147\n\t"
    "v_and_b32 v59, 0xffff0000, v147\n\t"
    "v_pk_add_f32 v[88:89], v[88:89], v[52:53]\n\t"
    "v_pk_add_f32 v[90:91], v[90:91], v[54:55]\n\t"
    "v_pk_add_f32 v[124:125], v[124:125], v[56:57]\n\t"
    "v_pk_add_f32 v[126:127], v[126:127], v[58:59]\n\t"
    "s_waitcnt vmcnt(8)\n\t"
    "v_lshlrev_b32 v52, 16, v148\n\t"
    "v_and_b32 v53, 0xffff0000, v148\n\t"
    "v_lshlrev_b32 v54, 16, v149\n\t"
    "v_and_b32 v55, 0xffff0000, v149\n\t"
    "v_lshlrev_b32 v56, 16, v150\n\t"
    "v_and_b32 v57, 0xffff0000, v150\n\t"
    "v_lshlrev_b32 v58, 16, v151\n\t"
    "v_and_b32 v59, 0xffff0000, v151\n\t"
    "v_pk_add_f32 v[88:89], v[88:89], v[52:53]\n\t"
    "v_pk_add_f32 v[90:91], v[90:91], v[54:55]\n\t"
    "v_pk_add_f32 v[124:125], v[124:125], v[56:57]\n\t"
    "v_pk_add_f32 v[126:127], v[126:127], v[58:59]\n\t"
    "s_waitcnt vmcnt(7)\n\t"
    "v_lshlrev_b32 v52, 16, v152\n\t"
    "v_and_b32 v53, 0xffff0000, v152\n\t"
    "v_lshlrev_b32 v54, 16, v153\n\t"
    "v_and_b32 v55, 0xffff0000, v153\n\t"
    "v_lshlrev_b32 v56, 16, v154\n\t"
    "v_and_b32 v57, 0xffff0000, v154\n\t"
    "v_lshlrev_b32 v58, 16, v155\n\t"
    "v_and_b32 v59, 0xffff0000, v155\n\t"
    "v_pk_add_f32 v[92:93], v[92:93], v[52:53]\n\t"
    "v_pk_add_f32 v[94:95], v[94:95], v[54:55]\n\t"
    "v_pk_add_f32 v[128:129], v[128:129], v[56:57]\n\t"
    "v_pk_add_f32 v[130:131], v[130:131], v[58:59]\n\t"
    "s_waitcnt vmcnt(6)\n\t"
    "v_lshlrev_b32 v52, 16, v156\n\t"
    "v_and_b32 v53, 0xffff0000, v156\n\t"
    "v_lshlrev_b32 v54, 16, v157\n\t"
    "v_and_b32 v55, 0xffff0000, v157\n\t"
    "v_lshlrev_b32 v56, 16, v158\n\t"
    "v_and_b32 v57, 0xffff0000, v158\n\t"
    "v_lshlrev_b32 v58, 16, v159\n\t"
    "v_and_b32 v59, 0xffff0000, v159\n\t"
    "v_pk_add_f32 v[92:93], v[92:93], v[52:53]\n\t"
    "v_pk_add_f32 v[94:95], v[94:95], v[54:55]\n\t"
    "v_pk_add_f32 v[128:129], v[128:129], v[56:57]\n\t"
    "v_pk_add_f32 v[130:131], v[130:131], v[58:59]\n\t"
    "s_waitcnt vmcnt(5)\n\t"
    "v_lshlrev_b32 v52, 16, v160\n\t"
    "v_and_b32 v53, 0xffff0000, v160\n\t"
    "v_lshlrev_b32 v54, 16, v161\n\t"
    "v_and_b32 v55, 0xffff0000, v161\n\t"
    "v_lshlrev_b32 v56, 16, v162\n\t"
    "v_and_b32 v57, 0xffff0000, v162\n\t"
    "v_lshlrev_b32 v58, 16, v163\n\t"
    "v_and_b32 v59, 0xffff0000, v163\n\t"
    "v_pk_add_f32 v[92:93], v[92:93], v[52:53]\n\t"
    "v_pk_add_f32 v[94:95], v[94:95], v[54:55]\n\t"
    "v_pk_add_f32 v[128:129], v[128:129], v[56:57]\n\t"
    "v_pk_add_f32 v[130:131], v[130:131], v[58:59]\n\t"
    "s_waitcnt vmcnt(4)\n\t"
    "v_lshlrev_b32 v52, 16, v164\n\t"
    "v_and_b32 v53, 0xffff0000, v164\n\t"
    "v_lshlrev_b32 v54, 16, v165\n\t"
    "v_and_b32 v55, 0xffff0000, v165\n\t"
    "v_lshlrev_b32 v56, 16, v166\n\t"
    "v_and_b32 v57, 0xffff0000, v166\n\t"
    "v_lshlrev_b32 v58, 16, v167\n\t"
    "v_and_b32 v59, 0xffff0000, v167\n\t"
    "v_pk_add_f32 v[92:93], v[92:93], v[52:53]\n\t"
    "v_pk_add_f32 v[94:95], v[94:95], v[54:55]\n\t"
    "v_pk_add_f32 v[128:129], v[128:129], v[56:57]\n\t"
    "v_pk_add_f32 v[130:131], v[130:131], v[58:59]\n\t"
    "s_waitcnt vmcnt(3)\n\t"
    "v_lshlrev_b32 v52, 16, v168\n\t"
    "v_and_b32 v53, 0xffff0000, v168\n\t"
    "v_lshlrev_b32 v54, 16, v169\n\t"
    "v_and_b32 v55, 0xffff0000, v169\n\t"
    "v_lshlrev_b32 v56, 16, v170\n\t"
    "v_and_b32 v57, 0xffff0000, v170\n\t"
    "v_lshlrev_b32 v58, 16, v171\n\t"
    "v_and_b32 v59, 0xffff0000, v171\n\t"
    "v_pk_add_f32 v[96:97], v[96:97], v[52:53]\n\t"
    "v_pk_add_f32 v[98:99], v[98:99], v[54:55]\n\t"
    "v_pk_add_f32 v[132:133], v[132:133], v[56:57]\n\t"
    "v_pk_add_f32 v[134:135], v[134:135], v[58:59]\n\t"
    "s_waitcnt vmcnt(2)\n\t"
    "v_lshlrev_b32 v52, 16, v172\n\t"
    "v_and_b32 v53, 0xffff0000, v172\n\t"
    "v_lshlrev_b32 v54, 16, v173\n\t"
    "v_and_b32 v55, 0xffff0000, v173\n\t"
    "v_lshlrev_b32 v56, 16, v174\n\t"
    "v_and_b32 v57, 0xffff0000, v174\n\t"
    "v_lshlrev_b32 v58, 16, v175\n\t"
    "v_and_b32 v59, 0xffff0000, v175\n\t"
    "v_pk_add_f32 v[96:97], v[96:97], v[52:53]\n\t"
    "v_pk_add_f32 v[98:99], v[98:99], v[54:55]\n\t"
    "v_pk_add_f32 v[132:133], v[132:133], v[56:57]\n\t"
    "v_pk_add_f32 v[134:135], v[134:135], v[58:59]\n\t"
    "s_waitcnt vmcnt(1)\n\t"
    "v_lshlrev_b32 v52, 16, v176\n\t"
    "v_and_b32 v53, 0xffff0000, v176\n\t"
    "v_lshlrev_b32 v54, 16, v177\n\t"
    "v_and_b32 v55, 0xffff0000, v177\n\t"
    "v_lshlrev_b32 v56, 16, v178\n\t"
    "v_and_b32 v57, 0xffff0000, v178\n\t"
    "v_lshlrev_b32 v58, 16, v179\n\t"
    "v_and_b32 v59, 0xffff0000, v179\n\t"
    "v_pk_add_f32 v[96:97], v[96:97], v[52:53]\n\t"
    "v_pk_add_f32 v[98:99], v[98:99], v[54:55]\n\t"
    "v_pk_add_f32 v[132:133], v[132:133], v[56:57]\n\t"
    "v_pk_add_f32 v[134:135], v[134:135], v[58:59]\n\t"
    "s_waitcnt vmcnt(0)\n\t"
    "v_lshlrev_b32 v52, 16, v180\n\t"
    "v_and_b32 v53, 0xffff0000, v180\n\t"
    "v_lshlrev_b32 v54, 16, v181\n\t"
    "v_and_b32 v55, 0xffff0000, v181\n\t"
    "v_lshlrev_b32 v56, 16, v182\n\t"
    "v_and_b32 v57, 0xffff0000, v182\n\t"
    "v_lshlrev_b32 v58, 16, v183\n\t"
    "v_and_b32 v59, 0xffff0000, v183\n\t"
    "v_pk_add_f32 v[96:97], v[96:97], v[52:53]\n\t"
    "v_pk_add_f32 v[98:99], v[98:99], v[54:55]\n\t"
    "v_pk_add_f32 v[132:133], v[132:133], v[56:57]\n\t"
    "v_pk_add_f32 v[134:135], v[134:135], v[58:59]\n\t"
    "v_cvt_pk_bf16_f32 v40, v88, v89\n\t"
    "v_cvt_pk_bf16_f32 v41, v90, v91\n\t"
    "v_cvt_pk_bf16_f32 v42, v92, v93\n\t"
    "v_cvt_pk_bf16_f32 v43, v94, v95\n\t"
    "s_nop 0\n\t"
    "v_permlane16_swap_b32 v40, v42\n\t"
    "v_permlane16_swap_b32 v41, v43\n\t"
    "buffer_store_dwordx4 v[40:43], v37, s[52:55], 0 offen offset:192\n\t"
    "v_cvt_pk_bf16_f32 v44, v96, v97\n\t"
    "v_cvt_pk_bf16_f32 v45, v98, v99\n\t"
    "buffer_store_dwordx2 v[44:45], %18, s[52:55], 0 offen offset:256\n\t"
    "v_cvt_pk_bf16_f32 v44, v124, v125\n\t"
    "v_cvt_pk_bf16_f32 v45, v126, v127\n\t"
    "v_cvt_pk_bf16_f32 v46, v128, v129\n\t"
    "v_cvt_pk_bf16_f32 v47, v130, v131\n\t"
    "s_nop 0\n\t"
    "v_permlane16_swap_b32 v44, v46\n\t"
    "v_permlane16_swap_b32 v45, v47\n\t"
    "buffer_store_dwordx4 v[44:47], v37, s[52:55], s71 offen offset:192\n\t"
    "v_cvt_pk_bf16_f32 v40, v132, v133\n\t"
    "v_cvt_pk_bf16_f32 v41, v134, v135\n\t"
    "buffer_store_dwordx2 v[40:41], %18, s[52:55], s71 offen offset:256\n\t"
    "s_branch o_end_%=\n\t"
    "; ---- epiB\n\t"
    "o_epiB_%=:\n\t"
    "s_mov_b32 s67, 0x480\n\t"
    "s_mov_b32 s71, 0x9480\n\t"
    "s_waitcnt lgkmcnt(15)\n\t"
    "v_pk_add_f32 v[64:65], v[64:65], v[136:137]\n\t"
    "v_pk_add_f32 v[66:67], v[66:67], v[138:139]\n\t"
    "v_pk_add_f32 v[68:69], v[68:69], v[140:141]\n\t"
    "v_pk_add_f32 v[70:71], v[70:71], v[142:143]\n\t"
    "v_cvt_pk_bf16_f32 v40, v64, v65\n\t"
    "v_cvt_pk_bf16_f32 v41, v66, v67\n\t"
    "v_cvt_pk_bf16_f32 v42, v68, v69\n\t"
    "v_cvt_pk_bf16_f32 v43, v70, v71\n\t"
    "s_nop 0\n\t"
    "v_permlane16_swap_b32 v40, v42\n\t"
    "v_permlane16_swap_b32 v41, v43\n\t"
    "buffer_store_dwordx4 v[40:43], v37, s[52:55], s67 offen offset:0\n\t"
    "s_waitcnt lgkmcnt(7)\n\t"
    "v_pk_add_f32 v[100:101], v[100:101], v[172:173]\n\t"
    "v_pk_add_f32 v[102:103], v[102:103], v[174:175]\n\t"
    "v_pk_add_f32 v[104:105], v[104:105], v[176:177]\n\t"
    "v_pk_add_f32 v[106:107], v[106:107], v[178:179]\n\t"
    "v_cvt_pk_bf16_f32 v44, v100, v101\n\t"
    "v_cvt_pk_bf16_f32 v45, v102, v103\n\t"
    "v_cvt_pk_bf16_f32 v46, v104, v105\n\t"
    "v_cvt_pk_bf16_f32 v47, v106, v107\n\t"
    "s_nop 0\n\t"
    "v_permlane16_swap_b32 v44, v46\n\t"
    "v_permlane16_swap_b32 v45, v47\n\t"
    "buffer_store_dwordx4 v[44:47], v37, s[52:55], s71 offen offset:0\n\t"
    "v_pk_add_f32 v[72:73], v[72:73], v[144:145]\n\t"
    "v_pk_add_f32 v[74:75], v[74:75], v[146:147]\n\t"
    "v_pk_add_f32 v[76:77], v[76:77], v[148:149]\n\t"
    "v_pk_add_f32 v[78:79], v[78:79], v[150:151]\n\t"
    "v_cvt_pk_bf16_f32 v40, v72, v73\n\t"
    "v_cvt_pk_bf16_f32 v41, v74, v75\n\t"
    "v_cvt_pk_bf16_f32 v42, v76, v77\n\t"
    "v_cvt_pk_bf16_f32 v43, v78, v79\n\t"
    "s_nop 0\n\t"
    "v_permlane16_swap_b32 v40, v42\n\t"
    "v_permlane16_swap_b32 v41, v43\n\t"
    "buffer_store_dwordx4 v[40:43], v37, s[52:55], s67 offen offset:64\n\t"
    "s_waitcnt lgkmcnt(5)\n\t"
    "v_pk_add_f32 v[108:109], v[108:109], v[180:181]\n\t"
    "v_pk_add_f32 v[110:111], v[110:111], v[182:183]\n\t"
    "v_pk_add_f32 v[112:113], v[112:113], v[184:185]\n\t"
    "v_pk_add_f32 v[114:115], v[114:115], v[186:187]\n\t"
    "v_cvt_pk_bf16_f32 v44, v108, v109\n\t"
    "v_cvt_pk_bf16_f32 v45, v110, v111\n\t"
    "v_cvt_pk_bf16_f32 v46, v112, v113\n\t"
    "v_cvt_pk_bf16_f32 v47, v114, v115\n\t"
    "s_nop 0\n\t"
    "v_permlane16_swap_b32 v44, v46\n\t"
    "v_permlane16_swap_b32 v45, v47\n\t"
    "buffer_store_dwordx4 v[44:47], v37, s[52:55], s71 offen offset:64\n\t"
    "v_pk_add_f32 v[80:81], v[80:81], v[152:153]\n\t"
    "v_pk_add_f32 v[82:83], v[82:83], v[154:155]\n\t"
    "v_pk_add_f32 v[84:85], v[84:85], v[156:157]\n\t"
    "v_pk_add_f32 v[86:87], v[86:87], v[158:159]\n\t"
    "v_cvt_pk_bf16_f32 v40, v80, v81\n\t"
    "v_cvt_pk_bf16_f32 v41, v82, v83\n\t"
    "v_cvt_pk_bf16_f32 v42, v84, v85\n\t"
    "v_cvt_pk_bf16_f32 v43, v86, v87\n\t"
    "s_nop 0\n\t"
    "v_permlane16_swap_b32 v40, v42\n\t"
    "v_permlane16_swap_b32 v41, v43\n\t"
    "buffer_store_dwordx4 v[40:43], v37, s[52:55], s67 offen offset:128\n\t"
    "s_waitcnt lgkmcnt(3)\n\t"
    "v_pk_add_f32 v[116:117], v[116:117], v[188:189]\n\t"
    "v_pk_add_f32 v[118:119], v[118:119], v[190:191]\n\t"
    "v_pk_add_f32 v[120:121], v[120:121], v[192:193]\n\t"
    "v_pk_add_f32 v[122:123], v[122:123], v[194:195]\n\t"
    "v_cvt_pk_bf16_f32 v44, v116, v117\n\t"
    "v_cvt_pk_bf16_f32 v45, v118, v119\n\t"
    "v_cvt_pk_bf16_f32 v46, v120, v121\n\t"
    "v_cvt_pk_bf16_f32 v47, v122, v123\n\t"
    "s_nop 0\n\t"
    "v_permlane16_swap_b32 v44, v46\n\t"
    "v_permlane16_swap_b32 v45, v47\n\t"
    "buffer_store_dwordx4 v[44:47], v37, s[52:55], s71 offen offset:128\n\t"
    "v_pk_add_f32 v[88:89], v[88:89], v[160:161]\n\t"
    "v_pk_add_f32 v[90:91], v[90:91], v[162:163]\n\t"
    "v_pk_add_f32 v[92:93], v[92:93], v[164:165]\n\t"
    "v_pk_add_f32 v[94:95], v[94:95], v[166:167]\n\t"
    "v_cvt_pk_bf16_f32 v40, v88, v89\n\t"
    "v_cvt_pk_bf16_f32 v41, v90, v91\n\t"
    "v_cvt_pk_bf16_f32 v42, v92, v93\n\t"
    "v_cvt_pk_bf16_f32 v43, v94, v95\n\t"
    "s_nop 0\n\t"
    "v_permlane16_swap_b32 v40, v42\n\t"
    "v_permlane16_swap_b32 v41, v43\n\t"
    "buffer_store_dwordx4 v[40:43], v37, s[52:55], s67 offen offset:192\n\t"
    "s_waitcnt lgkmcnt(1)\n\t"
    "v_pk_add_f32 v[124:125], v[124:125], v[196:197]\n\t"
    "v_pk_add_f32 v[126:127], v[126:127], v[198:199]\n\t"
    "v_pk_add_f32 v[128:129], v[128:129], v[200:201]\n\t"
    "v_pk_add_f32 v[130:131], v[130:131], v[202:203]\n\t"
    "v_cvt_pk_bf16_f32 v44, v124, v125\n\t"
    "v_cvt_pk_bf16_f32 v45, v126, v127\n\t"
    "v_cvt_pk_bf16_f32 v46, v128, v129\n\t"
    "v_cvt_pk_bf16_f32 v47, v130, v131\n\t"
    "s_nop 0\n\t"
    "v_permlane16_swap_b32 v44, v46\n\t"
    "v_permlane16_swap_b32 v45, v47\n\t"
    "buffer_store_dwordx4 v[44:47], v37, s[52:55], s71 offen offset:192\n\t"
    "v_pk_add_f32 v[96:97], v[96:97], v[168:169]\n\t"
    "v_pk_add_f32 v[98:99], v[98:99], v[170:171]\n\t"
    "v_cvt_pk_bf16_f32 v40, v96, v97\n\t"
    "v_cvt_pk_bf16_f32 v41, v98, v99\n\t"
    "buffer_store_dwordx2 v[40:41], %18, s[52:55], s67 offen offset:256\n\t"
    "s_waitcnt lgkmcnt(0)\n\t"
    "v_pk_add_f32 v[132:133], v[132:133], v[204:205]\n\t"
    "v_pk_add_f32 v[134:135], v[134:135], v[206:207]\n\t"
    "v_cvt_pk_bf16_f32 v44, v132, v133\n\t"
    "v_cvt_pk_bf16_f32 v45, v134, v135\n\t"
    "buffer_store_dwordx2 v[44:45], %18, s[52:55], s71 offen offset:256\n\t"
    "; ---- end\n\t"
    "o_end_%=:\n\t"
    "s_nop 1\n\t"
    "kmain_code_end:\n\t"
      :: "s"(w), "s"(bid), "s"(uni64(wo + offT)), "s"(4096u * 1152u - offT), "s"(nst), "s"(uni64(ws)), "s"(uni64(bwo + ct * 288)), "s"(576u * 1152u - ct * 288u), "s"(sk == 3 ? 9 : 0), "s"(k0),
         "s"(uni64(out + (size_t)(rt * 128 + 32 * w) * 2304 + ct * 288)),
         "v"(l * 16), "v"(16384 + (32 * (w & 1) + 8 * (l >> 5) + 4 * ((l >> 4) & 1) + ((l >> 2) & 3)) * 288 + 8 * (l & 3)), "v"(vo[0]), "v"(vo[1]), "v"(vo[2]), "v"(vo[3]), "v"(vo[4]),
         "v"((l & 15) * 2304 + (l >> 4) * 8), "s"(tag),
         "v"(FL_CTX + ((hp >> 2) + 8 * (hp & 3) + 32 * xr) * 128), "v"(FL_CTX + (8 * (l & 31) + (rt >> 1)) * 128), "v"(FL_P + ((bid & ~3) + ((sk + 1 + l % 3) & 3)) * 128), "s"(sk) DBGOP(2)
      : "v30", "v31", "v32", "v33", "v34", "v35", "v36", "v37", "v38", "v39", "v40", "v41", "v42", "v43", "v44", "v45", "v46", "v47", "v48", "v49", "v50", "v51", "v52", "v53", "v54", "v55", "v56", "v57", "v58", "v59", "v60", "v61", "v62", "v63", "v64", "v65", "v66", "v67", "v68", "v69", "v70", "v71", "v72", "v73", "v74", "v75", "v76", "v77", "v78", "v79", "v80", "v81", "v82", "v83", "v84", "v85", "v86", "v87", "v88", "v89", "v90", "v91", "v92", "v93", "v94", "v95", "v96", "v97", "v98", "v99", "v100", "v101", "v102", "v103", "v104", "v105", "v106", "v107", "v108", "v109", "v110", "v111", "v112", "v113", "v114", "v115", "v116", "v117", "v118", "v119", "v120", "v121", "v122", "v123", "v124", "v125", "v126", "v127", "v128", "v129", "v130", "v131", "v132", "v133", "v134", "v135", "v136", "v137", "v138", "v139", "v140", "v141", "v142", "v143", "v144", "v145", "v146", "v147", "v148", "v149", "v150", "v151", "v152", "v153", "v154", "v155", "v156", "v157", "v158", "v159", "v160", "v161", "v162", "v163", "v164", "v165", "v166", "v167", "v168", "v169", "v170", "v171", "v172", "v173", "v174", "v175", "v176", "v177", "v178", "v179", "v180", "v181", "v182", "v183", "v184", "v185", "v186", "v187", "v188", "v189", "v190", "v191", "v192", "v193", "v194", "v195", "v196", "v197", "v198", "v199", "v200", "v201", "v202", "v203", "v204", "v205", "v206", "v207", "a0", "a1", "a2", "a3", "a4", "a5", "a6", "a7", "a8", "a9", "a10", "a11", "a12", "a13", "a14", "a15", "a16", "a17", "a18", "a19", "a20", "a21", "a22", "a23", "a24", "a25", "a26", "a27", "a28", "a29", "a30", "a31", "a32", "a33", "a34", "a35", "a36", "a37", "a38", "a39", "a40", "a41", "a42", "a43", "a44", "a45", "a46", "a47", "a48", "a49", "a50", "a51", "a52", "a53", "a54", "a55", "a56", "a57", "a58", "a59", "a60", "a61", "a62", "a63", "a64", "a65", "a66", "a67", "a68", "a69", "a70", "a71", "a72", "a73", "a74", "a75", "a76", "a77", "a78", "a79", "a80", "a81", "a82", "a83", "a84", "a85", "a86", "a87", "a88", "a89", "a90", "a91", "a92", "a93", "a94", "a95", "a96", "a97", "a98", "a99", "a100", "a101", "a102", "a103", "s40", "s41", "s42", "s43", "s44", "s45", "s46", "s47", "s48", "s49", "s50", "s51", "s52", "s53", "s54", "s55", "s56", "s57", "s58", "s59", "s60", "s61", "s62", "s63", "s64", "s65", "s66", "s67", "s68", "s69", "s70", "s71", "s72", "s73", "s74", "s75", "s76", "s77", "s78", "s79", "s80", "s81", "s82", "s83", "vcc", "scc", "memory"); }
  if (bid == 3 && w == 0) {
    const int l = fresh_lane(); gu64* f = (gu64*)(ws + WS_FLAG + FL_KV + l * 128);
    for (int i = 0; i < (1 << 18); ++i) {
      const u64 a = __hip_atomic_load(f, RLX_AGENT), b = __hip_atomic_load(f + 1024, RLX_AGENT), c = __hip_atomic_load(f + 2048, RLX_AGENT), d = __hip_atomic_load(f + 3072, RLX_AGENT);
      if (__all(a == tag && b == tag && c == tag && d == tag)) break;
      __builtin_amdgcn_s_sleep(2);
    }
    if (l == 0) __hip_atomic_store((gu64*)(ws + WS_FLAG + FL_SEED), tag, RLX_AGENT);
  }
}

extern "C" void kernel_launch(void* const* d_in, const int* in_sizes, int n_in, void* d_out, int out_size, void* d_ws, size_t ws_size, hipStream_t stream) {
  const char *x = (const char*)d_in[0], *ckv = (const char*)d_in[1], *wq = (const char*)d_in[2], *wuk = (const char*)d_in[3], *wuv = (const char*)d_in[4], *wo = (const char*)d_in[5];
  const char *bx = (const char*)d_in[6], *bwqkv = (const char*)d_in[7], *bwphi = (const char*)d_in[8]; const char* bwo = (const char*)d_in[9];
  char* ws = (char*)d_ws;
  (void)hipFuncSetAttribute((const void*)k_main, hipFuncAttributeMaxDynamicSharedMemorySize, 163840);
  k_main<<<256, 256, 163840, stream>>>(x, ckv, wq, wuk, wuv, wo, bx, bwqkv, bwphi, bwo, ws, (char*)d_out);
}
```
